```python
import torch

def _softmax(x, axis=-1):
    return torch.softmax(x, dim=axis)
def _gelu(x):
    return torch.nn.functional.gelu(x, approximate="tanh")
def _silu(x):
    return torch.nn.functional.silu(x)
def _sigmoid(x):
    return torch.sigmoid(x)
def _layernorm(x, axis=-1, eps=1e-5):
    m = x.mean(dim=axis, keepdim=True)
    v = x.var(dim=axis, keepdim=True, unbiased=False)
    return (x - m) / torch.sqrt(v + eps)
def _scatter_add(tgt, idx, src, axis=0):
    return tgt.clone().index_add_(axis, idx, src)
def _segment_sum(src, seg, n):
    out = torch.zeros((n,) + src.shape[1:], dtype=src.dtype)
    return out.index_add_(0, seg, src)
def _pad(x, pw):
    flat = tuple(v for pair in reversed(pw) for v in pair)
    return torch.nn.functional.pad(x, flat)

kernel_name = 'concat_heads__gqa_block_cached__gqa_block_cached__near_ridge'

def setup_inputs():

    _w = (lambda a: (lambda t: t.to(torch.bfloat16) if t.is_floating_point() else t)(torch.as_tensor(a)))
    def _A_setup():
        B, Sq, D = 16, 128, 448
        Hq, Hkv, Sk, Dh = 16, 8, 1024, 256
        assert Hq % Hkv == 0, (Hq, Hkv)
        return {
            "x": _w((lambda *s: torch.randn(*s, dtype=torch.bfloat16) * 0.8)(B, Sq, D)),
            "k_cache": _w((lambda *s: torch.randn(*s, dtype=torch.bfloat16) * 0.8)(B, Hkv, Sk, Dh)),
            "v_cache": _w((lambda *s: torch.randn(*s, dtype=torch.bfloat16) * 0.05)(B, Hkv, Sk, Dh)),
            "wq": _w((lambda *s: torch.randn(*s, dtype=torch.bfloat16) * 0.8)(D, Hq * Dh) * (D ** -0.5)),
            "wo": _w((lambda *s: torch.randn(*s, dtype=torch.bfloat16) * 0.05)(Hq * Dh, D) * ((Hq * Dh) ** -0.5)),
        }
    _A_in = _A_setup()

    def _B_setup():
        B, Sq, D = 16, 128, 448
        Hq, Hkv, Sk, Dh = 16, 8, 1024, 256
        assert Hq % Hkv == 0, (Hq, Hkv)
        return {
            "x": _w((lambda *s: torch.randn(*s, dtype=torch.bfloat16) * 0.8)(B, Sq, D)),
            "k_cache": _w((lambda *s: torch.randn(*s, dtype=torch.bfloat16) * 0.8)(B, Hkv, Sk, Dh)),
            "v_cache": _w((lambda *s: torch.randn(*s, dtype=torch.bfloat16) * 0.05)(B, Hkv, Sk, Dh)),
            "wq": _w((lambda *s: torch.randn(*s, dtype=torch.bfloat16) * 0.8)(D, Hq * Dh) * (D ** -0.5)),
            "wo": _w((lambda *s: torch.randn(*s, dtype=torch.bfloat16) * 0.05)(Hq * Dh, D) * ((Hq * Dh) ** -0.5)),
        }
    _B_in = _B_setup()

    return {
        "A_x": _A_in["x"],
        "A_k_cache": _A_in["k_cache"],
        "A_v_cache": _A_in["v_cache"],
        "A_wq": _A_in["wq"],
        "A_wo": _A_in["wo"],
        "B_x": _B_in["x"],
        "B_k_cache": _B_in["k_cache"],
        "B_v_cache": _B_in["v_cache"],
        "B_wq": _B_in["wq"],
        "B_wo": _B_in["wo"],
    }

def reference(A_x, A_k_cache, A_v_cache, A_wq, A_wo, B_x, B_k_cache, B_v_cache, B_wq, B_wo):
    def _A_ref(x, k_cache, v_cache, wq, wo):
        B, Sq, D = x.shape
        Hq, Dh = 16, 256
        Hkv = k_cache.shape[1]
        rep = Hq // Hkv
        q = (lambda x, *d: x.permute(*d))((x @ wq).reshape(B, Sq, Hq, Dh), 0, 2, 1, 3)
        k = (lambda x, n, axis: torch.repeat_interleave(x, n, dim=axis))(k_cache, rep, axis=1)
        v = (lambda x, n, axis: torch.repeat_interleave(x, n, dim=axis))(v_cache, rep, axis=1)
        scores = torch.einsum("bhqd,bhkd->bhqk", q, k) * (Dh ** -0.5)
        p = _softmax(scores, axis=-1)
        o = torch.einsum("bhqk,bhkd->bhqd", p, v)
        return (lambda x, *d: x.permute(*d))(o, 0, 2, 1, 3).reshape(B, Sq, Hq * Dh) @ wo

    def _B_ref(x, k_cache, v_cache, wq, wo):
        B, Sq, D = x.shape
        Hq, Dh = 16, 256
        Hkv = k_cache.shape[1]
        rep = Hq // Hkv
        q = (lambda x, *d: x.permute(*d))((x @ wq).reshape(B, Sq, Hq, Dh), 0, 2, 1, 3)
        k = (lambda x, n, axis: torch.repeat_interleave(x, n, dim=axis))(k_cache, rep, axis=1)
        v = (lambda x, n, axis: torch.repeat_interleave(x, n, dim=axis))(v_cache, rep, axis=1)
        scores = torch.einsum("bhqd,bhkd->bhqk", q, k) * (Dh ** -0.5)
        p = _softmax(scores, axis=-1)
        o = torch.einsum("bhqk,bhkd->bhqd", p, v)
        return (lambda x, *d: x.permute(*d))(o, 0, 2, 1, 3).reshape(B, Sq, Hq * Dh) @ wo

    a = _A_ref(A_x, A_k_cache, A_v_cache, A_wq, A_wo)
    b = _B_ref(B_x, B_k_cache, B_v_cache, B_wq, B_wo)
    return torch.concatenate([a, b], axis=-1)
```

```cpp
#include <hip/hip_runtime.h>
#include <hip/hip_bf16.h>
typedef unsigned short u16;
__device__ __forceinline__ float b2f(u16 x){ return __uint_as_float(((unsigned)x)<<16); }
__device__ __forceinline__ u16 f2b(float f){ unsigned u=__float_as_uint(f); u += 0x7fffu + ((u>>16)&1u); return (u16)(u>>16); }
__global__ void gemm(const u16* A, const u16* B, u16* C, int M, int N, int K, int ldc){
  __shared__ float As[16][17], Bs[16][17];
  int tx=threadIdx.x, ty=threadIdx.y; int row=blockIdx.y*16+ty, col=blockIdx.x*16+tx;
  float acc=0;
  for(int k0=0;k0<K;k0+=16){
    As[ty][tx]=b2f(A[(size_t)row*K+k0+tx]); Bs[ty][tx]=b2f(B[(size_t)(k0+ty)*N+col]);
    __syncthreads();
    for(int k=0;k<16;k++) acc+=As[ty][k]*Bs[k][tx];
    __syncthreads();
  }
  C[(size_t)row*ldc+col]=f2b(acc);
}
__global__ void attn(const u16* Q, const u16* Kc, const u16* Vc, u16* O){
  int q=blockIdx.x, h=blockIdx.y, b=blockIdx.z; int t=threadIdx.x;
  __shared__ float qs[256], s[1024], red[256];
  qs[t]=b2f(Q[((size_t)(b*128+q))*4096+h*256+t]); __syncthreads();
  const u16* Kb=Kc+((size_t)(b*8+h/2))*1024*256; const u16* Vb=Vc+((size_t)(b*8+h/2))*1024*256;
  float mx=-1e30f;
  for(int j=t;j<1024;j+=256){ float a=0; for(int d=0;d<256;d++) a+=qs[d]*b2f(Kb[(size_t)j*256+d]); a*=0.0625f; s[j]=a; mx=fmaxf(mx,a);}
  red[t]=mx; __syncthreads(); for(int o=128;o>0;o>>=1){ if(t<o) red[t]=fmaxf(red[t],red[t+o]); __syncthreads(); } mx=red[0]; __syncthreads();
  float sum=0; for(int j=t;j<1024;j+=256){ float e=__expf(s[j]-mx); s[j]=e; sum+=e; }
  red[t]=sum; __syncthreads(); for(int o=128;o>0;o>>=1){ if(t<o) red[t]+=red[t+o]; __syncthreads(); } sum=red[0];
  float a=0; for(int j=0;j<1024;j++) a+=s[j]*b2f(Vb[(size_t)j*256+t]);
  O[((size_t)(b*128+q))*4096+h*256+t]=f2b(a/sum);
}
extern "C" void kernel_launch(void* const* d_in, const int* in_sizes, int n_in, void* d_out, int out_size, void* d_ws, size_t ws_size, hipStream_t stream){
  for(int p=0;p<2;p++){
    const u16* x=(const u16*)d_in[5*p+0]; const u16* k=(const u16*)d_in[5*p+1]; const u16* v=(const u16*)d_in[5*p+2];
    const u16* wq=(const u16*)d_in[5*p+3]; const u16* wo=(const u16*)d_in[5*p+4];
    u16* Q=(u16*)d_ws + (size_t)p*2*2048*4096; u16* O=Q+(size_t)2048*4096;
    gemm<<<dim3(4096/16,2048/16),dim3(16,16),0,stream>>>(x,wq,Q,2048,4096,448,4096);
    attn<<<dim3(128,16,16),256,0,stream>>>(Q,k,v,O);
    gemm<<<dim3(448/16,2048/16),dim3(16,16),0,stream>>>(O,wo,(u16*)d_out+p*448,2048,448,4096,896);
  }
}
```

```cpp
#include <hip/hip_runtime.h>
#include <hip/hip_bf16.h>

extern "C" __global__ __launch_bounds__(256, 1) __attribute__((aligned(4096))) void concat_heads__gqa_block_cached__gqa_block_cached__near_ridge_kernel(
    const void* ax, const void* awq, const void* bx, const void* bwq, const void* ak, const void* av,
    const void* awo, void* ws, const void* bk, const void* bv, const void* bwo, void* out) {
  __shared__ __attribute__((aligned(16))) char lds[163840];
  unsigned tid = threadIdx.x;
  asm volatile(
"s_memrealtime s[98:99]\n"
"s_and_b32 s34, %2, 7\n"
"s_lshr_b32 s35, %2, 3\n"
"s_lshr_b32 s74, s34, 2\n"
"s_lshl_b32 s75, s74, 4\n"
"s_load_dwordx4 s[76:79], %1, s75\n"
"v_lshrrev_b32 v1, 6, v0\n"
"v_and_b32 v207, 63, v0\n"
"v_readfirstlane_b32 s70, v1\n"
"s_and_b32 s88, s35, 1\n"
"s_lshr_b32 s89, s35, 1\n"
"s_bfe_u32 s35, s34, 2\n"
"s_lshl_b32 s35, s35, 4\n"
"s_add_u32 s35, s35, s89\n"
"s_and_b32 s34, s34, 3\n"
"s_lshl_b32 s34, s34, 1\n"
"s_add_u32 s34, s34, s88\n"
"s_mul_i32 s90, s88, 3\n"
"s_min_u32 s90, s90, 6\n"
"s_lshl_b32 s36, s90, 19\n"
"s_add_u32 s37, s36, 524288\n"
"s_cmp_eq_u32 s37, 3670016\n"
"s_cselect_b32 s37, 0, s37\n"
"s_add_u32 s38, s37, 524288\n"
"s_cmp_eq_u32 s38, 3670016\n"
"s_cselect_b32 s38, 0, s38\n"
"s_add_u32 s39, s38, 524288\n"
"s_cmp_eq_u32 s39, 3670016\n"
"s_cselect_b32 s39, 0, s39\n"
"s_add_u32 s40, s39, 524288\n"
"s_cmp_eq_u32 s40, 3670016\n"
"s_cselect_b32 s40, 0, s40\n"
"s_add_u32 s41, s40, 524288\n"
"s_cmp_eq_u32 s41, 3670016\n"
"s_cselect_b32 s41, 0, s41\n"
"s_add_u32 s42, s41, 524288\n"
"s_cmp_eq_u32 s42, 3670016\n"
"s_cselect_b32 s42, 0, s42\n"
"s_lshl_b32 s60, s70, 12\n"
"s_mul_i32 s61, s70, 21504\n"
"v_bfe_u32 v2, v207, 2, 3\n"
"v_lshl_add_u32 v3, v1, 3, v2\n"
"v_lshrrev_b32 v4, 5, v207\n"
"v_lshlrev_b32 v4, 6, v4\n"
"v_and_b32 v5, 3, v207\n"
"v_and_b32 v6, 1, v1\n"
"v_xor_b32 v6, v6, v5\n"
"v_lshl_add_u32 v6, v6, 4, v4\n"
"v_lshrrev_b32 v7, 2, v2\n"
"v_lshlrev_b32 v7, 1, v7\n"
"v_xor_b32 v7, v7, v5\n"
"v_lshl_add_u32 v7, v7, 4, v4\n"
"v_lshl_add_u32 v247, v3, 9, v6\n"
"v_lshl_add_u32 v248, v3, 9, v7\n"
"v_lshl_add_u32 v204, v3, 13, v7\n"
"v_add_u32 v205, 262144, v204\n"
"v_mul_u32_u24 v8, 896, v3\n"
"v_add_u32 v249, v8, v7\n"
"v_bfe_u32 v2, v207, 1, 3\n"
"v_and_b32 v3, 1, v207\n"
"v_lshlrev_b32 v3, 7, v3\n"
"v_lshl_add_u32 v242, v2, 13, v3\n"
"s_lshl_b32 s75, s74, 5\n"
"s_add_u32 s75, s75, 32\n"
"s_add_u32 s90, s75, 8\n"
"s_add_u32 s91, s75, 16\n"
"s_waitcnt lgkmcnt(0)\n"
"s_load_dwordx2 s[8:9], %1, s75\n"
"s_load_dwordx2 s[12:13], %1, s90\n"
"s_load_dwordx2 s[20:21], %1, s91\n"
"s_load_dwordx2 s[28:29], %1, 0x38\n"
"s_load_dwordx2 s[46:47], %1, 0x58\n"
"s_mul_i32 s88, s35, 114688\n"
"s_add_u32 s24, s76, s88\n"
"s_addc_u32 s25, s77, 0\n"
"s_and_b32 s25, s25, 0xffff\n"
"s_mov_b32 s26, 114688\n"
"s_mov_b32 s27, 0x20000\n"
"s_lshl_b32 s88, s34, 10\n"
"s_sub_u32 s91, 3670016, s88\n"
"s_add_u32 s16, s78, s88\n"
"s_addc_u32 s17, s79, 0\n"
"s_and_b32 s17, s17, 0xffff\n"
"s_mov_b32 s18, s91\n"
"s_mov_b32 s19, 0x20000\n"
"s_lshl_b32 s5, s89, 2\n"
"s_add_u32 s5, s5, s70\n"
"s_lshl_b32 s5, s5, 0\n"
"s_mul_i32 s66, s5, 57344\n"
"s_lshr_b32 s65, s36, 12\n"
"s_add_u32 s65, s65, s61\n"
"s_add_u32 m0, s60, 0\n"
"s_nop 0\n"
"buffer_load_dwordx4 v249, s[24:27], s65 offen lds\n"
"s_add_u32 s65, s65, 7168\n"
"s_add_u32 m0, s60, 1024\n"
"s_nop 0\n"
"buffer_load_dwordx4 v249, s[24:27], s65 offen lds\n"
"s_add_u32 s65, s65, 7168\n"
"s_add_u32 m0, s60, 2048\n"
"s_nop 0\n"
"buffer_load_dwordx4 v249, s[24:27], s65 offen lds\n"
"s_add_u32 s65, s65, 7168\n"
"s_add_u32 m0, s60, 3072\n"
"s_nop 0\n"
"buffer_load_dwordx4 v249, s[24:27], s65 offen lds\n"
"s_add_u32 m0, s60, 32768\n"
"s_nop 0\n"
"buffer_load_dwordx4 v204, s[16:19], s36 offen lds\n"
"s_add_u32 m0, s60, 33664\n"
"s_nop 0\n"
"buffer_load_dwordx4 v204, s[16:19], s36 offen offset:128 lds\n"
"s_add_u32 m0, s60, 34816\n"
"s_nop 0\n"
"buffer_load_dwordx4 v205, s[16:19], s36 offen lds\n"
"s_add_u32 m0, s60, 35712\n"
"s_nop 0\n"
"buffer_load_dwordx4 v205, s[16:19], s36 offen offset:128 lds\n"
"buffer_load_dword v255, v242, s[16:19], s66 offen offset:0\n"
"s_mov_b32 s4, s34\n"
"s_add_u32 s52, s60, 0\n"
"s_add_u32 s56, s60, 16384\n"
"s_add_u32 s53, s60, 896\n"
"s_add_u32 s57, s60, 17280\n"
"s_add_u32 s54, s60, 1792\n"
"s_add_u32 s58, s60, 18176\n"
"s_add_u32 s55, s60, 2688\n"
"s_add_u32 s59, s60, 19072\n"
"s_mov_b32 s67, 0x3db8aa3b\n"
"s_mov_b64 s[68:69], 0\n"
"s_mov_b64 s[44:45], 0\n"
"s_mov_b32 s71, 0\n"
"s_mov_b32 s48, 0\n"
"s_mov_b64 s[50:51], 0\n"
"s_mov_b32 s43, 0\n"
"s_mov_b32 s63, 0\n"
"s_mov_b32 s64, 131072\n"
"s_getpc_b64 s[76:77]\n"
"CS_%=:\n"
"s_and_b32 s77, s77, 0xffff\n"
"s_mov_b32 s78, CE_%=-CS_%=\n"
"s_mov_b32 s79, 0x20000\n"
"s_lshr_b32 s72, %2, 3\n"
"s_and_b32 s90, s72, 3\n"
"s_lshl_b32 s90, s90, 7\n"
"s_lshr_b32 s72, s72, 2\n"
"s_lshl_b32 s72, s72, 14\n"
"s_add_u32 s72, s72, s90\n"
"buffer_load_dword v255, v247, s[76:79], s72 offen offset:0\n"
"v_and_b32 v241, 7, v207\n"
"s_lshl_b32 s6, s4, 11\n"
"s_add_u32 s6, s6, 917504\n"
"v_lshl_add_u32 v240, v241, 8, s6\n"
"s_lshl_b32 s7, s4, 8\n"
"s_add_u32 s7, s7, 917504\n"
"v_lshl_add_u32 v241, v241, 11, s7\n"
"v_lshrrev_b32 v2, 3, v207\n"
"v_add_u32 v2, s70, v2\n"
"v_lshl_add_u32 v241, v2, 21, v241\n"
"v_bfe_u32 v2, v207, 3, 1\n"
"v_bfe_u32 v3, v207, 4, 1\n"
"v_lshrrev_b32 v4, 5, v207\n"
"v_lshl_add_u32 v5, v3, 1, v4\n"
"v_xor_b32 v5, v5, v2\n"
"v_and_b32 v6, 7, v207\n"
"v_lshlrev_b32 v6, 6, v6\n"
"v_lshl_add_u32 v7, v5, 4, v6\n"
"v_lshl_add_u32 v250, v2, 12, v7\n"
"v_mov_b32 v244, v250\n"
"v_bfe_u32 v7, v207, 2, 3\n"
"v_lshlrev_b32 v7, 6, v7\n"
"v_lshl_add_u32 v7, v3, 5, v7\n"
"v_and_b32 v8, 3, v207\n"
"v_lshl_add_u32 v7, v8, 3, v7\n"
"v_lshl_add_u32 v251, v4, 12, v7\n"
"v_add_u32 v192, 0, v251\n"
"v_xor_b32 v193, 32, v192\n"
"v_add_u32 v194, 65536, v251\n"
"v_xor_b32 v195, 32, v194\n"
"v_add_u32 v196, 131072, v251\n"
"v_xor_b32 v197, 32, v196\n"
"v_bfe_u32 v8, v207, 2, 1\n"
"v_lshlrev_b32 v8, 1, v8\n"
"v_xor_b32 v8, v8, v4\n"
"v_lshl_add_u32 v8, v8, 4, v6\n"
"v_lshl_add_u32 v8, v3, 3, v8\n"
"v_lshl_add_u32 v8, v2, 10, v8\n"
"v_add_u32 v8, s60, v8\n"
"v_add_u32 v198, 0, v8\n"
"v_xor_b32 v199, 32, v198\n"
"v_add_u32 v200, 65536, v8\n"
"v_xor_b32 v201, 32, v200\n"
"v_add_u32 v202, 131072, v8\n"
"v_xor_b32 v203, 32, v202\n"
"s_lshr_b32 s65, s37, 12\n"
"s_add_u32 s65, s65, s61\n"
"s_add_u32 m0, s60, 49152\n"
"s_nop 0\n"
"buffer_load_dwordx4 v249, s[24:27], s65 offen lds\n"
"s_add_u32 s65, s65, 7168\n"
"s_add_u32 m0, s60, 50176\n"
"s_nop 0\n"
"buffer_load_dwordx4 v249, s[24:27], s65 offen lds\n"
"s_add_u32 s65, s65, 7168\n"
"s_add_u32 m0, s60, 51200\n"
"s_nop 0\n"
"buffer_load_dwordx4 v249, s[24:27], s65 offen lds\n"
"s_add_u32 s65, s65, 7168\n"
"s_add_u32 m0, s60, 52224\n"
"s_nop 0\n"
"buffer_load_dwordx4 v249, s[24:27], s65 offen lds\n"
"s_add_u32 m0, s60, 65536\n"
"s_nop 0\n"
"buffer_load_dwordx4 v204, s[16:19], s37 offen lds\n"
"s_add_u32 m0, s60, 66432\n"
"s_nop 0\n"
"buffer_load_dwordx4 v204, s[16:19], s37 offen offset:128 lds\n"
"s_add_u32 m0, s60, 67584\n"
"s_nop 0\n"
"buffer_load_dwordx4 v205, s[16:19], s37 offen lds\n"
"s_add_u32 m0, s60, 68480\n"
"s_nop 0\n"
"buffer_load_dwordx4 v205, s[16:19], s37 offen offset:128 lds\n"
"s_waitcnt lgkmcnt(0) \n"
"s_waitcnt vmcnt(10)\n"
"s_barrier\n"
"ds_read_b64 a[0:1], v198 offset:0\n"
"ds_read_b64 a[2:3], v199 offset:0\n"
"ds_read_b64 a[4:5], v198 offset:2048\n"
"ds_read_b64 a[6:7], v199 offset:2048\n"
"ds_read_b64 a[8:9], v198 offset:512\n"
"ds_read_b64 a[10:11], v199 offset:512\n"
"ds_read_b64 a[12:13], v198 offset:2560\n"
"ds_read_b64 a[14:15], v199 offset:2560\n"
"ds_read_b64_tr_b16 v[208:209], v192 offset:32768\n"
"ds_read_b64_tr_b16 v[210:211], v192 offset:40960\n"
"ds_read_b64_tr_b16 v[212:213], v192 offset:34816\n"
"ds_read_b64_tr_b16 v[214:215], v192 offset:43008\n"
"ds_read_b64_tr_b16 v[216:217], v193 offset:32768\n"
"ds_read_b64_tr_b16 v[218:219], v193 offset:40960\n"
"ds_read_b64_tr_b16 v[220:221], v193 offset:34816\n"
"s_waitcnt lgkmcnt(14)\n"
"ds_read_b64_tr_b16 v[222:223], v193 offset:43008\n"
"s_waitcnt lgkmcnt(14)\n"
"ds_read_b64_tr_b16 v[224:225], v192 offset:33280\n"
"s_waitcnt lgkmcnt(14)\n"
"ds_read_b64_tr_b16 v[226:227], v192 offset:41472\n"
"s_waitcnt lgkmcnt(14)\n"
"ds_read_b64_tr_b16 v[228:229], v192 offset:35328\n"
"s_waitcnt lgkmcnt(14)\n"
"ds_read_b64_tr_b16 v[230:231], v192 offset:43520\n"
"s_waitcnt lgkmcnt(8)\n"
"v_mfma_f32_16x16x32_bf16 v[64:67], v[208:211], a[0:3], 0\n"
"s_lshr_b32 s65, s38, 12\n"
"s_add_u32 s65, s65, s61\n"
"ds_read_b64_tr_b16 v[232:233], v193 offset:33280\n"
"v_mfma_f32_16x16x32_bf16 v[64:67], v[212:215], a[8:11], v[64:67]\n"
"s_add_u32 m0, s60, 81920\n"
"ds_read_b64_tr_b16 v[234:235], v193 offset:41472\n"
"v_mfma_f32_16x16x32_bf16 v[68:71], v[212:215], a[12:15], 0\n"
"buffer_load_dwordx4 v249, s[24:27], s65 offen lds\n"
"v_mfma_f32_16x16x32_bf16 v[68:71], v[208:211], a[4:7], v[68:71]\n"
"s_waitcnt lgkmcnt(6)\n"
"s_add_u32 s65, s65, 7168\n"
"ds_read_b64_tr_b16 v[236:237], v193 offset:35328\n"
"ds_read_b64_tr_b16 v[238:239], v193 offset:43520\n"
"v_mfma_f32_16x16x32_bf16 v[76:79], v[216:219], a[4:7], 0\n"
"s_add_u32 m0, s60, 82944\n"
"ds_read_b64_tr_b16 v[208:209], v192 offset:33792\n"
"v_mfma_f32_16x16x32_bf16 v[76:79], v[220:223], a[12:15], v[76:79]\n"
"buffer_load_dwordx4 v249, s[24:27], s65 offen lds\n"
"v_mfma_f32_16x16x32_bf16 v[72:75], v[220:223], a[8:11], 0\n"
"s_add_u32 s65, s65, 7168\n"
"s_add_u32 m0, s60, 83968\n"
"ds_read_b64_tr_b16 v[210:211], v192 offset:41984\n"
"ds_read_b64_tr_b16 v[212:213], v192 offset:35840\n"
"v_mfma_f32_16x16x32_bf16 v[72:75], v[216:219], a[0:3], v[72:75]\n"
"s_waitcnt lgkmcnt(7)\n"
"buffer_load_dwordx4 v249, s[24:27], s65 offen lds\n"
"v_mfma_f32_16x16x32_bf16 v[80:83], v[224:227], a[0:3], 0\n"
"s_add_u32 s65, s65, 7168\n"
"s_add_u32 m0, s60, 84992\n"
"ds_read_b64_tr_b16 v[214:215], v192 offset:44032\n"
"ds_read_b64_tr_b16 v[216:217], v193 offset:33792\n"
"v_mfma_f32_16x16x32_bf16 v[80:83], v[228:231], a[8:11], v[80:83]\n"
"buffer_load_dwordx4 v249, s[24:27], s65 offen lds\n"
"v_mfma_f32_16x16x32_bf16 v[84:87], v[228:231], a[12:15], 0\n"
"s_add_u32 m0, s60, 98304\n"
"ds_read_b64_tr_b16 v[218:219], v193 offset:41984\n"
"ds_read_b64_tr_b16 v[220:221], v193 offset:35840\n"
"v_mfma_f32_16x16x32_bf16 v[84:87], v[224:227], a[4:7], v[84:87]\n"
"s_waitcnt lgkmcnt(7)\n"
"buffer_load_dwordx4 v204, s[16:19], s38 offen lds\n"
"v_mfma_f32_16x16x32_bf16 v[92:95], v[232:235], a[4:7], 0\n"
"s_add_u32 m0, s60, 99200\n"
"ds_read_b64_tr_b16 v[222:223], v193 offset:44032\n"
"ds_read_b64_tr_b16 v[224:225], v192 offset:34304\n"
"v_mfma_f32_16x16x32_bf16 v[92:95], v[236:239], a[12:15], v[92:95]\n"
"buffer_load_dwordx4 v204, s[16:19], s38 offen offset:128 lds\n"
"v_mfma_f32_16x16x32_bf16 v[88:91], v[236:239], a[8:11], 0\n"
"s_add_u32 m0, s60, 100352\n"
"ds_read_b64_tr_b16 v[226:227], v192 offset:42496\n"
"ds_read_b64_tr_b16 v[228:229], v192 offset:36352\n"
"v_mfma_f32_16x16x32_bf16 v[88:91], v[232:235], a[0:3], v[88:91]\n"
"s_waitcnt lgkmcnt(7)\n"
"buffer_load_dwordx4 v205, s[16:19], s38 offen lds\n"
"v_mfma_f32_16x16x32_bf16 v[96:99], v[208:211], a[0:3], 0\n"
"s_mov_b64 exec, 0\n"
"s_cmp_eq_u32 s70, 0\n"
"s_cbranch_scc1 IWA0_%=\n"
"s_cmp_eq_u32 s70, 1\n"
"s_cbranch_scc1 IWA1_%=\n"
"s_cmp_eq_u32 s70, 2\n"
"s_cbranch_scc1 IWA2_%=\n"
"s_branch IWA3_%=\n"
"IRA_%=:\n"
"s_mov_b64 exec, -1\n"
"s_nop 1\n"
"s_lshl_b32 s88, s35, 3\n"
"s_add_u32 s88, s88, s34\n"
"s_lshl_b32 s91, s88, 19\n"
"s_add_u32 s8, s8, s91\n"
"s_addc_u32 s9, s9, 0\n"
"s_and_b32 s9, s9, 0xffff\n"
"s_mov_b32 s10, 524288\n"
"s_mov_b32 s11, 0x20000\n"
"s_add_u32 s12, s12, s91\n"
"s_addc_u32 s13, s13, 0\n"
"s_and_b32 s13, s13, 0xffff\n"
"s_mov_b32 s14, 524288\n"
"s_mov_b32 s15, 0x20000\n"
"s_lshl_b32 s91, s74, 2\n"
"s_lshr_b32 s90, s35, 2\n"
"s_add_u32 s91, s91, s90\n"
"s_add_u32 s91, s91, s88\n"
"s_mul_i32 s91, s91, 1\n"
"s_and_b32 s91, s91, 31\n"
"s_lshl_b32 s91, s91, 14\n"
"s_mov_b32 s73, s91\n"
"s_mul_i32 s91, s34, 458752\n"
"s_add_u32 s20, s20, s91\n"
"s_addc_u32 s21, s21, 0\n"
"s_and_b32 s21, s21, 0xffff\n"
"s_mov_b32 s22, 458752\n"
"s_mov_b32 s23, 0x20000\n"
"s_lshl_b32 s91, s74, 4\n"
"s_add_u32 s91, s91, s35\n"
"s_mul_i32 s91, s91, 1196032\n"
"s_add_u32 s28, s28, s91\n"
"s_addc_u32 s29, s29, 0\n"
"s_and_b32 s29, s29, 0xffff\n"
"s_mov_b32 s30, 1196032\n"
"s_mov_b32 s31, 0x20000\n"
"s_mul_i32 s91, s88, 28672\n"
"s_mul_i32 s90, s74, 896\n"
"s_add_u32 s91, s91, s90\n"
"s_add_u32 s46, s46, s91\n"
"s_addc_u32 s47, s47, 0\n"
"s_mov_b32 s99, s46\n"
"s_mov_b32 s33, s47\n"
"s_add_u32 s62, s73, 81920\n"
"s_and_b32 s62, s62, 524287\n"
"s_add_u32 s34, s73, 65536\n"
"s_and_b32 s34, s34, 524287\n"
"s_waitcnt vmcnt(7)\n"
"s_barrier ; mid\n"
"v_mfma_f32_16x16x32_bf16 v[96:99], v[212:215], a[8:11], v[96:99]\n"
"s_add_u32 m0, s60, 101248\n"
"ds_read_b64 a[16:17], v198 offset:49152\n"
"ds_read_b64 a[18:19], v199 offset:49152\n"
"v_mfma_f32_16x16x32_bf16 v[100:103], v[212:215], a[12:15], 0\n"
"buffer_load_dwordx4 v205, s[16:19], s38 offen offset:128 lds\n"
"v_mfma_f32_16x16x32_bf16 v[100:103], v[208:211], a[4:7], v[100:103]\n"
"s_waitcnt lgkmcnt(5)\n"
"s_lshr_b32 s65, s39, 12\n"
"ds_read_b64 a[20:21], v198 offset:51200\n"
"ds_read_b64_tr_b16 v[230:231], v192 offset:44544\n"
"ds_read_b64_tr_b16 v[232:233], v193 offset:34304\n"
"v_mfma_f32_16x16x32_bf16 v[108:111], v[216:219], a[4:7], 0\n"
"s_add_u32 s65, s65, s61\n"
"s_add_u32 m0, s60, 114688\n"
"ds_read_b64 a[22:23], v199 offset:51200\n"
"ds_read_b64_tr_b16 v[234:235], v193 offset:42496\n"
"v_mfma_f32_16x16x32_bf16 v[108:111], v[220:223], a[12:15], v[108:111]\n"
"buffer_load_dwordx4 v249, s[24:27], s65 offen lds\n"
"v_mfma_f32_16x16x32_bf16 v[104:107], v[220:223], a[8:11], 0\n"
"s_add_u32 s65, s65, 7168\n"
"s_add_u32 m0, s60, 115712\n"
"ds_read_b64 a[24:25], v198 offset:49664\n"
"ds_read_b64 a[26:27], v199 offset:49664\n"
"ds_read_b64_tr_b16 v[236:237], v193 offset:36352\n"
"v_mfma_f32_16x16x32_bf16 v[104:107], v[216:219], a[0:3], v[104:107]\n"
"s_waitcnt lgkmcnt(6)\n"
"buffer_load_dwordx4 v249, s[24:27], s65 offen lds\n"
"v_mfma_f32_16x16x32_bf16 v[112:115], v[224:227], a[0:3], 0\n"
"s_add_u32 s65, s65, 7168\n"
"s_add_u32 m0, s60, 116736\n"
"ds_read_b64 a[28:29], v198 offset:51712\n"
"ds_read_b64_tr_b16 v[238:239], v193 offset:44544\n"
"ds_read_b64_tr_b16 v[208:209], v194 offset:0\n"
"v_mfma_f32_16x16x32_bf16 v[112:115], v[228:231], a[8:11], v[112:115]\n"
"buffer_load_dwordx4 v249, s[24:27], s65 offen lds\n"
"v_mfma_f32_16x16x32_bf16 v[116:119], v[228:231], a[12:15], 0\n"
"s_add_u32 s65, s65, 7168\n"
"s_add_u32 m0, s60, 117760\n"
"ds_read_b64 a[30:31], v199 offset:51712\n"
"ds_read_b64_tr_b16 v[210:211], v194 offset:8192\n"
"ds_read_b64_tr_b16 v[212:213], v194 offset:2048\n"
"v_mfma_f32_16x16x32_bf16 v[116:119], v[224:227], a[4:7], v[116:119]\n"
"s_waitcnt lgkmcnt(4)\n"
"buffer_load_dwordx4 v249, s[24:27], s65 offen lds\n"
"v_mfma_f32_16x16x32_bf16 v[124:127], v[232:235], a[4:7], 0\n"
"ds_read_b64_tr_b16 v[214:215], v194 offset:10240\n"
"ds_read_b64_tr_b16 v[216:217], v195 offset:0\n"
"ds_read_b64_tr_b16 v[218:219], v195 offset:8192\n"
"v_mfma_f32_16x16x32_bf16 v[124:127], v[236:239], a[12:15], v[124:127]\n"
"ds_read_b64_tr_b16 v[220:221], v195 offset:2048\n"
"ds_read_b64_tr_b16 v[222:223], v195 offset:10240\n"
"v_mfma_f32_16x16x32_bf16 v[120:123], v[236:239], a[8:11], 0\n"
"s_barrier\n"
"v_mfma_f32_16x16x32_bf16 v[120:123], v[232:235], a[0:3], v[120:123]\n"
"s_waitcnt lgkmcnt(4)\n"
"ds_read_b64_tr_b16 v[224:225], v194 offset:512\n"
"ds_read_b64_tr_b16 v[226:227], v194 offset:8704\n"
"v_mfma_f32_16x16x32_bf16 v[64:67], v[208:211], a[16:19], v[64:67]\n"
"s_add_u32 m0, s60, 0\n"
"ds_read_b64_tr_b16 v[228:229], v194 offset:2560\n"
"ds_read_b64_tr_b16 v[230:231], v194 offset:10752\n"
"v_mfma_f32_16x16x32_bf16 v[64:67], v[212:215], a[24:27], v[64:67]\n"
"buffer_load_dwordx4 v204, s[16:19], s39 offen lds\n"
"v_mfma_f32_16x16x32_bf16 v[68:71], v[212:215], a[28:31], v[68:71]\n"
"s_add_u32 m0, s60, 896\n"
"ds_read_b64_tr_b16 v[232:233], v195 offset:512\n"
"ds_read_b64_tr_b16 v[234:235], v195 offset:8704\n"
"v_mfma_f32_16x16x32_bf16 v[68:71], v[208:211], a[20:23], v[68:71]\n"
"s_waitcnt lgkmcnt(6)\n"
"buffer_load_dwordx4 v204, s[16:19], s39 offen offset:128 lds\n"
"v_mfma_f32_16x16x32_bf16 v[76:79], v[216:219], a[20:23], v[76:79]\n"
"s_add_u32 m0, s60, 2048\n"
"ds_read_b64_tr_b16 v[236:237], v195 offset:2560\n"
"ds_read_b64_tr_b16 v[238:239], v195 offset:10752\n"
"v_mfma_f32_16x16x32_bf16 v[76:79], v[220:223], a[28:31], v[76:79]\n"
"buffer_load_dwordx4 v205, s[16:19], s39 offen lds\n"
"v_mfma_f32_16x16x32_bf16 v[72:75], v[220:223], a[24:27], v[72:75]\n"
"ds_read_b64_tr_b16 v[208:209], v194 offset:1024\n"
"ds_read_b64_tr_b16 v[210:211], v194 offset:9216\n"
"v_mfma_f32_16x16x32_bf16 v[72:75], v[216:219], a[16:19], v[72:75]\n"
"s_waitcnt lgkmcnt(6)\n"
"s_add_u32 m0, s60, 2944\n"
"ds_read_b64_tr_b16 v[212:213], v194 offset:3072\n"
"ds_read_b64_tr_b16 v[214:215], v194 offset:11264\n"
"v_mfma_f32_16x16x32_bf16 v[80:83], v[224:227], a[16:19], v[80:83]\n"
"buffer_load_dwordx4 v205, s[16:19], s39 offen offset:128 lds\n"
"v_mfma_f32_16x16x32_bf16 v[80:83], v[228:231], a[24:27], v[80:83]\n"
"s_lshr_b32 s65, s40, 12\n"
"s_add_u32 s65, s65, s61\n"
"ds_read_b64_tr_b16 v[216:217], v195 offset:1024\n"
"ds_read_b64_tr_b16 v[218:219], v195 offset:9216\n"
"v_mfma_f32_16x16x32_bf16 v[84:87], v[228:231], a[28:31], v[84:87]\n"
"s_add_u32 m0, s60, 32768\n"
"ds_read_b64_tr_b16 v[220:221], v195 offset:3072\n"
"v_mfma_f32_16x16x32_bf16 v[84:87], v[224:227], a[20:23], v[84:87]\n"
"s_waitcnt lgkmcnt(7)\n"
"buffer_load_dwordx4 v249, s[24:27], s65 offen lds\n"
"v_mfma_f32_16x16x32_bf16 v[92:95], v[232:235], a[20:23], v[92:95]\n"
"s_add_u32 s65, s65, 7168\n"
"s_add_u32 m0, s60, 33792\n"
"ds_read_b64_tr_b16 v[222:223], v195 offset:11264\n"
"ds_read_b64_tr_b16 v[224:225], v194 offset:1536\n"
"v_mfma_f32_16x16x32_bf16 v[92:95], v[236:239], a[28:31], v[92:95]\n"
"buffer_load_dwordx4 v249, s[24:27], s65 offen lds\n"
"v_mfma_f32_16x16x32_bf16 v[88:91], v[236:239], a[24:27], v[88:91]\n"
"s_add_u32 s65, s65, 7168\n"
"s_add_u32 m0, s60, 34816\n"
"ds_read_b64_tr_b16 v[226:227], v194 offset:9728\n"
"ds_read_b64_tr_b16 v[228:229], v194 offset:3584\n"
"v_mfma_f32_16x16x32_bf16 v[88:91], v[232:235], a[16:19], v[88:91]\n"
"s_waitcnt lgkmcnt(7)\n"
"buffer_load_dwordx4 v249, s[24:27], s65 offen lds\n"
"v_mfma_f32_16x16x32_bf16 v[96:99], v[208:211], a[16:19], v[96:99]\n"
"s_mov_b64 exec, 0\n"
"s_cmp_eq_u32 s70, 0\n"
"s_cbranch_scc1 IWC0_%=\n"
"s_cmp_eq_u32 s70, 1\n"
"s_cbranch_scc1 IWC1_%=\n"
"s_cmp_eq_u32 s70, 2\n"
"s_cbranch_scc1 IWC2_%=\n"
"s_branch IWC3_%=\n"
"IRC_%=:\n"
"s_mov_b64 exec, -1\n"
"s_nop 1\n"
"s_waitcnt vmcnt(11)\n"
"s_barrier ; mid\n"
"v_mfma_f32_16x16x32_bf16 v[96:99], v[212:215], a[24:27], v[96:99]\n"
"s_add_u32 s65, s65, 7168\n"
"s_add_u32 m0, s60, 35840\n"
"ds_read_b64 a[32:33], v200 offset:16384\n"
"ds_read_b64 a[34:35], v201 offset:16384\n"
"v_mfma_f32_16x16x32_bf16 v[100:103], v[212:215], a[28:31], v[100:103]\n"
"buffer_load_dwordx4 v249, s[24:27], s65 offen lds\n"
"v_mfma_f32_16x16x32_bf16 v[100:103], v[208:211], a[20:23], v[100:103]\n"
"s_waitcnt lgkmcnt(5)\n"
"s_add_u32 m0, s60, 131072\n"
"ds_read_b64 a[36:37], v200 offset:18432\n"
"ds_read_b64 a[38:39], v201 offset:18432\n"
"ds_read_b64_tr_b16 v[230:231], v194 offset:11776\n"
"v_mfma_f32_16x16x32_bf16 v[108:111], v[216:219], a[20:23], v[108:111]\n"
"buffer_load_dwordx4 v204, s[16:19], s40 offen lds\n"
"v_mfma_f32_16x16x32_bf16 v[108:111], v[220:223], a[28:31], v[108:111]\n"
"s_add_u32 m0, s60, 131968\n"
"ds_read_b64 a[40:41], v200 offset:16896\n"
"ds_read_b64_tr_b16 v[232:233], v195 offset:1536\n"
"ds_read_b64_tr_b16 v[234:235], v195 offset:9728\n"
"v_mfma_f32_16x16x32_bf16 v[104:107], v[220:223], a[24:27], v[104:107]\n"
"buffer_load_dwordx4 v204, s[16:19], s40 offen offset:128 lds\n"
"v_mfma_f32_16x16x32_bf16 v[104:107], v[216:219], a[16:19], v[104:107]\n"
"s_waitcnt lgkmcnt(3)\n"
"s_add_u32 m0, s60, 133120\n"
"ds_read_b64 a[42:43], v201 offset:16896\n"
"ds_read_b64 a[44:45], v200 offset:18944\n"
"ds_read_b64_tr_b16 v[236:237], v195 offset:3584\n"
"v_mfma_f32_16x16x32_bf16 v[112:115], v[224:227], a[16:19], v[112:115]\n"
"buffer_load_dwordx4 v205, s[16:19], s40 offen lds\n"
"v_mfma_f32_16x16x32_bf16 v[112:115], v[228:231], a[24:27], v[112:115]\n"
"s_add_u32 m0, s60, 134016\n"
"ds_read_b64 a[46:47], v201 offset:18944\n"
"ds_read_b64_tr_b16 v[238:239], v195 offset:11776\n"
"ds_read_b64_tr_b16 v[208:209], v194 offset:32768\n"
"v_mfma_f32_16x16x32_bf16 v[116:119], v[228:231], a[28:31], v[116:119]\n"
"buffer_load_dwordx4 v205, s[16:19], s40 offen offset:128 lds\n"
"v_mfma_f32_16x16x32_bf16 v[116:119], v[224:227], a[20:23], v[116:119]\n"
"s_waitcnt lgkmcnt(1)\n"
"ds_read_b64_tr_b16 v[210:211], v194 offset:40960\n"
"ds_read_b64_tr_b16 v[212:213], v194 offset:34816\n"
"ds_read_b64_tr_b16 v[214:215], v194 offset:43008\n"
"v_mfma_f32_16x16x32_bf16 v[124:127], v[232:235], a[20:23], v[124:127]\n"
"ds_read_b64_tr_b16 v[216:217], v195 offset:32768\n"
"ds_read_b64_tr_b16 v[218:219], v195 offset:40960\n"
"v_mfma_f32_16x16x32_bf16 v[124:127], v[236:239], a[28:31], v[124:127]\n"
"ds_read_b64_tr_b16 v[220:221], v195 offset:34816\n"
"ds_read_b64_tr_b16 v[222:223], v195 offset:43008\n"
"v_mfma_f32_16x16x32_bf16 v[120:123], v[236:239], a[24:27], v[120:123]\n"
"s_barrier\n"
"v_mfma_f32_16x16x32_bf16 v[120:123], v[232:235], a[16:19], v[120:123]\n"
"s_waitcnt lgkmcnt(4)\n"
"ds_read_b64_tr_b16 v[224:225], v194 offset:33280\n"
"ds_read_b64_tr_b16 v[226:227], v194 offset:41472\n"
"v_mfma_f32_16x16x32_bf16 v[64:67], v[208:211], a[32:35], v[64:67]\n"
"s_lshr_b32 s65, s41, 12\n"
"s_add_u32 s65, s65, s61\n"
"ds_read_b64_tr_b16 v[228:229], v194 offset:35328\n"
"ds_read_b64_tr_b16 v[230:231], v194 offset:43520\n"
"v_mfma_f32_16x16x32_bf16 v[64:67], v[212:215], a[40:43], v[64:67]\n"
"s_add_u32 m0, s60, 49152\n"
"ds_read_b64_tr_b16 v[232:233], v195 offset:33280\n"
"ds_read_b64_tr_b16 v[234:235], v195 offset:41472\n"
"v_mfma_f32_16x16x32_bf16 v[68:71], v[212:215], a[44:47], v[68:71]\n"
"buffer_load_dwordx4 v249, s[24:27], s65 offen lds\n"
"v_mfma_f32_16x16x32_bf16 v[68:71], v[208:211], a[36:39], v[68:71]\n"
"s_waitcnt lgkmcnt(6)\n"
"s_add_u32 s65, s65, 7168\n"
"ds_read_b64_tr_b16 v[236:237], v195 offset:35328\n"
"ds_read_b64_tr_b16 v[238:239], v195 offset:43520\n"
"v_mfma_f32_16x16x32_bf16 v[76:79], v[216:219], a[36:39], v[76:79]\n"
"s_add_u32 m0, s60, 50176\n"
"ds_read_b64_tr_b16 v[208:209], v194 offset:33792\n"
"v_mfma_f32_16x16x32_bf16 v[76:79], v[220:223], a[44:47], v[76:79]\n"
"buffer_load_dwordx4 v249, s[24:27], s65 offen lds\n"
"v_mfma_f32_16x16x32_bf16 v[72:75], v[220:223], a[40:43], v[72:75]\n"
"s_add_u32 s65, s65, 7168\n"
"s_add_u32 m0, s60, 51200\n"
"ds_read_b64_tr_b16 v[210:211], v194 offset:41984\n"
"ds_read_b64_tr_b16 v[212:213], v194 offset:35840\n"
"v_mfma_f32_16x16x32_bf16 v[72:75], v[216:219], a[32:35], v[72:75]\n"
"s_waitcnt lgkmcnt(7)\n"
"buffer_load_dwordx4 v249, s[24:27], s65 offen lds\n"
"v_mfma_f32_16x16x32_bf16 v[80:83], v[224:227], a[32:35], v[80:83]\n"
"s_add_u32 s65, s65, 7168\n"
"s_add_u32 m0, s60, 52224\n"
"ds_read_b64_tr_b16 v[214:215], v194 offset:44032\n"
"ds_read_b64_tr_b16 v[216:217], v195 offset:33792\n"
"v_mfma_f32_16x16x32_bf16 v[80:83], v[228:231], a[40:43], v[80:83]\n"
"buffer_load_dwordx4 v249, s[24:27], s65 offen lds\n"
"v_mfma_f32_16x16x32_bf16 v[84:87], v[228:231], a[44:47], v[84:87]\n"
"s_add_u32 m0, s60, 65536\n"
"ds_read_b64_tr_b16 v[218:219], v195 offset:41984\n"
"ds_read_b64_tr_b16 v[220:221], v195 offset:35840\n"
"v_mfma_f32_16x16x32_bf16 v[84:87], v[224:227], a[36:39], v[84:87]\n"
"s_waitcnt lgkmcnt(7)\n"
"buffer_load_dwordx4 v204, s[16:19], s41 offen lds\n"
"v_mfma_f32_16x16x32_bf16 v[92:95], v[232:235], a[36:39], v[92:95]\n"
"s_add_u32 m0, s60, 66432\n"
"ds_read_b64_tr_b16 v[222:223], v195 offset:44032\n"
"ds_read_b64_tr_b16 v[224:225], v194 offset:34304\n"
"v_mfma_f32_16x16x32_bf16 v[92:95], v[236:239], a[44:47], v[92:95]\n"
"buffer_load_dwordx4 v204, s[16:19], s41 offen offset:128 lds\n"
"v_mfma_f32_16x16x32_bf16 v[88:91], v[236:239], a[40:43], v[88:91]\n"
"s_add_u32 m0, s60, 67584\n"
"ds_read_b64_tr_b16 v[226:227], v194 offset:42496\n"
"ds_read_b64_tr_b16 v[228:229], v194 offset:36352\n"
"v_mfma_f32_16x16x32_bf16 v[88:91], v[232:235], a[32:35], v[88:91]\n"
"s_waitcnt lgkmcnt(7)\n"
"buffer_load_dwordx4 v205, s[16:19], s41 offen lds\n"
"v_mfma_f32_16x16x32_bf16 v[96:99], v[208:211], a[32:35], v[96:99]\n"
"s_waitcnt vmcnt(15)\n"
"s_barrier ; mid\n"
"v_mfma_f32_16x16x32_bf16 v[96:99], v[212:215], a[40:43], v[96:99]\n"
"s_add_u32 m0, s60, 68480\n"
"ds_read_b64 a[48:49], v200 offset:49152\n"
"ds_read_b64 a[50:51], v201 offset:49152\n"
"v_mfma_f32_16x16x32_bf16 v[100:103], v[212:215], a[44:47], v[100:103]\n"
"buffer_load_dwordx4 v205, s[16:19], s41 offen offset:128 lds\n"
"v_mfma_f32_16x16x32_bf16 v[100:103], v[208:211], a[36:39], v[100:103]\n"
"s_waitcnt lgkmcnt(5)\n"
"s_lshr_b32 s65, s42, 12\n"
"ds_read_b64 a[52:53], v200 offset:51200\n"
"ds_read_b64_tr_b16 v[230:231], v194 offset:44544\n"
"ds_read_b64_tr_b16 v[232:233], v195 offset:34304\n"
"v_mfma_f32_16x16x32_bf16 v[108:111], v[216:219], a[36:39], v[108:111]\n"
"IWA0_%=:\n"
"s_cbranch_execz IRA_%=\n"
"s_add_u32 s65, s65, s61\n"
"s_add_u32 m0, s60, 147456\n"
"ds_read_b64 a[54:55], v201 offset:51200\n"
"ds_read_b64_tr_b16 v[234:235], v195 offset:42496\n"
"v_mfma_f32_16x16x32_bf16 v[108:111], v[220:223], a[44:47], v[108:111]\n"
"buffer_load_dwordx4 v249, s[24:27], s65 offen lds\n"
"v_mfma_f32_16x16x32_bf16 v[104:107], v[220:223], a[40:43], v[104:107]\n"
"s_add_u32 s65, s65, 7168\n"
"s_add_u32 m0, s60, 148480\n"
"ds_read_b64 a[56:57], v200 offset:49664\n"
"ds_read_b64 a[58:59], v201 offset:49664\n"
"ds_read_b64_tr_b16 v[236:237], v195 offset:36352\n"
"v_mfma_f32_16x16x32_bf16 v[104:107], v[216:219], a[32:35], v[104:107]\n"
"s_waitcnt lgkmcnt(6)\n"
"buffer_load_dwordx4 v249, s[24:27], s65 offen lds\n"
"v_mfma_f32_16x16x32_bf16 v[112:115], v[224:227], a[32:35], v[112:115]\n"
"s_add_u32 s65, s65, 7168\n"
"s_add_u32 m0, s60, 149504\n"
"ds_read_b64 a[60:61], v200 offset:51712\n"
"ds_read_b64_tr_b16 v[238:239], v195 offset:44544\n"
"ds_read_b64_tr_b16 v[208:209], v192 offset:0\n"
"v_mfma_f32_16x16x32_bf16 v[112:115], v[228:231], a[40:43], v[112:115]\n"
"buffer_load_dwordx4 v249, s[24:27], s65 offen lds\n"
"v_mfma_f32_16x16x32_bf16 v[116:119], v[228:231], a[44:47], v[116:119]\n"
"s_add_u32 s65, s65, 7168\n"
"s_add_u32 m0, s60, 150528\n"
"ds_read_b64 a[62:63], v201 offset:51712\n"
"ds_read_b64_tr_b16 v[210:211], v192 offset:8192\n"
"ds_read_b64_tr_b16 v[212:213], v192 offset:2048\n"
"v_mfma_f32_16x16x32_bf16 v[116:119], v[224:227], a[36:39], v[116:119]\n"
"s_waitcnt lgkmcnt(4)\n"
"buffer_load_dwordx4 v249, s[24:27], s65 offen lds\n"
"v_mfma_f32_16x16x32_bf16 v[124:127], v[232:235], a[36:39], v[124:127]\n"
"s_mul_i32 s75, s5, 57344\n"
"ds_read_b64_tr_b16 v[214:215], v192 offset:10240\n"
"ds_read_b64_tr_b16 v[216:217], v193 offset:0\n"
"ds_read_b64_tr_b16 v[218:219], v193 offset:8192\n"
"v_mfma_f32_16x16x32_bf16 v[124:127], v[236:239], a[44:47], v[124:127]\n"
"buffer_load_dword v207, v242, s[16:19], s75 offen offset:256\n"
"v_mfma_f32_16x16x32_bf16 v[120:123], v[236:239], a[40:43], v[120:123]\n"
"s_waitcnt vmcnt(13)\n"
"s_barrier\n"
"v_mfma_f32_16x16x32_bf16 v[120:123], v[232:235], a[32:35], v[120:123]\n"
"s_waitcnt lgkmcnt(2)\n"
"ds_read_b64_tr_b16 v[220:221], v193 offset:2048\n"
"ds_read_b64_tr_b16 v[222:223], v193 offset:10240\n"
"ds_read_b64_tr_b16 v[224:225], v192 offset:512\n"
"v_mfma_f32_16x16x32_bf16 v[64:67], v[208:211], a[48:51], v[64:67]\n"
"s_add_u32 m0, s60, 81920\n"
"ds_read_b64 a[64:65], v198 offset:32768\n"
"ds_read_b64_tr_b16 v[226:227], v192 offset:8704\n"
"v_mfma_f32_16x16x32_bf16 v[64:67], v[212:215], a[56:59], v[64:67]\n"
"buffer_load_dwordx4 v204, s[16:19], s42 offen lds\n"
"v_mfma_f32_16x16x32_bf16 v[68:71], v[212:215], a[60:63], v[68:71]\n"
"ds_read_b64_tr_b16 v[228:229], v192 offset:2560\n"
"ds_read_b64_tr_b16 v[230:231], v192 offset:10752\n"
"v_mfma_f32_16x16x32_bf16 v[68:71], v[208:211], a[52:55], v[68:71]\n"
"s_waitcnt lgkmcnt(5)\n"
"ds_read_b64 a[66:67], v199 offset:32768\n"
"ds_read_b64_tr_b16 v[232:233], v193 offset:512\n"
"v_mfma_f32_16x16x32_bf16 v[76:79], v[216:219], a[52:55], v[76:79]\n"
"s_add_u32 m0, s60, 82816\n"
"ds_read_b64 a[68:69], v198 offset:34816\n"
"ds_read_b64_tr_b16 v[234:235], v193 offset:8704\n"
"v_mfma_f32_16x16x32_bf16 v[76:79], v[220:223], a[60:63], v[76:79]\n"
"buffer_load_dwordx4 v204, s[16:19], s42 offen offset:128 lds\n"
"v_mfma_f32_16x16x32_bf16 v[72:75], v[220:223], a[56:59], v[72:75]\n"
"ds_read_b64_tr_b16 v[236:237], v193 offset:2560\n"
"ds_read_b64_tr_b16 v[238:239], v193 offset:10752\n"
"v_mfma_f32_16x16x32_bf16 v[72:75], v[216:219], a[48:51], v[72:75]\n"
"s_waitcnt lgkmcnt(6)\n"
"ds_read_b64_tr_b16 v[208:209], v192 offset:1024\n"
"ds_read_b64_tr_b16 v[210:211], v192 offset:9216\n"
"v_mfma_f32_16x16x32_bf16 v[80:83], v[224:227], a[48:51], v[80:83]\n"
"s_add_u32 m0, s60, 83968\n"
"ds_read_b64 a[70:71], v199 offset:34816\n"
"ds_read_b64_tr_b16 v[212:213], v192 offset:3072\n"
"v_mfma_f32_16x16x32_bf16 v[80:83], v[228:231], a[56:59], v[80:83]\n"
"buffer_load_dwordx4 v205, s[16:19], s42 offen lds\n"
"v_mfma_f32_16x16x32_bf16 v[84:87], v[228:231], a[60:63], v[84:87]\n"
"s_add_u32 m0, s60, 84864\n"
"ds_read_b64_tr_b16 v[214:215], v192 offset:11264\n"
"ds_read_b64_tr_b16 v[216:217], v193 offset:1024\n"
"v_mfma_f32_16x16x32_bf16 v[84:87], v[224:227], a[52:55], v[84:87]\n"
"s_waitcnt lgkmcnt(6)\n"
"buffer_load_dwordx4 v205, s[16:19], s42 offen offset:128 lds\n"
"v_mfma_f32_16x16x32_bf16 v[92:95], v[232:235], a[52:55], v[92:95]\n"
"ds_read_b64_tr_b16 v[218:219], v193 offset:9216\n"
"ds_read_b64_tr_b16 v[220:221], v193 offset:3072\n"
"v_mfma_f32_16x16x32_bf16 v[92:95], v[236:239], a[60:63], v[92:95]\n"
"ds_read_b64 a[72:73], v198 offset:33280\n"
"ds_read_b64_tr_b16 v[222:223], v193 offset:11264\n"
"v_mfma_f32_16x16x32_bf16 v[88:91], v[236:239], a[56:59], v[88:91]\n"
"s_add_u32 s65, s36, 256\n"
"s_add_u32 m0, s60, 98304\n"
"ds_read_b64 a[74:75], v199 offset:33280\n"
"ds_read_b64_tr_b16 v[224:225], v192 offset:1536\n"
"v_mfma_f32_16x16x32_bf16 v[88:91], v[232:235], a[48:51], v[88:91]\n"
"s_waitcnt lgkmcnt(7)\n"
"buffer_load_dwordx4 v204, s[16:19], s65 offen lds\n"
"v_mfma_f32_16x16x32_bf16 v[96:99], v[208:211], a[48:51], v[96:99]\n"
"ds_read_b64_tr_b16 v[226:227], v192 offset:9728\n"
"ds_read_b64_tr_b16 v[228:229], v192 offset:3584\n"
"v_mfma_f32_16x16x32_bf16 v[96:99], v[212:215], a[56:59], v[96:99]\n"
"s_add_u32 m0, s60, 99200\n"
"ds_read_b64_tr_b16 v[230:231], v192 offset:11776\n"
"ds_read_b64_tr_b16 v[232:233], v193 offset:1536\n"
"v_mfma_f32_16x16x32_bf16 v[100:103], v[212:215], a[60:63], v[100:103]\n"
"buffer_load_dwordx4 v204, s[16:19], s65 offen offset:128 lds\n"
"v_mfma_f32_16x16x32_bf16 v[100:103], v[208:211], a[52:55], v[100:103]\n"
"s_waitcnt lgkmcnt(6)\n"
"ds_read_b64_tr_b16 v[234:235], v193 offset:9728\n"
"ds_read_b64_tr_b16 v[236:237], v193 offset:3584\n"
"v_mfma_f32_16x16x32_bf16 v[108:111], v[216:219], a[52:55], v[108:111]\n"
"ds_read_b64 a[76:77], v198 offset:35328\n"
"ds_read_b64_tr_b16 v[238:239], v193 offset:11776\n"
"v_mfma_f32_16x16x32_bf16 v[108:111], v[220:223], a[60:63], v[108:111]\n"
"s_add_u32 m0, s60, 100352\n"
"ds_read_b64 a[78:79], v199 offset:35328\n"
"ds_read_b64_tr_b16 v[208:209], v196 offset:0\n"
"v_mfma_f32_16x16x32_bf16 v[104:107], v[220:223], a[56:59], v[104:107]\n"
"buffer_load_dwordx4 v205, s[16:19], s65 offen lds\n"
"v_mfma_f32_16x16x32_bf16 v[104:107], v[216:219], a[48:51], v[104:107]\n"
"s_waitcnt lgkmcnt(7)\n"
"ds_read_b64_tr_b16 v[210:211], v196 offset:8192\n"
"ds_read_b64_tr_b16 v[212:213], v196 offset:2048\n"
"v_mfma_f32_16x16x32_bf16 v[112:115], v[224:227], a[48:51], v[112:115]\n"
"s_add_u32 m0, s60, 101248\n"
"ds_read_b64_tr_b16 v[214:215], v196 offset:10240\n"
"ds_read_b64_tr_b16 v[216:217], v197 offset:0\n"
"v_mfma_f32_16x16x32_bf16 v[112:115], v[228:231], a[56:59], v[112:115]\n"
"buffer_load_dwordx4 v205, s[16:19], s65 offen offset:128 lds\n"
"v_mfma_f32_16x16x32_bf16 v[116:119], v[228:231], a[60:63], v[116:119]\n"
"ds_read_b64_tr_b16 v[218:219], v197 offset:8192\n"
"ds_read_b64_tr_b16 v[220:221], v197 offset:2048\n"
"v_mfma_f32_16x16x32_bf16 v[116:119], v[224:227], a[52:55], v[116:119]\n"
"s_waitcnt lgkmcnt(8)\n"
"ds_read_b64_tr_b16 v[222:223], v197 offset:10240\n"
"v_mfma_f32_16x16x32_bf16 v[124:127], v[232:235], a[52:55], v[124:127]\n"
"ds_read_b64_tr_b16 v[224:225], v196 offset:512\n"
"v_mfma_f32_16x16x32_bf16 v[124:127], v[236:239], a[60:63], v[124:127]\n"
"ds_read_b64_tr_b16 v[226:227], v196 offset:8704\n"
"v_mfma_f32_16x16x32_bf16 v[120:123], v[236:239], a[56:59], v[120:123]\n"
"s_waitcnt vmcnt(13)\n"
"s_barrier\n"
"v_mfma_f32_16x16x32_bf16 v[120:123], v[232:235], a[48:51], v[120:123]\n"
"s_waitcnt lgkmcnt(6)\n"
"ds_read_b64_tr_b16 v[228:229], v196 offset:2560\n"
"ds_read_b64_tr_b16 v[230:231], v196 offset:10752\n"
"v_mfma_f32_16x16x32_bf16 v[64:67], v[208:211], a[64:67], v[64:67]\n"
"s_add_u32 s65, s37, 256\n"
"ds_read_b64 a[80:81], v198 offset:49152\n"
"ds_read_b64_tr_b16 v[232:233], v197 offset:512\n"
"v_mfma_f32_16x16x32_bf16 v[64:67], v[212:215], a[72:75], v[64:67]\n"
"s_add_u32 m0, s60, 0\n"
"ds_read_b64_tr_b16 v[234:235], v197 offset:8704\n"
"v_mfma_f32_16x16x32_bf16 v[68:71], v[212:215], a[76:79], v[68:71]\n"
"buffer_load_dwordx4 v204, s[16:19], s65 offen lds\n"
"v_mfma_f32_16x16x32_bf16 v[68:71], v[208:211], a[68:71], v[68:71]\n"
"s_waitcnt lgkmcnt(7)\n"
"ds_read_b64_tr_b16 v[236:237], v197 offset:2560\n"
"ds_read_b64_tr_b16 v[238:239], v197 offset:10752\n"
"v_mfma_f32_16x16x32_bf16 v[76:79], v[216:219], a[68:71], v[76:79]\n"
"ds_read_b64 a[82:83], v199 offset:49152\n"
"ds_read_b64_tr_b16 v[208:209], v196 offset:1024\n"
"v_mfma_f32_16x16x32_bf16 v[76:79], v[220:223], a[76:79], v[76:79]\n"
"s_add_u32 m0, s60, 896\n"
"ds_read_b64_tr_b16 v[210:211], v196 offset:9216\n"
"v_mfma_f32_16x16x32_bf16 v[72:75], v[220:223], a[72:75], v[72:75]\n"
"buffer_load_dwordx4 v204, s[16:19], s65 offen offset:128 lds\n"
"v_mfma_f32_16x16x32_bf16 v[72:75], v[216:219], a[64:67], v[72:75]\n"
"s_waitcnt lgkmcnt(8)\n"
"ds_read_b64_tr_b16 v[212:213], v196 offset:3072\n"
"ds_read_b64_tr_b16 v[214:215], v196 offset:11264\n"
"v_mfma_f32_16x16x32_bf16 v[80:83], v[224:227], a[64:67], v[80:83]\n"
"ds_read_b64 a[84:85], v198 offset:51200\n"
"ds_read_b64_tr_b16 v[216:217], v197 offset:1024\n"
"v_mfma_f32_16x16x32_bf16 v[80:83], v[228:231], a[72:75], v[80:83]\n"
"ds_read_b64_tr_b16 v[218:219], v197 offset:9216\n"
"v_mfma_f32_16x16x32_bf16 v[84:87], v[228:231], a[76:79], v[84:87]\n"
"ds_read_b64 a[86:87], v199 offset:51200\n"
"ds_read_b64_tr_b16 v[220:221], v197 offset:3072\n"
"v_mfma_f32_16x16x32_bf16 v[84:87], v[224:227], a[68:71], v[84:87]\n"
"s_waitcnt lgkmcnt(10)\n"
"ds_read_b64_tr_b16 v[222:223], v197 offset:11264\n"
"v_mfma_f32_16x16x32_bf16 v[92:95], v[232:235], a[68:71], v[92:95]\n"
"s_add_u32 m0, s60, 2048\n"
"ds_read_b64 a[88:89], v198 offset:49664\n"
"ds_read_b64_tr_b16 v[224:225], v196 offset:1536\n"
"v_mfma_f32_16x16x32_bf16 v[92:95], v[236:239], a[76:79], v[92:95]\n"
"buffer_load_dwordx4 v205, s[16:19], s65 offen lds\n"
"v_mfma_f32_16x16x32_bf16 v[88:91], v[236:239], a[72:75], v[88:91]\n"
"ds_read_b64_tr_b16 v[226:227], v196 offset:9728\n"
"ds_read_b64_tr_b16 v[228:229], v196 offset:3584\n"
"v_mfma_f32_16x16x32_bf16 v[88:91], v[232:235], a[64:67], v[88:91]\n"
"s_waitcnt lgkmcnt(10)\n"
"ds_read_b64_tr_b16 v[230:231], v196 offset:11776\n"
"v_mfma_f32_16x16x32_bf16 v[96:99], v[208:211], a[64:67], v[96:99]\n"
"s_add_u32 m0, s60, 2944\n"
"ds_read_b64 a[90:91], v199 offset:49664\n"
"ds_read_b64_tr_b16 v[232:233], v197 offset:1536\n"
"v_mfma_f32_16x16x32_bf16 v[96:99], v[212:215], a[72:75], v[96:99]\n"
"buffer_load_dwordx4 v205, s[16:19], s65 offen offset:128 lds\n"
"v_mfma_f32_16x16x32_bf16 v[100:103], v[212:215], a[76:79], v[100:103]\n"
"ds_read_b64_tr_b16 v[234:235], v197 offset:9728\n"
"ds_read_b64_tr_b16 v[236:237], v197 offset:3584\n"
"v_mfma_f32_16x16x32_bf16 v[100:103], v[208:211], a[68:71], v[100:103]\n"
"s_waitcnt lgkmcnt(9)\n"
"ds_read_b64_tr_b16 v[238:239], v197 offset:11776\n"
"v_mfma_f32_16x16x32_bf16 v[108:111], v[216:219], a[68:71], v[108:111]\n"
"ds_read_b64 a[92:93], v198 offset:51712\n"
"ds_read_b64_tr_b16 v[208:209], v194 offset:0\n"
"v_mfma_f32_16x16x32_bf16 v[108:111], v[220:223], a[76:79], v[108:111]\n"
"ds_read_b64_tr_b16 v[210:211], v194 offset:8192\n"
"v_mfma_f32_16x16x32_bf16 v[104:107], v[220:223], a[72:75], v[104:107]\n"
"buffer_load_dwordx4 v[252:255], v240, s[28:31], 0 offen offset:128 sc1\n"
"v_mfma_f32_16x16x32_bf16 v[104:107], v[216:219], a[64:67], v[104:107]\n"
"s_waitcnt lgkmcnt(8)\n"
"ds_read_b64_tr_b16 v[212:213], v194 offset:2048\n"
"ds_read_b64_tr_b16 v[214:215], v194 offset:10240\n"
"v_mfma_f32_16x16x32_bf16 v[112:115], v[224:227], a[64:67], v[112:115]\n"
"ds_read_b64 a[94:95], v199 offset:51712\n"
"ds_read_b64_tr_b16 v[216:217], v195 offset:0\n"
"v_mfma_f32_16x16x32_bf16 v[112:115], v[228:231], a[72:75], v[112:115]\n"
"ds_read_b64_tr_b16 v[218:219], v195 offset:8192\n"
"v_mfma_f32_16x16x32_bf16 v[116:119], v[228:231], a[76:79], v[116:119]\n"
"ds_read_b64_tr_b16 v[220:221], v195 offset:2048\n"
"v_mfma_f32_16x16x32_bf16 v[116:119], v[224:227], a[68:71], v[116:119]\n"
"s_waitcnt lgkmcnt(9)\n"
"ds_read_b64_tr_b16 v[222:223], v195 offset:10240\n"
"v_mfma_f32_16x16x32_bf16 v[124:127], v[232:235], a[68:71], v[124:127]\n"
"ds_read_b64_tr_b16 v[224:225], v194 offset:512\n"
"v_mfma_f32_16x16x32_bf16 v[124:127], v[236:239], a[76:79], v[124:127]\n"
"ds_read_b64_tr_b16 v[226:227], v194 offset:8704\n"
"v_mfma_f32_16x16x32_bf16 v[120:123], v[236:239], a[72:75], v[120:123]\n"
"s_waitcnt vmcnt(9)\n"
"s_barrier\n"
"v_mfma_f32_16x16x32_bf16 v[120:123], v[232:235], a[64:67], v[120:123]\n"
"s_waitcnt lgkmcnt(7)\n"
"ds_read_b64_tr_b16 v[228:229], v194 offset:2560\n"
"ds_read_b64_tr_b16 v[230:231], v194 offset:10752\n"
"v_mfma_f32_16x16x32_bf16 v[64:67], v[208:211], a[80:83], v[64:67]\n"
"s_add_u32 s65, s38, 256\n"
"ds_read_b64 a[96:97], v202 offset:16384\n"
"ds_read_b64_tr_b16 v[232:233], v195 offset:512\n"
"v_mfma_f32_16x16x32_bf16 v[64:67], v[212:215], a[88:91], v[64:67]\n"
"s_waitcnt lgkmcnt(10)\n"
"s_add_u32 m0, s60, 32768\n"
"ds_read_b64_tr_b16 v[234:235], v195 offset:8704\n"
"v_mfma_f32_16x16x32_bf16 v[68:71], v[212:215], a[92:95], v[68:71]\n"
"buffer_load_dwordx4 v204, s[16:19], s65 offen lds\n"
"v_mfma_f32_16x16x32_bf16 v[68:71], v[208:211], a[84:87], v[68:71]\n"
"s_waitcnt lgkmcnt(7)\n"
"ds_read_b64_tr_b16 v[236:237], v195 offset:2560\n"
"ds_read_b64_tr_b16 v[238:239], v195 offset:10752\n"
"v_mfma_f32_16x16x32_bf16 v[76:79], v[216:219], a[84:87], v[76:79]\n"
"ds_read_b64 a[98:99], v203 offset:16384\n"
"ds_read_b64_tr_b16 v[208:209], v194 offset:1024\n"
"v_mfma_f32_16x16x32_bf16 v[76:79], v[220:223], a[92:95], v[76:79]\n"
"s_add_u32 m0, s60, 33664\n"
"ds_read_b64_tr_b16 v[210:211], v194 offset:9216\n"
"v_mfma_f32_16x16x32_bf16 v[72:75], v[220:223], a[88:91], v[72:75]\n"
"buffer_load_dwordx4 v204, s[16:19], s65 offen offset:128 lds\n"
"v_mfma_f32_16x16x32_bf16 v[72:75], v[216:219], a[80:83], v[72:75]\n"
"s_waitcnt lgkmcnt(8)\n"
"ds_read_b64_tr_b16 v[212:213], v194 offset:3072\n"
"ds_read_b64_tr_b16 v[214:215], v194 offset:11264\n"
"v_mfma_f32_16x16x32_bf16 v[80:83], v[224:227], a[80:83], v[80:83]\n"
"ds_read_b64 a[100:101], v202 offset:18432\n"
"ds_read_b64_tr_b16 v[216:217], v195 offset:1024\n"
"v_mfma_f32_16x16x32_bf16 v[80:83], v[228:231], a[88:91], v[80:83]\n"
"ds_read_b64_tr_b16 v[218:219], v195 offset:9216\n"
"v_mfma_f32_16x16x32_bf16 v[84:87], v[228:231], a[92:95], v[84:87]\n"
"ds_read_b64 a[102:103], v203 offset:18432\n"
"ds_read_b64_tr_b16 v[220:221], v195 offset:3072\n"
"v_mfma_f32_16x16x32_bf16 v[84:87], v[224:227], a[84:87], v[84:87]\n"
"s_waitcnt lgkmcnt(10)\n"
"ds_read_b64_tr_b16 v[222:223], v195 offset:11264\n"
"v_mfma_f32_16x16x32_bf16 v[92:95], v[232:235], a[84:87], v[92:95]\n"
"s_add_u32 m0, s60, 34816\n"
"ds_read_b64 a[104:105], v202 offset:16896\n"
"ds_read_b64_tr_b16 v[224:225], v194 offset:1536\n"
"v_mfma_f32_16x16x32_bf16 v[92:95], v[236:239], a[92:95], v[92:95]\n"
"buffer_load_dwordx4 v205, s[16:19], s65 offen lds\n"
"v_mfma_f32_16x16x32_bf16 v[88:91], v[236:239], a[88:91], v[88:91]\n"
"ds_read_b64_tr_b16 v[226:227], v194 offset:9728\n"
"ds_read_b64_tr_b16 v[228:229], v194 offset:3584\n"
"v_mfma_f32_16x16x32_bf16 v[88:91], v[232:235], a[80:83], v[88:91]\n"
"s_waitcnt lgkmcnt(10)\n"
"ds_read_b64_tr_b16 v[230:231], v194 offset:11776\n"
"v_mfma_f32_16x16x32_bf16 v[96:99], v[208:211], a[80:83], v[96:99]\n"
"s_add_u32 m0, s60, 35712\n"
"ds_read_b64 a[106:107], v203 offset:16896\n"
"ds_read_b64_tr_b16 v[232:233], v195 offset:1536\n"
"v_mfma_f32_16x16x32_bf16 v[96:99], v[212:215], a[88:91], v[96:99]\n"
"buffer_load_dwordx4 v205, s[16:19], s65 offen offset:128 lds\n"
"v_mfma_f32_16x16x32_bf16 v[100:103], v[212:215], a[92:95], v[100:103]\n"
"ds_read_b64_tr_b16 v[234:235], v195 offset:9728\n"
"ds_read_b64_tr_b16 v[236:237], v195 offset:3584\n"
"v_mfma_f32_16x16x32_bf16 v[100:103], v[208:211], a[84:87], v[100:103]\n"
"s_waitcnt lgkmcnt(9)\n"
"ds_read_b64_tr_b16 v[238:239], v195 offset:11776\n"
"v_mfma_f32_16x16x32_bf16 v[108:111], v[216:219], a[84:87], v[108:111]\n"
"ds_read_b64 a[108:109], v202 offset:18944\n"
"ds_read_b64_tr_b16 v[208:209], v194 offset:16384\n"
"v_mfma_f32_16x16x32_bf16 v[108:111], v[220:223], a[92:95], v[108:111]\n"
"ds_read_b64_tr_b16 v[210:211], v194 offset:24576\n"
"v_mfma_f32_16x16x32_bf16 v[104:107], v[220:223], a[88:91], v[104:107]\n"
"s_mul_i32 s75, s5, 57344\n"
"ds_read_b64 a[110:111], v203 offset:18944\n"
"ds_read_b64_tr_b16 v[212:213], v194 offset:18432\n"
"v_mfma_f32_16x16x32_bf16 v[104:107], v[216:219], a[80:83], v[104:107]\n"
"s_waitcnt lgkmcnt(10)\n"
"buffer_load_dword v207, v242, s[16:19], s75 offen offset:512\n"
"v_mfma_f32_16x16x32_bf16 v[112:115], v[224:227], a[80:83], v[112:115]\n"
"ds_read_b64_tr_b16 v[214:215], v194 offset:26624\n"
"ds_read_b64_tr_b16 v[216:217], v195 offset:16384\n"
"v_mfma_f32_16x16x32_bf16 v[112:115], v[228:231], a[88:91], v[112:115]\n"
"ds_read_b64_tr_b16 v[218:219], v195 offset:24576\n"
"v_mfma_f32_16x16x32_bf16 v[116:119], v[228:231], a[92:95], v[116:119]\n"
"ds_read_b64_tr_b16 v[220:221], v195 offset:18432\n"
"v_mfma_f32_16x16x32_bf16 v[116:119], v[224:227], a[84:87], v[116:119]\n"
"s_waitcnt lgkmcnt(9)\n"
"ds_read_b64_tr_b16 v[222:223], v195 offset:26624\n"
"v_mfma_f32_16x16x32_bf16 v[124:127], v[232:235], a[84:87], v[124:127]\n"
"ds_read_b64_tr_b16 v[224:225], v194 offset:16896\n"
"v_mfma_f32_16x16x32_bf16 v[124:127], v[236:239], a[92:95], v[124:127]\n"
"ds_read_b64_tr_b16 v[226:227], v194 offset:25088\n"
"v_mfma_f32_16x16x32_bf16 v[120:123], v[236:239], a[88:91], v[120:123]\n"
"s_waitcnt vmcnt(10)\n"
"s_barrier\n"
"v_mfma_f32_16x16x32_bf16 v[120:123], v[232:235], a[80:83], v[120:123]\n"
"s_waitcnt lgkmcnt(6)\n"
"ds_read_b64_tr_b16 v[228:229], v194 offset:18944\n"
"ds_read_b64_tr_b16 v[230:231], v194 offset:27136\n"
"v_mfma_f32_16x16x32_bf16 v[64:67], v[208:211], a[96:99], v[64:67]\n"
"s_add_u32 s65, s39, 256\n"
"ds_read_b64_tr_b16 v[232:233], v195 offset:16896\n"
"v_mfma_f32_16x16x32_bf16 v[64:67], v[212:215], a[104:107], v[64:67]\n"
"s_add_u32 m0, s60, 49152\n"
"ds_read_b64_tr_b16 v[234:235], v195 offset:25088\n"
"v_mfma_f32_16x16x32_bf16 v[68:71], v[212:215], a[108:111], v[68:71]\n"
"buffer_load_dwordx4 v204, s[16:19], s65 offen lds\n"
"v_mfma_f32_16x16x32_bf16 v[68:71], v[208:211], a[100:103], v[68:71]\n"
"s_waitcnt lgkmcnt(6)\n"
"ds_read_b64_tr_b16 v[236:237], v195 offset:18944\n"
"ds_read_b64_tr_b16 v[238:239], v195 offset:27136\n"
"v_mfma_f32_16x16x32_bf16 v[76:79], v[216:219], a[100:103], v[76:79]\n"
"ds_read_b64_tr_b16 v[208:209], v194 offset:17408\n"
"v_mfma_f32_16x16x32_bf16 v[76:79], v[220:223], a[108:111], v[76:79]\n"
"ds_read_b64_tr_b16 v[210:211], v194 offset:25600\n"
"v_mfma_f32_16x16x32_bf16 v[72:75], v[220:223], a[104:107], v[72:75]\n"
"ds_read_b64_tr_b16 v[212:213], v194 offset:19456\n"
"v_mfma_f32_16x16x32_bf16 v[72:75], v[216:219], a[96:99], v[72:75]\n"
"s_waitcnt lgkmcnt(7)\n"
"ds_read_b64_tr_b16 v[214:215], v194 offset:27648\n"
"v_mfma_f32_16x16x32_bf16 v[80:83], v[224:227], a[96:99], v[80:83]\n"
"s_add_u32 m0, s60, 50048\n"
"ds_read_b64_tr_b16 v[216:217], v195 offset:17408\n"
"v_mul_f32 v68, s67, v68\n"
"v_mfma_f32_16x16x32_bf16 v[80:83], v[228:231], a[104:107], v[80:83]\n"
"buffer_load_dwordx4 v204, s[16:19], s65 offen offset:128 lds\n"
"v_mfma_f32_16x16x32_bf16 v[84:87], v[228:231], a[108:111], v[84:87]\n"
"ds_read_b64_tr_b16 v[218:219], v195 offset:25600\n"
"ds_read_b64_tr_b16 v[220:221], v195 offset:19456\n"
"v_mfma_f32_16x16x32_bf16 v[84:87], v[224:227], a[100:103], v[84:87]\n"
"s_waitcnt lgkmcnt(7)\n"
"ds_read_b64_tr_b16 v[222:223], v195 offset:27648\n"
"v_mul_f32 v64, s67, v64\n"
"v_mfma_f32_16x16x32_bf16 v[92:95], v[232:235], a[100:103], v[92:95]\n"
"ds_read_b64_tr_b16 v[224:225], v194 offset:17920\n"
"v_mul_f32 v65, s67, v65\n"
"v_mfma_f32_16x16x32_bf16 v[92:95], v[236:239], a[108:111], v[92:95]\n"
"ds_read_b64_tr_b16 v[226:227], v194 offset:26112\n"
"v_mul_f32 v66, s67, v66\n"
"v_mfma_f32_16x16x32_bf16 v[88:91], v[236:239], a[104:107], v[88:91]\n"
"s_add_u32 m0, s60, 51200\n"
"ds_read_b64_tr_b16 v[228:229], v194 offset:19968\n"
"v_mul_f32 v67, s67, v67\n"
"v_mfma_f32_16x16x32_bf16 v[88:91], v[232:235], a[96:99], v[88:91]\n"
"s_waitcnt lgkmcnt(7)\n"
"buffer_load_dwordx4 v205, s[16:19], s65 offen lds\n"
"v_mfma_f32_16x16x32_bf16 v[96:99], v[208:211], a[96:99], v[96:99]\n"
"ds_read_b64_tr_b16 v[230:231], v194 offset:28160\n"
"ds_read_b64_tr_b16 v[232:233], v195 offset:17920\n"
"v_mfma_f32_16x16x32_bf16 v[96:99], v[212:215], a[104:107], v[96:99]\n"
"ds_read_b64_tr_b16 v[234:235], v195 offset:26112\n"
"v_cvt_pk_bf16_f32 v0, v64, v65\n"
"v_mfma_f32_16x16x32_bf16 v[100:103], v[212:215], a[108:111], v[100:103]\n"
"ds_read_b64_tr_b16 v[236:237], v195 offset:19968\n"
"v_cvt_pk_bf16_f32 v1, v66, v67\n"
"v_mfma_f32_16x16x32_bf16 v[100:103], v[208:211], a[100:103], v[100:103]\n"
"s_waitcnt lgkmcnt(7)\n"
"ds_read_b64_tr_b16 v[238:239], v195 offset:28160\n"
"v_mul_f32 v72, s67, v72\n"
"v_mfma_f32_16x16x32_bf16 v[108:111], v[216:219], a[100:103], v[108:111]\n"
"ds_read_b64_tr_b16 v[208:209], v194 offset:32768\n"
"v_mul_f32 v73, s67, v73\n"
"v_mfma_f32_16x16x32_bf16 v[108:111], v[220:223], a[108:111], v[108:111]\n"
"s_add_u32 m0, s60, 52096\n"
"ds_read_b64_tr_b16 v[210:211], v194 offset:40960\n"
"v_mul_f32 v74, s67, v74\n"
"v_mfma_f32_16x16x32_bf16 v[104:107], v[220:223], a[104:107], v[104:107]\n"
"buffer_load_dwordx4 v205, s[16:19], s65 offen offset:128 lds\n"
"v_mfma_f32_16x16x32_bf16 v[104:107], v[216:219], a[96:99], v[104:107]\n"
"s_waitcnt lgkmcnt(6)\n"
"ds_read_b64_tr_b16 v[212:213], v194 offset:34816\n"
"ds_read_b64_tr_b16 v[214:215], v194 offset:43008\n"
"v_mfma_f32_16x16x32_bf16 v[112:115], v[224:227], a[96:99], v[112:115]\n"
"ds_read_b64_tr_b16 v[216:217], v195 offset:32768\n"
"v_mul_f32 v75, s67, v75\n"
"v_mfma_f32_16x16x32_bf16 v[112:115], v[228:231], a[104:107], v[112:115]\n"
"ds_read_b64_tr_b16 v[218:219], v195 offset:40960\n"
"v_cvt_pk_bf16_f32 v2, v72, v73\n"
"v_mfma_f32_16x16x32_bf16 v[116:119], v[228:231], a[108:111], v[116:119]\n"
"ds_read_b64_tr_b16 v[220:221], v195 offset:34816\n"
"v_cvt_pk_bf16_f32 v3, v74, v75\n"
"v_mfma_f32_16x16x32_bf16 v[116:119], v[224:227], a[100:103], v[116:119]\n"
"s_waitcnt lgkmcnt(7)\n"
"ds_read_b64_tr_b16 v[222:223], v195 offset:43008\n"
"v_mul_f32 v69, s67, v69\n"
"v_mfma_f32_16x16x32_bf16 v[124:127], v[232:235], a[100:103], v[124:127]\n"
"ds_read_b64_tr_b16 v[224:225], v194 offset:33280\n"
"v_mul_f32 v70, s67, v70\n"
"v_mfma_f32_16x16x32_bf16 v[124:127], v[236:239], a[108:111], v[124:127]\n"
"ds_read_b64_tr_b16 v[226:227], v194 offset:41472\n"
"v_mul_f32 v71, s67, v71\n"
"v_mfma_f32_16x16x32_bf16 v[120:123], v[236:239], a[104:107], v[120:123]\n"
"s_waitcnt vmcnt(10)\n"
"s_barrier\n"
"v_mfma_f32_16x16x32_bf16 v[120:123], v[232:235], a[96:99], v[120:123]\n"
"s_waitcnt lgkmcnt(6)\n"
"ds_read_b64_tr_b16 v[228:229], v194 offset:35328\n"
"ds_read_b64_tr_b16 v[230:231], v194 offset:43520\n"
"v_mfma_f32_16x16x32_bf16 v[128:131], v[208:211], a[0:3], 0\n"
"s_add_u32 s65, s40, 256\n"
"ds_read_b64_tr_b16 v[232:233], v195 offset:33280\n"
"v_mul_f32 v76, s67, v76\n"
"v_mfma_f32_16x16x32_bf16 v[128:131], v[212:215], a[8:11], v[128:131]\n"
"s_add_u32 m0, s60, 65536\n"
"ds_read_b64_tr_b16 v[234:235], v195 offset:41472\n"
"v_mul_f32 v77, s67, v77\n"
"v_mfma_f32_16x16x32_bf16 v[132:135], v[212:215], a[12:15], 0\n"
"buffer_load_dwordx4 v204, s[16:19], s65 offen lds\n"
"v_mfma_f32_16x16x32_bf16 v[132:135], v[208:211], a[4:7], v[132:135]\n"
"s_waitcnt lgkmcnt(6)\n"
"ds_read_b64_tr_b16 v[236:237], v195 offset:35328\n"
"ds_read_b64_tr_b16 v[238:239], v195 offset:43520\n"
"v_mfma_f32_16x16x32_bf16 v[140:143], v[216:219], a[4:7], 0\n"
"ds_read_b64_tr_b16 v[208:209], v194 offset:33792\n"
"v_cvt_pk_bf16_f32 v32, v68, v69\n"
"v_mfma_f32_16x16x32_bf16 v[140:143], v[220:223], a[12:15], v[140:143]\n"
"ds_read_b64_tr_b16 v[210:211], v194 offset:41984\n"
"v_cvt_pk_bf16_f32 v33, v70, v71\n"
"v_mfma_f32_16x16x32_bf16 v[136:139], v[220:223], a[8:11], 0\n"
"ds_read_b64_tr_b16 v[212:213], v194 offset:35840\n"
"v_mul_f32 v78, s67, v78\n"
"v_mfma_f32_16x16x32_bf16 v[136:139], v[216:219], a[0:3], v[136:139]\n"
"s_waitcnt lgkmcnt(7)\n"
"ds_read_b64_tr_b16 v[214:215], v194 offset:44032\n"
"v_mul_f32 v79, s67, v79\n"
"v_mfma_f32_16x16x32_bf16 v[144:147], v[224:227], a[0:3], 0\n"
"s_add_u32 m0, s60, 66432\n"
"ds_read_b64_tr_b16 v[216:217], v195 offset:33792\n"
"v_permlane16_swap_b32 v0, v2\n"
"v_mfma_f32_16x16x32_bf16 v[144:147], v[228:231], a[8:11], v[144:147]\n"
"buffer_load_dwordx4 v204, s[16:19], s65 offen offset:128 lds\n"
"v_mfma_f32_16x16x32_bf16 v[148:151], v[228:231], a[12:15], 0\n"
"ds_read_b64_tr_b16 v[218:219], v195 offset:41984\n"
"ds_read_b64_tr_b16 v[220:221], v195 offset:35840\n"
"v_mfma_f32_16x16x32_bf16 v[148:151], v[224:227], a[4:7], v[148:151]\n"
"s_waitcnt lgkmcnt(7)\n"
"ds_read_b64_tr_b16 v[222:223], v195 offset:44032\n"
"v_cvt_pk_bf16_f32 v34, v76, v77\n"
"v_mfma_f32_16x16x32_bf16 v[156:159], v[232:235], a[4:7], 0\n"
"ds_read_b64_tr_b16 v[224:225], v194 offset:34304\n"
"v_cvt_pk_bf16_f32 v35, v78, v79\n"
"v_mfma_f32_16x16x32_bf16 v[156:159], v[236:239], a[12:15], v[156:159]\n"
"ds_read_b64_tr_b16 v[226:227], v194 offset:42496\n"
"v_permlane16_swap_b32 v1, v3\n"
"v_mfma_f32_16x16x32_bf16 v[152:155], v[236:239], a[8:11], 0\n"
"s_add_u32 m0, s60, 67584\n"
"ds_read_b64_tr_b16 v[228:229], v194 offset:36352\n"
"v_mul_f32 v80, s67, v80\n"
"v_mfma_f32_16x16x32_bf16 v[152:155], v[232:235], a[0:3], v[152:155]\n"
"s_waitcnt lgkmcnt(7)\n"
"buffer_load_dwordx4 v205, s[16:19], s65 offen lds\n"
"v_mfma_f32_16x16x32_bf16 v[160:163], v[208:211], a[0:3], 0\n"
"ds_read_b64_tr_b16 v[230:231], v194 offset:44544\n"
"ds_read_b64_tr_b16 v[232:233], v195 offset:34304\n"
"v_mfma_f32_16x16x32_bf16 v[160:163], v[212:215], a[8:11], v[160:163]\n"
"ds_read_b64_tr_b16 v[234:235], v195 offset:42496\n"
"v_mul_f32 v81, s67, v81\n"
"v_mfma_f32_16x16x32_bf16 v[164:167], v[212:215], a[12:15], 0\n"
"ds_read_b64_tr_b16 v[236:237], v195 offset:36352\n"
"v_mul_f32 v82, s67, v82\n"
"v_mfma_f32_16x16x32_bf16 v[164:167], v[208:211], a[4:7], v[164:167]\n"
"s_waitcnt lgkmcnt(7)\n"
"ds_read_b64_tr_b16 v[238:239], v195 offset:44544\n"
"v_mul_f32 v83, s67, v83\n"
"v_mfma_f32_16x16x32_bf16 v[172:175], v[216:219], a[4:7], 0\n"
"ds_read_b64_tr_b16 v[208:209], v192 offset:0\n"
"v_cvt_pk_bf16_f32 v4, v80, v81\n"
"v_mfma_f32_16x16x32_bf16 v[172:175], v[220:223], a[12:15], v[172:175]\n"
"s_add_u32 m0, s60, 68480\n"
"ds_read_b64_tr_b16 v[210:211], v192 offset:8192\n"
"v_mul_f32 v88, s67, v88\n"
"v_mfma_f32_16x16x32_bf16 v[168:171], v[220:223], a[8:11], 0\n"
"buffer_load_dwordx4 v205, s[16:19], s65 offen offset:128 lds\n"
"v_mfma_f32_16x16x32_bf16 v[168:171], v[216:219], a[0:3], v[168:171]\n"
"s_waitcnt lgkmcnt(6)\n"
"ds_read_b64_tr_b16 v[212:213], v192 offset:2048\n"
"ds_read_b64_tr_b16 v[214:215], v192 offset:10240\n"
"v_mfma_f32_16x16x32_bf16 v[176:179], v[224:227], a[0:3], 0\n"
"ds_read_b64_tr_b16 v[216:217], v193 offset:0\n"
"v_cvt_pk_bf16_f32 v5, v82, v83\n"
"v_mfma_f32_16x16x32_bf16 v[176:179], v[228:231], a[8:11], v[176:179]\n"
"ds_read_b64_tr_b16 v[218:219], v193 offset:8192\n"
"v_mul_f32 v89, s67, v89\n"
"v_mfma_f32_16x16x32_bf16 v[180:183], v[228:231], a[12:15], 0\n"
"ds_read_b64_tr_b16 v[220:221], v193 offset:2048\n"
"v_mul_f32 v90, s67, v90\n"
"v_mfma_f32_16x16x32_bf16 v[180:183], v[224:227], a[4:7], v[180:183]\n"
"s_waitcnt lgkmcnt(7)\n"
"ds_read_b64_tr_b16 v[222:223], v193 offset:10240\n"
"v_mul_f32 v91, s67, v91\n"
"v_mfma_f32_16x16x32_bf16 v[188:191], v[232:235], a[4:7], 0\n"
"ds_read_b64_tr_b16 v[224:225], v192 offset:512\n"
"v_cvt_pk_bf16_f32 v6, v88, v89\n"
"v_mfma_f32_16x16x32_bf16 v[188:191], v[236:239], a[12:15], v[188:191]\n"
"ds_read_b64_tr_b16 v[226:227], v192 offset:8704\n"
"v_cvt_pk_bf16_f32 v7, v90, v91\n"
"v_mfma_f32_16x16x32_bf16 v[184:187], v[236:239], a[8:11], 0\n"
"s_waitcnt vmcnt(9)\n"
"s_barrier\n"
"v_mfma_f32_16x16x32_bf16 v[184:187], v[232:235], a[0:3], v[184:187]\n"
"s_waitcnt lgkmcnt(6)\n"
"ds_read_b64_tr_b16 v[228:229], v192 offset:2560\n"
"ds_read_b64_tr_b16 v[230:231], v192 offset:10752\n"
"v_mfma_f32_16x16x32_bf16 v[128:131], v[208:211], a[16:19], v[128:131]\n"
"s_add_u32 s65, s41, 256\n"
"ds_read_b64_tr_b16 v[232:233], v193 offset:512\n"
"v_permlane16_swap_b32 v32, v34\n"
"v_mfma_f32_16x16x32_bf16 v[128:131], v[212:215], a[24:27], v[128:131]\n"
"s_add_u32 m0, s60, 81920\n"
"ds_read_b64_tr_b16 v[234:235], v193 offset:8704\n"
"v_permlane16_swap_b32 v33, v35\n"
"v_mfma_f32_16x16x32_bf16 v[132:135], v[212:215], a[28:31], v[132:135]\n"
"buffer_load_dwordx4 v204, s[16:19], s65 offen lds\n"
"v_mfma_f32_16x16x32_bf16 v[132:135], v[208:211], a[20:23], v[132:135]\n"
"s_waitcnt lgkmcnt(6)\n"
"ds_read_b64_tr_b16 v[236:237], v193 offset:2560\n"
"ds_read_b64_tr_b16 v[238:239], v193 offset:10752\n"
"v_mfma_f32_16x16x32_bf16 v[140:143], v[216:219], a[20:23], v[140:143]\n"
"IWA1_%=:\n"
"s_cbranch_execz IRA_%=\n"
"ds_read_b64_tr_b16 v[208:209], v192 offset:1024\n"
"v_mul_f32 v84, s67, v84\n"
"v_mfma_f32_16x16x32_bf16 v[140:143], v[220:223], a[28:31], v[140:143]\n"
"s_add_u32 m0, s60, 82816\n"
"ds_read_b64_tr_b16 v[210:211], v192 offset:9216\n"
"v_mul_f32 v85, s67, v85\n"
"v_mfma_f32_16x16x32_bf16 v[136:139], v[220:223], a[24:27], v[136:139]\n"
"buffer_load_dwordx4 v204, s[16:19], s65 offen offset:128 lds\n"
"v_mfma_f32_16x16x32_bf16 v[136:139], v[216:219], a[16:19], v[136:139]\n"
"s_waitcnt lgkmcnt(6)\n"
"ds_read_b64_tr_b16 v[212:213], v192 offset:3072\n"
"ds_read_b64_tr_b16 v[214:215], v192 offset:11264\n"
"v_mfma_f32_16x16x32_bf16 v[144:147], v[224:227], a[16:19], v[144:147]\n"
"ds_read_b64_tr_b16 v[216:217], v193 offset:1024\n"
"v_mul_f32 v86, s67, v86\n"
"v_mfma_f32_16x16x32_bf16 v[144:147], v[228:231], a[24:27], v[144:147]\n"
"ds_read_b64_tr_b16 v[218:219], v193 offset:9216\n"
"v_mul_f32 v87, s67, v87\n"
"v_mfma_f32_16x16x32_bf16 v[148:151], v[228:231], a[28:31], v[148:151]\n"
"ds_read_b64_tr_b16 v[220:221], v193 offset:3072\n"
"v_cvt_pk_bf16_f32 v36, v84, v85\n"
"v_mfma_f32_16x16x32_bf16 v[148:151], v[224:227], a[20:23], v[148:151]\n"
"s_waitcnt lgkmcnt(7)\n"
"ds_read_b64_tr_b16 v[222:223], v193 offset:11264\n"
"v_cvt_pk_bf16_f32 v37, v86, v87\n"
"v_mfma_f32_16x16x32_bf16 v[156:159], v[232:235], a[20:23], v[156:159]\n"
"s_add_u32 m0, s60, 83968\n"
"ds_read_b64_tr_b16 v[224:225], v192 offset:1536\n"
"v_mul_f32 v92, s67, v92\n"
"v_mfma_f32_16x16x32_bf16 v[156:159], v[236:239], a[28:31], v[156:159]\n"
"buffer_load_dwordx4 v205, s[16:19], s65 offen lds\n"
"v_mfma_f32_16x16x32_bf16 v[152:155], v[236:239], a[24:27], v[152:155]\n"
"ds_read_b64_tr_b16 v[226:227], v192 offset:9728\n"
"ds_read_b64_tr_b16 v[228:229], v192 offset:3584\n"
"v_mfma_f32_16x16x32_bf16 v[152:155], v[232:235], a[16:19], v[152:155]\n"
"s_waitcnt lgkmcnt(7)\n"
"ds_read_b64_tr_b16 v[230:231], v192 offset:11776\n"
"v_mul_f32 v93, s67, v93\n"
"v_mfma_f32_16x16x32_bf16 v[160:163], v[208:211], a[16:19], v[160:163]\n"
"s_add_u32 m0, s60, 84864\n"
"ds_read_b64_tr_b16 v[232:233], v193 offset:1536\n"
"v_mul_f32 v94, s67, v94\n"
"v_mfma_f32_16x16x32_bf16 v[160:163], v[212:215], a[24:27], v[160:163]\n"
"buffer_load_dwordx4 v205, s[16:19], s65 offen offset:128 lds\n"
"v_mfma_f32_16x16x32_bf16 v[164:167], v[212:215], a[28:31], v[164:167]\n"
"ds_read_b64_tr_b16 v[234:235], v193 offset:9728\n"
"ds_read_b64_tr_b16 v[236:237], v193 offset:3584\n"
"v_mfma_f32_16x16x32_bf16 v[164:167], v[208:211], a[20:23], v[164:167]\n"
"s_waitcnt lgkmcnt(7)\n"
"ds_read_b64_tr_b16 v[238:239], v193 offset:11776\n"
"v_mul_f32 v95, s67, v95\n"
"v_mfma_f32_16x16x32_bf16 v[172:175], v[216:219], a[20:23], v[172:175]\n"
"ds_read_b64_tr_b16 v[208:209], v192 offset:32768\n"
"v_cvt_pk_bf16_f32 v38, v92, v93\n"
"v_mfma_f32_16x16x32_bf16 v[172:175], v[220:223], a[28:31], v[172:175]\n"
"ds_read_b64_tr_b16 v[210:211], v192 offset:40960\n"
"v_cvt_pk_bf16_f32 v39, v94, v95\n"
"v_mfma_f32_16x16x32_bf16 v[168:171], v[220:223], a[24:27], v[168:171]\n"
"s_mul_i32 s75, s5, 57344\n"
"ds_read_b64_tr_b16 v[212:213], v192 offset:34816\n"
"v_permlane16_swap_b32 v4, v6\n"
"v_mfma_f32_16x16x32_bf16 v[168:171], v[216:219], a[16:19], v[168:171]\n"
"s_waitcnt lgkmcnt(7)\n"
"buffer_load_dword v207, v242, s[16:19], s75 offen offset:768\n"
"v_mfma_f32_16x16x32_bf16 v[176:179], v[224:227], a[16:19], v[176:179]\n"
"ds_read_b64_tr_b16 v[214:215], v192 offset:43008\n"
"ds_read_b64_tr_b16 v[216:217], v193 offset:32768\n"
"v_mfma_f32_16x16x32_bf16 v[176:179], v[228:231], a[24:27], v[176:179]\n"
"ds_read_b64_tr_b16 v[218:219], v193 offset:40960\n"
"v_permlane16_swap_b32 v5, v7\n"
"v_mfma_f32_16x16x32_bf16 v[180:183], v[228:231], a[28:31], v[180:183]\n"
"ds_read_b64_tr_b16 v[220:221], v193 offset:34816\n"
"v_mul_f32 v96, s67, v96\n"
"v_mfma_f32_16x16x32_bf16 v[180:183], v[224:227], a[20:23], v[180:183]\n"
"s_waitcnt lgkmcnt(7)\n"
"ds_read_b64_tr_b16 v[222:223], v193 offset:43008\n"
"v_mul_f32 v97, s67, v97\n"
"v_mfma_f32_16x16x32_bf16 v[188:191], v[232:235], a[20:23], v[188:191]\n"
"ds_read_b64_tr_b16 v[224:225], v192 offset:33280\n"
"v_mul_f32 v98, s67, v98\n"
"v_mfma_f32_16x16x32_bf16 v[188:191], v[236:239], a[28:31], v[188:191]\n"
"ds_read_b64_tr_b16 v[226:227], v192 offset:41472\n"
"v_mul_f32 v99, s67, v99\n"
"v_mfma_f32_16x16x32_bf16 v[184:187], v[236:239], a[24:27], v[184:187]\n"
"s_waitcnt vmcnt(9)\n"
"s_barrier\n"
"v_mfma_f32_16x16x32_bf16 v[184:187], v[232:235], a[16:19], v[184:187]\n"
"s_waitcnt lgkmcnt(6)\n"
"ds_read_b64_tr_b16 v[228:229], v192 offset:35328\n"
"ds_read_b64_tr_b16 v[230:231], v192 offset:43520\n"
"v_mfma_f32_16x16x32_bf16 v[128:131], v[208:211], a[32:35], v[128:131]\n"
"s_add_u32 s65, s42, 256\n"
"ds_read_b64_tr_b16 v[232:233], v193 offset:33280\n"
"v_mul_f32 v104, s67, v104\n"
"v_mfma_f32_16x16x32_bf16 v[128:131], v[212:215], a[40:43], v[128:131]\n"
"s_add_u32 m0, s60, 98304\n"
"ds_read_b64_tr_b16 v[234:235], v193 offset:41472\n"
"v_mul_f32 v105, s67, v105\n"
"v_mfma_f32_16x16x32_bf16 v[132:135], v[212:215], a[44:47], v[132:135]\n"
"buffer_load_dwordx4 v204, s[16:19], s65 offen lds\n"
"v_mfma_f32_16x16x32_bf16 v[132:135], v[208:211], a[36:39], v[132:135]\n"
"s_waitcnt lgkmcnt(6)\n"
"ds_read_b64_tr_b16 v[236:237], v193 offset:35328\n"
"ds_read_b64_tr_b16 v[238:239], v193 offset:43520\n"
"v_mfma_f32_16x16x32_bf16 v[140:143], v[216:219], a[36:39], v[140:143]\n"
"ds_read_b64_tr_b16 v[208:209], v192 offset:33792\n"
"v_cvt_pk_bf16_f32 v8, v96, v97\n"
"v_mfma_f32_16x16x32_bf16 v[140:143], v[220:223], a[44:47], v[140:143]\n"
"ds_read_b64_tr_b16 v[210:211], v192 offset:41984\n"
"v_cvt_pk_bf16_f32 v9, v98, v99\n"
"v_mfma_f32_16x16x32_bf16 v[136:139], v[220:223], a[40:43], v[136:139]\n"
"ds_read_b64_tr_b16 v[212:213], v192 offset:35840\n"
"v_mul_f32 v106, s67, v106\n"
"v_mfma_f32_16x16x32_bf16 v[136:139], v[216:219], a[32:35], v[136:139]\n"
"s_waitcnt lgkmcnt(7)\n"
"ds_read_b64_tr_b16 v[214:215], v192 offset:44032\n"
"v_mul_f32 v107, s67, v107\n"
"v_mfma_f32_16x16x32_bf16 v[144:147], v[224:227], a[32:35], v[144:147]\n"
"s_add_u32 m0, s60, 99200\n"
"ds_read_b64_tr_b16 v[216:217], v193 offset:33792\n"
"v_permlane16_swap_b32 v36, v38\n"
"v_mfma_f32_16x16x32_bf16 v[144:147], v[228:231], a[40:43], v[144:147]\n"
"buffer_load_dwordx4 v204, s[16:19], s65 offen offset:128 lds\n"
"v_mfma_f32_16x16x32_bf16 v[148:151], v[228:231], a[44:47], v[148:151]\n"
"ds_read_b64_tr_b16 v[218:219], v193 offset:41984\n"
"ds_read_b64_tr_b16 v[220:221], v193 offset:35840\n"
"v_mfma_f32_16x16x32_bf16 v[148:151], v[224:227], a[36:39], v[148:151]\n"
"s_waitcnt lgkmcnt(7)\n"
"ds_read_b64_tr_b16 v[222:223], v193 offset:44032\n"
"v_cvt_pk_bf16_f32 v10, v104, v105\n"
"v_mfma_f32_16x16x32_bf16 v[156:159], v[232:235], a[36:39], v[156:159]\n"
"ds_read_b64_tr_b16 v[224:225], v192 offset:34304\n"
"v_cvt_pk_bf16_f32 v11, v106, v107\n"
"v_mfma_f32_16x16x32_bf16 v[156:159], v[236:239], a[44:47], v[156:159]\n"
"ds_read_b64_tr_b16 v[226:227], v192 offset:42496\n"
"v_permlane16_swap_b32 v37, v39\n"
"v_mfma_f32_16x16x32_bf16 v[152:155], v[236:239], a[40:43], v[152:155]\n"
"s_add_u32 m0, s60, 100352\n"
"ds_read_b64_tr_b16 v[228:229], v192 offset:36352\n"
"v_mul_f32 v100, s67, v100\n"
"v_mfma_f32_16x16x32_bf16 v[152:155], v[232:235], a[32:35], v[152:155]\n"
"s_waitcnt lgkmcnt(7)\n"
"buffer_load_dwordx4 v205, s[16:19], s65 offen lds\n"
"v_mfma_f32_16x16x32_bf16 v[160:163], v[208:211], a[32:35], v[160:163]\n"
"ds_read_b64_tr_b16 v[230:231], v192 offset:44544\n"
"ds_read_b64_tr_b16 v[232:233], v193 offset:34304\n"
"v_mfma_f32_16x16x32_bf16 v[160:163], v[212:215], a[40:43], v[160:163]\n"
"ds_read_b64_tr_b16 v[234:235], v193 offset:42496\n"
"v_mul_f32 v101, s67, v101\n"
"v_mfma_f32_16x16x32_bf16 v[164:167], v[212:215], a[44:47], v[164:167]\n"
"ds_read_b64_tr_b16 v[236:237], v193 offset:36352\n"
"v_mul_f32 v102, s67, v102\n"
"v_mfma_f32_16x16x32_bf16 v[164:167], v[208:211], a[36:39], v[164:167]\n"
"s_waitcnt lgkmcnt(7)\n"
"ds_read_b64_tr_b16 v[238:239], v193 offset:44544\n"
"v_mul_f32 v103, s67, v103\n"
"v_mfma_f32_16x16x32_bf16 v[172:175], v[216:219], a[36:39], v[172:175]\n"
"ds_read_b64_tr_b16 v[208:209], v192 offset:49152\n"
"v_cvt_pk_bf16_f32 v40, v100, v101\n"
"v_mfma_f32_16x16x32_bf16 v[172:175], v[220:223], a[44:47], v[172:175]\n"
"s_add_u32 m0, s60, 101248\n"
"ds_read_b64_tr_b16 v[210:211], v192 offset:57344\n"
"v_mul_f32 v108, s67, v108\n"
"v_mfma_f32_16x16x32_bf16 v[168:171], v[220:223], a[40:43], v[168:171]\n"
"buffer_load_dwordx4 v205, s[16:19], s65 offen offset:128 lds\n"
"v_mfma_f32_16x16x32_bf16 v[168:171], v[216:219], a[32:35], v[168:171]\n"
"s_waitcnt lgkmcnt(6)\n"
"ds_read_b64_tr_b16 v[212:213], v192 offset:51200\n"
"ds_read_b64_tr_b16 v[214:215], v192 offset:59392\n"
"v_mfma_f32_16x16x32_bf16 v[176:179], v[224:227], a[32:35], v[176:179]\n"
"ds_read_b64_tr_b16 v[216:217], v193 offset:49152\n"
"v_cvt_pk_bf16_f32 v41, v102, v103\n"
"v_mfma_f32_16x16x32_bf16 v[176:179], v[228:231], a[40:43], v[176:179]\n"
"ds_read_b64_tr_b16 v[218:219], v193 offset:57344\n"
"v_mul_f32 v109, s67, v109\n"
"v_mfma_f32_16x16x32_bf16 v[180:183], v[228:231], a[44:47], v[180:183]\n"
"ds_read_b64_tr_b16 v[220:221], v193 offset:51200\n"
"v_mul_f32 v110, s67, v110\n"
"v_mfma_f32_16x16x32_bf16 v[180:183], v[224:227], a[36:39], v[180:183]\n"
"s_waitcnt lgkmcnt(7)\n"
"ds_read_b64_tr_b16 v[222:223], v193 offset:59392\n"
"v_mul_f32 v111, s67, v111\n"
"v_mfma_f32_16x16x32_bf16 v[188:191], v[232:235], a[36:39], v[188:191]\n"
"ds_read_b64_tr_b16 v[224:225], v192 offset:49664\n"
"v_cvt_pk_bf16_f32 v42, v108, v109\n"
"v_mfma_f32_16x16x32_bf16 v[188:191], v[236:239], a[44:47], v[188:191]\n"
"ds_read_b64_tr_b16 v[226:227], v192 offset:57856\n"
"v_cvt_pk_bf16_f32 v43, v110, v111\n"
"v_mfma_f32_16x16x32_bf16 v[184:187], v[236:239], a[40:43], v[184:187]\n"
"s_waitcnt vmcnt(9)\n"
"s_barrier\n"
"v_mfma_f32_16x16x32_bf16 v[184:187], v[232:235], a[32:35], v[184:187]\n"
"s_waitcnt lgkmcnt(6)\n"
"ds_read_b64_tr_b16 v[228:229], v192 offset:51712\n"
"ds_read_b64_tr_b16 v[230:231], v192 offset:59904\n"
"v_mfma_f32_16x16x32_bf16 v[128:131], v[208:211], a[48:51], v[128:131]\n"
"s_add_u32 s65, s36, 512\n"
"s_add_u32 m0, s60, 114688\n"
"ds_read_b64_tr_b16 v[232:233], v193 offset:49664\n"
"v_add_u32 v252, 1, v252\n"
"v_mfma_f32_16x16x32_bf16 v[128:131], v[212:215], a[56:59], v[128:131]\n"
"ds_read_b64_tr_b16 v[234:235], v193 offset:57856\n"
"v_add_u32 v253, 1, v253\n"
"v_mfma_f32_16x16x32_bf16 v[132:135], v[212:215], a[60:63], v[132:135]\n"
"ds_read_b64_tr_b16 v[236:237], v193 offset:51712\n"
"v_add_u32 v254, 1, v254\n"
"v_mfma_f32_16x16x32_bf16 v[132:135], v[208:211], a[52:55], v[132:135]\n"
"s_waitcnt lgkmcnt(7)\n"
"ds_read_b64_tr_b16 v[238:239], v193 offset:59904\n"
"v_add_u32 v255, 1, v255\n"
"v_mfma_f32_16x16x32_bf16 v[140:143], v[216:219], a[52:55], v[140:143]\n"
"buffer_load_dwordx4 v204, s[16:19], s65 offen lds\n"
"v_mfma_f32_16x16x32_bf16 v[140:143], v[220:223], a[60:63], v[140:143]\n"
"s_add_u32 m0, s60, 115584\n"
"ds_read_b64_tr_b16 v[208:209], v192 offset:50176\n"
"ds_read_b64_tr_b16 v[210:211], v192 offset:58368\n"
"v_mfma_f32_16x16x32_bf16 v[136:139], v[220:223], a[56:59], v[136:139]\n"
"buffer_load_dwordx4 v204, s[16:19], s65 offen offset:128 lds\n"
"v_mfma_f32_16x16x32_bf16 v[136:139], v[216:219], a[48:51], v[136:139]\n"
"s_waitcnt lgkmcnt(6)\n"
"ds_read_b64_tr_b16 v[212:213], v192 offset:52224\n"
"ds_read_b64_tr_b16 v[214:215], v192 offset:60416\n"
"v_mfma_f32_16x16x32_bf16 v[144:147], v[224:227], a[48:51], v[144:147]\n"
"s_add_u32 m0, s60, 116736\n"
"ds_read_b64_tr_b16 v[216:217], v193 offset:50176\n"
"v_permlane16_swap_b32 v8, v10\n"
"v_mfma_f32_16x16x32_bf16 v[144:147], v[228:231], a[56:59], v[144:147]\n"
"buffer_load_dwordx4 v205, s[16:19], s65 offen lds\n"
"v_mfma_f32_16x16x32_bf16 v[148:151], v[228:231], a[60:63], v[148:151]\n"
"s_add_u32 m0, s60, 117632\n"
"ds_read_b64_tr_b16 v[218:219], v193 offset:58368\n"
"ds_read_b64_tr_b16 v[220:221], v193 offset:52224\n"
"v_mfma_f32_16x16x32_bf16 v[148:151], v[224:227], a[52:55], v[148:151]\n"
"s_waitcnt lgkmcnt(7)\n"
"buffer_load_dwordx4 v205, s[16:19], s65 offen offset:128 lds\n"
"v_mfma_f32_16x16x32_bf16 v[156:159], v[232:235], a[52:55], v[156:159]\n"
"s_add_u32 s66, s73, 0\n"
"s_and_b32 s66, s66, 524287\n"
"ds_read_b64_tr_b16 v[222:223], v193 offset:60416\n"
"ds_read_b64_tr_b16 v[224:225], v192 offset:50688\n"
"v_mfma_f32_16x16x32_bf16 v[156:159], v[236:239], a[60:63], v[156:159]\n"
"s_add_u32 m0, s52, 0\n"
"ds_read_b64_tr_b16 v[226:227], v192 offset:58880\n"
"v_permlane16_swap_b32 v9, v11\n"
"v_mfma_f32_16x16x32_bf16 v[152:155], v[236:239], a[56:59], v[152:155]\n"
"buffer_load_dwordx4 v247, s[8:11], s66 offen nt lds\n"
"v_mfma_f32_16x16x32_bf16 v[152:155], v[232:235], a[48:51], v[152:155]\n"
"s_waitcnt lgkmcnt(6)\n"
"ds_read_b64_tr_b16 v[228:229], v192 offset:52736\n"
"ds_read_b64_tr_b16 v[230:231], v192 offset:60928\n"
"v_mfma_f32_16x16x32_bf16 v[160:163], v[208:211], a[48:51], v[160:163]\n"
"s_add_u32 m0, s53, 0\n"
"ds_read_b64_tr_b16 v[232:233], v193 offset:50688\n"
"v_mul_f32 v112, s67, v112\n"
"v_mfma_f32_16x16x32_bf16 v[160:163], v[212:215], a[56:59], v[160:163]\n"
"buffer_load_dwordx4 v247, s[8:11], s66 offen offset:128 nt lds\n"
"v_mfma_f32_16x16x32_bf16 v[164:167], v[212:215], a[60:63], v[164:167]\n"
"s_add_u32 m0, s54, 0\n"
"ds_read_b64_tr_b16 v[234:235], v193 offset:58880\n"
"ds_read_b64_tr_b16 v[236:237], v193 offset:52736\n"
"v_mfma_f32_16x16x32_bf16 v[164:167], v[208:211], a[52:55], v[164:167]\n"
"s_waitcnt lgkmcnt(7)\n"
"buffer_load_dwordx4 v247, s[8:11], s66 offen offset:256 nt lds\n"
"v_mfma_f32_16x16x32_bf16 v[172:175], v[216:219], a[52:55], v[172:175]\n"
"ds_read_b64_tr_b16 v[238:239], v193 offset:60928\n"
"ds_read_b64_tr_b16 v[208:209], v194 offset:0\n"
"v_mfma_f32_16x16x32_bf16 v[172:175], v[220:223], a[60:63], v[172:175]\n"
"s_add_u32 m0, s55, 0\n"
"ds_read_b64_tr_b16 v[210:211], v194 offset:8192\n"
"v_mul_f32 v113, s67, v113\n"
"v_mfma_f32_16x16x32_bf16 v[168:171], v[220:223], a[56:59], v[168:171]\n"
"buffer_load_dwordx4 v247, s[8:11], s66 offen offset:384 nt lds\n"
"v_mfma_f32_16x16x32_bf16 v[168:171], v[216:219], a[48:51], v[168:171]\n"
"s_waitcnt lgkmcnt(6)\n"
"ds_read_b64_tr_b16 v[212:213], v194 offset:2048\n"
"ds_read_b64_tr_b16 v[214:215], v194 offset:10240\n"
"v_mfma_f32_16x16x32_bf16 v[176:179], v[224:227], a[48:51], v[176:179]\n"
"buffer_store_dwordx4 v[252:255], v241, s[28:31], 0 offen offset:0 sc1\n"
"v_mfma_f32_16x16x32_bf16 v[176:179], v[228:231], a[56:59], v[176:179]\n"
"ds_read_b64_tr_b16 v[216:217], v195 offset:0\n"
"ds_read_b64_tr_b16 v[218:219], v195 offset:8192\n"
"v_mfma_f32_16x16x32_bf16 v[180:183], v[228:231], a[60:63], v[180:183]\n"
"ds_read_b64_tr_b16 v[220:221], v195 offset:2048\n"
"v_mul_f32 v114, s67, v114\n"
"v_mfma_f32_16x16x32_bf16 v[180:183], v[224:227], a[52:55], v[180:183]\n"
"s_waitcnt lgkmcnt(7)\n"
"ds_read_b64_tr_b16 v[222:223], v195 offset:10240\n"
"v_mul_f32 v115, s67, v115\n"
"v_mfma_f32_16x16x32_bf16 v[188:191], v[232:235], a[52:55], v[188:191]\n"
"ds_read_b64_tr_b16 v[224:225], v194 offset:512\n"
"v_cvt_pk_bf16_f32 v12, v112, v113\n"
"v_mfma_f32_16x16x32_bf16 v[188:191], v[236:239], a[60:63], v[188:191]\n"
"ds_read_b64_tr_b16 v[226:227], v194 offset:8704\n"
"v_cvt_pk_bf16_f32 v13, v114, v115\n"
"v_mfma_f32_16x16x32_bf16 v[184:187], v[236:239], a[56:59], v[184:187]\n"
"s_waitcnt vmcnt(14)\n"
"s_barrier\n"
"v_mfma_f32_16x16x32_bf16 v[184:187], v[232:235], a[48:51], v[184:187]\n"
"s_waitcnt lgkmcnt(6)\n"
"ds_read_b64_tr_b16 v[228:229], v194 offset:2560\n"
"ds_read_b64_tr_b16 v[230:231], v194 offset:10752\n"
"v_mfma_f32_16x16x32_bf16 v[128:131], v[208:211], a[64:67], v[128:131]\n"
"s_add_u32 s65, s37, 512\n"
"ds_read_b64_tr_b16 v[232:233], v195 offset:512\n"
"v_mul_f32 v120, s67, v120\n"
"v_mfma_f32_16x16x32_bf16 v[128:131], v[212:215], a[72:75], v[128:131]\n"
"s_add_u32 m0, s60, 49152\n"
"ds_read_b64_tr_b16 v[234:235], v195 offset:8704\n"
"v_mul_f32 v121, s67, v121\n"
"v_mfma_f32_16x16x32_bf16 v[132:135], v[212:215], a[76:79], v[132:135]\n"
"buffer_load_dwordx4 v204, s[16:19], s65 offen lds\n"
"v_mfma_f32_16x16x32_bf16 v[132:135], v[208:211], a[68:71], v[132:135]\n"
"s_waitcnt lgkmcnt(6)\n"
"ds_read_b64_tr_b16 v[236:237], v195 offset:2560\n"
"ds_read_b64_tr_b16 v[238:239], v195 offset:10752\n"
"v_mfma_f32_16x16x32_bf16 v[140:143], v[216:219], a[68:71], v[140:143]\n"
"ds_read_b64_tr_b16 v[208:209], v194 offset:1024\n"
"v_mul_f32 v122, s67, v122\n"
"v_mfma_f32_16x16x32_bf16 v[140:143], v[220:223], a[76:79], v[140:143]\n"
"ds_read_b64_tr_b16 v[210:211], v194 offset:9216\n"
"v_mul_f32 v123, s67, v123\n"
"v_mfma_f32_16x16x32_bf16 v[136:139], v[220:223], a[72:75], v[136:139]\n"
"ds_read_b64_tr_b16 v[212:213], v194 offset:3072\n"
"v_cvt_pk_bf16_f32 v14, v120, v121\n"
"v_mfma_f32_16x16x32_bf16 v[136:139], v[216:219], a[64:67], v[136:139]\n"
"s_waitcnt lgkmcnt(7)\n"
"ds_read_b64_tr_b16 v[214:215], v194 offset:11264\n"
"v_cvt_pk_bf16_f32 v15, v122, v123\n"
"v_mfma_f32_16x16x32_bf16 v[144:147], v[224:227], a[64:67], v[144:147]\n"
"s_add_u32 m0, s60, 50048\n"
"ds_read_b64_tr_b16 v[216:217], v195 offset:1024\n"
"v_permlane16_swap_b32 v40, v42\n"
"v_mfma_f32_16x16x32_bf16 v[144:147], v[228:231], a[72:75], v[144:147]\n"
"buffer_load_dwordx4 v204, s[16:19], s65 offen offset:128 lds\n"
"v_mfma_f32_16x16x32_bf16 v[148:151], v[228:231], a[76:79], v[148:151]\n"
"ds_read_b64_tr_b16 v[218:219], v195 offset:9216\n"
"ds_read_b64_tr_b16 v[220:221], v195 offset:3072\n"
"v_mfma_f32_16x16x32_bf16 v[148:151], v[224:227], a[68:71], v[148:151]\n"
"s_waitcnt lgkmcnt(7)\n"
"ds_read_b64_tr_b16 v[222:223], v195 offset:11264\n"
"v_permlane16_swap_b32 v41, v43\n"
"v_mfma_f32_16x16x32_bf16 v[156:159], v[232:235], a[68:71], v[156:159]\n"
"ds_read_b64_tr_b16 v[224:225], v194 offset:1536\n"
"v_mul_f32 v116, s67, v116\n"
"v_mfma_f32_16x16x32_bf16 v[156:159], v[236:239], a[76:79], v[156:159]\n"
"ds_read_b64_tr_b16 v[226:227], v194 offset:9728\n"
"v_mul_f32 v117, s67, v117\n"
"v_mfma_f32_16x16x32_bf16 v[152:155], v[236:239], a[72:75], v[152:155]\n"
"s_add_u32 m0, s60, 51200\n"
"ds_read_b64_tr_b16 v[228:229], v194 offset:3584\n"
"v_mul_f32 v118, s67, v118\n"
"v_mfma_f32_16x16x32_bf16 v[152:155], v[232:235], a[64:67], v[152:155]\n"
"s_waitcnt lgkmcnt(7)\n"
"buffer_load_dwordx4 v205, s[16:19], s65 offen lds\n"
"v_mfma_f32_16x16x32_bf16 v[160:163], v[208:211], a[64:67], v[160:163]\n"
"ds_read_b64_tr_b16 v[230:231], v194 offset:11776\n"
"ds_read_b64_tr_b16 v[232:233], v195 offset:1536\n"
"v_mfma_f32_16x16x32_bf16 v[160:163], v[212:215], a[72:75], v[160:163]\n"
"ds_read_b64_tr_b16 v[234:235], v195 offset:9728\n"
"v_mul_f32 v119, s67, v119\n"
"v_mfma_f32_16x16x32_bf16 v[164:167], v[212:215], a[76:79], v[164:167]\n"
"ds_read_b64_tr_b16 v[236:237], v195 offset:3584\n"
"v_cvt_pk_bf16_f32 v44, v116, v117\n"
"v_mfma_f32_16x16x32_bf16 v[164:167], v[208:211], a[68:71], v[164:167]\n"
"s_waitcnt lgkmcnt(7)\n"
"ds_read_b64_tr_b16 v[238:239], v195 offset:11776\n"
"v_cvt_pk_bf16_f32 v45, v118, v119\n"
"v_mfma_f32_16x16x32_bf16 v[172:175], v[216:219], a[68:71], v[172:175]\n"
"ds_read_b64_tr_b16 v[208:209], v194 offset:16384\n"
"v_mul_f32 v124, s67, v124\n"
"v_mfma_f32_16x16x32_bf16 v[172:175], v[220:223], a[76:79], v[172:175]\n"
"s_add_u32 m0, s60, 52096\n"
"ds_read_b64_tr_b16 v[210:211], v194 offset:24576\n"
"v_mul_f32 v125, s67, v125\n"
"v_mfma_f32_16x16x32_bf16 v[168:171], v[220:223], a[72:75], v[168:171]\n"
"buffer_load_dwordx4 v205, s[16:19], s65 offen offset:128 lds\n"
"v_mfma_f32_16x16x32_bf16 v[168:171], v[216:219], a[64:67], v[168:171]\n"
"s_waitcnt lgkmcnt(6)\n"
"ds_read_b64_tr_b16 v[212:213], v194 offset:18432\n"
"ds_read_b64_tr_b16 v[214:215], v194 offset:26624\n"
"v_mfma_f32_16x16x32_bf16 v[176:179], v[224:227], a[64:67], v[176:179]\n"
"ds_read_b64_tr_b16 v[216:217], v195 offset:16384\n"
"v_mul_f32 v126, s67, v126\n"
"v_mfma_f32_16x16x32_bf16 v[176:179], v[228:231], a[72:75], v[176:179]\n"
"ds_read_b64_tr_b16 v[218:219], v195 offset:24576\n"
"v_mul_f32 v127, s67, v127\n"
"v_mfma_f32_16x16x32_bf16 v[180:183], v[228:231], a[76:79], v[180:183]\n"
"ds_read_b64_tr_b16 v[220:221], v195 offset:18432\n"
"v_cvt_pk_bf16_f32 v46, v124, v125\n"
"v_mfma_f32_16x16x32_bf16 v[180:183], v[224:227], a[68:71], v[180:183]\n"
"s_waitcnt lgkmcnt(7)\n"
"ds_read_b64_tr_b16 v[222:223], v195 offset:26624\n"
"v_cvt_pk_bf16_f32 v47, v126, v127\n"
"v_mfma_f32_16x16x32_bf16 v[188:191], v[232:235], a[68:71], v[188:191]\n"
"ds_read_b64_tr_b16 v[224:225], v194 offset:16896\n"
"v_permlane16_swap_b32 v12, v14\n"
"v_mfma_f32_16x16x32_bf16 v[188:191], v[236:239], a[76:79], v[188:191]\n"
"ds_read_b64_tr_b16 v[226:227], v194 offset:25088\n"
"v_permlane16_swap_b32 v13, v15\n"
"v_mfma_f32_16x16x32_bf16 v[184:187], v[236:239], a[72:75], v[184:187]\n"
"s_waitcnt vmcnt(13)\n"
"s_barrier\n"
"v_mfma_f32_16x16x32_bf16 v[184:187], v[232:235], a[64:67], v[184:187]\n"
"s_waitcnt lgkmcnt(6)\n"
"ds_read_b64_tr_b16 v[228:229], v194 offset:18944\n"
"ds_read_b64_tr_b16 v[230:231], v194 offset:27136\n"
"v_mfma_f32_16x16x32_bf16 v[128:131], v[208:211], a[80:83], v[128:131]\n"
"s_add_u32 s65, s38, 512\n"
"ds_read_b64_tr_b16 v[232:233], v195 offset:16896\n"
"v_permlane16_swap_b32 v44, v46\n"
"v_mfma_f32_16x16x32_bf16 v[128:131], v[212:215], a[88:91], v[128:131]\n"
"s_add_u32 m0, s60, 65536\n"
"ds_read_b64_tr_b16 v[234:235], v195 offset:25088\n"
"v_permlane16_swap_b32 v45, v47\n"
"v_mfma_f32_16x16x32_bf16 v[132:135], v[212:215], a[92:95], v[132:135]\n"
"buffer_load_dwordx4 v204, s[16:19], s65 offen lds\n"
"v_mfma_f32_16x16x32_bf16 v[132:135], v[208:211], a[84:87], v[132:135]\n"
"s_waitcnt lgkmcnt(6)\n"
"ds_read_b64_tr_b16 v[236:237], v195 offset:18944\n"
"ds_read_b64_tr_b16 v[238:239], v195 offset:27136\n"
"v_mfma_f32_16x16x32_bf16 v[140:143], v[216:219], a[84:87], v[140:143]\n"
"ds_read_b64_tr_b16 v[208:209], v194 offset:17408\n"
"v_mfma_f32_16x16x32_bf16 v[140:143], v[220:223], a[92:95], v[140:143]\n"
"ds_read_b64_tr_b16 v[210:211], v194 offset:25600\n"
"v_mfma_f32_16x16x32_bf16 v[136:139], v[220:223], a[88:91], v[136:139]\n"
"ds_read_b64_tr_b16 v[212:213], v194 offset:19456\n"
"v_mfma_f32_16x16x32_bf16 v[136:139], v[216:219], a[80:83], v[136:139]\n"
"s_waitcnt lgkmcnt(7)\n"
"ds_read_b64_tr_b16 v[214:215], v194 offset:27648\n"
"v_mfma_f32_16x16x32_bf16 v[144:147], v[224:227], a[80:83], v[144:147]\n"
"s_add_u32 m0, s60, 66432\n"
"ds_read_b64_tr_b16 v[216:217], v195 offset:17408\n"
"v_mfma_f32_16x16x32_bf16 v[144:147], v[228:231], a[88:91], v[144:147]\n"
"buffer_load_dwordx4 v204, s[16:19], s65 offen offset:128 lds\n"
"v_mfma_f32_16x16x32_bf16 v[148:151], v[228:231], a[92:95], v[148:151]\n"
"ds_read_b64_tr_b16 v[218:219], v195 offset:25600\n"
"ds_read_b64_tr_b16 v[220:221], v195 offset:19456\n"
"v_mfma_f32_16x16x32_bf16 v[148:151], v[224:227], a[84:87], v[148:151]\n"
"s_waitcnt lgkmcnt(7)\n"
"ds_read_b64_tr_b16 v[222:223], v195 offset:27648\n"
"v_mfma_f32_16x16x32_bf16 v[156:159], v[232:235], a[84:87], v[156:159]\n"
"ds_read_b64_tr_b16 v[224:225], v194 offset:17920\n"
"v_mfma_f32_16x16x32_bf16 v[156:159], v[236:239], a[92:95], v[156:159]\n"
"ds_read_b64_tr_b16 v[226:227], v194 offset:26112\n"
"v_mfma_f32_16x16x32_bf16 v[152:155], v[236:239], a[88:91], v[152:155]\n"
"s_add_u32 m0, s60, 67584\n"
"ds_read_b64_tr_b16 v[228:229], v194 offset:19968\n"
"v_mfma_f32_16x16x32_bf16 v[152:155], v[232:235], a[80:83], v[152:155]\n"
"s_waitcnt lgkmcnt(7)\n"
"buffer_load_dwordx4 v205, s[16:19], s65 offen lds\n"
"v_mfma_f32_16x16x32_bf16 v[160:163], v[208:211], a[80:83], v[160:163]\n"
"ds_read_b64_tr_b16 v[230:231], v194 offset:28160\n"
"ds_read_b64_tr_b16 v[232:233], v195 offset:17920\n"
"v_mfma_f32_16x16x32_bf16 v[160:163], v[212:215], a[88:91], v[160:163]\n"
"ds_read_b64_tr_b16 v[234:235], v195 offset:26112\n"
"v_mfma_f32_16x16x32_bf16 v[164:167], v[212:215], a[92:95], v[164:167]\n"
"ds_read_b64_tr_b16 v[236:237], v195 offset:19968\n"
"v_mfma_f32_16x16x32_bf16 v[164:167], v[208:211], a[84:87], v[164:167]\n"
"s_waitcnt lgkmcnt(7)\n"
"ds_read_b64_tr_b16 v[238:239], v195 offset:28160\n"
"v_mfma_f32_16x16x32_bf16 v[172:175], v[216:219], a[84:87], v[172:175]\n"
"ds_read_b64_tr_b16 v[208:209], v194 offset:32768\n"
"v_mfma_f32_16x16x32_bf16 v[172:175], v[220:223], a[92:95], v[172:175]\n"
"s_add_u32 m0, s60, 68480\n"
"ds_read_b64_tr_b16 v[210:211], v194 offset:40960\n"
"v_mfma_f32_16x16x32_bf16 v[168:171], v[220:223], a[88:91], v[168:171]\n"
"buffer_load_dwordx4 v205, s[16:19], s65 offen offset:128 lds\n"
"v_mfma_f32_16x16x32_bf16 v[168:171], v[216:219], a[80:83], v[168:171]\n"
"s_waitcnt lgkmcnt(6)\n"
"ds_read_b64_tr_b16 v[212:213], v194 offset:34816\n"
"ds_read_b64_tr_b16 v[214:215], v194 offset:43008\n"
"v_mfma_f32_16x16x32_bf16 v[176:179], v[224:227], a[80:83], v[176:179]\n"
"ds_read_b64_tr_b16 v[216:217], v195 offset:32768\n"
"v_mfma_f32_16x16x32_bf16 v[176:179], v[228:231], a[88:91], v[176:179]\n"
"ds_read_b64_tr_b16 v[218:219], v195 offset:40960\n"
"v_mfma_f32_16x16x32_bf16 v[180:183], v[228:231], a[92:95], v[180:183]\n"
"ds_read_b64_tr_b16 v[220:221], v195 offset:34816\n"
"v_mfma_f32_16x16x32_bf16 v[180:183], v[224:227], a[84:87], v[180:183]\n"
"s_waitcnt lgkmcnt(7)\n"
"ds_read_b64_tr_b16 v[222:223], v195 offset:43008\n"
"v_mfma_f32_16x16x32_bf16 v[188:191], v[232:235], a[84:87], v[188:191]\n"
"ds_read_b64_tr_b16 v[224:225], v194 offset:33280\n"
"v_mfma_f32_16x16x32_bf16 v[188:191], v[236:239], a[92:95], v[188:191]\n"
"ds_read_b64_tr_b16 v[226:227], v194 offset:41472\n"
"v_mfma_f32_16x16x32_bf16 v[184:187], v[236:239], a[88:91], v[184:187]\n"
"s_waitcnt vmcnt(13)\n"
"s_barrier\n"
"v_mfma_f32_16x16x32_bf16 v[184:187], v[232:235], a[80:83], v[184:187]\n"
"s_waitcnt lgkmcnt(6)\n"
"ds_read_b64_tr_b16 v[228:229], v194 offset:35328\n"
"ds_read_b64_tr_b16 v[230:231], v194 offset:43520\n"
"v_mfma_f32_16x16x32_bf16 v[128:131], v[208:211], a[96:99], v[128:131]\n"
"s_add_u32 s65, s39, 512\n"
"s_add_u32 m0, s60, 81920\n"
"ds_read_b64_tr_b16 v[232:233], v195 offset:33280\n"
"v_mfma_f32_16x16x32_bf16 v[128:131], v[212:215], a[104:107], v[128:131]\n"
"buffer_load_dwordx4 v204, s[16:19], s65 offen lds\n"
"v_mfma_f32_16x16x32_bf16 v[132:135], v[212:215], a[108:111], v[132:135]\n"
"ds_read_b64_tr_b16 v[234:235], v195 offset:41472\n"
"ds_read_b64_tr_b16 v[236:237], v195 offset:35328\n"
"v_mfma_f32_16x16x32_bf16 v[132:135], v[208:211], a[100:103], v[132:135]\n"
"s_waitcnt lgkmcnt(7)\n"
"ds_read_b64_tr_b16 v[238:239], v195 offset:43520\n"
"v_mfma_f32_16x16x32_bf16 v[140:143], v[216:219], a[100:103], v[140:143]\n"
"s_add_u32 m0, s60, 82816\n"
"ds_read_b64_tr_b16 v[208:209], v194 offset:33792\n"
"v_mfma_f32_16x16x32_bf16 v[140:143], v[220:223], a[108:111], v[140:143]\n"
"buffer_load_dwordx4 v204, s[16:19], s65 offen offset:128 lds\n"
"v_mfma_f32_16x16x32_bf16 v[136:139], v[220:223], a[104:107], v[136:139]\n"
"ds_read_b64_tr_b16 v[210:211], v194 offset:41984\n"
"ds_read_b64_tr_b16 v[212:213], v194 offset:35840\n"
"v_mfma_f32_16x16x32_bf16 v[136:139], v[216:219], a[96:99], v[136:139]\n"
"s_waitcnt lgkmcnt(7)\n"
"ds_read_b64_tr_b16 v[214:215], v194 offset:44032\n"
"v_mfma_f32_16x16x32_bf16 v[144:147], v[224:227], a[96:99], v[144:147]\n"
"s_add_u32 m0, s60, 83968\n"
"ds_read_b64_tr_b16 v[216:217], v195 offset:33792\n"
"v_mul_f32 v132, s67, v132\n"
"v_mfma_f32_16x16x32_bf16 v[144:147], v[228:231], a[104:107], v[144:147]\n"
"buffer_load_dwordx4 v205, s[16:19], s65 offen lds\n"
"v_mfma_f32_16x16x32_bf16 v[148:151], v[228:231], a[108:111], v[148:151]\n"
"s_add_u32 m0, s60, 84864\n"
"ds_read_b64_tr_b16 v[218:219], v195 offset:41984\n"
"ds_read_b64_tr_b16 v[220:221], v195 offset:35840\n"
"v_mfma_f32_16x16x32_bf16 v[148:151], v[224:227], a[100:103], v[148:151]\n"
"s_waitcnt lgkmcnt(7)\n"
"buffer_load_dwordx4 v205, s[16:19], s65 offen offset:128 lds\n"
"v_mfma_f32_16x16x32_bf16 v[156:159], v[232:235], a[100:103], v[156:159]\n"
"ds_read_b64_tr_b16 v[222:223], v195 offset:44032\n"
"ds_read_b64_tr_b16 v[224:225], v194 offset:34304\n"
"v_mfma_f32_16x16x32_bf16 v[156:159], v[236:239], a[108:111], v[156:159]\n"
"ds_read_b64_tr_b16 v[226:227], v194 offset:42496\n"
"v_mul_f32 v128, s67, v128\n"
"v_mfma_f32_16x16x32_bf16 v[152:155], v[236:239], a[104:107], v[152:155]\n"
"s_add_u32 s66, s73, 16384\n"
"s_and_b32 s66, s66, 524287\n"
"ds_read_b64_tr_b16 v[228:229], v194 offset:36352\n"
"v_mfma_f32_16x16x32_bf16 v[152:155], v[232:235], a[96:99], v[152:155]\n"
"s_waitcnt lgkmcnt(7)\n"
"s_add_u32 m0, s52, 32768\n"
"ds_read_b64_tr_b16 v[230:231], v194 offset:44544\n"
"v_mfma_f32_16x16x32_bf16 v[160:163], v[208:211], a[96:99], v[160:163]\n"
"buffer_load_dwordx4 v247, s[8:11], s66 offen nt lds\n"
"v_mfma_f32_16x16x32_bf16 v[160:163], v[212:215], a[104:107], v[160:163]\n"
"s_add_u32 m0, s53, 32768\n"
"ds_read_b64_tr_b16 v[232:233], v195 offset:34304\n"
"ds_read_b64_tr_b16 v[234:235], v195 offset:42496\n"
"v_mfma_f32_16x16x32_bf16 v[164:167], v[212:215], a[108:111], v[164:167]\n"
"buffer_load_dwordx4 v247, s[8:11], s66 offen offset:128 nt lds\n"
"v_mfma_f32_16x16x32_bf16 v[164:167], v[208:211], a[100:103], v[164:167]\n"
"s_waitcnt lgkmcnt(6)\n"
"ds_read_b64_tr_b16 v[236:237], v195 offset:36352\n"
"ds_read_b64_tr_b16 v[238:239], v195 offset:44544\n"
"v_mfma_f32_16x16x32_bf16 v[172:175], v[216:219], a[100:103], v[172:175]\n"
"ds_read_b64_tr_b16 v[208:209], v194 offset:49152\n"
"v_mul_f32 v129, s67, v129\n"
"v_mfma_f32_16x16x32_bf16 v[172:175], v[220:223], a[108:111], v[172:175]\n"
"s_add_u32 m0, s54, 32768\n"
"ds_read_b64_tr_b16 v[210:211], v194 offset:57344\n"
"v_mul_f32 v130, s67, v130\n"
"v_mfma_f32_16x16x32_bf16 v[168:171], v[220:223], a[104:107], v[168:171]\n"
"buffer_load_dwordx4 v247, s[8:11], s66 offen offset:256 nt lds\n"
"v_mfma_f32_16x16x32_bf16 v[168:171], v[216:219], a[96:99], v[168:171]\n"
"s_waitcnt lgkmcnt(6)\n"
"ds_read_b64_tr_b16 v[212:213], v194 offset:51200\n"
"ds_read_b64_tr_b16 v[214:215], v194 offset:59392\n"
"v_mfma_f32_16x16x32_bf16 v[176:179], v[224:227], a[96:99], v[176:179]\n"
"s_add_u32 m0, s55, 32768\n"
"ds_read_b64_tr_b16 v[216:217], v195 offset:49152\n"
"v_mul_f32 v131, s67, v131\n"
"v_mfma_f32_16x16x32_bf16 v[176:179], v[228:231], a[104:107], v[176:179]\n"
"buffer_load_dwordx4 v247, s[8:11], s66 offen offset:384 nt lds\n"
"v_mfma_f32_16x16x32_bf16 v[180:183], v[228:231], a[108:111], v[180:183]\n"
"IWA2_%=:\n"
"s_cbranch_execz IRA_%=\n"
"ds_read_b64_tr_b16 v[218:219], v195 offset:57344\n"
"ds_read_b64_tr_b16 v[220:221], v195 offset:51200\n"
"v_mfma_f32_16x16x32_bf16 v[180:183], v[224:227], a[100:103], v[180:183]\n"
"s_waitcnt lgkmcnt(7)\n"
"ds_read_b64_tr_b16 v[222:223], v195 offset:59392\n"
"v_cvt_pk_bf16_f32 v16, v128, v129\n"
"v_mfma_f32_16x16x32_bf16 v[188:191], v[232:235], a[100:103], v[188:191]\n"
"ds_read_b64_tr_b16 v[224:225], v194 offset:49664\n"
"v_cvt_pk_bf16_f32 v17, v130, v131\n"
"v_mfma_f32_16x16x32_bf16 v[188:191], v[236:239], a[108:111], v[188:191]\n"
"ds_read_b64_tr_b16 v[226:227], v194 offset:57856\n"
"v_mul_f32 v136, s67, v136\n"
"v_mfma_f32_16x16x32_bf16 v[184:187], v[236:239], a[104:107], v[184:187]\n"
"s_waitcnt vmcnt(12)\n"
"s_barrier\n"
"v_mfma_f32_16x16x32_bf16 v[184:187], v[232:235], a[96:99], v[184:187]\n"
"s_waitcnt lgkmcnt(6)\n"
"ds_read_b64_tr_b16 v[228:229], v194 offset:51712\n"
"ds_read_b64_tr_b16 v[230:231], v194 offset:59904\n"
"v_mfma_f32_16x16x32_bf16 a[128:131], v[208:211], a[0:3], 0\n"
"s_add_u32 s65, s40, 512\n"
"ds_read_b64_tr_b16 v[232:233], v195 offset:49664\n"
"v_mul_f32 v137, s67, v137\n"
"v_mfma_f32_16x16x32_bf16 a[128:131], v[212:215], a[8:11], a[128:131]\n"
"s_add_u32 m0, s60, 98304\n"
"ds_read_b64_tr_b16 v[234:235], v195 offset:57856\n"
"v_mul_f32 v138, s67, v138\n"
"v_mfma_f32_16x16x32_bf16 a[132:135], v[212:215], a[12:15], 0\n"
"buffer_load_dwordx4 v204, s[16:19], s65 offen lds\n"
"v_mfma_f32_16x16x32_bf16 a[132:135], v[208:211], a[4:7], a[132:135]\n"
"s_waitcnt lgkmcnt(6)\n"
"ds_read_b64_tr_b16 v[236:237], v195 offset:51712\n"
"ds_read_b64_tr_b16 v[238:239], v195 offset:59904\n"
"v_mfma_f32_16x16x32_bf16 a[140:143], v[216:219], a[4:7], 0\n"
"ds_read_b64_tr_b16 v[208:209], v194 offset:50176\n"
"v_mul_f32 v139, s67, v139\n"
"v_mfma_f32_16x16x32_bf16 a[140:143], v[220:223], a[12:15], a[140:143]\n"
"ds_read_b64_tr_b16 v[210:211], v194 offset:58368\n"
"v_cvt_pk_bf16_f32 v18, v136, v137\n"
"v_mfma_f32_16x16x32_bf16 a[136:139], v[220:223], a[8:11], 0\n"
"ds_read_b64_tr_b16 v[212:213], v194 offset:52224\n"
"v_cvt_pk_bf16_f32 v19, v138, v139\n"
"v_mfma_f32_16x16x32_bf16 a[136:139], v[216:219], a[0:3], a[136:139]\n"
"s_waitcnt lgkmcnt(7)\n"
"ds_read_b64_tr_b16 v[214:215], v194 offset:60416\n"
"v_mul_f32 v133, s67, v133\n"
"v_mfma_f32_16x16x32_bf16 a[144:147], v[224:227], a[0:3], 0\n"
"s_add_u32 m0, s60, 99200\n"
"ds_read_b64_tr_b16 v[216:217], v195 offset:50176\n"
"v_mul_f32 v134, s67, v134\n"
"v_mfma_f32_16x16x32_bf16 a[144:147], v[228:231], a[8:11], a[144:147]\n"
"buffer_load_dwordx4 v204, s[16:19], s65 offen offset:128 lds\n"
"v_mfma_f32_16x16x32_bf16 a[148:151], v[228:231], a[12:15], 0\n"
"ds_read_b64_tr_b16 v[218:219], v195 offset:58368\n"
"ds_read_b64_tr_b16 v[220:221], v195 offset:52224\n"
"v_mfma_f32_16x16x32_bf16 a[148:151], v[224:227], a[4:7], a[148:151]\n"
"s_waitcnt lgkmcnt(7)\n"
"ds_read_b64_tr_b16 v[222:223], v195 offset:60416\n"
"v_mul_f32 v135, s67, v135\n"
"v_mfma_f32_16x16x32_bf16 a[156:159], v[232:235], a[4:7], 0\n"
"ds_read_b64_tr_b16 v[224:225], v194 offset:50688\n"
"v_cvt_pk_bf16_f32 v48, v132, v133\n"
"v_mfma_f32_16x16x32_bf16 a[156:159], v[236:239], a[12:15], a[156:159]\n"
"ds_read_b64_tr_b16 v[226:227], v194 offset:58880\n"
"v_cvt_pk_bf16_f32 v49, v134, v135\n"
"v_mfma_f32_16x16x32_bf16 a[152:155], v[236:239], a[8:11], 0\n"
"s_add_u32 m0, s60, 100352\n"
"ds_read_b64_tr_b16 v[228:229], v194 offset:52736\n"
"v_mul_f32 v140, s67, v140\n"
"v_mfma_f32_16x16x32_bf16 a[152:155], v[232:235], a[0:3], a[152:155]\n"
"s_waitcnt lgkmcnt(7)\n"
"buffer_load_dwordx4 v205, s[16:19], s65 offen lds\n"
"v_mfma_f32_16x16x32_bf16 a[160:163], v[208:211], a[0:3], 0\n"
"ds_read_b64_tr_b16 v[230:231], v194 offset:60928\n"
"ds_read_b64_tr_b16 v[232:233], v195 offset:50688\n"
"v_mfma_f32_16x16x32_bf16 a[160:163], v[212:215], a[8:11], a[160:163]\n"
"ds_read_b64_tr_b16 v[234:235], v195 offset:58880\n"
"v_mul_f32 v141, s67, v141\n"
"v_mfma_f32_16x16x32_bf16 a[164:167], v[212:215], a[12:15], 0\n"
"ds_read_b64_tr_b16 v[236:237], v195 offset:52736\n"
"v_mul_f32 v142, s67, v142\n"
"v_mfma_f32_16x16x32_bf16 a[164:167], v[208:211], a[4:7], a[164:167]\n"
"s_waitcnt lgkmcnt(7)\n"
"ds_read_b64_tr_b16 v[238:239], v195 offset:60928\n"
"v_mul_f32 v143, s67, v143\n"
"v_mfma_f32_16x16x32_bf16 a[172:175], v[216:219], a[4:7], 0\n"
"ds_read_b64_tr_b16 v[208:209], v192 offset:49152\n"
"v_cvt_pk_bf16_f32 v50, v140, v141\n"
"v_mfma_f32_16x16x32_bf16 a[172:175], v[220:223], a[12:15], a[172:175]\n"
"s_add_u32 m0, s60, 101248\n"
"ds_read_b64_tr_b16 v[210:211], v192 offset:57344\n"
"v_permlane16_swap_b32 v16, v18\n"
"v_mfma_f32_16x16x32_bf16 a[168:171], v[220:223], a[8:11], 0\n"
"buffer_load_dwordx4 v205, s[16:19], s65 offen offset:128 lds\n"
"v_mfma_f32_16x16x32_bf16 a[168:171], v[216:219], a[0:3], a[168:171]\n"
"s_waitcnt lgkmcnt(6)\n"
"ds_read_b64_tr_b16 v[212:213], v192 offset:51200\n"
"ds_read_b64_tr_b16 v[214:215], v192 offset:59392\n"
"v_mfma_f32_16x16x32_bf16 a[176:179], v[224:227], a[0:3], 0\n"
"ds_read_b64_tr_b16 v[216:217], v193 offset:49152\n"
"v_cvt_pk_bf16_f32 v51, v142, v143\n"
"v_mfma_f32_16x16x32_bf16 a[176:179], v[228:231], a[8:11], a[176:179]\n"
"ds_read_b64_tr_b16 v[218:219], v193 offset:57344\n"
"v_permlane16_swap_b32 v17, v19\n"
"v_mfma_f32_16x16x32_bf16 a[180:183], v[228:231], a[12:15], 0\n"
"ds_read_b64_tr_b16 v[220:221], v193 offset:51200\n"
"v_mul_f32 v144, s67, v144\n"
"v_mfma_f32_16x16x32_bf16 a[180:183], v[224:227], a[4:7], a[180:183]\n"
"s_waitcnt lgkmcnt(7)\n"
"ds_read_b64_tr_b16 v[222:223], v193 offset:59392\n"
"v_mul_f32 v145, s67, v145\n"
"v_mfma_f32_16x16x32_bf16 a[188:191], v[232:235], a[4:7], 0\n"
"ds_read_b64_tr_b16 v[224:225], v192 offset:49664\n"
"v_mul_f32 v146, s67, v146\n"
"v_mfma_f32_16x16x32_bf16 a[188:191], v[236:239], a[12:15], a[188:191]\n"
"ds_read_b64_tr_b16 v[226:227], v192 offset:57856\n"
"v_mul_f32 v147, s67, v147\n"
"v_mfma_f32_16x16x32_bf16 a[184:187], v[236:239], a[8:11], 0\n"
"s_waitcnt vmcnt(12)\n"
"s_barrier\n"
"v_mfma_f32_16x16x32_bf16 a[184:187], v[232:235], a[0:3], a[184:187]\n"
"s_waitcnt lgkmcnt(6)\n"
"ds_read_b64_tr_b16 v[228:229], v192 offset:51712\n"
"ds_read_b64_tr_b16 v[230:231], v192 offset:59904\n"
"v_mfma_f32_16x16x32_bf16 a[128:131], v[208:211], a[16:19], a[128:131]\n"
"s_add_u32 s65, s41, 512\n"
"ds_read_b64_tr_b16 v[232:233], v193 offset:49664\n"
"v_mul_f32 v152, s67, v152\n"
"v_mfma_f32_16x16x32_bf16 a[128:131], v[212:215], a[24:27], a[128:131]\n"
"s_add_u32 m0, s60, 114688\n"
"ds_read_b64_tr_b16 v[234:235], v193 offset:57856\n"
"v_mul_f32 v153, s67, v153\n"
"v_mfma_f32_16x16x32_bf16 a[132:135], v[212:215], a[28:31], a[132:135]\n"
"buffer_load_dwordx4 v204, s[16:19], s65 offen lds\n"
"v_mfma_f32_16x16x32_bf16 a[132:135], v[208:211], a[20:23], a[132:135]\n"
"s_waitcnt lgkmcnt(6)\n"
"ds_read_b64_tr_b16 v[236:237], v193 offset:51712\n"
"ds_read_b64_tr_b16 v[238:239], v193 offset:59904\n"
"v_mfma_f32_16x16x32_bf16 a[140:143], v[216:219], a[20:23], a[140:143]\n"
"ds_read_b64_tr_b16 v[208:209], v192 offset:50176\n"
"v_cvt_pk_bf16_f32 v20, v144, v145\n"
"v_mfma_f32_16x16x32_bf16 a[140:143], v[220:223], a[28:31], a[140:143]\n"
"ds_read_b64_tr_b16 v[210:211], v192 offset:58368\n"
"v_cvt_pk_bf16_f32 v21, v146, v147\n"
"v_mfma_f32_16x16x32_bf16 a[136:139], v[220:223], a[24:27], a[136:139]\n"
"ds_read_b64_tr_b16 v[212:213], v192 offset:52224\n"
"v_mul_f32 v154, s67, v154\n"
"v_mfma_f32_16x16x32_bf16 a[136:139], v[216:219], a[16:19], a[136:139]\n"
"s_waitcnt lgkmcnt(7)\n"
"ds_read_b64_tr_b16 v[214:215], v192 offset:60416\n"
"v_mul_f32 v155, s67, v155\n"
"v_mfma_f32_16x16x32_bf16 a[144:147], v[224:227], a[16:19], a[144:147]\n"
"s_add_u32 m0, s60, 115584\n"
"ds_read_b64_tr_b16 v[216:217], v193 offset:50176\n"
"v_permlane16_swap_b32 v48, v50\n"
"v_mfma_f32_16x16x32_bf16 a[144:147], v[228:231], a[24:27], a[144:147]\n"
"buffer_load_dwordx4 v204, s[16:19], s65 offen offset:128 lds\n"
"v_mfma_f32_16x16x32_bf16 a[148:151], v[228:231], a[28:31], a[148:151]\n"
"ds_read_b64_tr_b16 v[218:219], v193 offset:58368\n"
"ds_read_b64_tr_b16 v[220:221], v193 offset:52224\n"
"v_mfma_f32_16x16x32_bf16 a[148:151], v[224:227], a[20:23], a[148:151]\n"
"s_waitcnt lgkmcnt(7)\n"
"ds_read_b64_tr_b16 v[222:223], v193 offset:60416\n"
"v_cvt_pk_bf16_f32 v22, v152, v153\n"
"v_mfma_f32_16x16x32_bf16 a[156:159], v[232:235], a[20:23], a[156:159]\n"
"ds_read_b64_tr_b16 v[224:225], v192 offset:50688\n"
"v_cvt_pk_bf16_f32 v23, v154, v155\n"
"v_mfma_f32_16x16x32_bf16 a[156:159], v[236:239], a[28:31], a[156:159]\n"
"ds_read_b64_tr_b16 v[226:227], v192 offset:58880\n"
"v_permlane16_swap_b32 v49, v51\n"
"v_mfma_f32_16x16x32_bf16 a[152:155], v[236:239], a[24:27], a[152:155]\n"
"s_add_u32 m0, s60, 116736\n"
"ds_read_b64_tr_b16 v[228:229], v192 offset:52736\n"
"v_mul_f32 v148, s67, v148\n"
"v_mfma_f32_16x16x32_bf16 a[152:155], v[232:235], a[16:19], a[152:155]\n"
"s_waitcnt lgkmcnt(7)\n"
"buffer_load_dwordx4 v205, s[16:19], s65 offen lds\n"
"v_mfma_f32_16x16x32_bf16 a[160:163], v[208:211], a[16:19], a[160:163]\n"
"ds_read_b64_tr_b16 v[230:231], v192 offset:60928\n"
"ds_read_b64_tr_b16 v[232:233], v193 offset:50688\n"
"v_mfma_f32_16x16x32_bf16 a[160:163], v[212:215], a[24:27], a[160:163]\n"
"ds_read_b64_tr_b16 v[234:235], v193 offset:58880\n"
"v_mul_f32 v149, s67, v149\n"
"v_mfma_f32_16x16x32_bf16 a[164:167], v[212:215], a[28:31], a[164:167]\n"
"ds_read_b64_tr_b16 v[236:237], v193 offset:52736\n"
"v_mul_f32 v150, s67, v150\n"
"v_mfma_f32_16x16x32_bf16 a[164:167], v[208:211], a[20:23], a[164:167]\n"
"s_waitcnt lgkmcnt(7)\n"
"ds_read_b64_tr_b16 v[238:239], v193 offset:60928\n"
"v_mul_f32 v151, s67, v151\n"
"v_mfma_f32_16x16x32_bf16 a[172:175], v[216:219], a[20:23], a[172:175]\n"
"ds_read_b64_tr_b16 v[208:209], v194 offset:0\n"
"v_cvt_pk_bf16_f32 v52, v148, v149\n"
"v_mfma_f32_16x16x32_bf16 a[172:175], v[220:223], a[28:31], a[172:175]\n"
"s_add_u32 m0, s60, 117632\n"
"ds_read_b64_tr_b16 v[210:211], v194 offset:8192\n"
"v_mul_f32 v156, s67, v156\n"
"v_mfma_f32_16x16x32_bf16 a[168:171], v[220:223], a[24:27], a[168:171]\n"
"buffer_load_dwordx4 v205, s[16:19], s65 offen offset:128 lds\n"
"v_mfma_f32_16x16x32_bf16 a[168:171], v[216:219], a[16:19], a[168:171]\n"
"s_waitcnt lgkmcnt(6)\n"
"ds_read_b64_tr_b16 v[212:213], v194 offset:2048\n"
"ds_read_b64_tr_b16 v[214:215], v194 offset:10240\n"
"v_mfma_f32_16x16x32_bf16 a[176:179], v[224:227], a[16:19], a[176:179]\n"
"ds_read_b64_tr_b16 v[216:217], v195 offset:0\n"
"v_cvt_pk_bf16_f32 v53, v150, v151\n"
"v_mfma_f32_16x16x32_bf16 a[176:179], v[228:231], a[24:27], a[176:179]\n"
"ds_read_b64_tr_b16 v[218:219], v195 offset:8192\n"
"v_mul_f32 v157, s67, v157\n"
"v_mfma_f32_16x16x32_bf16 a[180:183], v[228:231], a[28:31], a[180:183]\n"
"ds_read_b64_tr_b16 v[220:221], v195 offset:2048\n"
"v_mul_f32 v158, s67, v158\n"
"v_mfma_f32_16x16x32_bf16 a[180:183], v[224:227], a[20:23], a[180:183]\n"
"s_waitcnt lgkmcnt(7)\n"
"ds_read_b64_tr_b16 v[222:223], v195 offset:10240\n"
"v_mul_f32 v159, s67, v159\n"
"v_mfma_f32_16x16x32_bf16 a[188:191], v[232:235], a[20:23], a[188:191]\n"
"ds_read_b64_tr_b16 v[224:225], v194 offset:512\n"
"v_cvt_pk_bf16_f32 v54, v156, v157\n"
"v_mfma_f32_16x16x32_bf16 a[188:191], v[236:239], a[28:31], a[188:191]\n"
"ds_read_b64_tr_b16 v[226:227], v194 offset:8704\n"
"v_cvt_pk_bf16_f32 v55, v158, v159\n"
"v_mfma_f32_16x16x32_bf16 a[184:187], v[236:239], a[24:27], a[184:187]\n"
"s_waitcnt vmcnt(12)\n"
"s_barrier\n"
"v_mfma_f32_16x16x32_bf16 a[184:187], v[232:235], a[16:19], a[184:187]\n"
"s_waitcnt lgkmcnt(6)\n"
"ds_read_b64_tr_b16 v[228:229], v194 offset:2560\n"
"ds_read_b64_tr_b16 v[230:231], v194 offset:10752\n"
"v_mfma_f32_16x16x32_bf16 a[128:131], v[208:211], a[32:35], a[128:131]\n"
"s_add_u32 s65, s42, 512\n"
"s_add_u32 m0, s60, 131072\n"
"ds_read_b64_tr_b16 v[232:233], v195 offset:512\n"
"v_mfma_f32_16x16x32_bf16 a[128:131], v[212:215], a[40:43], a[128:131]\n"
"buffer_load_dwordx4 v204, s[16:19], s65 offen lds\n"
"v_mfma_f32_16x16x32_bf16 a[132:135], v[212:215], a[44:47], a[132:135]\n"
"ds_read_b64_tr_b16 v[234:235], v195 offset:8704\n"
"ds_read_b64_tr_b16 v[236:237], v195 offset:2560\n"
"v_mfma_f32_16x16x32_bf16 a[132:135], v[208:211], a[36:39], a[132:135]\n"
"s_waitcnt lgkmcnt(7)\n"
"ds_read_b64_tr_b16 v[238:239], v195 offset:10752\n"
"v_permlane16_swap_b32 v20, v22\n"
"v_mfma_f32_16x16x32_bf16 a[140:143], v[216:219], a[36:39], a[140:143]\n"
"s_add_u32 m0, s60, 131968\n"
"ds_read_b64_tr_b16 v[208:209], v194 offset:1024\n"
"v_permlane16_swap_b32 v21, v23\n"
"v_mfma_f32_16x16x32_bf16 a[140:143], v[220:223], a[44:47], a[140:143]\n"
"buffer_load_dwordx4 v204, s[16:19], s65 offen offset:128 lds\n"
"v_mfma_f32_16x16x32_bf16 a[136:139], v[220:223], a[40:43], a[136:139]\n"
"ds_read_b64_tr_b16 v[210:211], v194 offset:9216\n"
"ds_read_b64_tr_b16 v[212:213], v194 offset:3072\n"
"v_mfma_f32_16x16x32_bf16 a[136:139], v[216:219], a[32:35], a[136:139]\n"
"s_waitcnt lgkmcnt(7)\n"
"ds_read_b64_tr_b16 v[214:215], v194 offset:11264\n"
"v_mul_f32 v160, s67, v160\n"
"v_mfma_f32_16x16x32_bf16 a[144:147], v[224:227], a[32:35], a[144:147]\n"
"s_add_u32 m0, s60, 133120\n"
"ds_read_b64_tr_b16 v[216:217], v195 offset:1024\n"
"v_mul_f32 v161, s67, v161\n"
"v_mfma_f32_16x16x32_bf16 a[144:147], v[228:231], a[40:43], a[144:147]\n"
"buffer_load_dwordx4 v205, s[16:19], s65 offen lds\n"
"v_mfma_f32_16x16x32_bf16 a[148:151], v[228:231], a[44:47], a[148:151]\n"
"s_add_u32 m0, s60, 134016\n"
"ds_read_b64_tr_b16 v[218:219], v195 offset:9216\n"
"ds_read_b64_tr_b16 v[220:221], v195 offset:3072\n"
"v_mfma_f32_16x16x32_bf16 a[148:151], v[224:227], a[36:39], a[148:151]\n"
"s_waitcnt lgkmcnt(7)\n"
"buffer_load_dwordx4 v205, s[16:19], s65 offen offset:128 lds\n"
"v_mfma_f32_16x16x32_bf16 a[156:159], v[232:235], a[36:39], a[156:159]\n"
"ds_read_b64_tr_b16 v[222:223], v195 offset:11264\n"
"ds_read_b64_tr_b16 v[224:225], v194 offset:1536\n"
"v_mfma_f32_16x16x32_bf16 a[156:159], v[236:239], a[44:47], a[156:159]\n"
"ds_read_b64_tr_b16 v[226:227], v194 offset:9728\n"
"v_mul_f32 v162, s67, v162\n"
"v_mfma_f32_16x16x32_bf16 a[152:155], v[236:239], a[40:43], a[152:155]\n"
"s_add_u32 s66, s73, 0\n"
"s_and_b32 s66, s66, 524287\n"
"ds_read_b64_tr_b16 v[228:229], v194 offset:3584\n"
"v_mfma_f32_16x16x32_bf16 a[152:155], v[232:235], a[32:35], a[152:155]\n"
"s_waitcnt lgkmcnt(7)\n"
"s_add_u32 m0, s56, 32768\n"
"ds_read_b64_tr_b16 v[230:231], v194 offset:11776\n"
"v_mfma_f32_16x16x32_bf16 a[160:163], v[208:211], a[32:35], a[160:163]\n"
"buffer_load_dwordx4 v248, s[12:15], s66 offen lds\n"
"v_mfma_f32_16x16x32_bf16 a[160:163], v[212:215], a[40:43], a[160:163]\n"
"s_add_u32 m0, s57, 32768\n"
"ds_read_b64_tr_b16 v[232:233], v195 offset:1536\n"
"ds_read_b64_tr_b16 v[234:235], v195 offset:9728\n"
"v_mfma_f32_16x16x32_bf16 a[164:167], v[212:215], a[44:47], a[164:167]\n"
"buffer_load_dwordx4 v248, s[12:15], s66 offen offset:128 lds\n"
"v_mfma_f32_16x16x32_bf16 a[164:167], v[208:211], a[36:39], a[164:167]\n"
"s_waitcnt lgkmcnt(6)\n"
"ds_read_b64_tr_b16 v[236:237], v195 offset:3584\n"
"ds_read_b64_tr_b16 v[238:239], v195 offset:11776\n"
"v_mfma_f32_16x16x32_bf16 a[172:175], v[216:219], a[36:39], a[172:175]\n"
"ds_read_b64_tr_b16 v[208:209], v194 offset:16384\n"
"v_mul_f32 v163, s67, v163\n"
"v_mfma_f32_16x16x32_bf16 a[172:175], v[220:223], a[44:47], a[172:175]\n"
"s_add_u32 m0, s58, 32768\n"
"ds_read_b64_tr_b16 v[210:211], v194 offset:24576\n"
"v_mul_f32 v168, s67, v168\n"
"v_mfma_f32_16x16x32_bf16 a[168:171], v[220:223], a[40:43], a[168:171]\n"
"buffer_load_dwordx4 v248, s[12:15], s66 offen offset:256 lds\n"
"v_mfma_f32_16x16x32_bf16 a[168:171], v[216:219], a[32:35], a[168:171]\n"
"s_waitcnt lgkmcnt(6)\n"
"ds_read_b64_tr_b16 v[212:213], v194 offset:18432\n"
"ds_read_b64_tr_b16 v[214:215], v194 offset:26624\n"
"v_mfma_f32_16x16x32_bf16 a[176:179], v[224:227], a[32:35], a[176:179]\n"
"s_add_u32 m0, s59, 32768\n"
"ds_read_b64_tr_b16 v[216:217], v195 offset:16384\n"
"v_mul_f32 v169, s67, v169\n"
"v_mfma_f32_16x16x32_bf16 a[176:179], v[228:231], a[40:43], a[176:179]\n"
"buffer_load_dwordx4 v248, s[12:15], s66 offen offset:384 lds\n"
"v_mfma_f32_16x16x32_bf16 a[180:183], v[228:231], a[44:47], a[180:183]\n"
"ds_read_b64_tr_b16 v[218:219], v195 offset:24576\n"
"ds_read_b64_tr_b16 v[220:221], v195 offset:18432\n"
"v_mfma_f32_16x16x32_bf16 a[180:183], v[224:227], a[36:39], a[180:183]\n"
"s_waitcnt lgkmcnt(7)\n"
"ds_read_b64_tr_b16 v[222:223], v195 offset:26624\n"
"v_cvt_pk_bf16_f32 v24, v160, v161\n"
"v_mfma_f32_16x16x32_bf16 a[188:191], v[232:235], a[36:39], a[188:191]\n"
"ds_read_b64_tr_b16 v[224:225], v194 offset:16896\n"
"v_cvt_pk_bf16_f32 v25, v162, v163\n"
"v_mfma_f32_16x16x32_bf16 a[188:191], v[236:239], a[44:47], a[188:191]\n"
"ds_read_b64_tr_b16 v[226:227], v194 offset:25088\n"
"v_mul_f32 v170, s67, v170\n"
"v_mfma_f32_16x16x32_bf16 a[184:187], v[236:239], a[40:43], a[184:187]\n"
"s_waitcnt vmcnt(12)\n"
"s_barrier\n"
"v_mfma_f32_16x16x32_bf16 a[184:187], v[232:235], a[32:35], a[184:187]\n"
"s_waitcnt lgkmcnt(6)\n"
"ds_read_b64_tr_b16 v[228:229], v194 offset:18944\n"
"ds_read_b64_tr_b16 v[230:231], v194 offset:27136\n"
"v_mfma_f32_16x16x32_bf16 a[128:131], v[208:211], a[48:51], a[128:131]\n"
"s_add_u32 s65, s36, 768\n"
"ds_read_b64_tr_b16 v[232:233], v195 offset:16896\n"
"v_mul_f32 v171, s67, v171\n"
"v_mfma_f32_16x16x32_bf16 a[128:131], v[212:215], a[56:59], a[128:131]\n"
"s_add_u32 m0, s60, 147456\n"
"ds_read_b64_tr_b16 v[234:235], v195 offset:25088\n"
"v_permlane16_swap_b32 v52, v54\n"
"v_mfma_f32_16x16x32_bf16 a[132:135], v[212:215], a[60:63], a[132:135]\n"
"buffer_load_dwordx4 v204, s[16:19], s65 offen lds\n"
"v_mfma_f32_16x16x32_bf16 a[132:135], v[208:211], a[52:55], a[132:135]\n"
"s_waitcnt lgkmcnt(6)\n"
"ds_read_b64_tr_b16 v[236:237], v195 offset:18944\n"
"ds_read_b64_tr_b16 v[238:239], v195 offset:27136\n"
"v_mfma_f32_16x16x32_bf16 a[140:143], v[216:219], a[52:55], a[140:143]\n"
"ds_read_b64_tr_b16 v[208:209], v194 offset:17408\n"
"v_cvt_pk_bf16_f32 v26, v168, v169\n"
"v_mfma_f32_16x16x32_bf16 a[140:143], v[220:223], a[60:63], a[140:143]\n"
"ds_read_b64_tr_b16 v[210:211], v194 offset:25600\n"
"v_cvt_pk_bf16_f32 v27, v170, v171\n"
"v_mfma_f32_16x16x32_bf16 a[136:139], v[220:223], a[56:59], a[136:139]\n"
"ds_read_b64_tr_b16 v[212:213], v194 offset:19456\n"
"v_permlane16_swap_b32 v53, v55\n"
"v_mfma_f32_16x16x32_bf16 a[136:139], v[216:219], a[48:51], a[136:139]\n"
"s_waitcnt lgkmcnt(7)\n"
"ds_read_b64_tr_b16 v[214:215], v194 offset:27648\n"
"v_mul_f32 v164, s67, v164\n"
"v_mfma_f32_16x16x32_bf16 a[144:147], v[224:227], a[48:51], a[144:147]\n"
"s_add_u32 m0, s60, 148352\n"
"ds_read_b64_tr_b16 v[216:217], v195 offset:17408\n"
"v_mul_f32 v165, s67, v165\n"
"v_mfma_f32_16x16x32_bf16 a[144:147], v[228:231], a[56:59], a[144:147]\n"
"buffer_load_dwordx4 v204, s[16:19], s65 offen offset:128 lds\n"
"v_mfma_f32_16x16x32_bf16 a[148:151], v[228:231], a[60:63], a[148:151]\n"
"ds_read_b64_tr_b16 v[218:219], v195 offset:25600\n"
"ds_read_b64_tr_b16 v[220:221], v195 offset:19456\n"
"v_mfma_f32_16x16x32_bf16 a[148:151], v[224:227], a[52:55], a[148:151]\n"
"s_waitcnt lgkmcnt(7)\n"
"ds_read_b64_tr_b16 v[222:223], v195 offset:27648\n"
"v_mul_f32 v166, s67, v166\n"
"v_mfma_f32_16x16x32_bf16 a[156:159], v[232:235], a[52:55], a[156:159]\n"
"ds_read_b64_tr_b16 v[224:225], v194 offset:17920\n"
"v_mul_f32 v167, s67, v167\n"
"v_mfma_f32_16x16x32_bf16 a[156:159], v[236:239], a[60:63], a[156:159]\n"
"ds_read_b64_tr_b16 v[226:227], v194 offset:26112\n"
"v_cvt_pk_bf16_f32 v56, v164, v165\n"
"v_mfma_f32_16x16x32_bf16 a[152:155], v[236:239], a[56:59], a[152:155]\n"
"s_add_u32 m0, s60, 149504\n"
"ds_read_b64_tr_b16 v[228:229], v194 offset:19968\n"
"v_mul_f32 v172, s67, v172\n"
"v_mfma_f32_16x16x32_bf16 a[152:155], v[232:235], a[48:51], a[152:155]\n"
"s_waitcnt lgkmcnt(7)\n"
"buffer_load_dwordx4 v205, s[16:19], s65 offen lds\n"
"v_mfma_f32_16x16x32_bf16 a[160:163], v[208:211], a[48:51], a[160:163]\n"
"ds_read_b64_tr_b16 v[230:231], v194 offset:28160\n"
"ds_read_b64_tr_b16 v[232:233], v195 offset:17920\n"
"v_mfma_f32_16x16x32_bf16 a[160:163], v[212:215], a[56:59], a[160:163]\n"
"ds_read_b64_tr_b16 v[234:235], v195 offset:26112\n"
"v_cvt_pk_bf16_f32 v57, v166, v167\n"
"v_mfma_f32_16x16x32_bf16 a[164:167], v[212:215], a[60:63], a[164:167]\n"
"ds_read_b64_tr_b16 v[236:237], v195 offset:19968\n"
"v_mul_f32 v173, s67, v173\n"
"v_mfma_f32_16x16x32_bf16 a[164:167], v[208:211], a[52:55], a[164:167]\n"
"s_waitcnt lgkmcnt(7)\n"
"ds_read_b64_tr_b16 v[238:239], v195 offset:28160\n"
"v_mul_f32 v174, s67, v174\n"
"v_mfma_f32_16x16x32_bf16 a[172:175], v[216:219], a[52:55], a[172:175]\n"
"ds_read_b64_tr_b16 v[208:209], v194 offset:32768\n"
"v_mul_f32 v175, s67, v175\n"
"v_mfma_f32_16x16x32_bf16 a[172:175], v[220:223], a[60:63], a[172:175]\n"
"s_add_u32 m0, s60, 150400\n"
"ds_read_b64_tr_b16 v[210:211], v194 offset:40960\n"
"v_permlane16_swap_b32 v24, v26\n"
"v_mfma_f32_16x16x32_bf16 a[168:171], v[220:223], a[56:59], a[168:171]\n"
"buffer_load_dwordx4 v205, s[16:19], s65 offen offset:128 lds\n"
"v_mfma_f32_16x16x32_bf16 a[168:171], v[216:219], a[48:51], a[168:171]\n"
"s_waitcnt lgkmcnt(6)\n"
"ds_read_b64_tr_b16 v[212:213], v194 offset:34816\n"
"ds_read_b64_tr_b16 v[214:215], v194 offset:43008\n"
"v_mfma_f32_16x16x32_bf16 a[176:179], v[224:227], a[48:51], a[176:179]\n"
"ds_read_b64_tr_b16 v[216:217], v195 offset:32768\n"
"v_cvt_pk_bf16_f32 v58, v172, v173\n"
"v_mfma_f32_16x16x32_bf16 a[176:179], v[228:231], a[56:59], a[176:179]\n"
"ds_read_b64_tr_b16 v[218:219], v195 offset:40960\n"
"v_cvt_pk_bf16_f32 v59, v174, v175\n"
"v_mfma_f32_16x16x32_bf16 a[180:183], v[228:231], a[60:63], a[180:183]\n"
"ds_read_b64_tr_b16 v[220:221], v195 offset:34816\n"
"v_permlane16_swap_b32 v25, v27\n"
"v_mfma_f32_16x16x32_bf16 a[180:183], v[224:227], a[52:55], a[180:183]\n"
"s_waitcnt lgkmcnt(7)\n"
"ds_read_b64_tr_b16 v[222:223], v195 offset:43008\n"
"v_mul_f32 v176, s67, v176\n"
"v_mfma_f32_16x16x32_bf16 a[188:191], v[232:235], a[52:55], a[188:191]\n"
"ds_read_b64_tr_b16 v[224:225], v194 offset:33280\n"
"v_mul_f32 v177, s67, v177\n"
"v_mfma_f32_16x16x32_bf16 a[188:191], v[236:239], a[60:63], a[188:191]\n"
"ds_read_b64_tr_b16 v[226:227], v194 offset:41472\n"
"v_mul_f32 v178, s67, v178\n"
"v_mfma_f32_16x16x32_bf16 a[184:187], v[236:239], a[56:59], a[184:187]\n"
"s_waitcnt vmcnt(12)\n"
"s_barrier\n"
"v_mfma_f32_16x16x32_bf16 a[184:187], v[232:235], a[48:51], a[184:187]\n"
"s_waitcnt lgkmcnt(6)\n"
"ds_read_b64_tr_b16 v[228:229], v194 offset:35328\n"
"ds_read_b64_tr_b16 v[230:231], v194 offset:43520\n"
"v_mfma_f32_16x16x32_bf16 a[128:131], v[208:211], a[64:67], a[128:131]\n"
"s_add_u32 s65, s37, 768\n"
"ds_read_b64_tr_b16 v[232:233], v195 offset:33280\n"
"v_mul_f32 v179, s67, v179\n"
"v_mfma_f32_16x16x32_bf16 a[128:131], v[212:215], a[72:75], a[128:131]\n"
"s_add_u32 m0, s60, 16384\n"
"ds_read_b64_tr_b16 v[234:235], v195 offset:41472\n"
"v_mul_f32 v184, s67, v184\n"
"v_mfma_f32_16x16x32_bf16 a[132:135], v[212:215], a[76:79], a[132:135]\n"
"buffer_load_dwordx4 v204, s[16:19], s65 offen lds\n"
"v_mfma_f32_16x16x32_bf16 a[132:135], v[208:211], a[68:71], a[132:135]\n"
"s_waitcnt lgkmcnt(6)\n"
"ds_read_b64_tr_b16 v[236:237], v195 offset:35328\n"
"ds_read_b64_tr_b16 v[238:239], v195 offset:43520\n"
"v_mfma_f32_16x16x32_bf16 a[140:143], v[216:219], a[68:71], a[140:143]\n"
"ds_read_b64_tr_b16 v[208:209], v194 offset:33792\n"
"v_cvt_pk_bf16_f32 v28, v176, v177\n"
"v_mfma_f32_16x16x32_bf16 a[140:143], v[220:223], a[76:79], a[140:143]\n"
"ds_read_b64_tr_b16 v[210:211], v194 offset:41984\n"
"v_cvt_pk_bf16_f32 v29, v178, v179\n"
"v_mfma_f32_16x16x32_bf16 a[136:139], v[220:223], a[72:75], a[136:139]\n"
"ds_read_b64_tr_b16 v[212:213], v194 offset:35840\n"
"v_mul_f32 v185, s67, v185\n"
"v_mfma_f32_16x16x32_bf16 a[136:139], v[216:219], a[64:67], a[136:139]\n"
"s_waitcnt lgkmcnt(7)\n"
"ds_read_b64_tr_b16 v[214:215], v194 offset:44032\n"
"v_mul_f32 v186, s67, v186\n"
"v_mfma_f32_16x16x32_bf16 a[144:147], v[224:227], a[64:67], a[144:147]\n"
"s_add_u32 m0, s60, 17280\n"
"ds_read_b64_tr_b16 v[216:217], v195 offset:33792\n"
"v_mul_f32 v187, s67, v187\n"
"v_mfma_f32_16x16x32_bf16 a[144:147], v[228:231], a[72:75], a[144:147]\n"
"buffer_load_dwordx4 v204, s[16:19], s65 offen offset:128 lds\n"
"v_mfma_f32_16x16x32_bf16 a[148:151], v[228:231], a[76:79], a[148:151]\n"
"ds_read_b64_tr_b16 v[218:219], v195 offset:41984\n"
"ds_read_b64_tr_b16 v[220:221], v195 offset:35840\n"
"v_mfma_f32_16x16x32_bf16 a[148:151], v[224:227], a[68:71], a[148:151]\n"
"s_waitcnt lgkmcnt(7)\n"
"ds_read_b64_tr_b16 v[222:223], v195 offset:44032\n"
"v_cvt_pk_bf16_f32 v30, v184, v185\n"
"v_mfma_f32_16x16x32_bf16 a[156:159], v[232:235], a[68:71], a[156:159]\n"
"ds_read_b64_tr_b16 v[224:225], v194 offset:34304\n"
"v_cvt_pk_bf16_f32 v31, v186, v187\n"
"v_mfma_f32_16x16x32_bf16 a[156:159], v[236:239], a[76:79], a[156:159]\n"
"ds_read_b64_tr_b16 v[226:227], v194 offset:42496\n"
"v_permlane16_swap_b32 v56, v58\n"
"v_mfma_f32_16x16x32_bf16 a[152:155], v[236:239], a[72:75], a[152:155]\n"
"s_add_u32 m0, s60, 18432\n"
"ds_read_b64_tr_b16 v[228:229], v194 offset:36352\n"
"v_permlane16_swap_b32 v57, v59\n"
"v_mfma_f32_16x16x32_bf16 a[152:155], v[232:235], a[64:67], a[152:155]\n"
"s_waitcnt lgkmcnt(7)\n"
"buffer_load_dwordx4 v205, s[16:19], s65 offen lds\n"
"v_mfma_f32_16x16x32_bf16 a[160:163], v[208:211], a[64:67], a[160:163]\n"
"ds_read_b64_tr_b16 v[230:231], v194 offset:44544\n"
"ds_read_b64_tr_b16 v[232:233], v195 offset:34304\n"
"v_mfma_f32_16x16x32_bf16 a[160:163], v[212:215], a[72:75], a[160:163]\n"
"ds_read_b64_tr_b16 v[234:235], v195 offset:42496\n"
"v_mul_f32 v180, s67, v180\n"
"v_mfma_f32_16x16x32_bf16 a[164:167], v[212:215], a[76:79], a[164:167]\n"
"ds_read_b64_tr_b16 v[236:237], v195 offset:36352\n"
"v_mul_f32 v181, s67, v181\n"
"v_mfma_f32_16x16x32_bf16 a[164:167], v[208:211], a[68:71], a[164:167]\n"
"s_waitcnt lgkmcnt(7)\n"
"ds_read_b64_tr_b16 v[238:239], v195 offset:44544\n"
"v_mul_f32 v182, s67, v182\n"
"v_mfma_f32_16x16x32_bf16 a[172:175], v[216:219], a[68:71], a[172:175]\n"
"ds_read_b64_tr_b16 v[208:209], v194 offset:49152\n"
"v_mul_f32 v183, s67, v183\n"
"v_mfma_f32_16x16x32_bf16 a[172:175], v[220:223], a[76:79], a[172:175]\n"
"s_add_u32 m0, s60, 19328\n"
"ds_read_b64_tr_b16 v[210:211], v194 offset:57344\n"
"v_mul_f32 v188, s67, v188\n"
"v_mfma_f32_16x16x32_bf16 a[168:171], v[220:223], a[72:75], a[168:171]\n"
"buffer_load_dwordx4 v205, s[16:19], s65 offen offset:128 lds\n"
"v_mfma_f32_16x16x32_bf16 a[168:171], v[216:219], a[64:67], a[168:171]\n"
"s_waitcnt lgkmcnt(6)\n"
"ds_read_b64_tr_b16 v[212:213], v194 offset:51200\n"
"ds_read_b64_tr_b16 v[214:215], v194 offset:59392\n"
"v_mfma_f32_16x16x32_bf16 a[176:179], v[224:227], a[64:67], a[176:179]\n"
"ds_read_b64_tr_b16 v[216:217], v195 offset:49152\n"
"v_cvt_pk_bf16_f32 v60, v180, v181\n"
"v_mfma_f32_16x16x32_bf16 a[176:179], v[228:231], a[72:75], a[176:179]\n"
"ds_read_b64_tr_b16 v[218:219], v195 offset:57344\n"
"v_cvt_pk_bf16_f32 v61, v182, v183\n"
"v_mfma_f32_16x16x32_bf16 a[180:183], v[228:231], a[76:79], a[180:183]\n"
"ds_read_b64_tr_b16 v[220:221], v195 offset:51200\n"
"v_mul_f32 v189, s67, v189\n"
"v_mfma_f32_16x16x32_bf16 a[180:183], v[224:227], a[68:71], a[180:183]\n"
"s_waitcnt lgkmcnt(7)\n"
"ds_read_b64_tr_b16 v[222:223], v195 offset:59392\n"
"v_mul_f32 v190, s67, v190\n"
"v_mfma_f32_16x16x32_bf16 a[188:191], v[232:235], a[68:71], a[188:191]\n"
"ds_read_b64_tr_b16 v[224:225], v194 offset:49664\n"
"v_mul_f32 v191, s67, v191\n"
"v_mfma_f32_16x16x32_bf16 a[188:191], v[236:239], a[76:79], a[188:191]\n"
"ds_read_b64_tr_b16 v[226:227], v194 offset:57856\n"
"v_cvt_pk_bf16_f32 v62, v188, v189\n"
"v_mfma_f32_16x16x32_bf16 a[184:187], v[236:239], a[72:75], a[184:187]\n"
"s_waitcnt vmcnt(12)\n"
"s_barrier\n"
"v_mfma_f32_16x16x32_bf16 a[184:187], v[232:235], a[64:67], a[184:187]\n"
"s_waitcnt lgkmcnt(6)\n"
"ds_read_b64_tr_b16 v[228:229], v194 offset:51712\n"
"ds_read_b64_tr_b16 v[230:231], v194 offset:59904\n"
"v_mfma_f32_16x16x32_bf16 a[128:131], v[208:211], a[80:83], a[128:131]\n"
"s_add_u32 s65, s38, 768\n"
"s_add_u32 m0, s60, 98304\n"
"ds_read_b64_tr_b16 v[232:233], v195 offset:49664\n"
"v_mfma_f32_16x16x32_bf16 a[128:131], v[212:215], a[88:91], a[128:131]\n"
"buffer_load_dwordx4 v204, s[16:19], s65 offen lds\n"
"v_mfma_f32_16x16x32_bf16 a[132:135], v[212:215], a[92:95], a[132:135]\n"
"ds_read_b64_tr_b16 v[234:235], v195 offset:57856\n"
"ds_read_b64_tr_b16 v[236:237], v195 offset:51712\n"
"v_mfma_f32_16x16x32_bf16 a[132:135], v[208:211], a[84:87], a[132:135]\n"
"s_waitcnt lgkmcnt(7)\n"
"ds_read_b64_tr_b16 v[238:239], v195 offset:59904\n"
"v_cvt_pk_bf16_f32 v63, v190, v191\n"
"v_mfma_f32_16x16x32_bf16 a[140:143], v[216:219], a[84:87], a[140:143]\n"
"s_add_u32 m0, s60, 99200\n"
"ds_read_b64_tr_b16 v[208:209], v194 offset:50176\n"
"v_permlane16_swap_b32 v28, v30\n"
"v_mfma_f32_16x16x32_bf16 a[140:143], v[220:223], a[92:95], a[140:143]\n"
"buffer_load_dwordx4 v204, s[16:19], s65 offen offset:128 lds\n"
"v_mfma_f32_16x16x32_bf16 a[136:139], v[220:223], a[88:91], a[136:139]\n"
"ds_read_b64_tr_b16 v[210:211], v194 offset:58368\n"
"ds_read_b64_tr_b16 v[212:213], v194 offset:52224\n"
"v_mfma_f32_16x16x32_bf16 a[136:139], v[216:219], a[80:83], a[136:139]\n"
"s_waitcnt lgkmcnt(7)\n"
"ds_read_b64_tr_b16 v[214:215], v194 offset:60416\n"
"v_permlane16_swap_b32 v29, v31\n"
"v_mfma_f32_16x16x32_bf16 a[144:147], v[224:227], a[80:83], a[144:147]\n"
"s_add_u32 m0, s60, 100352\n"
"ds_read_b64_tr_b16 v[216:217], v195 offset:50176\n"
"v_permlane16_swap_b32 v60, v62\n"
"v_mfma_f32_16x16x32_bf16 a[144:147], v[228:231], a[88:91], a[144:147]\n"
"buffer_load_dwordx4 v205, s[16:19], s65 offen lds\n"
"v_mfma_f32_16x16x32_bf16 a[148:151], v[228:231], a[92:95], a[148:151]\n"
"IWA3_%=:\n"
"s_cbranch_execz IRA_%=\n"
"s_add_u32 m0, s60, 101248\n"
"ds_read_b64_tr_b16 v[218:219], v195 offset:58368\n"
"ds_read_b64_tr_b16 v[220:221], v195 offset:52224\n"
"v_mfma_f32_16x16x32_bf16 a[148:151], v[224:227], a[84:87], a[148:151]\n"
"s_waitcnt lgkmcnt(7)\n"
"buffer_load_dwordx4 v205, s[16:19], s65 offen offset:128 lds\n"
"v_mfma_f32_16x16x32_bf16 a[156:159], v[232:235], a[84:87], a[156:159]\n"
"ds_read_b64_tr_b16 v[222:223], v195 offset:60416\n"
"ds_read_b64_tr_b16 v[224:225], v194 offset:50688\n"
"v_mfma_f32_16x16x32_bf16 a[156:159], v[236:239], a[92:95], a[156:159]\n"
"ds_read_b64_tr_b16 v[226:227], v194 offset:58880\n"
"v_permlane16_swap_b32 v61, v63\n"
"v_mfma_f32_16x16x32_bf16 a[152:155], v[236:239], a[88:91], a[152:155]\n"
"s_add_u32 s66, s73, 32768\n"
"s_and_b32 s66, s66, 524287\n"
"ds_read_b64_tr_b16 v[228:229], v194 offset:52736\n"
"v_mfma_f32_16x16x32_bf16 a[152:155], v[232:235], a[80:83], a[152:155]\n"
"s_waitcnt lgkmcnt(7)\n"
"s_add_u32 m0, s52, 65536\n"
"ds_read_b64_tr_b16 v[230:231], v194 offset:60928\n"
"v_mfma_f32_16x16x32_bf16 a[160:163], v[208:211], a[80:83], a[160:163]\n"
"buffer_load_dwordx4 v247, s[8:11], s66 offen nt lds\n"
"v_mfma_f32_16x16x32_bf16 a[160:163], v[212:215], a[88:91], a[160:163]\n"
"s_add_u32 m0, s53, 65536\n"
"ds_read_b64_tr_b16 v[232:233], v195 offset:50688\n"
"ds_read_b64_tr_b16 v[234:235], v195 offset:58880\n"
"v_mfma_f32_16x16x32_bf16 a[164:167], v[212:215], a[92:95], a[164:167]\n"
"buffer_load_dwordx4 v247, s[8:11], s66 offen offset:128 nt lds\n"
"v_mfma_f32_16x16x32_bf16 a[164:167], v[208:211], a[84:87], a[164:167]\n"
"s_waitcnt lgkmcnt(6)\n"
"ds_read_b64_tr_b16 v[236:237], v195 offset:52736\n"
"ds_read_b64_tr_b16 v[238:239], v195 offset:60928\n"
"v_mfma_f32_16x16x32_bf16 a[172:175], v[216:219], a[84:87], a[172:175]\n"
"ds_read_b64_tr_b16 v[208:209], v196 offset:0\n"
"v_mfma_f32_16x16x32_bf16 a[172:175], v[220:223], a[92:95], a[172:175]\n"
"s_add_u32 m0, s54, 65536\n"
"ds_read_b64_tr_b16 v[210:211], v196 offset:8192\n"
"v_mfma_f32_16x16x32_bf16 a[168:171], v[220:223], a[88:91], a[168:171]\n"
"buffer_load_dwordx4 v247, s[8:11], s66 offen offset:256 nt lds\n"
"v_mfma_f32_16x16x32_bf16 a[168:171], v[216:219], a[80:83], a[168:171]\n"
"s_waitcnt lgkmcnt(6)\n"
"ds_read_b64_tr_b16 v[212:213], v196 offset:2048\n"
"ds_read_b64_tr_b16 v[214:215], v196 offset:10240\n"
"v_mfma_f32_16x16x32_bf16 a[176:179], v[224:227], a[80:83], a[176:179]\n"
"s_add_u32 m0, s55, 65536\n"
"ds_read_b64_tr_b16 v[216:217], v197 offset:0\n"
"v_mfma_f32_16x16x32_bf16 a[176:179], v[228:231], a[88:91], a[176:179]\n"
"buffer_load_dwordx4 v247, s[8:11], s66 offen offset:384 nt lds\n"
"v_mfma_f32_16x16x32_bf16 a[180:183], v[228:231], a[92:95], a[180:183]\n"
"ds_read_b64_tr_b16 v[218:219], v197 offset:8192\n"
"ds_read_b64_tr_b16 v[220:221], v197 offset:2048\n"
"v_mfma_f32_16x16x32_bf16 a[180:183], v[224:227], a[84:87], a[180:183]\n"
"s_waitcnt lgkmcnt(7)\n"
"ds_read_b64_tr_b16 v[222:223], v197 offset:10240\n"
"v_mfma_f32_16x16x32_bf16 a[188:191], v[232:235], a[84:87], a[188:191]\n"
"ds_read_b64_tr_b16 v[224:225], v196 offset:512\n"
"v_mfma_f32_16x16x32_bf16 a[188:191], v[236:239], a[92:95], a[188:191]\n"
"ds_read_b64_tr_b16 v[226:227], v196 offset:8704\n"
"v_mfma_f32_16x16x32_bf16 a[184:187], v[236:239], a[88:91], a[184:187]\n"
"s_waitcnt vmcnt(12)\n"
"s_barrier\n"
"v_mfma_f32_16x16x32_bf16 a[184:187], v[232:235], a[80:83], a[184:187]\n"
"s_waitcnt lgkmcnt(6)\n"
"ds_read_b64_tr_b16 v[228:229], v196 offset:2560\n"
"ds_read_b64_tr_b16 v[230:231], v196 offset:10752\n"
"v_mfma_f32_16x16x32_bf16 a[128:131], v[208:211], a[96:99], a[128:131]\n"
"s_add_u32 s65, s39, 768\n"
"ds_read_b64_tr_b16 v[232:233], v197 offset:512\n"
"v_mfma_f32_16x16x32_bf16 a[128:131], v[212:215], a[104:107], a[128:131]\n"
"s_add_u32 m0, s60, 114688\n"
"ds_read_b64_tr_b16 v[234:235], v197 offset:8704\n"
"v_mfma_f32_16x16x32_bf16 a[132:135], v[212:215], a[108:111], a[132:135]\n"
"buffer_load_dwordx4 v204, s[16:19], s65 offen lds\n"
"v_mfma_f32_16x16x32_bf16 a[132:135], v[208:211], a[100:103], a[132:135]\n"
"s_waitcnt lgkmcnt(6)\n"
"ds_read_b64_tr_b16 v[236:237], v197 offset:2560\n"
"ds_read_b64_tr_b16 v[238:239], v197 offset:10752\n"
"v_mfma_f32_16x16x32_bf16 a[140:143], v[216:219], a[100:103], a[140:143]\n"
"ds_read_b64_tr_b16 v[208:209], v196 offset:1024\n"
"v_mfma_f32_16x16x32_bf16 a[140:143], v[220:223], a[108:111], a[140:143]\n"
"ds_read_b64_tr_b16 v[210:211], v196 offset:9216\n"
"v_mfma_f32_16x16x32_bf16 a[136:139], v[220:223], a[104:107], a[136:139]\n"
"ds_read_b64_tr_b16 v[212:213], v196 offset:3072\n"
"v_mfma_f32_16x16x32_bf16 a[136:139], v[216:219], a[96:99], a[136:139]\n"
"s_waitcnt lgkmcnt(7)\n"
"ds_read_b64_tr_b16 v[214:215], v196 offset:11264\n"
"v_mfma_f32_16x16x32_bf16 a[144:147], v[224:227], a[96:99], a[144:147]\n"
"s_add_u32 m0, s60, 115584\n"
"ds_read_b64_tr_b16 v[216:217], v197 offset:1024\n"
"v_mfma_f32_16x16x32_bf16 a[144:147], v[228:231], a[104:107], a[144:147]\n"
"buffer_load_dwordx4 v204, s[16:19], s65 offen offset:128 lds\n"
"v_mfma_f32_16x16x32_bf16 a[148:151], v[228:231], a[108:111], a[148:151]\n"
"ds_read_b64_tr_b16 v[218:219], v197 offset:9216\n"
"ds_read_b64_tr_b16 v[220:221], v197 offset:3072\n"
"v_mfma_f32_16x16x32_bf16 a[148:151], v[224:227], a[100:103], a[148:151]\n"
"s_waitcnt lgkmcnt(7)\n"
"ds_read_b64_tr_b16 v[222:223], v197 offset:11264\n"
"v_accvgpr_read_b32 v252, a128\n"
"v_mfma_f32_16x16x32_bf16 a[156:159], v[232:235], a[100:103], a[156:159]\n"
"ds_read_b64_tr_b16 v[224:225], v196 offset:1536\n"
"v_accvgpr_read_b32 v253, a129\n"
"v_mfma_f32_16x16x32_bf16 a[156:159], v[236:239], a[108:111], a[156:159]\n"
"ds_read_b64_tr_b16 v[226:227], v196 offset:9728\n"
"v_accvgpr_read_b32 v254, a130\n"
"v_mfma_f32_16x16x32_bf16 a[152:155], v[236:239], a[104:107], a[152:155]\n"
"s_add_u32 m0, s60, 116736\n"
"ds_read_b64_tr_b16 v[228:229], v196 offset:3584\n"
"v_mul_f32 v252, s67, v252\n"
"v_mfma_f32_16x16x32_bf16 a[152:155], v[232:235], a[96:99], a[152:155]\n"
"s_waitcnt lgkmcnt(7)\n"
"buffer_load_dwordx4 v205, s[16:19], s65 offen lds\n"
"v_mfma_f32_16x16x32_bf16 a[160:163], v[208:211], a[96:99], a[160:163]\n"
"ds_read_b64_tr_b16 v[230:231], v196 offset:11776\n"
"ds_read_b64_tr_b16 v[232:233], v197 offset:1536\n"
"v_mfma_f32_16x16x32_bf16 a[160:163], v[212:215], a[104:107], a[160:163]\n"
"ds_read_b64_tr_b16 v[234:235], v197 offset:9728\n"
"v_accvgpr_read_b32 v255, a131\n"
"v_mfma_f32_16x16x32_bf16 a[164:167], v[212:215], a[108:111], a[164:167]\n"
"ds_read_b64_tr_b16 v[236:237], v197 offset:3584\n"
"v_mul_f32 v253, s67, v253\n"
"v_mfma_f32_16x16x32_bf16 a[164:167], v[208:211], a[100:103], a[164:167]\n"
"s_waitcnt lgkmcnt(7)\n"
"ds_read_b64_tr_b16 v[238:239], v197 offset:11776\n"
"v_mul_f32 v254, s67, v254\n"
"v_mfma_f32_16x16x32_bf16 a[172:175], v[216:219], a[100:103], a[172:175]\n"
"ds_read_b64_tr_b16 v[208:209], v196 offset:16384\n"
"v_mul_f32 v255, s67, v255\n"
"v_mfma_f32_16x16x32_bf16 a[172:175], v[220:223], a[108:111], a[172:175]\n"
"s_add_u32 m0, s60, 117632\n"
"ds_read_b64_tr_b16 v[210:211], v196 offset:24576\n"
"v_mfma_f32_16x16x32_bf16 a[168:171], v[220:223], a[104:107], a[168:171]\n"
"buffer_load_dwordx4 v205, s[16:19], s65 offen offset:128 lds\n"
"v_mfma_f32_16x16x32_bf16 a[168:171], v[216:219], a[96:99], a[168:171]\n"
"s_waitcnt lgkmcnt(6)\n"
"ds_read_b64_tr_b16 v[212:213], v196 offset:18432\n"
"ds_read_b64_tr_b16 v[214:215], v196 offset:26624\n"
"v_mfma_f32_16x16x32_bf16 a[176:179], v[224:227], a[96:99], a[176:179]\n"
"ds_read_b64_tr_b16 v[216:217], v197 offset:16384\n"
"v_cvt_pk_bf16_f32 v64, v252, v253\n"
"v_mfma_f32_16x16x32_bf16 a[176:179], v[228:231], a[104:107], a[176:179]\n"
"ds_read_b64_tr_b16 v[218:219], v197 offset:24576\n"
"v_cvt_pk_bf16_f32 v65, v254, v255\n"
"v_mfma_f32_16x16x32_bf16 a[180:183], v[228:231], a[108:111], a[180:183]\n"
"ds_read_b64_tr_b16 v[220:221], v197 offset:18432\n"
"v_accvgpr_read_b32 v252, a136\n"
"v_mfma_f32_16x16x32_bf16 a[180:183], v[224:227], a[100:103], a[180:183]\n"
"s_waitcnt lgkmcnt(7)\n"
"ds_read_b64_tr_b16 v[222:223], v197 offset:26624\n"
"v_accvgpr_read_b32 v253, a137\n"
"v_mfma_f32_16x16x32_bf16 a[188:191], v[232:235], a[100:103], a[188:191]\n"
"ds_read_b64_tr_b16 v[224:225], v196 offset:16896\n"
"v_accvgpr_read_b32 v254, a138\n"
"v_mfma_f32_16x16x32_bf16 a[188:191], v[236:239], a[108:111], a[188:191]\n"
"ds_read_b64_tr_b16 v[226:227], v196 offset:25088\n"
"v_accvgpr_read_b32 v255, a139\n"
"v_mfma_f32_16x16x32_bf16 a[184:187], v[236:239], a[104:107], a[184:187]\n"
"s_waitcnt vmcnt(12)\n"
"s_barrier\n"
"v_mfma_f32_16x16x32_bf16 a[184:187], v[232:235], a[96:99], a[184:187]\n"
"s_waitcnt lgkmcnt(6)\n"
"ds_read_b64_tr_b16 v[228:229], v196 offset:18944\n"
"ds_read_b64_tr_b16 v[230:231], v196 offset:27136\n"
"v_mfma_f32_16x16x32_bf16 v[128:131], v[208:211], a[0:3], 0\n"
"s_add_u32 s65, s40, 768\n"
"ds_read_b64_tr_b16 v[232:233], v197 offset:16896\n"
"v_mul_f32 v252, s67, v252\n"
"v_mfma_f32_16x16x32_bf16 v[128:131], v[212:215], a[8:11], v[128:131]\n"
"s_add_u32 m0, s60, 131072\n"
"ds_read_b64_tr_b16 v[234:235], v197 offset:25088\n"
"v_mul_f32 v253, s67, v253\n"
"v_mfma_f32_16x16x32_bf16 v[132:135], v[212:215], a[12:15], 0\n"
"buffer_load_dwordx4 v204, s[16:19], s65 offen lds\n"
"v_mfma_f32_16x16x32_bf16 v[132:135], v[208:211], a[4:7], v[132:135]\n"
"s_waitcnt lgkmcnt(6)\n"
"ds_read_b64_tr_b16 v[236:237], v197 offset:18944\n"
"ds_read_b64_tr_b16 v[238:239], v197 offset:27136\n"
"v_mfma_f32_16x16x32_bf16 v[140:143], v[216:219], a[4:7], 0\n"
"ds_read_b64_tr_b16 v[208:209], v196 offset:17408\n"
"v_mul_f32 v254, s67, v254\n"
"v_mfma_f32_16x16x32_bf16 v[140:143], v[220:223], a[12:15], v[140:143]\n"
"ds_read_b64_tr_b16 v[210:211], v196 offset:25600\n"
"v_mul_f32 v255, s67, v255\n"
"v_mfma_f32_16x16x32_bf16 v[136:139], v[220:223], a[8:11], 0\n"
"ds_read_b64_tr_b16 v[212:213], v196 offset:19456\n"
"v_cvt_pk_bf16_f32 v66, v252, v253\n"
"v_mfma_f32_16x16x32_bf16 v[136:139], v[216:219], a[0:3], v[136:139]\n"
"s_waitcnt lgkmcnt(7)\n"
"ds_read_b64_tr_b16 v[214:215], v196 offset:27648\n"
"v_cvt_pk_bf16_f32 v67, v254, v255\n"
"v_mfma_f32_16x16x32_bf16 v[144:147], v[224:227], a[0:3], 0\n"
"s_add_u32 m0, s60, 131968\n"
"ds_read_b64_tr_b16 v[216:217], v197 offset:17408\n"
"v_permlane16_swap_b32 v64, v66\n"
"v_mfma_f32_16x16x32_bf16 v[144:147], v[228:231], a[8:11], v[144:147]\n"
"buffer_load_dwordx4 v204, s[16:19], s65 offen offset:128 lds\n"
"v_mfma_f32_16x16x32_bf16 v[148:151], v[228:231], a[12:15], 0\n"
"ds_read_b64_tr_b16 v[218:219], v197 offset:25600\n"
"ds_read_b64_tr_b16 v[220:221], v197 offset:19456\n"
"v_mfma_f32_16x16x32_bf16 v[148:151], v[224:227], a[4:7], v[148:151]\n"
"s_waitcnt lgkmcnt(7)\n"
"ds_read_b64_tr_b16 v[222:223], v197 offset:27648\n"
"v_accvgpr_read_b32 v252, a132\n"
"v_mfma_f32_16x16x32_bf16 v[156:159], v[232:235], a[4:7], 0\n"
"ds_read_b64_tr_b16 v[224:225], v196 offset:17920\n"
"v_accvgpr_read_b32 v253, a133\n"
"v_mfma_f32_16x16x32_bf16 v[156:159], v[236:239], a[12:15], v[156:159]\n"
"ds_read_b64_tr_b16 v[226:227], v196 offset:26112\n"
"v_accvgpr_read_b32 v254, a134\n"
"v_mfma_f32_16x16x32_bf16 v[152:155], v[236:239], a[8:11], 0\n"
"s_add_u32 m0, s60, 133120\n"
"ds_read_b64_tr_b16 v[228:229], v196 offset:19968\n"
"v_mul_f32 v252, s67, v252\n"
"v_mfma_f32_16x16x32_bf16 v[152:155], v[232:235], a[0:3], v[152:155]\n"
"s_waitcnt lgkmcnt(7)\n"
"buffer_load_dwordx4 v205, s[16:19], s65 offen lds\n"
"v_mfma_f32_16x16x32_bf16 v[160:163], v[208:211], a[0:3], 0\n"
"ds_read_b64_tr_b16 v[230:231], v196 offset:28160\n"
"ds_read_b64_tr_b16 v[232:233], v197 offset:17920\n"
"v_mfma_f32_16x16x32_bf16 v[160:163], v[212:215], a[8:11], v[160:163]\n"
"ds_read_b64_tr_b16 v[234:235], v197 offset:26112\n"
"v_accvgpr_read_b32 v255, a135\n"
"v_mfma_f32_16x16x32_bf16 v[164:167], v[212:215], a[12:15], 0\n"
"ds_read_b64_tr_b16 v[236:237], v197 offset:19968\n"
"v_mul_f32 v253, s67, v253\n"
"v_mfma_f32_16x16x32_bf16 v[164:167], v[208:211], a[4:7], v[164:167]\n"
"s_waitcnt lgkmcnt(7)\n"
"ds_read_b64_tr_b16 v[238:239], v197 offset:28160\n"
"v_mul_f32 v254, s67, v254\n"
"v_mfma_f32_16x16x32_bf16 v[172:175], v[216:219], a[4:7], 0\n"
"ds_read_b64_tr_b16 v[208:209], v192 offset:16384\n"
"v_mul_f32 v255, s67, v255\n"
"v_mfma_f32_16x16x32_bf16 v[172:175], v[220:223], a[12:15], v[172:175]\n"
"s_add_u32 m0, s60, 134016\n"
"ds_read_b64_tr_b16 v[210:211], v192 offset:24576\n"
"v_permlane16_swap_b32 v65, v67\n"
"v_mfma_f32_16x16x32_bf16 v[168:171], v[220:223], a[8:11], 0\n"
"buffer_load_dwordx4 v205, s[16:19], s65 offen offset:128 lds\n"
"v_mfma_f32_16x16x32_bf16 v[168:171], v[216:219], a[0:3], v[168:171]\n"
"s_waitcnt lgkmcnt(6)\n"
"ds_read_b64_tr_b16 v[212:213], v192 offset:18432\n"
"ds_read_b64_tr_b16 v[214:215], v192 offset:26624\n"
"v_mfma_f32_16x16x32_bf16 v[176:179], v[224:227], a[0:3], 0\n"
"ds_read_b64_tr_b16 v[216:217], v193 offset:16384\n"
"v_cvt_pk_bf16_f32 v96, v252, v253\n"
"v_mfma_f32_16x16x32_bf16 v[176:179], v[228:231], a[8:11], v[176:179]\n"
"ds_read_b64_tr_b16 v[218:219], v193 offset:24576\n"
"v_cvt_pk_bf16_f32 v97, v254, v255\n"
"v_mfma_f32_16x16x32_bf16 v[180:183], v[228:231], a[12:15], 0\n"
"ds_read_b64_tr_b16 v[220:221], v193 offset:18432\n"
"v_accvgpr_read_b32 v252, a140\n"
"v_mfma_f32_16x16x32_bf16 v[180:183], v[224:227], a[4:7], v[180:183]\n"
"s_waitcnt lgkmcnt(7)\n"
"ds_read_b64_tr_b16 v[222:223], v193 offset:26624\n"
"v_accvgpr_read_b32 v253, a141\n"
"v_mfma_f32_16x16x32_bf16 v[188:191], v[232:235], a[4:7], 0\n"
"ds_read_b64_tr_b16 v[224:225], v192 offset:16896\n"
"v_accvgpr_read_b32 v254, a142\n"
"v_mfma_f32_16x16x32_bf16 v[188:191], v[236:239], a[12:15], v[188:191]\n"
"ds_read_b64_tr_b16 v[226:227], v192 offset:25088\n"
"v_accvgpr_read_b32 v255, a143\n"
"v_mfma_f32_16x16x32_bf16 v[184:187], v[236:239], a[8:11], 0\n"
"s_waitcnt vmcnt(12)\n"
"s_barrier\n"
"v_mfma_f32_16x16x32_bf16 v[184:187], v[232:235], a[0:3], v[184:187]\n"
"s_waitcnt lgkmcnt(6)\n"
"ds_read_b64_tr_b16 v[228:229], v192 offset:18944\n"
"ds_read_b64_tr_b16 v[230:231], v192 offset:27136\n"
"v_mfma_f32_16x16x32_bf16 v[128:131], v[208:211], a[16:19], v[128:131]\n"
"s_add_u32 s65, s41, 768\n"
"s_add_u32 m0, s60, 147456\n"
"ds_read_b64_tr_b16 v[232:233], v193 offset:16896\n"
"v_mfma_f32_16x16x32_bf16 v[128:131], v[212:215], a[24:27], v[128:131]\n"
"buffer_load_dwordx4 v204, s[16:19], s65 offen lds\n"
"v_mfma_f32_16x16x32_bf16 v[132:135], v[212:215], a[28:31], v[132:135]\n"
"ds_read_b64_tr_b16 v[234:235], v193 offset:25088\n"
"ds_read_b64_tr_b16 v[236:237], v193 offset:18944\n"
"v_mfma_f32_16x16x32_bf16 v[132:135], v[208:211], a[20:23], v[132:135]\n"
"s_waitcnt lgkmcnt(7)\n"
"ds_read_b64_tr_b16 v[238:239], v193 offset:27136\n"
"v_mul_f32 v252, s67, v252\n"
"v_mfma_f32_16x16x32_bf16 v[140:143], v[216:219], a[20:23], v[140:143]\n"
"s_add_u32 m0, s60, 148352\n"
"ds_read_b64_tr_b16 v[208:209], v192 offset:17408\n"
"v_mul_f32 v253, s67, v253\n"
"v_mfma_f32_16x16x32_bf16 v[140:143], v[220:223], a[28:31], v[140:143]\n"
"buffer_load_dwordx4 v204, s[16:19], s65 offen offset:128 lds\n"
"v_mfma_f32_16x16x32_bf16 v[136:139], v[220:223], a[24:27], v[136:139]\n"
"ds_read_b64_tr_b16 v[210:211], v192 offset:25600\n"
"ds_read_b64_tr_b16 v[212:213], v192 offset:19456\n"
"v_mfma_f32_16x16x32_bf16 v[136:139], v[216:219], a[16:19], v[136:139]\n"
"s_waitcnt lgkmcnt(7)\n"
"ds_read_b64_tr_b16 v[214:215], v192 offset:27648\n"
"v_mul_f32 v254, s67, v254\n"
"v_mfma_f32_16x16x32_bf16 v[144:147], v[224:227], a[16:19], v[144:147]\n"
"s_add_u32 m0, s60, 149504\n"
"ds_read_b64_tr_b16 v[216:217], v193 offset:17408\n"
"v_mul_f32 v255, s67, v255\n"
"v_mfma_f32_16x16x32_bf16 v[144:147], v[228:231], a[24:27], v[144:147]\n"
"buffer_load_dwordx4 v205, s[16:19], s65 offen lds\n"
"v_mfma_f32_16x16x32_bf16 v[148:151], v[228:231], a[28:31], v[148:151]\n"
"s_add_u32 m0, s60, 150400\n"
"ds_read_b64_tr_b16 v[218:219], v193 offset:25600\n"
"ds_read_b64_tr_b16 v[220:221], v193 offset:19456\n"
"v_mfma_f32_16x16x32_bf16 v[148:151], v[224:227], a[20:23], v[148:151]\n"
"s_waitcnt lgkmcnt(7)\n"
"buffer_load_dwordx4 v205, s[16:19], s65 offen offset:128 lds\n"
"v_mfma_f32_16x16x32_bf16 v[156:159], v[232:235], a[20:23], v[156:159]\n"
"ds_read_b64_tr_b16 v[222:223], v193 offset:27648\n"
"ds_read_b64_tr_b16 v[224:225], v192 offset:17920\n"
"v_mfma_f32_16x16x32_bf16 v[156:159], v[236:239], a[28:31], v[156:159]\n"
"ds_read_b64_tr_b16 v[226:227], v192 offset:26112\n"
"v_cvt_pk_bf16_f32 v98, v252, v253\n"
"v_mfma_f32_16x16x32_bf16 v[152:155], v[236:239], a[24:27], v[152:155]\n"
"s_add_u32 s66, s73, 16384\n"
"s_and_b32 s66, s66, 524287\n"
"ds_read_b64_tr_b16 v[228:229], v192 offset:19968\n"
"v_mfma_f32_16x16x32_bf16 v[152:155], v[232:235], a[16:19], v[152:155]\n"
"s_waitcnt lgkmcnt(7)\n"
"s_add_u32 m0, s56, 65536\n"
"ds_read_b64_tr_b16 v[230:231], v192 offset:28160\n"
"v_mfma_f32_16x16x32_bf16 v[160:163], v[208:211], a[16:19], v[160:163]\n"
"buffer_load_dwordx4 v248, s[12:15], s66 offen lds\n"
"v_mfma_f32_16x16x32_bf16 v[160:163], v[212:215], a[24:27], v[160:163]\n"
"s_add_u32 m0, s57, 65536\n"
"ds_read_b64_tr_b16 v[232:233], v193 offset:17920\n"
"ds_read_b64_tr_b16 v[234:235], v193 offset:26112\n"
"v_mfma_f32_16x16x32_bf16 v[164:167], v[212:215], a[28:31], v[164:167]\n"
"buffer_load_dwordx4 v248, s[12:15], s66 offen offset:128 lds\n"
"v_mfma_f32_16x16x32_bf16 v[164:167], v[208:211], a[20:23], v[164:167]\n"
"s_waitcnt lgkmcnt(6)\n"
"ds_read_b64_tr_b16 v[236:237], v193 offset:19968\n"
"ds_read_b64_tr_b16 v[238:239], v193 offset:28160\n"
"v_mfma_f32_16x16x32_bf16 v[172:175], v[216:219], a[20:23], v[172:175]\n"
"ds_read_b64_tr_b16 v[208:209], v194 offset:32768\n"
"v_cvt_pk_bf16_f32 v99, v254, v255\n"
"v_mfma_f32_16x16x32_bf16 v[172:175], v[220:223], a[28:31], v[172:175]\n"
"s_add_u32 m0, s58, 65536\n"
"ds_read_b64_tr_b16 v[210:211], v194 offset:40960\n"
"v_permlane16_swap_b32 v96, v98\n"
"v_mfma_f32_16x16x32_bf16 v[168:171], v[220:223], a[24:27], v[168:171]\n"
"buffer_load_dwordx4 v248, s[12:15], s66 offen offset:256 lds\n"
"v_mfma_f32_16x16x32_bf16 v[168:171], v[216:219], a[16:19], v[168:171]\n"
"s_waitcnt lgkmcnt(6)\n"
"ds_read_b64_tr_b16 v[212:213], v194 offset:34816\n"
"ds_read_b64_tr_b16 v[214:215], v194 offset:43008\n"
"v_mfma_f32_16x16x32_bf16 v[176:179], v[224:227], a[16:19], v[176:179]\n"
"s_add_u32 m0, s59, 65536\n"
"ds_read_b64_tr_b16 v[216:217], v195 offset:32768\n"
"v_permlane16_swap_b32 v97, v99\n"
"v_mfma_f32_16x16x32_bf16 v[176:179], v[228:231], a[24:27], v[176:179]\n"
"buffer_load_dwordx4 v248, s[12:15], s66 offen offset:384 lds\n"
"v_mfma_f32_16x16x32_bf16 v[180:183], v[228:231], a[28:31], v[180:183]\n"
"ds_read_b64_tr_b16 v[218:219], v195 offset:40960\n"
"ds_read_b64_tr_b16 v[220:221], v195 offset:34816\n"
"v_mfma_f32_16x16x32_bf16 v[180:183], v[224:227], a[20:23], v[180:183]\n"
"s_waitcnt lgkmcnt(7)\n"
"ds_read_b64_tr_b16 v[222:223], v195 offset:43008\n"
"v_accvgpr_read_b32 v252, a144\n"
"v_mfma_f32_16x16x32_bf16 v[188:191], v[232:235], a[20:23], v[188:191]\n"
"ds_read_b64_tr_b16 v[224:225], v194 offset:33280\n"
"v_accvgpr_read_b32 v253, a145\n"
"v_mfma_f32_16x16x32_bf16 v[188:191], v[236:239], a[28:31], v[188:191]\n"
"ds_read_b64_tr_b16 v[226:227], v194 offset:41472\n"
"v_accvgpr_read_b32 v254, a146\n"
"v_mfma_f32_16x16x32_bf16 v[184:187], v[236:239], a[24:27], v[184:187]\n"
"s_waitcnt vmcnt(12)\n"
"s_barrier\n"
"v_mfma_f32_16x16x32_bf16 v[184:187], v[232:235], a[16:19], v[184:187]\n"
"s_waitcnt lgkmcnt(6)\n"
"ds_read_b64_tr_b16 v[228:229], v194 offset:35328\n"
"ds_read_b64_tr_b16 v[230:231], v194 offset:43520\n"
"v_mfma_f32_16x16x32_bf16 v[128:131], v[208:211], a[32:35], v[128:131]\n"
"s_add_u32 s65, s42, 768\n"
"ds_read_b64_tr_b16 v[232:233], v195 offset:33280\n"
"v_mul_f32 v252, s67, v252\n"
"v_mfma_f32_16x16x32_bf16 v[128:131], v[212:215], a[40:43], v[128:131]\n"
"s_add_u32 m0, s60, 16384\n"
"ds_read_b64_tr_b16 v[234:235], v195 offset:41472\n"
"v_mul_f32 v253, s67, v253\n"
"v_mfma_f32_16x16x32_bf16 v[132:135], v[212:215], a[44:47], v[132:135]\n"
"buffer_load_dwordx4 v204, s[16:19], s65 offen lds\n"
"v_mfma_f32_16x16x32_bf16 v[132:135], v[208:211], a[36:39], v[132:135]\n"
"s_waitcnt lgkmcnt(6)\n"
"ds_read_b64_tr_b16 v[236:237], v195 offset:35328\n"
"ds_read_b64_tr_b16 v[238:239], v195 offset:43520\n"
"v_mfma_f32_16x16x32_bf16 v[140:143], v[216:219], a[36:39], v[140:143]\n"
"ds_read_b64_tr_b16 v[208:209], v194 offset:33792\n"
"v_accvgpr_read_b32 v255, a147\n"
"v_mfma_f32_16x16x32_bf16 v[140:143], v[220:223], a[44:47], v[140:143]\n"
"ds_read_b64_tr_b16 v[210:211], v194 offset:41984\n"
"v_mul_f32 v254, s67, v254\n"
"v_mfma_f32_16x16x32_bf16 v[136:139], v[220:223], a[40:43], v[136:139]\n"
"ds_read_b64_tr_b16 v[212:213], v194 offset:35840\n"
"v_mul_f32 v255, s67, v255\n"
"v_mfma_f32_16x16x32_bf16 v[136:139], v[216:219], a[32:35], v[136:139]\n"
"s_waitcnt lgkmcnt(7)\n"
"ds_read_b64_tr_b16 v[214:215], v194 offset:44032\n"
"v_cvt_pk_bf16_f32 v68, v252, v253\n"
"v_mfma_f32_16x16x32_bf16 v[144:147], v[224:227], a[32:35], v[144:147]\n"
"s_add_u32 m0, s60, 17280\n"
"ds_read_b64_tr_b16 v[216:217], v195 offset:33792\n"
"v_mfma_f32_16x16x32_bf16 v[144:147], v[228:231], a[40:43], v[144:147]\n"
"buffer_load_dwordx4 v204, s[16:19], s65 offen offset:128 lds\n"
"v_mfma_f32_16x16x32_bf16 v[148:151], v[228:231], a[44:47], v[148:151]\n"
"ds_read_b64_tr_b16 v[218:219], v195 offset:41984\n"
"ds_read_b64_tr_b16 v[220:221], v195 offset:35840\n"
"v_mfma_f32_16x16x32_bf16 v[148:151], v[224:227], a[36:39], v[148:151]\n"
"s_waitcnt lgkmcnt(7)\n"
"ds_read_b64_tr_b16 v[222:223], v195 offset:44032\n"
"v_cvt_pk_bf16_f32 v69, v254, v255\n"
"v_mfma_f32_16x16x32_bf16 v[156:159], v[232:235], a[36:39], v[156:159]\n"
"ds_read_b64_tr_b16 v[224:225], v194 offset:34304\n"
"v_accvgpr_read_b32 v252, a152\n"
"v_mfma_f32_16x16x32_bf16 v[156:159], v[236:239], a[44:47], v[156:159]\n"
"ds_read_b64_tr_b16 v[226:227], v194 offset:42496\n"
"v_accvgpr_read_b32 v253, a153\n"
"v_mfma_f32_16x16x32_bf16 v[152:155], v[236:239], a[40:43], v[152:155]\n"
"s_add_u32 m0, s60, 18432\n"
"ds_read_b64_tr_b16 v[228:229], v194 offset:36352\n"
"v_mul_f32 v252, s67, v252\n"
"v_mfma_f32_16x16x32_bf16 v[152:155], v[232:235], a[32:35], v[152:155]\n"
"s_waitcnt lgkmcnt(7)\n"
"buffer_load_dwordx4 v205, s[16:19], s65 offen lds\n"
"v_mfma_f32_16x16x32_bf16 v[160:163], v[208:211], a[32:35], v[160:163]\n"
"ds_read_b64_tr_b16 v[230:231], v194 offset:44544\n"
"ds_read_b64_tr_b16 v[232:233], v195 offset:34304\n"
"v_mfma_f32_16x16x32_bf16 v[160:163], v[212:215], a[40:43], v[160:163]\n"
"ds_read_b64_tr_b16 v[234:235], v195 offset:42496\n"
"v_accvgpr_read_b32 v254, a154\n"
"v_mfma_f32_16x16x32_bf16 v[164:167], v[212:215], a[44:47], v[164:167]\n"
"ds_read_b64_tr_b16 v[236:237], v195 offset:36352\n"
"v_accvgpr_read_b32 v255, a155\n"
"v_mfma_f32_16x16x32_bf16 v[164:167], v[208:211], a[36:39], v[164:167]\n"
"s_waitcnt lgkmcnt(7)\n"
"ds_read_b64_tr_b16 v[238:239], v195 offset:44544\n"
"v_mul_f32 v253, s67, v253\n"
"v_mfma_f32_16x16x32_bf16 v[172:175], v[216:219], a[36:39], v[172:175]\n"
"ds_read_b64_tr_b16 v[208:209], v194 offset:49152\n"
"v_mul_f32 v254, s67, v254\n"
"v_mfma_f32_16x16x32_bf16 v[172:175], v[220:223], a[44:47], v[172:175]\n"
"s_add_u32 m0, s60, 19328\n"
"ds_read_b64_tr_b16 v[210:211], v194 offset:57344\n"
"v_mul_f32 v255, s67, v255\n"
"v_mfma_f32_16x16x32_bf16 v[168:171], v[220:223], a[40:43], v[168:171]\n"
"buffer_load_dwordx4 v205, s[16:19], s65 offen offset:128 lds\n"
"v_mfma_f32_16x16x32_bf16 v[168:171], v[216:219], a[32:35], v[168:171]\n"
"s_waitcnt lgkmcnt(6)\n"
"ds_read_b64_tr_b16 v[212:213], v194 offset:51200\n"
"ds_read_b64_tr_b16 v[214:215], v194 offset:59392\n"
"v_mfma_f32_16x16x32_bf16 v[176:179], v[224:227], a[32:35], v[176:179]\n"
"ds_read_b64_tr_b16 v[216:217], v195 offset:49152\n"
"v_cvt_pk_bf16_f32 v70, v252, v253\n"
"v_mfma_f32_16x16x32_bf16 v[176:179], v[228:231], a[40:43], v[176:179]\n"
"ds_read_b64_tr_b16 v[218:219], v195 offset:57344\n"
"v_cvt_pk_bf16_f32 v71, v254, v255\n"
"v_mfma_f32_16x16x32_bf16 v[180:183], v[228:231], a[44:47], v[180:183]\n"
"ds_read_b64_tr_b16 v[220:221], v195 offset:51200\n"
"v_accvgpr_read_b32 v252, a148\n"
"v_mfma_f32_16x16x32_bf16 v[180:183], v[224:227], a[36:39], v[180:183]\n"
"s_waitcnt lgkmcnt(7)\n"
"ds_read_b64_tr_b16 v[222:223], v195 offset:59392\n"
"v_accvgpr_read_b32 v253, a149\n"
"v_mfma_f32_16x16x32_bf16 v[188:191], v[232:235], a[36:39], v[188:191]\n"
"ds_read_b64_tr_b16 v[224:225], v194 offset:49664\n"
"v_accvgpr_read_b32 v254, a150\n"
"v_mfma_f32_16x16x32_bf16 v[188:191], v[236:239], a[44:47], v[188:191]\n"
"ds_read_b64_tr_b16 v[226:227], v194 offset:57856\n"
"v_accvgpr_read_b32 v255, a151\n"
"v_mfma_f32_16x16x32_bf16 v[184:187], v[236:239], a[40:43], v[184:187]\n"
"s_waitcnt vmcnt(12)\n"
"s_barrier\n"
"v_mfma_f32_16x16x32_bf16 v[184:187], v[232:235], a[32:35], v[184:187]\n"
"s_waitcnt lgkmcnt(6)\n"
"ds_read_b64_tr_b16 v[228:229], v194 offset:51712\n"
"ds_read_b64_tr_b16 v[230:231], v194 offset:59904\n"
"v_mfma_f32_16x16x32_bf16 v[128:131], v[208:211], a[48:51], v[128:131]\n"
"s_add_u32 s66, s73, 49152\n"
"ds_read_b64_tr_b16 v[232:233], v195 offset:49664\n"
"v_mul_f32 v252, s67, v252\n"
"v_mfma_f32_16x16x32_bf16 v[128:131], v[212:215], a[56:59], v[128:131]\n"
"s_and_b32 s66, s66, 524287\n"
"ds_read_b64_tr_b16 v[234:235], v195 offset:57856\n"
"v_mul_f32 v253, s67, v253\n"
"v_mfma_f32_16x16x32_bf16 v[132:135], v[212:215], a[60:63], v[132:135]\n"
"s_add_u32 m0, s52, 98304\n"
"ds_read_b64_tr_b16 v[236:237], v195 offset:51712\n"
"v_mul_f32 v254, s67, v254\n"
"v_mfma_f32_16x16x32_bf16 v[132:135], v[208:211], a[52:55], v[132:135]\n"
"s_waitcnt lgkmcnt(7)\n"
"buffer_load_dwordx4 v247, s[8:11], s66 offen nt lds\n"
"v_mfma_f32_16x16x32_bf16 v[140:143], v[216:219], a[52:55], v[140:143]\n"
"ds_read_b64_tr_b16 v[238:239], v195 offset:59904\n"
"ds_read_b64_tr_b16 v[208:209], v194 offset:50176\n"
"v_mfma_f32_16x16x32_bf16 v[140:143], v[220:223], a[60:63], v[140:143]\n"
"ds_read_b64_tr_b16 v[210:211], v194 offset:58368\n"
"v_mul_f32 v255, s67, v255\n"
"v_mfma_f32_16x16x32_bf16 v[136:139], v[220:223], a[56:59], v[136:139]\n"
"ds_read_b64_tr_b16 v[212:213], v194 offset:52224\n"
"v_cvt_pk_bf16_f32 v100, v252, v253\n"
"v_mfma_f32_16x16x32_bf16 v[136:139], v[216:219], a[48:51], v[136:139]\n"
"s_waitcnt lgkmcnt(7)\n"
"ds_read_b64_tr_b16 v[214:215], v194 offset:60416\n"
"v_cvt_pk_bf16_f32 v101, v254, v255\n"
"v_mfma_f32_16x16x32_bf16 v[144:147], v[224:227], a[48:51], v[144:147]\n"
"s_add_u32 m0, s53, 98304\n"
"ds_read_b64_tr_b16 v[216:217], v195 offset:50176\n"
"v_permlane16_swap_b32 v68, v70\n"
"v_mfma_f32_16x16x32_bf16 v[144:147], v[228:231], a[56:59], v[144:147]\n"
"buffer_load_dwordx4 v247, s[8:11], s66 offen offset:128 nt lds\n"
"v_mfma_f32_16x16x32_bf16 v[148:151], v[228:231], a[60:63], v[148:151]\n"
"ds_read_b64_tr_b16 v[218:219], v195 offset:58368\n"
"ds_read_b64_tr_b16 v[220:221], v195 offset:52224\n"
"v_mfma_f32_16x16x32_bf16 v[148:151], v[224:227], a[52:55], v[148:151]\n"
"s_waitcnt lgkmcnt(7)\n"
"ds_read_b64_tr_b16 v[222:223], v195 offset:60416\n"
"v_accvgpr_read_b32 v252, a156\n"
"v_mfma_f32_16x16x32_bf16 v[156:159], v[232:235], a[52:55], v[156:159]\n"
"ds_read_b64_tr_b16 v[224:225], v194 offset:50688\n"
"v_accvgpr_read_b32 v253, a157\n"
"v_mfma_f32_16x16x32_bf16 v[156:159], v[236:239], a[60:63], v[156:159]\n"
"ds_read_b64_tr_b16 v[226:227], v194 offset:58880\n"
"v_accvgpr_read_b32 v254, a158\n"
"v_mfma_f32_16x16x32_bf16 v[152:155], v[236:239], a[56:59], v[152:155]\n"
"s_add_u32 m0, s54, 98304\n"
"ds_read_b64_tr_b16 v[228:229], v194 offset:52736\n"
"v_mul_f32 v252, s67, v252\n"
"v_mfma_f32_16x16x32_bf16 v[152:155], v[232:235], a[48:51], v[152:155]\n"
"s_waitcnt lgkmcnt(7)\n"
"buffer_load_dwordx4 v247, s[8:11], s66 offen offset:256 nt lds\n"
"v_mfma_f32_16x16x32_bf16 v[160:163], v[208:211], a[48:51], v[160:163]\n"
"ds_read_b64_tr_b16 v[230:231], v194 offset:60928\n"
"ds_read_b64_tr_b16 v[232:233], v195 offset:50688\n"
"v_mfma_f32_16x16x32_bf16 v[160:163], v[212:215], a[56:59], v[160:163]\n"
"ds_read_b64_tr_b16 v[234:235], v195 offset:58880\n"
"v_accvgpr_read_b32 v255, a159\n"
"v_mfma_f32_16x16x32_bf16 v[164:167], v[212:215], a[60:63], v[164:167]\n"
"ds_read_b64_tr_b16 v[236:237], v195 offset:52736\n"
"v_mul_f32 v253, s67, v253\n"
"v_mfma_f32_16x16x32_bf16 v[164:167], v[208:211], a[52:55], v[164:167]\n"
"s_waitcnt lgkmcnt(7)\n"
"ds_read_b64_tr_b16 v[238:239], v195 offset:60928\n"
"v_mul_f32 v254, s67, v254\n"
"v_mfma_f32_16x16x32_bf16 v[172:175], v[216:219], a[52:55], v[172:175]\n"
"ds_read_b64_tr_b16 v[208:209], v196 offset:0\n"
"v_mul_f32 v255, s67, v255\n"
"v_mfma_f32_16x16x32_bf16 v[172:175], v[220:223], a[60:63], v[172:175]\n"
"s_add_u32 m0, s55, 98304\n"
"ds_read_b64_tr_b16 v[210:211], v196 offset:8192\n"
"v_permlane16_swap_b32 v69, v71\n"
"v_mfma_f32_16x16x32_bf16 v[168:171], v[220:223], a[56:59], v[168:171]\n"
"buffer_load_dwordx4 v247, s[8:11], s66 offen offset:384 nt lds\n"
"v_mfma_f32_16x16x32_bf16 v[168:171], v[216:219], a[48:51], v[168:171]\n"
"s_waitcnt lgkmcnt(6)\n"
"ds_read_b64_tr_b16 v[212:213], v196 offset:2048\n"
"ds_read_b64_tr_b16 v[214:215], v196 offset:10240\n"
"v_mfma_f32_16x16x32_bf16 v[176:179], v[224:227], a[48:51], v[176:179]\n"
"ds_read_b64_tr_b16 v[216:217], v197 offset:0\n"
"v_cvt_pk_bf16_f32 v102, v252, v253\n"
"v_mfma_f32_16x16x32_bf16 v[176:179], v[228:231], a[56:59], v[176:179]\n"
"IWC0_%=:\n"
"s_cbranch_execz IRC_%=\n"
"ds_read_b64_tr_b16 v[218:219], v197 offset:8192\n"
"v_cvt_pk_bf16_f32 v103, v254, v255\n"
"v_mfma_f32_16x16x32_bf16 v[180:183], v[228:231], a[60:63], v[180:183]\n"
"ds_read_b64_tr_b16 v[220:221], v197 offset:2048\n"
"v_accvgpr_read_b32 v252, a160\n"
"v_mfma_f32_16x16x32_bf16 v[180:183], v[224:227], a[52:55], v[180:183]\n"
"s_waitcnt lgkmcnt(7)\n"
"ds_read_b64_tr_b16 v[222:223], v197 offset:10240\n"
"v_accvgpr_read_b32 v253, a161\n"
"v_mfma_f32_16x16x32_bf16 v[188:191], v[232:235], a[52:55], v[188:191]\n"
"ds_read_b64_tr_b16 v[224:225], v196 offset:512\n"
"v_accvgpr_read_b32 v254, a162\n"
"v_mfma_f32_16x16x32_bf16 v[188:191], v[236:239], a[60:63], v[188:191]\n"
"ds_read_b64_tr_b16 v[226:227], v196 offset:8704\n"
"v_accvgpr_read_b32 v255, a163\n"
"v_mfma_f32_16x16x32_bf16 v[184:187], v[236:239], a[56:59], v[184:187]\n"
"s_waitcnt vmcnt(12)\n"
"s_barrier\n"
"v_mfma_f32_16x16x32_bf16 v[184:187], v[232:235], a[48:51], v[184:187]\n"
"s_waitcnt lgkmcnt(6)\n"
"ds_read_b64_tr_b16 v[228:229], v196 offset:2560\n"
"ds_read_b64_tr_b16 v[230:231], v196 offset:10752\n"
"v_mfma_f32_16x16x32_bf16 v[128:131], v[208:211], a[64:67], v[128:131]\n"
"ds_read_b64_tr_b16 v[232:233], v197 offset:512\n"
"v_mul_f32 v252, s67, v252\n"
"v_mfma_f32_16x16x32_bf16 v[128:131], v[212:215], a[72:75], v[128:131]\n"
"ds_read_b64_tr_b16 v[234:235], v197 offset:8704\n"
"v_mul_f32 v253, s67, v253\n"
"v_mfma_f32_16x16x32_bf16 v[132:135], v[212:215], a[76:79], v[132:135]\n"
"ds_read_b64_tr_b16 v[236:237], v197 offset:2560\n"
"v_mul_f32 v254, s67, v254\n"
"v_mfma_f32_16x16x32_bf16 v[132:135], v[208:211], a[68:71], v[132:135]\n"
"s_waitcnt lgkmcnt(7)\n"
"ds_read_b64_tr_b16 v[238:239], v197 offset:10752\n"
"v_mul_f32 v255, s67, v255\n"
"v_mfma_f32_16x16x32_bf16 v[140:143], v[216:219], a[68:71], v[140:143]\n"
"ds_read_b64_tr_b16 v[208:209], v196 offset:1024\n"
"v_cvt_pk_bf16_f32 v72, v252, v253\n"
"v_mfma_f32_16x16x32_bf16 v[140:143], v[220:223], a[76:79], v[140:143]\n"
"ds_read_b64_tr_b16 v[210:211], v196 offset:9216\n"
"v_cvt_pk_bf16_f32 v73, v254, v255\n"
"v_mfma_f32_16x16x32_bf16 v[136:139], v[220:223], a[72:75], v[136:139]\n"
"ds_read_b64_tr_b16 v[212:213], v196 offset:3072\n"
"v_accvgpr_read_b32 v252, a168\n"
"v_mfma_f32_16x16x32_bf16 v[136:139], v[216:219], a[64:67], v[136:139]\n"
"s_waitcnt lgkmcnt(7)\n"
"ds_read_b64_tr_b16 v[214:215], v196 offset:11264\n"
"v_accvgpr_read_b32 v253, a169\n"
"v_mfma_f32_16x16x32_bf16 v[144:147], v[224:227], a[64:67], v[144:147]\n"
"ds_read_b64_tr_b16 v[216:217], v197 offset:1024\n"
"v_accvgpr_read_b32 v254, a170\n"
"v_mfma_f32_16x16x32_bf16 v[144:147], v[228:231], a[72:75], v[144:147]\n"
"ds_read_b64_tr_b16 v[218:219], v197 offset:9216\n"
"v_accvgpr_read_b32 v255, a171\n"
"v_mfma_f32_16x16x32_bf16 v[148:151], v[228:231], a[76:79], v[148:151]\n"
"ds_read_b64_tr_b16 v[220:221], v197 offset:3072\n"
"v_mul_f32 v252, s67, v252\n"
"v_mfma_f32_16x16x32_bf16 v[148:151], v[224:227], a[68:71], v[148:151]\n"
"s_waitcnt lgkmcnt(7)\n"
"ds_read_b64_tr_b16 v[222:223], v197 offset:11264\n"
"v_mul_f32 v253, s67, v253\n"
"v_mfma_f32_16x16x32_bf16 v[156:159], v[232:235], a[68:71], v[156:159]\n"
"ds_read_b64_tr_b16 v[224:225], v196 offset:1536\n"
"v_mul_f32 v254, s67, v254\n"
"v_mfma_f32_16x16x32_bf16 v[156:159], v[236:239], a[76:79], v[156:159]\n"
"ds_read_b64_tr_b16 v[226:227], v196 offset:9728\n"
"v_mul_f32 v255, s67, v255\n"
"v_mfma_f32_16x16x32_bf16 v[152:155], v[236:239], a[72:75], v[152:155]\n"
"ds_read_b64_tr_b16 v[228:229], v196 offset:3584\n"
"v_cvt_pk_bf16_f32 v74, v252, v253\n"
"v_mfma_f32_16x16x32_bf16 v[152:155], v[232:235], a[64:67], v[152:155]\n"
"s_waitcnt lgkmcnt(7)\n"
"ds_read_b64_tr_b16 v[230:231], v196 offset:11776\n"
"v_cvt_pk_bf16_f32 v75, v254, v255\n"
"v_mfma_f32_16x16x32_bf16 v[160:163], v[208:211], a[64:67], v[160:163]\n"
"ds_read_b64_tr_b16 v[232:233], v197 offset:1536\n"
"v_permlane16_swap_b32 v100, v102\n"
"v_mfma_f32_16x16x32_bf16 v[160:163], v[212:215], a[72:75], v[160:163]\n"
"ds_read_b64_tr_b16 v[234:235], v197 offset:9728\n"
"v_permlane16_swap_b32 v101, v103\n"
"v_mfma_f32_16x16x32_bf16 v[164:167], v[212:215], a[76:79], v[164:167]\n"
"ds_read_b64_tr_b16 v[236:237], v197 offset:3584\n"
"v_accvgpr_read_b32 v252, a164\n"
"v_mfma_f32_16x16x32_bf16 v[164:167], v[208:211], a[68:71], v[164:167]\n"
"s_waitcnt lgkmcnt(7)\n"
"ds_read_b64_tr_b16 v[238:239], v197 offset:11776\n"
"v_accvgpr_read_b32 v253, a165\n"
"v_mfma_f32_16x16x32_bf16 v[172:175], v[216:219], a[68:71], v[172:175]\n"
"ds_read_b64_tr_b16 v[208:209], v196 offset:16384\n"
"v_accvgpr_read_b32 v254, a166\n"
"v_mfma_f32_16x16x32_bf16 v[172:175], v[220:223], a[76:79], v[172:175]\n"
"ds_read_b64_tr_b16 v[210:211], v196 offset:24576\n"
"v_accvgpr_read_b32 v255, a167\n"
"v_mfma_f32_16x16x32_bf16 v[168:171], v[220:223], a[72:75], v[168:171]\n"
"ds_read_b64_tr_b16 v[212:213], v196 offset:18432\n"
"v_mul_f32 v252, s67, v252\n"
"v_mfma_f32_16x16x32_bf16 v[168:171], v[216:219], a[64:67], v[168:171]\n"
"s_waitcnt lgkmcnt(7)\n"
"ds_read_b64_tr_b16 v[214:215], v196 offset:26624\n"
"v_mul_f32 v253, s67, v253\n"
"v_mfma_f32_16x16x32_bf16 v[176:179], v[224:227], a[64:67], v[176:179]\n"
"ds_read_b64_tr_b16 v[216:217], v197 offset:16384\n"
"v_mul_f32 v254, s67, v254\n"
"v_mfma_f32_16x16x32_bf16 v[176:179], v[228:231], a[72:75], v[176:179]\n"
"ds_read_b64_tr_b16 v[218:219], v197 offset:24576\n"
"v_mul_f32 v255, s67, v255\n"
"v_mfma_f32_16x16x32_bf16 v[180:183], v[228:231], a[76:79], v[180:183]\n"
"ds_read_b64_tr_b16 v[220:221], v197 offset:18432\n"
"v_cvt_pk_bf16_f32 v104, v252, v253\n"
"v_mfma_f32_16x16x32_bf16 v[180:183], v[224:227], a[68:71], v[180:183]\n"
"s_waitcnt lgkmcnt(7)\n"
"ds_read_b64_tr_b16 v[222:223], v197 offset:26624\n"
"v_cvt_pk_bf16_f32 v105, v254, v255\n"
"v_mfma_f32_16x16x32_bf16 v[188:191], v[232:235], a[68:71], v[188:191]\n"
"ds_read_b64_tr_b16 v[224:225], v196 offset:16896\n"
"v_accvgpr_read_b32 v252, a172\n"
"v_mfma_f32_16x16x32_bf16 v[188:191], v[236:239], a[76:79], v[188:191]\n"
"ds_read_b64_tr_b16 v[226:227], v196 offset:25088\n"
"v_accvgpr_read_b32 v253, a173\n"
"v_mfma_f32_16x16x32_bf16 v[184:187], v[236:239], a[72:75], v[184:187]\n"
"s_waitcnt vmcnt(4)\n"
"s_barrier\n"
"v_mfma_f32_16x16x32_bf16 v[184:187], v[232:235], a[64:67], v[184:187]\n"
"s_waitcnt lgkmcnt(6)\n"
"ds_read_b64_tr_b16 v[228:229], v196 offset:18944\n"
"ds_read_b64_tr_b16 v[230:231], v196 offset:27136\n"
"v_mfma_f32_16x16x32_bf16 v[128:131], v[208:211], a[80:83], v[128:131]\n"
"s_add_u32 s66, s73, 32768\n"
"ds_read_b64_tr_b16 v[232:233], v197 offset:16896\n"
"v_mul_f32 v252, s67, v252\n"
"v_mfma_f32_16x16x32_bf16 v[128:131], v[212:215], a[88:91], v[128:131]\n"
"s_and_b32 s66, s66, 524287\n"
"ds_read_b64_tr_b16 v[234:235], v197 offset:25088\n"
"v_mul_f32 v253, s67, v253\n"
"v_mfma_f32_16x16x32_bf16 v[132:135], v[212:215], a[92:95], v[132:135]\n"
"s_add_u32 m0, s56, 98304\n"
"ds_read_b64_tr_b16 v[236:237], v197 offset:18944\n"
"v_permlane16_swap_b32 v72, v74\n"
"v_mfma_f32_16x16x32_bf16 v[132:135], v[208:211], a[84:87], v[132:135]\n"
"s_waitcnt lgkmcnt(7)\n"
"buffer_load_dwordx4 v248, s[12:15], s66 offen lds\n"
"v_mfma_f32_16x16x32_bf16 v[140:143], v[216:219], a[84:87], v[140:143]\n"
"ds_read_b64_tr_b16 v[238:239], v197 offset:27136\n"
"ds_read_b64_tr_b16 v[208:209], v196 offset:17408\n"
"v_mfma_f32_16x16x32_bf16 v[140:143], v[220:223], a[92:95], v[140:143]\n"
"ds_read_b64_tr_b16 v[210:211], v196 offset:25600\n"
"v_accvgpr_read_b32 v254, a174\n"
"v_mfma_f32_16x16x32_bf16 v[136:139], v[220:223], a[88:91], v[136:139]\n"
"ds_read_b64_tr_b16 v[212:213], v196 offset:19456\n"
"v_accvgpr_read_b32 v255, a175\n"
"v_mfma_f32_16x16x32_bf16 v[136:139], v[216:219], a[80:83], v[136:139]\n"
"s_waitcnt lgkmcnt(7)\n"
"ds_read_b64_tr_b16 v[214:215], v196 offset:27648\n"
"v_mul_f32 v254, s67, v254\n"
"v_mfma_f32_16x16x32_bf16 v[144:147], v[224:227], a[80:83], v[144:147]\n"
"s_add_u32 m0, s57, 98304\n"
"ds_read_b64_tr_b16 v[216:217], v197 offset:17408\n"
"v_mul_f32 v255, s67, v255\n"
"v_mfma_f32_16x16x32_bf16 v[144:147], v[228:231], a[88:91], v[144:147]\n"
"buffer_load_dwordx4 v248, s[12:15], s66 offen offset:128 lds\n"
"v_mfma_f32_16x16x32_bf16 v[148:151], v[228:231], a[92:95], v[148:151]\n"
"ds_read_b64_tr_b16 v[218:219], v197 offset:25600\n"
"ds_read_b64_tr_b16 v[220:221], v197 offset:19456\n"
"v_mfma_f32_16x16x32_bf16 v[148:151], v[224:227], a[84:87], v[148:151]\n"
"s_waitcnt lgkmcnt(7)\n"
"ds_read_b64_tr_b16 v[222:223], v197 offset:27648\n"
"v_cvt_pk_bf16_f32 v106, v252, v253\n"
"v_mfma_f32_16x16x32_bf16 v[156:159], v[232:235], a[84:87], v[156:159]\n"
"ds_read_b64_tr_b16 v[224:225], v196 offset:17920\n"
"v_cvt_pk_bf16_f32 v107, v254, v255\n"
"v_mfma_f32_16x16x32_bf16 v[156:159], v[236:239], a[92:95], v[156:159]\n"
"ds_read_b64_tr_b16 v[226:227], v196 offset:26112\n"
"v_permlane16_swap_b32 v73, v75\n"
"v_mfma_f32_16x16x32_bf16 v[152:155], v[236:239], a[88:91], v[152:155]\n"
"s_add_u32 m0, s58, 98304\n"
"ds_read_b64_tr_b16 v[228:229], v196 offset:19968\n"
"v_permlane16_swap_b32 v104, v106\n"
"v_mfma_f32_16x16x32_bf16 v[152:155], v[232:235], a[80:83], v[152:155]\n"
"s_waitcnt lgkmcnt(7)\n"
"buffer_load_dwordx4 v248, s[12:15], s66 offen offset:256 lds\n"
"v_mfma_f32_16x16x32_bf16 v[160:163], v[208:211], a[80:83], v[160:163]\n"
"ds_read_b64_tr_b16 v[230:231], v196 offset:28160\n"
"ds_read_b64_tr_b16 v[232:233], v197 offset:17920\n"
"v_mfma_f32_16x16x32_bf16 v[160:163], v[212:215], a[88:91], v[160:163]\n"
"ds_read_b64_tr_b16 v[234:235], v197 offset:26112\n"
"v_accvgpr_read_b32 v252, a176\n"
"v_mfma_f32_16x16x32_bf16 v[164:167], v[212:215], a[92:95], v[164:167]\n"
"ds_read_b64_tr_b16 v[236:237], v197 offset:19968\n"
"v_accvgpr_read_b32 v253, a177\n"
"v_mfma_f32_16x16x32_bf16 v[164:167], v[208:211], a[84:87], v[164:167]\n"
"s_waitcnt lgkmcnt(7)\n"
"ds_read_b64_tr_b16 v[238:239], v197 offset:28160\n"
"v_accvgpr_read_b32 v254, a178\n"
"v_mfma_f32_16x16x32_bf16 v[172:175], v[216:219], a[84:87], v[172:175]\n"
"ds_read_b64_tr_b16 v[208:209], v192 offset:16384\n"
"v_accvgpr_read_b32 v255, a179\n"
"v_mfma_f32_16x16x32_bf16 v[172:175], v[220:223], a[92:95], v[172:175]\n"
"s_add_u32 m0, s59, 98304\n"
"ds_read_b64_tr_b16 v[210:211], v192 offset:24576\n"
"v_mul_f32 v252, s67, v252\n"
"v_mfma_f32_16x16x32_bf16 v[168:171], v[220:223], a[88:91], v[168:171]\n"
"buffer_load_dwordx4 v248, s[12:15], s66 offen offset:384 lds\n"
"v_mfma_f32_16x16x32_bf16 v[168:171], v[216:219], a[80:83], v[168:171]\n"
"s_waitcnt lgkmcnt(6)\n"
"ds_read_b64_tr_b16 v[212:213], v192 offset:18432\n"
"ds_read_b64_tr_b16 v[214:215], v192 offset:26624\n"
"v_mfma_f32_16x16x32_bf16 v[176:179], v[224:227], a[80:83], v[176:179]\n"
"ds_read_b64_tr_b16 v[216:217], v193 offset:16384\n"
"v_mul_f32 v253, s67, v253\n"
"v_mfma_f32_16x16x32_bf16 v[176:179], v[228:231], a[88:91], v[176:179]\n"
"ds_read_b64_tr_b16 v[218:219], v193 offset:24576\n"
"v_mul_f32 v254, s67, v254\n"
"v_mfma_f32_16x16x32_bf16 v[180:183], v[228:231], a[92:95], v[180:183]\n"
"ds_read_b64_tr_b16 v[220:221], v193 offset:18432\n"
"v_mul_f32 v255, s67, v255\n"
"v_mfma_f32_16x16x32_bf16 v[180:183], v[224:227], a[84:87], v[180:183]\n"
"s_waitcnt lgkmcnt(7)\n"
"ds_read_b64_tr_b16 v[222:223], v193 offset:26624\n"
"v_cvt_pk_bf16_f32 v76, v252, v253\n"
"v_mfma_f32_16x16x32_bf16 v[188:191], v[232:235], a[84:87], v[188:191]\n"
"ds_read_b64_tr_b16 v[224:225], v192 offset:16896\n"
"v_cvt_pk_bf16_f32 v77, v254, v255\n"
"v_mfma_f32_16x16x32_bf16 v[188:191], v[236:239], a[92:95], v[188:191]\n"
"ds_read_b64_tr_b16 v[226:227], v192 offset:25088\n"
"v_accvgpr_read_b32 v252, a184\n"
"v_mfma_f32_16x16x32_bf16 v[184:187], v[236:239], a[88:91], v[184:187]\n"
"s_barrier\n"
"v_mfma_f32_16x16x32_bf16 v[184:187], v[232:235], a[80:83], v[184:187]\n"
"s_waitcnt lgkmcnt(6)\n"
"ds_read_b64_tr_b16 v[228:229], v192 offset:18944\n"
"ds_read_b64_tr_b16 v[230:231], v192 offset:27136\n"
"v_mfma_f32_16x16x32_bf16 v[128:131], v[208:211], a[96:99], v[128:131]\n"
"s_add_u32 s66, s73, 65536\n"
"ds_read_b64_tr_b16 v[232:233], v193 offset:16896\n"
"v_mul_f32 v252, s67, v252\n"
"v_mfma_f32_16x16x32_bf16 v[128:131], v[212:215], a[104:107], v[128:131]\n"
"s_and_b32 s66, s66, 524287\n"
"ds_read_b64_tr_b16 v[234:235], v193 offset:25088\n"
"v_permlane16_swap_b32 v105, v107\n"
"v_mfma_f32_16x16x32_bf16 v[132:135], v[212:215], a[108:111], v[132:135]\n"
"s_add_u32 m0, s52, 131072\n"
"ds_read_b64_tr_b16 v[236:237], v193 offset:18944\n"
"v_mfma_f32_16x16x32_bf16 v[132:135], v[208:211], a[100:103], v[132:135]\n"
"s_waitcnt lgkmcnt(7)\n"
"buffer_load_dwordx4 v247, s[8:11], s66 offen nt lds\n"
"v_mfma_f32_16x16x32_bf16 v[140:143], v[216:219], a[100:103], v[140:143]\n"
"ds_read_b64_tr_b16 v[238:239], v193 offset:27136\n"
"ds_read_b64_tr_b16 v[208:209], v192 offset:17408\n"
"v_mfma_f32_16x16x32_bf16 v[140:143], v[220:223], a[108:111], v[140:143]\n"
"ds_read_b64_tr_b16 v[210:211], v192 offset:25600\n"
"v_accvgpr_read_b32 v253, a185\n"
"v_mfma_f32_16x16x32_bf16 v[136:139], v[220:223], a[104:107], v[136:139]\n"
"ds_read_b64_tr_b16 v[212:213], v192 offset:19456\n"
"v_accvgpr_read_b32 v254, a186\n"
"v_mfma_f32_16x16x32_bf16 v[136:139], v[216:219], a[96:99], v[136:139]\n"
"s_waitcnt lgkmcnt(7)\n"
"ds_read_b64_tr_b16 v[214:215], v192 offset:27648\n"
"v_accvgpr_read_b32 v255, a187\n"
"v_mfma_f32_16x16x32_bf16 v[144:147], v[224:227], a[96:99], v[144:147]\n"
"s_add_u32 m0, s53, 131072\n"
"ds_read_b64_tr_b16 v[216:217], v193 offset:17408\n"
"v_mul_f32 v253, s67, v253\n"
"v_mfma_f32_16x16x32_bf16 v[144:147], v[228:231], a[104:107], v[144:147]\n"
"buffer_load_dwordx4 v247, s[8:11], s66 offen offset:128 nt lds\n"
"v_mfma_f32_16x16x32_bf16 v[148:151], v[228:231], a[108:111], v[148:151]\n"
"ds_read_b64_tr_b16 v[218:219], v193 offset:25600\n"
"ds_read_b64_tr_b16 v[220:221], v193 offset:19456\n"
"v_mfma_f32_16x16x32_bf16 v[148:151], v[224:227], a[100:103], v[148:151]\n"
"s_waitcnt lgkmcnt(7)\n"
"ds_read_b64_tr_b16 v[222:223], v193 offset:27648\n"
"v_mul_f32 v254, s67, v254\n"
"v_mfma_f32_16x16x32_bf16 v[156:159], v[232:235], a[100:103], v[156:159]\n"
"ds_read_b64_tr_b16 v[224:225], v192 offset:17920\n"
"v_mul_f32 v255, s67, v255\n"
"v_mfma_f32_16x16x32_bf16 v[156:159], v[236:239], a[108:111], v[156:159]\n"
"ds_read_b64_tr_b16 v[226:227], v192 offset:26112\n"
"v_cvt_pk_bf16_f32 v78, v252, v253\n"
"v_mfma_f32_16x16x32_bf16 v[152:155], v[236:239], a[104:107], v[152:155]\n"
"s_add_u32 m0, s54, 131072\n"
"ds_read_b64_tr_b16 v[228:229], v192 offset:19968\n"
"v_permlane16_swap_b32 v76, v78\n"
"v_mfma_f32_16x16x32_bf16 v[152:155], v[232:235], a[96:99], v[152:155]\n"
"s_waitcnt lgkmcnt(7)\n"
"buffer_load_dwordx4 v247, s[8:11], s66 offen offset:256 nt lds\n"
"v_mfma_f32_16x16x32_bf16 v[160:163], v[208:211], a[96:99], v[160:163]\n"
"ds_read_b64_tr_b16 v[230:231], v192 offset:28160\n"
"ds_read_b64_tr_b16 v[232:233], v193 offset:17920\n"
"v_mfma_f32_16x16x32_bf16 v[160:163], v[212:215], a[104:107], v[160:163]\n"
"ds_read_b64_tr_b16 v[234:235], v193 offset:26112\n"
"v_cvt_pk_bf16_f32 v79, v254, v255\n"
"v_mfma_f32_16x16x32_bf16 v[164:167], v[212:215], a[108:111], v[164:167]\n"
"ds_read_b64_tr_b16 v[236:237], v193 offset:19968\n"
"v_accvgpr_read_b32 v252, a180\n"
"v_mfma_f32_16x16x32_bf16 v[164:167], v[208:211], a[100:103], v[164:167]\n"
"s_waitcnt lgkmcnt(7)\n"
"ds_read_b64_tr_b16 v[238:239], v193 offset:28160\n"
"ds_read_b128 v[208:211], v244 offset:0\n"
"v_mfma_f32_16x16x32_bf16 v[172:175], v[216:219], a[100:103], v[172:175]\n"
"ds_read_b128 v[212:215], v244 offset:512\n"
"v_accvgpr_read_b32 v253, a181\n"
"v_mfma_f32_16x16x32_bf16 v[172:175], v[220:223], a[108:111], v[172:175]\n"
"s_add_u32 m0, s55, 131072\n"
"v_permlane16_swap_b32 v77, v79\n"
"v_mfma_f32_16x16x32_bf16 v[168:171], v[220:223], a[104:107], v[168:171]\n"
"buffer_load_dwordx4 v247, s[8:11], s66 offen offset:384 nt lds\n"
"v_mfma_f32_16x16x32_bf16 v[168:171], v[216:219], a[96:99], v[168:171]\n"
"s_waitcnt lgkmcnt(6)\n"
"v_accvgpr_read_b32 v254, a182\n"
"v_accvgpr_read_b32 v255, a183\n"
"v_mfma_f32_16x16x32_bf16 v[176:179], v[224:227], a[96:99], v[176:179]\n"
"ds_read_b128 v[216:219], v244 offset:1024\n"
"v_mul_f32 v252, s67, v252\n"
"v_mfma_f32_16x16x32_bf16 v[176:179], v[228:231], a[104:107], v[176:179]\n"
"v_mul_f32 v253, s67, v253\n"
"v_mul_f32 v254, s67, v254\n"
"v_mfma_f32_16x16x32_bf16 v[180:183], v[228:231], a[108:111], v[180:183]\n"
"v_mul_f32 v255, s67, v255\n"
"v_cvt_pk_bf16_f32 v108, v252, v253\n"
"v_mfma_f32_16x16x32_bf16 v[180:183], v[224:227], a[100:103], v[180:183]\n"
"s_waitcnt lgkmcnt(3)\n"
"v_cvt_pk_bf16_f32 v109, v254, v255\n"
"v_accvgpr_read_b32 v252, a188\n"
"v_mfma_f32_16x16x32_bf16 v[188:191], v[232:235], a[100:103], v[188:191]\n"
"ds_read_b128 v[220:223], v244 offset:1536\n"
"v_accvgpr_read_b32 v253, a189\n"
"v_mfma_f32_16x16x32_bf16 v[188:191], v[236:239], a[108:111], v[188:191]\n"
"v_accvgpr_read_b32 v254, a190\n"
"v_accvgpr_read_b32 v255, a191\n"
"v_mfma_f32_16x16x32_bf16 v[184:187], v[236:239], a[104:107], v[184:187]\n"
"s_barrier\n"
"v_mfma_f32_16x16x32_bf16 v[184:187], v[232:235], a[96:99], v[184:187]\n"
"s_waitcnt lgkmcnt(2)\n"
"v_mul_f32 v252, s67, v252\n"
"v_mul_f32 v253, s67, v253\n"
"v_mul_f32 v254, s67, v254\n"
"v_mul_f32 v255, s67, v255\n"
"v_cvt_pk_bf16_f32 v110, v252, v253\n"
"v_cvt_pk_bf16_f32 v111, v254, v255\n"
"v_mul_f32 v128, s67, v128\n"
"v_permlane16_swap_b32 v108, v110\n"
"v_permlane16_swap_b32 v109, v111\n"
"v_mul_f32 v129, s67, v129\n"
"v_mul_f32 v130, s67, v130\n"
"v_mul_f32 v131, s67, v131\n"
"v_cvt_pk_bf16_f32 v80, v128, v129\n"
"v_cvt_pk_bf16_f32 v81, v130, v131\n"
"v_mul_f32 v136, s67, v136\n"
"v_mul_f32 v137, s67, v137\n"
"v_mul_f32 v138, s67, v138\n"
"v_mul_f32 v139, s67, v139\n"
"v_cvt_pk_bf16_f32 v82, v136, v137\n"
"v_cvt_pk_bf16_f32 v83, v138, v139\n"
"v_mul_f32 v132, s67, v132\n"
"v_mul_f32 v133, s67, v133\n"
"v_mul_f32 v134, s67, v134\n"
"v_mul_f32 v135, s67, v135\n"
"v_cvt_pk_bf16_f32 v112, v132, v133\n"
"v_cvt_pk_bf16_f32 v113, v134, v135\n"
"v_mul_f32 v140, s67, v140\n"
"v_mul_f32 v141, s67, v141\n"
"v_mul_f32 v142, s67, v142\n"
"v_mul_f32 v143, s67, v143\n"
"v_cvt_pk_bf16_f32 v114, v140, v141\n"
"v_cvt_pk_bf16_f32 v115, v142, v143\n"
"v_permlane16_swap_b32 v80, v82\n"
"v_permlane16_swap_b32 v81, v83\n"
"v_mul_f32 v144, s67, v144\n"
"v_mul_f32 v145, s67, v145\n"
"v_mul_f32 v146, s67, v146\n"
"v_mul_f32 v147, s67, v147\n"
"v_cvt_pk_bf16_f32 v84, v144, v145\n"
"v_cvt_pk_bf16_f32 v85, v146, v147\n"
"v_mul_f32 v152, s67, v152\n"
"v_mul_f32 v153, s67, v153\n"
"v_mul_f32 v154, s67, v154\n"
"v_mul_f32 v155, s67, v155\n"
"v_cvt_pk_bf16_f32 v86, v152, v153\n"
"v_cvt_pk_bf16_f32 v87, v154, v155\n"
"v_permlane16_swap_b32 v112, v114\n"
"v_permlane16_swap_b32 v113, v115\n"
"v_mul_f32 v148, s67, v148\n"
"v_mul_f32 v149, s67, v149\n"
"v_mul_f32 v150, s67, v150\n"
"v_mul_f32 v151, s67, v151\n"
"v_cvt_pk_bf16_f32 v116, v148, v149\n"
"v_cvt_pk_bf16_f32 v117, v150, v151\n"
"v_mul_f32 v156, s67, v156\n"
"v_mul_f32 v157, s67, v157\n"
"v_mul_f32 v158, s67, v158\n"
"v_mul_f32 v159, s67, v159\n"
"v_cvt_pk_bf16_f32 v118, v156, v157\n"
"v_cvt_pk_bf16_f32 v119, v158, v159\n"
"v_permlane16_swap_b32 v84, v86\n"
"v_permlane16_swap_b32 v85, v87\n"
"v_mul_f32 v160, s67, v160\n"
"v_mul_f32 v161, s67, v161\n"
"v_mul_f32 v162, s67, v162\n"
"v_mul_f32 v163, s67, v163\n"
"v_cvt_pk_bf16_f32 v88, v160, v161\n"
"v_cvt_pk_bf16_f32 v89, v162, v163\n"
"v_mul_f32 v168, s67, v168\n"
"v_mul_f32 v169, s67, v169\n"
"v_mul_f32 v170, s67, v170\n"
"v_mul_f32 v171, s67, v171\n"
"v_cvt_pk_bf16_f32 v90, v168, v169\n"
"v_cvt_pk_bf16_f32 v91, v170, v171\n"
"v_permlane16_swap_b32 v116, v118\n"
"v_permlane16_swap_b32 v117, v119\n"
"v_mul_f32 v164, s67, v164\n"
"v_mul_f32 v165, s67, v165\n"
"v_mul_f32 v166, s67, v166\n"
"v_mul_f32 v167, s67, v167\n"
"v_cvt_pk_bf16_f32 v120, v164, v165\n"
"v_cvt_pk_bf16_f32 v121, v166, v167\n"
"v_mul_f32 v172, s67, v172\n"
"v_mul_f32 v173, s67, v173\n"
"v_mul_f32 v174, s67, v174\n"
"v_mul_f32 v175, s67, v175\n"
"v_cvt_pk_bf16_f32 v122, v172, v173\n"
"v_cvt_pk_bf16_f32 v123, v174, v175\n"
"v_permlane16_swap_b32 v88, v90\n"
"v_permlane16_swap_b32 v89, v91\n"
"v_mul_f32 v176, s67, v176\n"
"v_mul_f32 v177, s67, v177\n"
"v_mul_f32 v178, s67, v178\n"
"v_mul_f32 v179, s67, v179\n"
"v_cvt_pk_bf16_f32 v92, v176, v177\n"
"v_cvt_pk_bf16_f32 v93, v178, v179\n"
"v_mul_f32 v184, s67, v184\n"
"v_mul_f32 v185, s67, v185\n"
"v_mul_f32 v186, s67, v186\n"
"v_mul_f32 v187, s67, v187\n"
"v_cvt_pk_bf16_f32 v94, v184, v185\n"
"v_cvt_pk_bf16_f32 v95, v186, v187\n"
"v_permlane16_swap_b32 v120, v122\n"
"v_permlane16_swap_b32 v121, v123\n"
"v_mul_f32 v180, s67, v180\n"
"v_mul_f32 v181, s67, v181\n"
"v_mul_f32 v182, s67, v182\n"
"v_mul_f32 v183, s67, v183\n"
"v_cvt_pk_bf16_f32 v124, v180, v181\n"
"v_cvt_pk_bf16_f32 v125, v182, v183\n"
"v_mul_f32 v188, s67, v188\n"
"v_mul_f32 v189, s67, v189\n"
"v_mul_f32 v190, s67, v190\n"
"v_mul_f32 v191, s67, v191\n"
"v_cvt_pk_bf16_f32 v126, v188, v189\n"
"v_cvt_pk_bf16_f32 v127, v190, v191\n"
"v_permlane16_swap_b32 v92, v94\n"
"v_permlane16_swap_b32 v93, v95\n"
"v_permlane16_swap_b32 v124, v126\n"
"v_permlane16_swap_b32 v125, v127\n"
"A0_%=:\n"
"v_mfma_f32_16x16x32_bf16 v[128:131], v[208:211], v[0:3], 0\n"
"s_add_u32 s66, s73, 49152\n"
"s_and_b32 s66, s66, 524287\n"
"ds_read_b128 v[224:227], v244 offset:2048\n"
"v_mfma_f32_16x16x32_bf16 v[128:131], v[212:215], v[4:7], v[128:131]\n"
"s_lshr_b32 s61, %2, 3\n"
"s_add_u32 m0, s56, 131072\n"
"v_mfma_f32_16x16x32_bf16 v[136:139], v[212:215], v[36:39], 0\n"
"s_mul_i32 s61, s61, 12\n"
"v_mfma_f32_16x16x32_bf16 v[136:139], v[208:211], v[32:35], v[136:139]\n"
"s_lshr_b32 s61, s61, 5\n"
"v_mfma_f32_16x16x32_bf16 v[144:147], v[208:211], v[64:67], 0\n"
"s_add_u32 s61, s61, 2\n"
"ds_read_b128 v[228:231], v244 offset:2560\n"
"v_mfma_f32_16x16x32_bf16 v[144:147], v[212:215], v[68:71], v[144:147]\n"
"buffer_load_dwordx4 v248, s[12:15], s66 offen lds\n"
"v_mfma_f32_16x16x32_bf16 v[152:155], v[212:215], v[100:103], 0\n"
"s_add_u32 m0, s57, 131072\n"
"v_mfma_f32_16x16x32_bf16 v[152:155], v[208:211], v[96:99], v[152:155]\n"
"s_waitcnt lgkmcnt(2)\n"
"buffer_load_dwordx4 v248, s[12:15], s66 offen offset:128 lds\n"
"v_mfma_f32_16x16x32_bf16 v[152:155], v[216:219], v[104:107], v[152:155]\n"
"ds_read_b128 v[232:235], v244 offset:3072\n"
"v_mfma_f32_16x16x32_bf16 v[152:155], v[220:223], v[108:111], v[152:155]\n"
"s_add_u32 m0, s58, 131072\n"
"v_mfma_f32_16x16x32_bf16 v[144:147], v[220:223], v[76:79], v[144:147]\n"
"buffer_load_dwordx4 v248, s[12:15], s66 offen offset:256 lds\n"
"v_mfma_f32_16x16x32_bf16 v[144:147], v[216:219], v[72:75], v[144:147]\n"
"v_mfma_f32_16x16x32_bf16 v[136:139], v[216:219], v[40:43], v[136:139]\n"
"ds_read_b128 v[236:239], v244 offset:3584\n"
"v_mfma_f32_16x16x32_bf16 v[136:139], v[220:223], v[44:47], v[136:139]\n"
"s_add_u32 m0, s59, 131072\n"
"v_mfma_f32_16x16x32_bf16 v[128:131], v[220:223], v[12:15], v[128:131]\n"
"buffer_load_dwordx4 v248, s[12:15], s66 offen offset:384 lds\n"
"v_mfma_f32_16x16x32_bf16 v[128:131], v[216:219], v[8:11], v[128:131]\n"
"s_waitcnt lgkmcnt(2)\n"
"v_mfma_f32_16x16x32_bf16 v[128:131], v[224:227], v[16:19], v[128:131]\n"
"ds_read_b128 v[208:211], v244 offset:8192\n"
"v_mfma_f32_16x16x32_bf16 v[128:131], v[228:231], v[20:23], v[128:131]\n"
"v_mfma_f32_16x16x32_bf16 v[136:139], v[228:231], v[52:55], v[136:139]\n"
"v_mfma_f32_16x16x32_bf16 v[136:139], v[224:227], v[48:51], v[136:139]\n"
"v_mfma_f32_16x16x32_bf16 v[144:147], v[224:227], v[80:83], v[144:147]\n"
"s_add_u32 s64, s63, 32768\n"
"ds_read_b128 v[212:215], v244 offset:8704\n"
"v_mfma_f32_16x16x32_bf16 v[144:147], v[228:231], v[84:87], v[144:147]\n"
"s_cmp_eq_u32 s64, 163840\n"
"v_mfma_f32_16x16x32_bf16 v[152:155], v[228:231], v[116:119], v[152:155]\n"
"s_cselect_b32 s64, 0, s64\n"
"v_mfma_f32_16x16x32_bf16 v[152:155], v[224:227], v[112:115], v[152:155]\n"
"s_waitcnt lgkmcnt(2)\n"
"v_mfma_f32_16x16x32_bf16 v[152:155], v[232:235], v[120:123], v[152:155]\n"
"ds_read_b128 v[216:219], v244 offset:9216\n"
"v_mfma_f32_16x16x32_bf16 v[152:155], v[236:239], v[124:127], v[152:155]\n"
"v_mfma_f32_16x16x32_bf16 v[144:147], v[236:239], v[92:95], v[144:147]\n"
"v_mfma_f32_16x16x32_bf16 v[144:147], v[232:235], v[88:91], v[144:147]\n"
"v_mfma_f32_16x16x32_bf16 v[136:139], v[232:235], v[56:59], v[136:139]\n"
"ds_read_b128 v[220:223], v244 offset:9728\n"
"v_mfma_f32_16x16x32_bf16 v[136:139], v[236:239], v[60:63], v[136:139]\n"
"v_mfma_f32_16x16x32_bf16 v[128:131], v[236:239], v[28:31], v[128:131]\n"
"v_mfma_f32_16x16x32_bf16 v[128:131], v[232:235], v[24:27], v[128:131]\n"
"s_waitcnt lgkmcnt(2)\n"
"v_mfma_f32_16x16x32_bf16 v[132:135], v[208:211], v[0:3], 0\n"
"ds_read_b128 v[224:227], v244 offset:10240\n"
"v_mfma_f32_16x16x32_bf16 v[132:135], v[212:215], v[4:7], v[132:135]\n"
"v_mfma_f32_16x16x32_bf16 v[140:143], v[212:215], v[36:39], 0\n"
"s_nop 2\n"
"v_max3_f32 v240, v128, v129, v130\n"
"v_max3_f32 v241, v136, v137, v138\n"
"v_mfma_f32_16x16x32_bf16 v[140:143], v[208:211], v[32:35], v[140:143]\n"
"v_max3_f32 v242, v144, v145, v146\n"
"v_max3_f32 v243, v152, v153, v154\n"
"v_mfma_f32_16x16x32_bf16 v[148:151], v[208:211], v[64:67], 0\n"
"ds_read_b128 v[228:231], v244 offset:10752\n"
"v_max_f32 v240, v240, v131\n"
"v_mfma_f32_16x16x32_bf16 v[148:151], v[212:215], v[68:71], v[148:151]\n"
"v_max_f32 v241, v241, v139\n"
"v_max_f32 v242, v242, v147\n"
"v_mfma_f32_16x16x32_bf16 v[156:159], v[212:215], v[100:103], 0\n"
"v_max_f32 v243, v243, v155\n"
"v_mov_b32 v252, v240\n"
"v_mfma_f32_16x16x32_bf16 v[156:159], v[208:211], v[96:99], v[156:159]\n"
"s_waitcnt lgkmcnt(2)\n"
"v_permlane16_swap_b32 v252, v240\n"
"v_mfma_f32_16x16x32_bf16 v[156:159], v[216:219], v[104:107], v[156:159]\n"
"ds_read_b128 v[232:235], v244 offset:11264\n"
"v_mov_b32 v253, v241\n"
"v_mfma_f32_16x16x32_bf16 v[156:159], v[220:223], v[108:111], v[156:159]\n"
"s_nop 0\n"
"v_permlane16_swap_b32 v253, v241\n"
"v_mfma_f32_16x16x32_bf16 v[148:151], v[220:223], v[76:79], v[148:151]\n"
"v_mov_b32 v254, v242\n"
"v_mov_b32 v255, v243\n"
"v_mfma_f32_16x16x32_bf16 v[148:151], v[216:219], v[72:75], v[148:151]\n"
"v_permlane16_swap_b32 v254, v242\n"
"v_mfma_f32_16x16x32_bf16 v[140:143], v[216:219], v[40:43], v[140:143]\n"
"ds_read_b128 v[236:239], v244 offset:11776\n"
"v_add_u32 v244, s64, v250\n"
"v_mfma_f32_16x16x32_bf16 v[140:143], v[220:223], v[44:47], v[140:143]\n"
"v_permlane16_swap_b32 v255, v243\n"
"v_mfma_f32_16x16x32_bf16 v[132:135], v[220:223], v[12:15], v[132:135]\n"
"v_max_f32 v240, v240, v252\n"
"v_max_f32 v241, v241, v253\n"
"v_mfma_f32_16x16x32_bf16 v[132:135], v[216:219], v[8:11], v[132:135]\n"
"s_waitcnt lgkmcnt(2)\n"
"v_max_f32 v242, v242, v254\n"
"v_max_f32 v243, v243, v255\n"
"v_mfma_f32_16x16x32_bf16 v[132:135], v[224:227], v[16:19], v[132:135]\n"
"ds_read_b128 v[208:211], v244 offset:0\n"
"v_mov_b32 v252, v240\n"
"v_mfma_f32_16x16x32_bf16 v[132:135], v[228:231], v[20:23], v[132:135]\n"
"s_nop 0\n"
"v_permlane32_swap_b32 v252, v240\n"
"v_mfma_f32_16x16x32_bf16 v[140:143], v[228:231], v[52:55], v[140:143]\n"
"v_mov_b32 v253, v241\n"
"v_mov_b32 v254, v242\n"
"v_mfma_f32_16x16x32_bf16 v[140:143], v[224:227], v[48:51], v[140:143]\n"
"v_permlane32_swap_b32 v253, v241\n"
"v_mfma_f32_16x16x32_bf16 v[148:151], v[224:227], v[80:83], v[148:151]\n"
"ds_read_b128 v[212:215], v244 offset:512\n"
"v_mov_b32 v255, v243\n"
"v_mfma_f32_16x16x32_bf16 v[148:151], v[228:231], v[84:87], v[148:151]\n"
"v_permlane32_swap_b32 v254, v242\n"
"v_mfma_f32_16x16x32_bf16 v[156:159], v[228:231], v[116:119], v[156:159]\n"
"v_permlane32_swap_b32 v255, v243\n"
"v_mfma_f32_16x16x32_bf16 v[156:159], v[224:227], v[112:115], v[156:159]\n"
"s_waitcnt lgkmcnt(2)\n"
"v_max_f32 v240, v240, v252\n"
"v_max_f32 v241, v241, v253\n"
"v_mfma_f32_16x16x32_bf16 v[156:159], v[232:235], v[120:123], v[156:159]\n"
"ds_read_b128 v[216:219], v244 offset:1024\n"
"v_max_f32 v242, v242, v254\n"
"v_mfma_f32_16x16x32_bf16 v[156:159], v[236:239], v[124:127], v[156:159]\n"
"v_max_f32 v243, v243, v255\n"
"v_cndmask_b32 v192, -v240, v192, s[68:69]\n"
"v_mfma_f32_16x16x32_bf16 v[148:151], v[236:239], v[92:95], v[148:151]\n"
"v_cndmask_b32 v193, -v240, v193, s[68:69]\n"
"v_cndmask_b32 v194, -v240, v194, s[68:69]\n"
"v_mfma_f32_16x16x32_bf16 v[148:151], v[232:235], v[88:91], v[148:151]\n"
"v_cndmask_b32 v195, -v240, v195, s[68:69]\n"
"v_cndmask_b32 v196, -v241, v196, s[68:69]\n"
"v_mfma_f32_16x16x32_bf16 v[140:143], v[232:235], v[56:59], v[140:143]\n"
"ds_read_b128 v[220:223], v244 offset:1536\n"
"v_cndmask_b32 v197, -v241, v197, s[68:69]\n"
"v_mfma_f32_16x16x32_bf16 v[140:143], v[236:239], v[60:63], v[140:143]\n"
"v_cndmask_b32 v198, -v241, v198, s[68:69]\n"
"v_cndmask_b32 v199, -v241, v199, s[68:69]\n"
"v_mfma_f32_16x16x32_bf16 v[132:135], v[236:239], v[28:31], v[132:135]\n"
"s_barrier\n"
"v_mfma_f32_16x16x32_bf16 v[132:135], v[232:235], v[24:27], v[132:135]\n"
"s_waitcnt lgkmcnt(2)\n"
"v_cndmask_b32 v200, -v242, v200, s[68:69]\n"
"v_cndmask_b32 v201, -v242, v201, s[68:69]\n"
"v_cndmask_b32 v202, -v242, v202, s[68:69]\n"
"v_cndmask_b32 v203, -v242, v203, s[68:69]\n"
"v_cndmask_b32 v204, -v243, v204, s[68:69]\n"
"v_cndmask_b32 v205, -v243, v205, s[68:69]\n"
"v_cndmask_b32 v206, -v243, v206, s[68:69]\n"
"v_cndmask_b32 v207, -v243, v207, s[68:69]\n"
"v_mov_b32 v240, 0\n"
"v_mov_b32 v241, 0\n"
"v_mov_b32 v242, 0\n"
"v_mov_b32 v243, 0\n"
"v_mfma_f32_16x16x32_bf16 v[160:163], v[208:211], v[0:3], v[192:195]\n"
"IWC1_%=:\n"
"s_cbranch_execz IRC_%=\n"
"s_add_u32 m0, s45, s52\n"
"ds_read_b128 v[224:227], v244 offset:2048\n"
"v_add_f32 v152, v204, v152\n"
"v_mfma_f32_16x16x32_bf16 v[160:163], v[212:215], v[4:7], v[160:163]\n"
"buffer_load_dwordx4 v247, s[8:11], s62 offen nt lds\n"
"v_mfma_f32_16x16x32_bf16 v[168:171], v[212:215], v[36:39], v[196:199]\n"
"v_add_f32 v153, v204, v153\n"
"v_add_f32 v154, v204, v154\n"
"v_mfma_f32_16x16x32_bf16 v[168:171], v[208:211], v[32:35], v[168:171]\n"
"v_add_f32 v155, v204, v155\n"
"v_add_f32 v144, v200, v144\n"
"v_mfma_f32_16x16x32_bf16 v[176:179], v[208:211], v[64:67], v[200:203]\n"
"ds_read_b128 v[228:231], v244 offset:2560\n"
"v_add_f32 v145, v200, v145\n"
"v_mfma_f32_16x16x32_bf16 v[176:179], v[212:215], v[68:71], v[176:179]\n"
"v_add_f32 v146, v200, v146\n"
"v_add_f32 v147, v200, v147\n"
"v_mfma_f32_16x16x32_bf16 v[184:187], v[212:215], v[100:103], v[204:207]\n"
"v_add_f32 v136, v196, v136\n"
"v_add_f32 v137, v196, v137\n"
"v_mfma_f32_16x16x32_bf16 v[184:187], v[208:211], v[96:99], v[184:187]\n"
"s_waitcnt lgkmcnt(2)\n"
"v_add_f32 v138, v196, v138\n"
"v_add_f32 v139, v196, v139\n"
"v_mfma_f32_16x16x32_bf16 v[184:187], v[216:219], v[104:107], v[184:187]\n"
"ds_read_b128 v[232:235], v244 offset:3072\n"
"v_add_u32 v245, s64, v251\n"
"v_mfma_f32_16x16x32_bf16 v[184:187], v[220:223], v[108:111], v[184:187]\n"
"s_add_u32 m0, s45, s53\n"
"v_xor_b32 v246, 32, v245\n"
"v_add_f32 v128, v192, v128\n"
"v_mfma_f32_16x16x32_bf16 v[176:179], v[220:223], v[76:79], v[176:179]\n"
"buffer_load_dwordx4 v247, s[8:11], s62 offen offset:128 nt lds\n"
"v_mfma_f32_16x16x32_bf16 v[176:179], v[216:219], v[72:75], v[176:179]\n"
"v_add_f32 v129, v192, v129\n"
"v_add_f32 v130, v192, v130\n"
"v_mfma_f32_16x16x32_bf16 v[168:171], v[216:219], v[40:43], v[168:171]\n"
"ds_read_b128 v[236:239], v244 offset:3584\n"
"v_add_f32 v131, v192, v131\n"
"v_mfma_f32_16x16x32_bf16 v[168:171], v[220:223], v[44:47], v[168:171]\n"
"v_add_f32 v156, v204, v156\n"
"v_add_f32 v157, v204, v157\n"
"v_mfma_f32_16x16x32_bf16 v[160:163], v[220:223], v[12:15], v[160:163]\n"
"v_add_f32 v158, v204, v158\n"
"v_add_f32 v159, v204, v159\n"
"v_mfma_f32_16x16x32_bf16 v[160:163], v[216:219], v[8:11], v[160:163]\n"
"s_waitcnt lgkmcnt(2)\n"
"v_add_f32 v148, v200, v148\n"
"v_add_f32 v149, v200, v149\n"
"v_mfma_f32_16x16x32_bf16 v[160:163], v[224:227], v[16:19], v[160:163]\n"
"ds_read_b128 v[208:211], v244 offset:8192\n"
"v_add_f32 v150, v200, v150\n"
"v_mfma_f32_16x16x32_bf16 v[160:163], v[228:231], v[20:23], v[160:163]\n"
"v_add_f32 v151, v200, v151\n"
"v_add_f32 v140, v196, v140\n"
"v_mfma_f32_16x16x32_bf16 v[168:171], v[228:231], v[52:55], v[168:171]\n"
"s_add_u32 m0, s45, s54\n"
"v_add_f32 v141, v196, v141\n"
"v_add_f32 v142, v196, v142\n"
"v_mfma_f32_16x16x32_bf16 v[168:171], v[224:227], v[48:51], v[168:171]\n"
"buffer_load_dwordx4 v247, s[8:11], s62 offen offset:256 nt lds\n"
"v_mfma_f32_16x16x32_bf16 v[176:179], v[224:227], v[80:83], v[176:179]\n"
"ds_read_b128 v[212:215], v244 offset:8704\n"
"v_add_f32 v143, v196, v143\n"
"v_mfma_f32_16x16x32_bf16 v[176:179], v[228:231], v[84:87], v[176:179]\n"
"v_exp_f32 v152, v152\n"
"v_mfma_f32_16x16x32_bf16 v[184:187], v[228:231], v[116:119], v[184:187]\n"
"v_exp_f32 v153, v153\n"
"v_mfma_f32_16x16x32_bf16 v[184:187], v[224:227], v[112:115], v[184:187]\n"
"s_waitcnt lgkmcnt(2)\n"
"v_exp_f32 v154, v154\n"
"v_mfma_f32_16x16x32_bf16 v[184:187], v[232:235], v[120:123], v[184:187]\n"
"ds_read_b128 v[216:219], v244 offset:9216\n"
"v_add_f32 v243, v152, v243\n"
"v_mfma_f32_16x16x32_bf16 v[184:187], v[236:239], v[124:127], v[184:187]\n"
"v_add_f32 v243, v153, v243\n"
"v_add_f32 v243, v154, v243\n"
"v_mfma_f32_16x16x32_bf16 v[176:179], v[236:239], v[92:95], v[176:179]\n"
"v_exp_f32 v155, v155\n"
"v_mfma_f32_16x16x32_bf16 v[176:179], v[232:235], v[88:91], v[176:179]\n"
"s_add_u32 m0, s45, s55\n"
"v_add_f32 v243, v155, v243\n"
"v_cvt_pk_bf16_f32 v152, v152, v153\n"
"v_mfma_f32_16x16x32_bf16 v[168:171], v[232:235], v[56:59], v[168:171]\n"
"buffer_load_dwordx4 v247, s[8:11], s62 offen offset:384 nt lds\n"
"v_mfma_f32_16x16x32_bf16 v[168:171], v[236:239], v[60:63], v[168:171]\n"
"ds_read_b128 v[220:223], v244 offset:9728\n"
"v_cvt_pk_bf16_f32 v153, v154, v155\n"
"v_mfma_f32_16x16x32_bf16 v[160:163], v[236:239], v[28:31], v[160:163]\n"
"v_exp_f32 v144, v144\n"
"v_mfma_f32_16x16x32_bf16 v[160:163], v[232:235], v[24:27], v[160:163]\n"
"s_waitcnt lgkmcnt(2)\n"
"v_exp_f32 v145, v145\n"
"v_mfma_f32_16x16x32_bf16 v[164:167], v[208:211], v[0:3], v[192:195]\n"
"ds_read_b128 v[224:227], v244 offset:10240\n"
"v_add_f32 v242, v144, v242\n"
"v_mfma_f32_16x16x32_bf16 v[164:167], v[212:215], v[4:7], v[164:167]\n"
"v_add_f32 v242, v145, v242\n"
"v_cvt_pk_bf16_f32 v144, v144, v145\n"
"v_mfma_f32_16x16x32_bf16 v[172:175], v[212:215], v[36:39], v[196:199]\n"
"v_exp_f32 v146, v146\n"
"v_mfma_f32_16x16x32_bf16 v[172:175], v[208:211], v[32:35], v[172:175]\n"
"v_exp_f32 v147, v147\n"
"v_mfma_f32_16x16x32_bf16 v[180:183], v[208:211], v[64:67], v[200:203]\n"
"s_add_u32 m0, s45, s56\n"
"ds_read_b128 v[228:231], v244 offset:10752\n"
"v_add_f32 v242, v146, v242\n"
"v_mfma_f32_16x16x32_bf16 v[180:183], v[212:215], v[68:71], v[180:183]\n"
"buffer_load_dwordx4 v248, s[12:15], s34 offen lds\n"
"v_mfma_f32_16x16x32_bf16 v[188:191], v[212:215], v[100:103], v[204:207]\n"
"v_add_f32 v242, v147, v242\n"
"v_cvt_pk_bf16_f32 v145, v146, v147\n"
"v_mfma_f32_16x16x32_bf16 v[188:191], v[208:211], v[96:99], v[188:191]\n"
"s_waitcnt lgkmcnt(2)\n"
"v_exp_f32 v136, v136\n"
"v_mfma_f32_16x16x32_bf16 v[188:191], v[216:219], v[104:107], v[188:191]\n"
"ds_read_b128 v[232:235], v244 offset:11264\n"
"v_add_f32 v241, v136, v241\n"
"v_mfma_f32_16x16x32_bf16 v[188:191], v[220:223], v[108:111], v[188:191]\n"
"v_exp_f32 v137, v137\n"
"v_mfma_f32_16x16x32_bf16 v[180:183], v[220:223], v[76:79], v[180:183]\n"
"v_add_f32 v241, v137, v241\n"
"v_cvt_pk_bf16_f32 v136, v136, v137\n"
"v_mfma_f32_16x16x32_bf16 v[180:183], v[216:219], v[72:75], v[180:183]\n"
"v_exp_f32 v138, v138\n"
"v_mfma_f32_16x16x32_bf16 v[172:175], v[216:219], v[40:43], v[172:175]\n"
"ds_read_b128 v[236:239], v244 offset:11776\n"
"v_add_f32 v241, v138, v241\n"
"v_mfma_f32_16x16x32_bf16 v[172:175], v[220:223], v[44:47], v[172:175]\n"
"s_add_u32 m0, s45, s57\n"
"v_exp_f32 v139, v139\n"
"v_mfma_f32_16x16x32_bf16 v[164:167], v[220:223], v[12:15], v[164:167]\n"
"buffer_load_dwordx4 v248, s[12:15], s34 offen offset:128 lds\n"
"v_mfma_f32_16x16x32_bf16 v[164:167], v[216:219], v[8:11], v[164:167]\n"
"s_waitcnt lgkmcnt(2)\n"
"v_add_f32 v241, v139, v241\n"
"v_cvt_pk_bf16_f32 v137, v138, v139\n"
"v_mfma_f32_16x16x32_bf16 v[164:167], v[224:227], v[16:19], v[164:167]\n"
"ds_read_b64_tr_b16 v[208:209], v245 offset:16384\n"
"v_add_f32 v132, v192, v132\n"
"v_mfma_f32_16x16x32_bf16 v[164:167], v[228:231], v[20:23], v[164:167]\n"
"ds_read_b64_tr_b16 v[210:211], v245 offset:24576\n"
"v_add_f32 v133, v192, v133\n"
"v_mfma_f32_16x16x32_bf16 v[172:175], v[228:231], v[52:55], v[172:175]\n"
"v_exp_f32 v128, v128\n"
"v_mfma_f32_16x16x32_bf16 v[172:175], v[224:227], v[48:51], v[172:175]\n"
"v_exp_f32 v129, v129\n"
"v_mfma_f32_16x16x32_bf16 v[180:183], v[224:227], v[80:83], v[180:183]\n"
"ds_read_b64_tr_b16 v[212:213], v246 offset:16384\n"
"v_add_f32 v240, v128, v240\n"
"v_mfma_f32_16x16x32_bf16 v[180:183], v[228:231], v[84:87], v[180:183]\n"
"ds_read_b64_tr_b16 v[214:215], v246 offset:24576\n"
"v_add_f32 v240, v129, v240\n"
"v_mfma_f32_16x16x32_bf16 v[188:191], v[228:231], v[116:119], v[188:191]\n"
"s_add_u32 m0, s45, s58\n"
"v_exp_f32 v130, v130\n"
"v_mfma_f32_16x16x32_bf16 v[188:191], v[224:227], v[112:115], v[188:191]\n"
"s_waitcnt lgkmcnt(4)\n"
"buffer_load_dwordx4 v248, s[12:15], s34 offen offset:256 lds\n"
"v_mfma_f32_16x16x32_bf16 v[188:191], v[232:235], v[120:123], v[188:191]\n"
"ds_read_b64_tr_b16 v[216:217], v245 offset:16896\n"
"v_add_f32 v240, v130, v240\n"
"v_mfma_f32_16x16x32_bf16 v[188:191], v[236:239], v[124:127], v[188:191]\n"
"ds_read_b64_tr_b16 v[218:219], v245 offset:25088\n"
"v_cvt_pk_bf16_f32 v128, v128, v129\n"
"v_mfma_f32_16x16x32_bf16 v[180:183], v[236:239], v[92:95], v[180:183]\n"
"v_exp_f32 v131, v131\n"
"v_mfma_f32_16x16x32_bf16 v[180:183], v[232:235], v[88:91], v[180:183]\n"
"v_exp_f32 v156, v156\n"
"v_mfma_f32_16x16x32_bf16 v[172:175], v[232:235], v[56:59], v[172:175]\n"
"ds_read_b64_tr_b16 v[220:221], v246 offset:16896\n"
"v_add_f32 v240, v131, v240\n"
"v_mfma_f32_16x16x32_bf16 v[172:175], v[236:239], v[60:63], v[172:175]\n"
"ds_read_b64_tr_b16 v[222:223], v246 offset:25088\n"
"v_cvt_pk_bf16_f32 v129, v130, v131\n"
"v_mfma_f32_16x16x32_bf16 v[164:167], v[236:239], v[28:31], v[164:167]\n"
"v_exp_f32 v157, v157\n"
"v_exp_f32 v158, v158\n"
"v_exp_f32 v159, v159\n"
"v_add_f32 v243, v156, v243\n"
"v_add_f32 v243, v157, v243\n"
"v_add_f32 v243, v158, v243\n"
"v_add_f32 v243, v159, v243\n"
"v_cvt_pk_bf16_f32 v154, v156, v157\n"
"v_cvt_pk_bf16_f32 v155, v158, v159\n"
"v_exp_f32 v148, v148\n"
"v_exp_f32 v149, v149\n"
"v_exp_f32 v150, v150\n"
"v_exp_f32 v151, v151\n"
"v_add_f32 v242, v148, v242\n"
"v_add_f32 v242, v149, v242\n"
"v_add_f32 v242, v150, v242\n"
"v_add_f32 v242, v151, v242\n"
"v_cvt_pk_bf16_f32 v146, v148, v149\n"
"v_cvt_pk_bf16_f32 v147, v150, v151\n"
"v_exp_f32 v140, v140\n"
"v_exp_f32 v141, v141\n"
"v_exp_f32 v142, v142\n"
"v_exp_f32 v143, v143\n"
"v_add_f32 v241, v140, v241\n"
"v_add_f32 v241, v141, v241\n"
"v_add_f32 v241, v142, v241\n"
"v_add_f32 v241, v143, v241\n"
"v_cvt_pk_bf16_f32 v138, v140, v141\n"
"v_cvt_pk_bf16_f32 v139, v142, v143\n"
"v_add_f32 v134, v192, v134\n"
"v_add_f32 v135, v192, v135\n"
"v_exp_f32 v132, v132\n"
"v_exp_f32 v133, v133\n"
"v_exp_f32 v134, v134\n"
"v_exp_f32 v135, v135\n"
"v_add_f32 v240, v132, v240\n"
"v_add_f32 v240, v133, v240\n"
"v_add_f32 v240, v134, v240\n"
"v_add_f32 v240, v135, v240\n"
"v_cvt_pk_bf16_f32 v130, v132, v133\n"
"v_cvt_pk_bf16_f32 v131, v134, v135\n"
"v_mfma_f32_16x16x32_bf16 v[164:167], v[232:235], v[24:27], v[164:167]\n"
"s_waitcnt lgkmcnt(4)\n"
"v_mfma_f32_16x16x32_bf16 a[12:15], v[208:211], v[152:155], 0\n"
"s_add_u32 m0, s45, s59\n"
"ds_read_b64_tr_b16 v[224:225], v245 offset:17408\n"
"v_mfma_f32_16x16x32_bf16 a[28:31], v[212:215], v[152:155], 0\n"
"buffer_load_dwordx4 v248, s[12:15], s34 offen offset:384 lds\n"
"v_mfma_f32_16x16x32_bf16 a[24:27], v[212:215], v[144:147], 0\n"
"ds_read_b64_tr_b16 v[226:227], v245 offset:25600\n"
"v_mfma_f32_16x16x32_bf16 a[8:11], v[208:211], v[144:147], 0\n"
"v_exp_f32 v184, v184\n"
"v_mfma_f32_16x16x32_bf16 a[4:7], v[208:211], v[136:139], 0\n"
"ds_read_b64_tr_b16 v[228:229], v246 offset:17408\n"
"v_add_f32 v243, v184, v243\n"
"v_mfma_f32_16x16x32_bf16 a[20:23], v[212:215], v[136:139], 0\n"
"ds_read_b64_tr_b16 v[230:231], v246 offset:25600\n"
"v_mfma_f32_16x16x32_bf16 a[16:19], v[212:215], v[128:131], 0\n"
"v_exp_f32 v185, v185\n"
"v_mfma_f32_16x16x32_bf16 a[0:3], v[208:211], v[128:131], 0\n"
"s_waitcnt lgkmcnt(4)\n"
"v_exp_f32 v186, v186\n"
"v_mfma_f32_16x16x32_bf16 a[32:35], v[216:219], v[128:131], 0\n"
"buffer_load_dword v255, v247, s[76:79], s72 offen offset:0\n"
"v_mfma_f32_16x16x32_bf16 a[48:51], v[220:223], v[128:131], 0\n"
"ds_read_b64_tr_b16 v[232:233], v245 offset:17920\n"
"v_add_f32 v243, v185, v243\n"
"v_mfma_f32_16x16x32_bf16 a[52:55], v[220:223], v[136:139], 0\n"
"ds_read_b64_tr_b16 v[234:235], v245 offset:26112\n"
"v_add_f32 v243, v186, v243\n"
"v_mfma_f32_16x16x32_bf16 a[36:39], v[216:219], v[136:139], 0\n"
"v_exp_f32 v187, v187\n"
"v_mfma_f32_16x16x32_bf16 a[40:43], v[216:219], v[144:147], 0\n"
"ds_read_b64_tr_b16 v[236:237], v246 offset:17920\n"
"v_add_f32 v243, v187, v243\n"
"v_mfma_f32_16x16x32_bf16 a[56:59], v[220:223], v[144:147], 0\n"
"ds_read_b64_tr_b16 v[238:239], v246 offset:26112\n"
"v_cvt_pk_bf16_f32 v184, v184, v185\n"
"v_mfma_f32_16x16x32_bf16 a[60:63], v[220:223], v[152:155], 0\n"
"v_exp_f32 v176, v176\n"
"v_mfma_f32_16x16x32_bf16 a[44:47], v[216:219], v[152:155], 0\n"
"s_waitcnt lgkmcnt(4)\n"
"v_exp_f32 v177, v177\n"
"v_mfma_f32_16x16x32_bf16 a[76:79], v[224:227], v[152:155], 0\n"
"ds_read_b64_tr_b16 v[208:209], v245 offset:18432\n"
"v_cvt_pk_bf16_f32 v185, v186, v187\n"
"v_mfma_f32_16x16x32_bf16 a[92:95], v[228:231], v[152:155], 0\n"
"ds_read_b64_tr_b16 v[210:211], v245 offset:26624\n"
"v_add_f32 v242, v176, v242\n"
"v_mfma_f32_16x16x32_bf16 a[88:91], v[228:231], v[144:147], 0\n"
"v_exp_f32 v178, v178\n"
"v_mfma_f32_16x16x32_bf16 a[72:75], v[224:227], v[144:147], 0\n"
"v_exp_f32 v179, v179\n"
"v_mfma_f32_16x16x32_bf16 a[68:71], v[224:227], v[136:139], 0\n"
"s_add_u32 s63, s64, 32768\n"
"ds_read_b64_tr_b16 v[212:213], v246 offset:18432\n"
"v_add_f32 v242, v177, v242\n"
"v_mfma_f32_16x16x32_bf16 a[84:87], v[228:231], v[136:139], 0\n"
"s_cmp_eq_u32 s63, 163840\n"
"ds_read_b64_tr_b16 v[214:215], v246 offset:26624\n"
"v_add_f32 v242, v178, v242\n"
"v_mfma_f32_16x16x32_bf16 a[80:83], v[228:231], v[128:131], 0\n"
"s_cselect_b32 s63, 0, s63\n"
"v_add_f32 v242, v179, v242\n"
"v_cvt_pk_bf16_f32 v176, v176, v177\n"
"v_mfma_f32_16x16x32_bf16 a[64:67], v[224:227], v[128:131], 0\n"
"s_waitcnt lgkmcnt(4)\n"
"v_add_u32 v244, s63, v250\n"
"v_cvt_pk_bf16_f32 v177, v178, v179\n"
"v_mfma_f32_16x16x32_bf16 a[96:99], v[232:235], v[128:131], 0\n"
"s_mov_b32 s34, s62\n"
"ds_read_b64_tr_b16 v[216:217], v245 offset:18944\n"
"v_mfma_f32_16x16x32_bf16 a[112:115], v[236:239], v[128:131], 0\n"
"s_add_u32 s62, s62, 16384\n"
"ds_read_b64_tr_b16 v[218:219], v245 offset:27136\n"
"v_mfma_f32_16x16x32_bf16 a[116:119], v[236:239], v[136:139], 0\n"
"s_and_b32 s62, s62, 524287\n"
"v_mfma_f32_16x16x32_bf16 a[100:103], v[232:235], v[136:139], 0\n"
"s_add_u32 s45, s45, 32768\n"
"v_mfma_f32_16x16x32_bf16 a[104:107], v[232:235], v[144:147], 0\n"
"s_cmp_eq_u32 s45, 163840\n"
"ds_read_b64_tr_b16 v[220:221], v246 offset:18944\n"
"v_mfma_f32_16x16x32_bf16 a[120:123], v[236:239], v[144:147], 0\n"
"s_cselect_b32 s45, 0, s45\n"
"ds_read_b64_tr_b16 v[222:223], v246 offset:27136\n"
"v_mfma_f32_16x16x32_bf16 a[124:127], v[236:239], v[152:155], 0\n"
"s_sub_u32 s61, s61, 1\n"
"v_exp_f32 v168, v168\n"
"v_mfma_f32_16x16x32_bf16 a[108:111], v[232:235], v[152:155], 0\n"
"s_waitcnt lgkmcnt(4)\n"
"v_exp_f32 v169, v169\n"
"v_mfma_f32_16x16x32_bf16 a[140:143], v[208:211], v[152:155], 0\n"
"s_cmp_eq_u32 s61, 0\n"
"ds_read_b64_tr_b16 v[224:225], v245 offset:19456\n"
"v_add_f32 v241, v168, v241\n"
"v_mfma_f32_16x16x32_bf16 a[156:159], v[212:215], v[152:155], 0\n"
"s_cselect_b32 s47, 1, 0\n"
"ds_read_b64_tr_b16 v[226:227], v245 offset:27648\n"
"v_add_f32 v241, v169, v241\n"
"v_mfma_f32_16x16x32_bf16 a[152:155], v[212:215], v[144:147], 0\n"
"s_cselect_b32 s61, 12, s61\n"
"v_exp_f32 v170, v170\n"
"v_mfma_f32_16x16x32_bf16 a[136:139], v[208:211], v[144:147], 0\n"
"s_cmp_lt_u32 s44, 2\n"
"v_exp_f32 v171, v171\n"
"v_mfma_f32_16x16x32_bf16 a[132:135], v[208:211], v[136:139], 0\n"
"s_cselect_b32 s47, s47, 0\n"
"ds_read_b64_tr_b16 v[228:229], v246 offset:19456\n"
"v_add_f32 v241, v170, v241\n"
"v_mfma_f32_16x16x32_bf16 a[148:151], v[212:215], v[136:139], 0\n"
"s_add_u32 s71, s71, 1\n"
"ds_read_b64_tr_b16 v[230:231], v246 offset:27648\n"
"v_add_f32 v241, v171, v241\n"
"v_mfma_f32_16x16x32_bf16 a[144:147], v[212:215], v[128:131], 0\n"
"v_cvt_pk_bf16_f32 v168, v168, v169\n"
"v_cvt_pk_bf16_f32 v169, v170, v171\n"
"v_mfma_f32_16x16x32_bf16 a[128:131], v[208:211], v[128:131], 0\n"
"s_waitcnt lgkmcnt(4)\n"
"v_mfma_f32_16x16x32_bf16 a[160:163], v[216:219], v[128:131], 0\n"
"s_memrealtime s[16:17]\n"
"ds_read_b64_tr_b16 v[232:233], v245 offset:19968\n"
"v_mfma_f32_16x16x32_bf16 a[176:179], v[220:223], v[128:131], 0\n"
"ds_read_b64_tr_b16 v[234:235], v245 offset:28160\n"
"v_mfma_f32_16x16x32_bf16 a[180:183], v[220:223], v[136:139], 0\n"
"v_exp_f32 v160, v160\n"
"v_mfma_f32_16x16x32_bf16 a[164:167], v[216:219], v[136:139], 0\n"
"v_exp_f32 v161, v161\n"
"v_mfma_f32_16x16x32_bf16 a[168:171], v[216:219], v[144:147], 0\n"
"ds_read_b64_tr_b16 v[236:237], v246 offset:19968\n"
"v_add_f32 v240, v160, v240\n"
"v_mfma_f32_16x16x32_bf16 a[184:187], v[220:223], v[144:147], 0\n"
"ds_read_b64_tr_b16 v[238:239], v246 offset:28160\n"
"v_add_f32 v240, v161, v240\n"
"v_mfma_f32_16x16x32_bf16 a[188:191], v[220:223], v[152:155], 0\n"
"v_exp_f32 v162, v162\n"
"v_mfma_f32_16x16x32_bf16 a[172:175], v[216:219], v[152:155], 0\n"
"s_waitcnt lgkmcnt(4)\n"
"v_exp_f32 v163, v163\n"
"v_mfma_f32_16x16x32_bf16 a[204:207], v[224:227], v[152:155], 0\n"
"v_add_f32 v240, v162, v240\n"
"v_add_f32 v240, v163, v240\n"
"v_mfma_f32_16x16x32_bf16 a[220:223], v[228:231], v[152:155], 0\n"
"s_mov_b32 s49, 0\n"
"v_cvt_pk_bf16_f32 v160, v160, v161\n"
"v_cvt_pk_bf16_f32 v161, v162, v163\n"
"v_mfma_f32_16x16x32_bf16 a[216:219], v[228:231], v[144:147], 0\n"
"s_mov_b32 s49, 0\n"
"v_mfma_f32_16x16x32_bf16 a[200:203], v[224:227], v[144:147], 0\n"
"s_mov_b32 s49, 0\n"
"v_mfma_f32_16x16x32_bf16 a[196:199], v[224:227], v[136:139], 0\n"
"v_mfma_f32_16x16x32_bf16 a[212:215], v[228:231], v[136:139], 0\n"
"s_waitcnt vmcnt(8) lgkmcnt(0)\n"
"s_barrier\n"
"v_mfma_f32_16x16x32_bf16 a[208:211], v[228:231], v[128:131], 0\n"
"s_mov_b32 s49, 0\n"
"ds_read_b128 v[208:211], v244 offset:0\n"
"v_mfma_f32_16x16x32_bf16 a[192:195], v[224:227], v[128:131], 0\n"
"s_mov_b32 s49, 0\n"
"ds_read_b128 v[212:215], v244 offset:512\n"
"v_mfma_f32_16x16x32_bf16 a[224:227], v[232:235], v[128:131], 0\n"
"s_mov_b32 s49, 0\n"
"ds_read_b128 v[216:219], v244 offset:1024\n"
"v_mfma_f32_16x16x32_bf16 a[240:243], v[236:239], v[128:131], 0\n"
"s_mov_b32 s49, 0\n"
"v_exp_f32 v188, v188\n"
"v_mfma_f32_16x16x32_bf16 a[244:247], v[236:239], v[136:139], 0\n"
"s_mov_b32 s49, 0\n"
"v_exp_f32 v189, v189\n"
"v_mfma_f32_16x16x32_bf16 a[228:231], v[232:235], v[136:139], 0\n"
"s_sub_u32 s35, s16, s98\n"
"s_sub_u32 s35, s35, 1510\n"
"s_mul_i32 s35, s35, 146\n"
"s_ashr_i32 s35, s35, 10\n"
"s_min_i32 s35, s35, 8\n"
"s_max_i32 s35, s35, -6\n"
"s_sub_u32 s35, 520, s35\n"
"s_lshl_b32 s18, s16, 2\n"
"v_mfma_f32_16x16x32_bf16 a[232:235], v[232:235], v[144:147], 0\n"
"s_mov_b32 s49, 0\n"
"ds_read_b128 v[220:223], v244 offset:1536\n"
"v_add_f32 v243, v188, v243\n"
"v_mfma_f32_16x16x32_bf16 a[248:251], v[236:239], v[144:147], 0\n"
"s_mov_b32 s49, 0\n"
"v_exp_f32 v190, v190\n"
"v_mfma_f32_16x16x32_bf16 a[252:255], v[236:239], v[152:155], 0\n"
"v_exp_f32 v191, v191\n"
"v_mfma_f32_16x16x32_bf16 a[236:239], v[232:235], v[152:155], 0\n"
"s_waitcnt lgkmcnt(2)\n"
"v_add_f32 v243, v189, v243\n"
"v_add_f32 v243, v190, v243\n"
"v_mfma_f32_16x16x32_bf16 v[128:131], v[208:211], v[0:3], v[192:195]\n"
"s_mov_b64 exec, 0\n"
"s_cmp_eq_u32 s70, 0\n"
"s_cbranch_scc1 IWB0_%=\n"
"s_cmp_eq_u32 s70, 1\n"
"s_cbranch_scc1 IWB1_%=\n"
"s_cmp_eq_u32 s70, 2\n"
"s_cbranch_scc1 IWB2_%=\n"
"s_branch IWB3_%=\n"
"IRB_%=:\n"
"s_mov_b64 exec, -1\n"
"s_nop 1\n"
"LP_%=:\n"
"v_mfma_f32_16x16x32_bf16 v[128:131], v[212:215], v[4:7], v[128:131]\n"
"s_add_u32 m0, s45, s52\n"
"ds_read_b128 v[224:227], v244 offset:2048\n"
"v_add_f32 v243, v191, v243\n"
"v_mfma_f32_16x16x32_bf16 v[136:139], v[212:215], v[36:39], v[196:199]\n"
"buffer_load_dwordx4 v247, s[8:11], s62 offen nt lds\n"
"v_mfma_f32_16x16x32_bf16 v[136:139], v[208:211], v[32:35], v[136:139]\n"
"v_cvt_pk_bf16_f32 v186, v188, v189\n"
"v_cvt_pk_bf16_f32 v187, v190, v191\n"
"v_mfma_f32_16x16x32_bf16 v[144:147], v[208:211], v[64:67], v[200:203]\n"
"ds_read_b128 v[228:231], v244 offset:2560\n"
"v_mfma_f32_16x16x32_bf16 v[144:147], v[212:215], v[68:71], v[144:147]\n"
"v_mfma_f32_16x16x32_bf16 v[152:155], v[212:215], v[100:103], v[204:207]\n"
"v_exp_f32 v180, v180\n"
"v_mfma_f32_16x16x32_bf16 v[152:155], v[208:211], v[96:99], v[152:155]\n"
"s_waitcnt lgkmcnt(2)\n"
"v_exp_f32 v181, v181\n"
"v_mfma_f32_16x16x32_bf16 v[152:155], v[216:219], v[104:107], v[152:155]\n"
"ds_read_b128 v[232:235], v244 offset:3072\n"
"v_add_u32 v245, s63, v251\n"
"v_mfma_f32_16x16x32_bf16 v[152:155], v[220:223], v[108:111], v[152:155]\n"
"v_xor_b32 v246, 32, v245\n"
"v_add_f32 v242, v180, v242\n"
"v_mfma_f32_16x16x32_bf16 v[144:147], v[220:223], v[76:79], v[144:147]\n"
"s_add_u32 m0, s45, s53\n"
"v_exp_f32 v182, v182\n"
"v_mfma_f32_16x16x32_bf16 v[144:147], v[216:219], v[72:75], v[144:147]\n"
"buffer_load_dwordx4 v247, s[8:11], s62 offen offset:128 nt lds\n"
"v_mfma_f32_16x16x32_bf16 v[136:139], v[216:219], v[40:43], v[136:139]\n"
"ds_read_b128 v[236:239], v244 offset:3584\n"
"v_add_f32 v242, v181, v242\n"
"v_mfma_f32_16x16x32_bf16 v[136:139], v[220:223], v[44:47], v[136:139]\n"
"v_exp_f32 v183, v183\n"
"v_mfma_f32_16x16x32_bf16 v[128:131], v[220:223], v[12:15], v[128:131]\n"
"v_add_f32 v242, v182, v242\n"
"v_add_f32 v242, v183, v242\n"
"v_mfma_f32_16x16x32_bf16 v[128:131], v[216:219], v[8:11], v[128:131]\n"
"s_waitcnt lgkmcnt(2)\n"
"v_cvt_pk_bf16_f32 v178, v180, v181\n"
"v_cvt_pk_bf16_f32 v179, v182, v183\n"
"v_mfma_f32_16x16x32_bf16 v[128:131], v[224:227], v[16:19], v[128:131]\n"
"ds_read_b128 v[208:211], v244 offset:8192\n"
"v_mfma_f32_16x16x32_bf16 v[128:131], v[228:231], v[20:23], v[128:131]\n"
"v_mfma_f32_16x16x32_bf16 v[136:139], v[228:231], v[52:55], v[136:139]\n"
"v_mfma_f32_16x16x32_bf16 v[136:139], v[224:227], v[48:51], v[136:139]\n"
"v_mfma_f32_16x16x32_bf16 v[144:147], v[224:227], v[80:83], v[144:147]\n"
"s_add_u32 m0, s45, s54\n"
"ds_read_b128 v[212:215], v244 offset:8704\n"
"v_mfma_f32_16x16x32_bf16 v[144:147], v[228:231], v[84:87], v[144:147]\n"
"buffer_load_dwordx4 v247, s[8:11], s62 offen offset:256 nt lds\n"
"v_mfma_f32_16x16x32_bf16 v[152:155], v[228:231], v[116:119], v[152:155]\n"
"v_exp_f32 v172, v172\n"
"v_mfma_f32_16x16x32_bf16 v[152:155], v[224:227], v[112:115], v[152:155]\n"
"s_waitcnt lgkmcnt(2)\n"
"v_exp_f32 v173, v173\n"
"v_mfma_f32_16x16x32_bf16 v[152:155], v[232:235], v[120:123], v[152:155]\n"
"ds_read_b128 v[216:219], v244 offset:9216\n"
"v_add_f32 v241, v172, v241\n"
"v_mfma_f32_16x16x32_bf16 v[152:155], v[236:239], v[124:127], v[152:155]\n"
"v_exp_f32 v174, v174\n"
"v_mfma_f32_16x16x32_bf16 v[144:147], v[236:239], v[92:95], v[144:147]\n"
"v_exp_f32 v175, v175\n"
"v_mfma_f32_16x16x32_bf16 v[144:147], v[232:235], v[88:91], v[144:147]\n"
"v_add_f32 v241, v173, v241\n"
"v_add_f32 v241, v174, v241\n"
"v_mfma_f32_16x16x32_bf16 v[136:139], v[232:235], v[56:59], v[136:139]\n"
"ds_read_b128 v[220:223], v244 offset:9728\n"
"v_add_f32 v241, v175, v241\n"
"v_mfma_f32_16x16x32_bf16 v[136:139], v[236:239], v[60:63], v[136:139]\n"
"v_cvt_pk_bf16_f32 v170, v172, v173\n"
"v_cvt_pk_bf16_f32 v171, v174, v175\n"
"v_mfma_f32_16x16x32_bf16 v[128:131], v[236:239], v[28:31], v[128:131]\n"
"s_add_u32 m0, s45, s55\n"
"v_mfma_f32_16x16x32_bf16 v[128:131], v[232:235], v[24:27], v[128:131]\n"
"s_waitcnt lgkmcnt(2)\n"
"buffer_load_dwordx4 v247, s[8:11], s62 offen offset:384 nt lds\n"
"v_mfma_f32_16x16x32_bf16 v[132:135], v[208:211], v[0:3], v[192:195]\n"
"ds_read_b128 v[224:227], v244 offset:10240\n"
"v_mfma_f32_16x16x32_bf16 v[132:135], v[212:215], v[4:7], v[132:135]\n"
"v_mfma_f32_16x16x32_bf16 v[140:143], v[212:215], v[36:39], v[196:199]\n"
"v_exp_f32 v164, v164\n"
"v_mfma_f32_16x16x32_bf16 v[140:143], v[208:211], v[32:35], v[140:143]\n"
"v_exp_f32 v165, v165\n"
"v_mfma_f32_16x16x32_bf16 v[148:151], v[208:211], v[64:67], v[200:203]\n"
"ds_read_b128 v[228:231], v244 offset:10752\n"
"v_add_f32 v240, v164, v240\n"
"v_mfma_f32_16x16x32_bf16 v[148:151], v[212:215], v[68:71], v[148:151]\n"
"v_exp_f32 v166, v166\n"
"v_mfma_f32_16x16x32_bf16 v[156:159], v[212:215], v[100:103], v[204:207]\n"
"v_exp_f32 v167, v167\n"
"v_mfma_f32_16x16x32_bf16 v[156:159], v[208:211], v[96:99], v[156:159]\n"
"s_waitcnt lgkmcnt(2)\n"
"v_add_f32 v240, v165, v240\n"
"v_add_f32 v240, v166, v240\n"
"v_mfma_f32_16x16x32_bf16 v[156:159], v[216:219], v[104:107], v[156:159]\n"
"ds_read_b128 v[232:235], v244 offset:11264\n"
"v_add_f32 v240, v167, v240\n"
"v_mfma_f32_16x16x32_bf16 v[156:159], v[220:223], v[108:111], v[156:159]\n"
"s_add_u32 m0, s45, s56\n"
"v_cvt_pk_bf16_f32 v162, v164, v165\n"
"v_cvt_pk_bf16_f32 v163, v166, v167\n"
"v_mfma_f32_16x16x32_bf16 v[148:151], v[220:223], v[76:79], v[148:151]\n"
"buffer_load_dwordx4 v248, s[12:15], s34 offen lds\n"
"v_mfma_f32_16x16x32_bf16 v[148:151], v[216:219], v[72:75], v[148:151]\n"
"v_mfma_f32_16x16x32_bf16 v[140:143], v[216:219], v[40:43], v[140:143]\n"
"ds_read_b128 v[236:239], v244 offset:11776\n"
"v_mfma_f32_16x16x32_bf16 v[140:143], v[220:223], v[44:47], v[140:143]\n"
"v_mfma_f32_16x16x32_bf16 v[132:135], v[220:223], v[12:15], v[132:135]\n"
"v_mfma_f32_16x16x32_bf16 v[132:135], v[216:219], v[8:11], v[132:135]\n"
"s_waitcnt lgkmcnt(2)\n"
"v_mfma_f32_16x16x32_bf16 v[132:135], v[224:227], v[16:19], v[132:135]\n"
"ds_read_b64_tr_b16 v[208:209], v245 offset:16384\n"
"v_mfma_f32_16x16x32_bf16 v[132:135], v[228:231], v[20:23], v[132:135]\n"
"ds_read_b64_tr_b16 v[210:211], v245 offset:24576\n"
"v_mfma_f32_16x16x32_bf16 v[140:143], v[228:231], v[52:55], v[140:143]\n"
"v_mfma_f32_16x16x32_bf16 v[140:143], v[224:227], v[48:51], v[140:143]\n"
"s_add_u32 m0, s45, s57\n"
"v_mfma_f32_16x16x32_bf16 v[148:151], v[224:227], v[80:83], v[148:151]\n"
"buffer_load_dwordx4 v248, s[12:15], s34 offen offset:128 lds\n"
"v_mfma_f32_16x16x32_bf16 v[148:151], v[228:231], v[84:87], v[148:151]\n"
"ds_read_b64_tr_b16 v[212:213], v246 offset:16384\n"
"v_mfma_f32_16x16x32_bf16 v[156:159], v[228:231], v[116:119], v[156:159]\n"
"ds_read_b64_tr_b16 v[214:215], v246 offset:24576\n"
"v_mfma_f32_16x16x32_bf16 v[156:159], v[224:227], v[112:115], v[156:159]\n"
"s_waitcnt lgkmcnt(4)\n"
"v_mfma_f32_16x16x32_bf16 v[156:159], v[232:235], v[120:123], v[156:159]\n"
"ds_read_b64_tr_b16 v[216:217], v245 offset:16896\n"
"v_mfma_f32_16x16x32_bf16 v[156:159], v[236:239], v[124:127], v[156:159]\n"
"ds_read_b64_tr_b16 v[218:219], v245 offset:25088\n"
"v_mfma_f32_16x16x32_bf16 v[148:151], v[236:239], v[92:95], v[148:151]\n"
"v_mfma_f32_16x16x32_bf16 v[148:151], v[232:235], v[88:91], v[148:151]\n"
"v_mfma_f32_16x16x32_bf16 v[140:143], v[232:235], v[56:59], v[140:143]\n"
"ds_read_b64_tr_b16 v[220:221], v246 offset:16896\n"
"v_mfma_f32_16x16x32_bf16 v[140:143], v[236:239], v[60:63], v[140:143]\n"
"s_add_u32 m0, s45, s58\n"
"ds_read_b64_tr_b16 v[222:223], v246 offset:25088\n"
"v_mfma_f32_16x16x32_bf16 v[132:135], v[236:239], v[28:31], v[132:135]\n"
"buffer_load_dwordx4 v248, s[12:15], s34 offen offset:256 lds\n"
"v_mfma_f32_16x16x32_bf16 v[132:135], v[232:235], v[24:27], v[132:135]\n"
"s_waitcnt lgkmcnt(4)\n"
"v_mfma_f32_16x16x32_bf16 a[12:15], v[208:211], v[184:187], a[12:15]\n"
"ds_read_b64_tr_b16 v[224:225], v245 offset:17408\n"
"v_mfma_f32_16x16x32_bf16 a[28:31], v[212:215], v[184:187], a[28:31]\n"
"ds_read_b64_tr_b16 v[226:227], v245 offset:25600\n"
"v_mfma_f32_16x16x32_bf16 a[24:27], v[212:215], v[176:179], a[24:27]\n"
"v_exp_f32 v152, v152\n"
"v_mfma_f32_16x16x32_bf16 a[8:11], v[208:211], v[176:179], a[8:11]\n"
"v_exp_f32 v153, v153\n"
"v_mfma_f32_16x16x32_bf16 a[4:7], v[208:211], v[168:171], a[4:7]\n"
"ds_read_b64_tr_b16 v[228:229], v246 offset:17408\n"
"v_add_f32 v243, v152, v243\n"
"v_mfma_f32_16x16x32_bf16 a[20:23], v[212:215], v[168:171], a[20:23]\n"
"ds_read_b64_tr_b16 v[230:231], v246 offset:25600\n"
"v_add_f32 v243, v153, v243\n"
"v_mfma_f32_16x16x32_bf16 a[16:19], v[212:215], v[160:163], a[16:19]\n"
"v_exp_f32 v154, v154\n"
"v_mfma_f32_16x16x32_bf16 a[0:3], v[208:211], v[160:163], a[0:3]\n"
"s_waitcnt lgkmcnt(4)\n"
"v_exp_f32 v155, v155\n"
"v_mfma_f32_16x16x32_bf16 a[32:35], v[216:219], v[160:163], a[32:35]\n"
"s_add_u32 m0, s45, s59\n"
"ds_read_b64_tr_b16 v[232:233], v245 offset:17920\n"
"v_add_f32 v243, v154, v243\n"
"v_mfma_f32_16x16x32_bf16 a[48:51], v[220:223], v[160:163], a[48:51]\n"
"buffer_load_dwordx4 v248, s[12:15], s34 offen offset:384 lds\n"
"v_mfma_f32_16x16x32_bf16 a[52:55], v[220:223], v[168:171], a[52:55]\n"
"ds_read_b64_tr_b16 v[234:235], v245 offset:26112\n"
"v_add_f32 v243, v155, v243\n"
"v_mfma_f32_16x16x32_bf16 a[36:39], v[216:219], v[168:171], a[36:39]\n"
"v_cvt_pk_bf16_f32 v152, v152, v153\n"
"v_cvt_pk_bf16_f32 v153, v154, v155\n"
"v_mfma_f32_16x16x32_bf16 a[40:43], v[216:219], v[176:179], a[40:43]\n"
"ds_read_b64_tr_b16 v[236:237], v246 offset:17920\n"
"v_mfma_f32_16x16x32_bf16 a[56:59], v[220:223], v[176:179], a[56:59]\n"
"ds_read_b64_tr_b16 v[238:239], v246 offset:26112\n"
"v_mfma_f32_16x16x32_bf16 a[60:63], v[220:223], v[184:187], a[60:63]\n"
"v_exp_f32 v144, v144\n"
"v_mfma_f32_16x16x32_bf16 a[44:47], v[216:219], v[184:187], a[44:47]\n"
"s_waitcnt lgkmcnt(4)\n"
"v_exp_f32 v145, v145\n"
"v_mfma_f32_16x16x32_bf16 a[76:79], v[224:227], v[184:187], a[76:79]\n"
"ds_read_b64_tr_b16 v[208:209], v245 offset:18432\n"
"v_add_f32 v242, v144, v242\n"
"v_mfma_f32_16x16x32_bf16 a[92:95], v[228:231], v[184:187], a[92:95]\n"
"ds_read_b64_tr_b16 v[210:211], v245 offset:26624\n"
"v_add_f32 v242, v145, v242\n"
"v_mfma_f32_16x16x32_bf16 a[88:91], v[228:231], v[176:179], a[88:91]\n"
"v_exp_f32 v146, v146\n"
"v_mfma_f32_16x16x32_bf16 a[72:75], v[224:227], v[176:179], a[72:75]\n"
"v_exp_f32 v147, v147\n"
"v_mfma_f32_16x16x32_bf16 a[68:71], v[224:227], v[168:171], a[68:71]\n"
"s_add_u32 s64, s63, 32768\n"
"ds_read_b64_tr_b16 v[212:213], v246 offset:18432\n"
"v_add_f32 v242, v146, v242\n"
"v_mfma_f32_16x16x32_bf16 a[84:87], v[228:231], v[168:171], a[84:87]\n"
"s_cmp_eq_u32 s64, 163840\n"
"ds_read_b64_tr_b16 v[214:215], v246 offset:26624\n"
"v_add_f32 v242, v147, v242\n"
"v_mfma_f32_16x16x32_bf16 a[80:83], v[228:231], v[160:163], a[80:83]\n"
"s_cselect_b32 s64, 0, s64\n"
"v_cvt_pk_bf16_f32 v144, v144, v145\n"
"v_cvt_pk_bf16_f32 v145, v146, v147\n"
"v_mfma_f32_16x16x32_bf16 a[64:67], v[224:227], v[160:163], a[64:67]\n"
"s_waitcnt lgkmcnt(4)\n"
"v_add_u32 v244, s64, v250\n"
"v_mfma_f32_16x16x32_bf16 a[96:99], v[232:235], v[160:163], a[96:99]\n"
"s_mov_b32 s34, s62\n"
"ds_read_b64_tr_b16 v[216:217], v245 offset:18944\n"
"v_mfma_f32_16x16x32_bf16 a[112:115], v[236:239], v[160:163], a[112:115]\n"
"s_add_u32 s62, s62, 16384\n"
"ds_read_b64_tr_b16 v[218:219], v245 offset:27136\n"
"v_mfma_f32_16x16x32_bf16 a[116:119], v[236:239], v[168:171], a[116:119]\n"
"s_and_b32 s62, s62, 524287\n"
"v_mfma_f32_16x16x32_bf16 a[100:103], v[232:235], v[168:171], a[100:103]\n"
"s_add_u32 s45, s45, 32768\n"
"v_mfma_f32_16x16x32_bf16 a[104:107], v[232:235], v[176:179], a[104:107]\n"
"s_cmp_eq_u32 s45, 163840\n"
"ds_read_b64_tr_b16 v[220:221], v246 offset:18944\n"
"v_mfma_f32_16x16x32_bf16 a[120:123], v[236:239], v[176:179], a[120:123]\n"
"s_cselect_b32 s45, 0, s45\n"
"ds_read_b64_tr_b16 v[222:223], v246 offset:27136\n"
"v_mfma_f32_16x16x32_bf16 a[124:127], v[236:239], v[184:187], a[124:127]\n"
"s_sub_u32 s61, s61, 1\n"
"v_exp_f32 v136, v136\n"
"v_mfma_f32_16x16x32_bf16 a[108:111], v[232:235], v[184:187], a[108:111]\n"
"IWC2_%=:\n"
"s_cbranch_execz IRC_%=\n"
"s_waitcnt lgkmcnt(4)\n"
"v_exp_f32 v137, v137\n"
"v_mfma_f32_16x16x32_bf16 a[140:143], v[208:211], v[184:187], a[140:143]\n"
"s_cmp_eq_u32 s61, 0\n"
"ds_read_b64_tr_b16 v[224:225], v245 offset:19456\n"
"v_add_f32 v241, v136, v241\n"
"v_mfma_f32_16x16x32_bf16 a[156:159], v[212:215], v[184:187], a[156:159]\n"
"s_cselect_b32 s47, 1, 0\n"
"ds_read_b64_tr_b16 v[226:227], v245 offset:27648\n"
"v_add_f32 v241, v137, v241\n"
"v_mfma_f32_16x16x32_bf16 a[152:155], v[212:215], v[176:179], a[152:155]\n"
"s_cselect_b32 s61, 12, s61\n"
"v_exp_f32 v138, v138\n"
"v_mfma_f32_16x16x32_bf16 a[136:139], v[208:211], v[176:179], a[136:139]\n"
"s_cmp_lt_u32 s44, 2\n"
"v_exp_f32 v139, v139\n"
"v_mfma_f32_16x16x32_bf16 a[132:135], v[208:211], v[168:171], a[132:135]\n"
"s_cselect_b32 s47, s47, 0\n"
"ds_read_b64_tr_b16 v[228:229], v246 offset:19456\n"
"v_add_f32 v241, v138, v241\n"
"v_mfma_f32_16x16x32_bf16 a[148:151], v[212:215], v[168:171], a[148:151]\n"
"s_add_u32 s18, s18, s35\n"
"ds_read_b64_tr_b16 v[230:231], v246 offset:27648\n"
"v_add_f32 v241, v139, v241\n"
"v_mfma_f32_16x16x32_bf16 a[144:147], v[212:215], v[160:163], a[144:147]\n"
"v_cvt_pk_bf16_f32 v136, v136, v137\n"
"v_cvt_pk_bf16_f32 v137, v138, v139\n"
"v_mfma_f32_16x16x32_bf16 a[128:131], v[208:211], v[160:163], a[128:131]\n"
"s_waitcnt lgkmcnt(4)\n"
"v_mfma_f32_16x16x32_bf16 a[160:163], v[216:219], v[160:163], a[160:163]\n"
"s_memrealtime s[16:17]\n"
"ds_read_b64_tr_b16 v[232:233], v245 offset:19968\n"
"v_mfma_f32_16x16x32_bf16 a[176:179], v[220:223], v[160:163], a[176:179]\n"
"ds_read_b64_tr_b16 v[234:235], v245 offset:28160\n"
"v_mfma_f32_16x16x32_bf16 a[180:183], v[220:223], v[168:171], a[180:183]\n"
"v_exp_f32 v128, v128\n"
"v_mfma_f32_16x16x32_bf16 a[164:167], v[216:219], v[168:171], a[164:167]\n"
"v_exp_f32 v129, v129\n"
"v_mfma_f32_16x16x32_bf16 a[168:171], v[216:219], v[176:179], a[168:171]\n"
"ds_read_b64_tr_b16 v[236:237], v246 offset:19968\n"
"v_add_f32 v240, v128, v240\n"
"v_mfma_f32_16x16x32_bf16 a[184:187], v[220:223], v[176:179], a[184:187]\n"
"ds_read_b64_tr_b16 v[238:239], v246 offset:28160\n"
"v_add_f32 v240, v129, v240\n"
"v_mfma_f32_16x16x32_bf16 a[188:191], v[220:223], v[184:187], a[188:191]\n"
"v_exp_f32 v130, v130\n"
"v_mfma_f32_16x16x32_bf16 a[172:175], v[216:219], v[184:187], a[172:175]\n"
"s_waitcnt lgkmcnt(4)\n"
"v_exp_f32 v131, v131\n"
"v_mfma_f32_16x16x32_bf16 a[204:207], v[224:227], v[184:187], a[204:207]\n"
"v_add_f32 v240, v130, v240\n"
"v_add_f32 v240, v131, v240\n"
"v_mfma_f32_16x16x32_bf16 a[220:223], v[228:231], v[184:187], a[220:223]\n"
"s_getreg_b32 s46, hwreg(HW_REG_IB_STS, 0, 4)\n"
"v_cvt_pk_bf16_f32 v128, v128, v129\n"
"v_cvt_pk_bf16_f32 v129, v130, v131\n"
"v_mfma_f32_16x16x32_bf16 a[216:219], v[228:231], v[176:179], a[216:219]\n"
"s_cmp_ge_u32 s46, 8\n"
"v_mfma_f32_16x16x32_bf16 a[200:203], v[224:227], v[176:179], a[200:203]\n"
"s_cselect_b32 s49, 1, s47\n"
"v_mfma_f32_16x16x32_bf16 a[196:199], v[224:227], v[168:171], a[196:199]\n"
"s_or_b32 s48, s48, s49\n"
"s_cbranch_scc1 RSL1_%=\n"
"RRL1_%=:\n"
"v_mfma_f32_16x16x32_bf16 a[212:215], v[228:231], v[168:171], a[212:215]\n"
"s_waitcnt vmcnt(8) lgkmcnt(0)\n"
"s_barrier\n"
"v_mfma_f32_16x16x32_bf16 a[208:211], v[228:231], v[160:163], a[208:211]\n"
"s_lshl_b32 s16, s16, 2\n"
"ds_read_b128 v[208:211], v244 offset:0\n"
"v_mfma_f32_16x16x32_bf16 a[192:195], v[224:227], v[160:163], a[192:195]\n"
"s_sub_u32 s16, s18, s16\n"
"ds_read_b128 v[212:215], v244 offset:512\n"
"v_mfma_f32_16x16x32_bf16 a[224:227], v[232:235], v[160:163], a[224:227]\n"
"s_min_i32 s19, s16, 0\n"
"ds_read_b128 v[216:219], v244 offset:1024\n"
"v_mfma_f32_16x16x32_bf16 a[240:243], v[236:239], v[160:163], a[240:243]\n"
"s_ashr_i32 s16, s16, 3\n"
"v_exp_f32 v156, v156\n"
"v_mfma_f32_16x16x32_bf16 a[244:247], v[236:239], v[168:171], a[244:247]\n"
"s_min_i32 s16, s16, 32\n"
"v_exp_f32 v157, v157\n"
"v_mfma_f32_16x16x32_bf16 a[228:231], v[232:235], v[168:171], a[228:231]\n"
"s_sub_u32 s16, s16, 1\n"
"s_cmp_gt_i32 s16, 0\n"
"s_cbranch_scc1 65533\n"
"v_mfma_f32_16x16x32_bf16 a[232:235], v[232:235], v[176:179], a[232:235]\n"
"s_and_b32 s19, s19, s43\n"
"ds_read_b128 v[220:223], v244 offset:1536\n"
"v_add_f32 v243, v156, v243\n"
"v_mfma_f32_16x16x32_bf16 a[248:251], v[236:239], v[176:179], a[248:251]\n"
"s_sub_u32 s18, s18, s19\n"
"v_exp_f32 v158, v158\n"
"v_mfma_f32_16x16x32_bf16 a[252:255], v[236:239], v[184:187], a[252:255]\n"
"v_exp_f32 v159, v159\n"
"v_mfma_f32_16x16x32_bf16 a[236:239], v[232:235], v[184:187], a[236:239]\n"
"s_waitcnt lgkmcnt(2)\n"
"v_add_f32 v243, v157, v243\n"
"v_add_f32 v243, v158, v243\n"
"v_mfma_f32_16x16x32_bf16 v[160:163], v[208:211], v[0:3], v[192:195]\n"
"s_add_u32 m0, s45, s52\n"
"ds_read_b128 v[224:227], v244 offset:2048\n"
"v_add_f32 v243, v159, v243\n"
"v_mfma_f32_16x16x32_bf16 v[160:163], v[212:215], v[4:7], v[160:163]\n"
"buffer_load_dwordx4 v247, s[8:11], s62 offen nt lds\n"
"v_mfma_f32_16x16x32_bf16 v[168:171], v[212:215], v[36:39], v[196:199]\n"
"v_cvt_pk_bf16_f32 v154, v156, v157\n"
"v_cvt_pk_bf16_f32 v155, v158, v159\n"
"v_mfma_f32_16x16x32_bf16 v[168:171], v[208:211], v[32:35], v[168:171]\n"
"v_mfma_f32_16x16x32_bf16 v[176:179], v[208:211], v[64:67], v[200:203]\n"
"ds_read_b128 v[228:231], v244 offset:2560\n"
"v_mfma_f32_16x16x32_bf16 v[176:179], v[212:215], v[68:71], v[176:179]\n"
"v_mfma_f32_16x16x32_bf16 v[184:187], v[212:215], v[100:103], v[204:207]\n"
"v_exp_f32 v148, v148\n"
"v_mfma_f32_16x16x32_bf16 v[184:187], v[208:211], v[96:99], v[184:187]\n"
"s_waitcnt lgkmcnt(2)\n"
"v_exp_f32 v149, v149\n"
"v_mfma_f32_16x16x32_bf16 v[184:187], v[216:219], v[104:107], v[184:187]\n"
"ds_read_b128 v[232:235], v244 offset:3072\n"
"v_add_u32 v245, s64, v251\n"
"v_mfma_f32_16x16x32_bf16 v[184:187], v[220:223], v[108:111], v[184:187]\n"
"s_add_u32 m0, s45, s53\n"
"v_xor_b32 v246, 32, v245\n"
"v_add_f32 v242, v148, v242\n"
"v_mfma_f32_16x16x32_bf16 v[176:179], v[220:223], v[76:79], v[176:179]\n"
"buffer_load_dwordx4 v247, s[8:11], s62 offen offset:128 nt lds\n"
"v_mfma_f32_16x16x32_bf16 v[176:179], v[216:219], v[72:75], v[176:179]\n"
"v_exp_f32 v150, v150\n"
"v_mfma_f32_16x16x32_bf16 v[168:171], v[216:219], v[40:43], v[168:171]\n"
"ds_read_b128 v[236:239], v244 offset:3584\n"
"v_add_f32 v242, v149, v242\n"
"v_mfma_f32_16x16x32_bf16 v[168:171], v[220:223], v[44:47], v[168:171]\n"
"v_exp_f32 v151, v151\n"
"v_mfma_f32_16x16x32_bf16 v[160:163], v[220:223], v[12:15], v[160:163]\n"
"v_add_f32 v242, v150, v242\n"
"v_add_f32 v242, v151, v242\n"
"v_mfma_f32_16x16x32_bf16 v[160:163], v[216:219], v[8:11], v[160:163]\n"
"s_waitcnt lgkmcnt(2)\n"
"v_cvt_pk_bf16_f32 v146, v148, v149\n"
"v_cvt_pk_bf16_f32 v147, v150, v151\n"
"v_mfma_f32_16x16x32_bf16 v[160:163], v[224:227], v[16:19], v[160:163]\n"
"ds_read_b128 v[208:211], v244 offset:8192\n"
"v_mfma_f32_16x16x32_bf16 v[160:163], v[228:231], v[20:23], v[160:163]\n"
"v_mfma_f32_16x16x32_bf16 v[168:171], v[228:231], v[52:55], v[168:171]\n"
"s_add_u32 m0, s45, s54\n"
"v_mfma_f32_16x16x32_bf16 v[168:171], v[224:227], v[48:51], v[168:171]\n"
"buffer_load_dwordx4 v247, s[8:11], s62 offen offset:256 nt lds\n"
"v_mfma_f32_16x16x32_bf16 v[176:179], v[224:227], v[80:83], v[176:179]\n"
"ds_read_b128 v[212:215], v244 offset:8704\n"
"v_mfma_f32_16x16x32_bf16 v[176:179], v[228:231], v[84:87], v[176:179]\n"
"v_exp_f32 v140, v140\n"
"v_mfma_f32_16x16x32_bf16 v[184:187], v[228:231], v[116:119], v[184:187]\n"
"v_exp_f32 v141, v141\n"
"v_mfma_f32_16x16x32_bf16 v[184:187], v[224:227], v[112:115], v[184:187]\n"
"s_waitcnt lgkmcnt(2)\n"
"v_exp_f32 v142, v142\n"
"v_mfma_f32_16x16x32_bf16 v[184:187], v[232:235], v[120:123], v[184:187]\n"
"ds_read_b128 v[216:219], v244 offset:9216\n"
"v_add_f32 v241, v140, v241\n"
"v_mfma_f32_16x16x32_bf16 v[184:187], v[236:239], v[124:127], v[184:187]\n"
"v_exp_f32 v143, v143\n"
"v_mfma_f32_16x16x32_bf16 v[176:179], v[236:239], v[92:95], v[176:179]\n"
"v_add_f32 v241, v141, v241\n"
"v_add_f32 v241, v142, v241\n"
"v_mfma_f32_16x16x32_bf16 v[176:179], v[232:235], v[88:91], v[176:179]\n"
"s_add_u32 m0, s45, s55\n"
"v_add_f32 v241, v143, v241\n"
"v_cvt_pk_bf16_f32 v138, v140, v141\n"
"v_mfma_f32_16x16x32_bf16 v[168:171], v[232:235], v[56:59], v[168:171]\n"
"buffer_load_dwordx4 v247, s[8:11], s62 offen offset:384 nt lds\n"
"v_mfma_f32_16x16x32_bf16 v[168:171], v[236:239], v[60:63], v[168:171]\n"
"ds_read_b128 v[220:223], v244 offset:9728\n"
"v_cvt_pk_bf16_f32 v139, v142, v143\n"
"v_mfma_f32_16x16x32_bf16 v[160:163], v[236:239], v[28:31], v[160:163]\n"
"v_mfma_f32_16x16x32_bf16 v[160:163], v[232:235], v[24:27], v[160:163]\n"
"s_waitcnt lgkmcnt(2)\n"
"v_mfma_f32_16x16x32_bf16 v[164:167], v[208:211], v[0:3], v[192:195]\n"
"ds_read_b128 v[224:227], v244 offset:10240\n"
"v_mfma_f32_16x16x32_bf16 v[164:167], v[212:215], v[4:7], v[164:167]\n"
"v_mfma_f32_16x16x32_bf16 v[172:175], v[212:215], v[36:39], v[196:199]\n"
"v_exp_f32 v132, v132\n"
"v_mfma_f32_16x16x32_bf16 v[172:175], v[208:211], v[32:35], v[172:175]\n"
"v_exp_f32 v133, v133\n"
"v_mfma_f32_16x16x32_bf16 v[180:183], v[208:211], v[64:67], v[200:203]\n"
"s_add_u32 m0, s45, s56\n"
"ds_read_b128 v[228:231], v244 offset:10752\n"
"v_add_f32 v240, v132, v240\n"
"v_mfma_f32_16x16x32_bf16 v[180:183], v[212:215], v[68:71], v[180:183]\n"
"buffer_load_dwordx4 v248, s[12:15], s34 offen lds\n"
"v_mfma_f32_16x16x32_bf16 v[188:191], v[212:215], v[100:103], v[204:207]\n"
"v_exp_f32 v134, v134\n"
"v_mfma_f32_16x16x32_bf16 v[188:191], v[208:211], v[96:99], v[188:191]\n"
"s_waitcnt lgkmcnt(2)\n"
"v_exp_f32 v135, v135\n"
"v_mfma_f32_16x16x32_bf16 v[188:191], v[216:219], v[104:107], v[188:191]\n"
"ds_read_b128 v[232:235], v244 offset:11264\n"
"v_add_f32 v240, v133, v240\n"
"v_mfma_f32_16x16x32_bf16 v[188:191], v[220:223], v[108:111], v[188:191]\n"
"v_add_f32 v240, v134, v240\n"
"v_add_f32 v240, v135, v240\n"
"v_mfma_f32_16x16x32_bf16 v[180:183], v[220:223], v[76:79], v[180:183]\n"
"v_cvt_pk_bf16_f32 v130, v132, v133\n"
"v_cvt_pk_bf16_f32 v131, v134, v135\n"
"v_mfma_f32_16x16x32_bf16 v[180:183], v[216:219], v[72:75], v[180:183]\n"
"v_mfma_f32_16x16x32_bf16 v[172:175], v[216:219], v[40:43], v[172:175]\n"
"ds_read_b128 v[236:239], v244 offset:11776\n"
"v_mfma_f32_16x16x32_bf16 v[172:175], v[220:223], v[44:47], v[172:175]\n"
"s_add_u32 m0, s45, s57\n"
"v_mfma_f32_16x16x32_bf16 v[164:167], v[220:223], v[12:15], v[164:167]\n"
"buffer_load_dwordx4 v248, s[12:15], s34 offen offset:128 lds\n"
"v_mfma_f32_16x16x32_bf16 v[164:167], v[216:219], v[8:11], v[164:167]\n"
"s_waitcnt lgkmcnt(2)\n"
"v_mfma_f32_16x16x32_bf16 v[164:167], v[224:227], v[16:19], v[164:167]\n"
"ds_read_b64_tr_b16 v[208:209], v245 offset:16384\n"
"v_mfma_f32_16x16x32_bf16 v[164:167], v[228:231], v[20:23], v[164:167]\n"
"ds_read_b64_tr_b16 v[210:211], v245 offset:24576\n"
"v_mfma_f32_16x16x32_bf16 v[172:175], v[228:231], v[52:55], v[172:175]\n"
"v_mfma_f32_16x16x32_bf16 v[172:175], v[224:227], v[48:51], v[172:175]\n"
"v_mfma_f32_16x16x32_bf16 v[180:183], v[224:227], v[80:83], v[180:183]\n"
"ds_read_b64_tr_b16 v[212:213], v246 offset:16384\n"
"v_mfma_f32_16x16x32_bf16 v[180:183], v[228:231], v[84:87], v[180:183]\n"
"ds_read_b64_tr_b16 v[214:215], v246 offset:24576\n"
"v_mfma_f32_16x16x32_bf16 v[188:191], v[228:231], v[116:119], v[188:191]\n"
"s_add_u32 m0, s45, s58\n"
"v_mfma_f32_16x16x32_bf16 v[188:191], v[224:227], v[112:115], v[188:191]\n"
"s_waitcnt lgkmcnt(4)\n"
"buffer_load_dwordx4 v248, s[12:15], s34 offen offset:256 lds\n"
"v_mfma_f32_16x16x32_bf16 v[188:191], v[232:235], v[120:123], v[188:191]\n"
"ds_read_b64_tr_b16 v[216:217], v245 offset:16896\n"
"v_mfma_f32_16x16x32_bf16 v[188:191], v[236:239], v[124:127], v[188:191]\n"
"ds_read_b64_tr_b16 v[218:219], v245 offset:25088\n"
"v_mfma_f32_16x16x32_bf16 v[180:183], v[236:239], v[92:95], v[180:183]\n"
"v_mfma_f32_16x16x32_bf16 v[180:183], v[232:235], v[88:91], v[180:183]\n"
"v_mfma_f32_16x16x32_bf16 v[172:175], v[232:235], v[56:59], v[172:175]\n"
"ds_read_b64_tr_b16 v[220:221], v246 offset:16896\n"
"v_mfma_f32_16x16x32_bf16 v[172:175], v[236:239], v[60:63], v[172:175]\n"
"ds_read_b64_tr_b16 v[222:223], v246 offset:25088\n"
"v_mfma_f32_16x16x32_bf16 v[164:167], v[236:239], v[28:31], v[164:167]\n"
"v_mfma_f32_16x16x32_bf16 v[164:167], v[232:235], v[24:27], v[164:167]\n"
"s_waitcnt lgkmcnt(4)\n"
"v_mfma_f32_16x16x32_bf16 a[12:15], v[208:211], v[152:155], a[12:15]\n"
"s_add_u32 m0, s45, s59\n"
"ds_read_b64_tr_b16 v[224:225], v245 offset:17408\n"
"v_mfma_f32_16x16x32_bf16 a[28:31], v[212:215], v[152:155], a[28:31]\n"
"buffer_load_dwordx4 v248, s[12:15], s34 offen offset:384 lds\n"
"v_mfma_f32_16x16x32_bf16 a[24:27], v[212:215], v[144:147], a[24:27]\n"
"ds_read_b64_tr_b16 v[226:227], v245 offset:25600\n"
"v_mfma_f32_16x16x32_bf16 a[8:11], v[208:211], v[144:147], a[8:11]\n"
"v_exp_f32 v184, v184\n"
"v_mfma_f32_16x16x32_bf16 a[4:7], v[208:211], v[136:139], a[4:7]\n"
"ds_read_b64_tr_b16 v[228:229], v246 offset:17408\n"
"v_add_f32 v243, v184, v243\n"
"v_mfma_f32_16x16x32_bf16 a[20:23], v[212:215], v[136:139], a[20:23]\n"
"ds_read_b64_tr_b16 v[230:231], v246 offset:25600\n"
"v_mfma_f32_16x16x32_bf16 a[16:19], v[212:215], v[128:131], a[16:19]\n"
"v_exp_f32 v185, v185\n"
"v_mfma_f32_16x16x32_bf16 a[0:3], v[208:211], v[128:131], a[0:3]\n"
"s_waitcnt lgkmcnt(4)\n"
"v_exp_f32 v186, v186\n"
"v_mfma_f32_16x16x32_bf16 a[32:35], v[216:219], v[128:131], a[32:35]\n"
"buffer_load_dword v255, v247, s[76:79], s72 offen offset:0\n"
"v_mfma_f32_16x16x32_bf16 a[48:51], v[220:223], v[128:131], a[48:51]\n"
"ds_read_b64_tr_b16 v[232:233], v245 offset:17920\n"
"v_add_f32 v243, v185, v243\n"
"v_mfma_f32_16x16x32_bf16 a[52:55], v[220:223], v[136:139], a[52:55]\n"
"ds_read_b64_tr_b16 v[234:235], v245 offset:26112\n"
"v_add_f32 v243, v186, v243\n"
"v_mfma_f32_16x16x32_bf16 a[36:39], v[216:219], v[136:139], a[36:39]\n"
"v_exp_f32 v187, v187\n"
"v_mfma_f32_16x16x32_bf16 a[40:43], v[216:219], v[144:147], a[40:43]\n"
"ds_read_b64_tr_b16 v[236:237], v246 offset:17920\n"
"v_add_f32 v243, v187, v243\n"
"v_mfma_f32_16x16x32_bf16 a[56:59], v[220:223], v[144:147], a[56:59]\n"
"ds_read_b64_tr_b16 v[238:239], v246 offset:26112\n"
"v_cvt_pk_bf16_f32 v184, v184, v185\n"
"v_mfma_f32_16x16x32_bf16 a[60:63], v[220:223], v[152:155], a[60:63]\n"
"v_exp_f32 v176, v176\n"
"v_mfma_f32_16x16x32_bf16 a[44:47], v[216:219], v[152:155], a[44:47]\n"
"s_waitcnt lgkmcnt(4)\n"
"v_exp_f32 v177, v177\n"
"v_mfma_f32_16x16x32_bf16 a[76:79], v[224:227], v[152:155], a[76:79]\n"
"ds_read_b64_tr_b16 v[208:209], v245 offset:18432\n"
"v_cvt_pk_bf16_f32 v185, v186, v187\n"
"v_mfma_f32_16x16x32_bf16 a[92:95], v[228:231], v[152:155], a[92:95]\n"
"ds_read_b64_tr_b16 v[210:211], v245 offset:26624\n"
"v_add_f32 v242, v176, v242\n"
"v_mfma_f32_16x16x32_bf16 a[88:91], v[228:231], v[144:147], a[88:91]\n"
"v_exp_f32 v178, v178\n"
"v_mfma_f32_16x16x32_bf16 a[72:75], v[224:227], v[144:147], a[72:75]\n"
"v_exp_f32 v179, v179\n"
"v_mfma_f32_16x16x32_bf16 a[68:71], v[224:227], v[136:139], a[68:71]\n"
"s_add_u32 s63, s64, 32768\n"
"ds_read_b64_tr_b16 v[212:213], v246 offset:18432\n"
"v_add_f32 v242, v177, v242\n"
"v_mfma_f32_16x16x32_bf16 a[84:87], v[228:231], v[136:139], a[84:87]\n"
"s_cmp_eq_u32 s63, 163840\n"
"ds_read_b64_tr_b16 v[214:215], v246 offset:26624\n"
"v_add_f32 v242, v178, v242\n"
"v_mfma_f32_16x16x32_bf16 a[80:83], v[228:231], v[128:131], a[80:83]\n"
"s_cselect_b32 s63, 0, s63\n"
"v_add_f32 v242, v179, v242\n"
"v_cvt_pk_bf16_f32 v176, v176, v177\n"
"v_mfma_f32_16x16x32_bf16 a[64:67], v[224:227], v[128:131], a[64:67]\n"
"s_waitcnt lgkmcnt(4)\n"
"v_add_u32 v244, s63, v250\n"
"v_cvt_pk_bf16_f32 v177, v178, v179\n"
"v_mfma_f32_16x16x32_bf16 a[96:99], v[232:235], v[128:131], a[96:99]\n"
"s_mov_b32 s34, s62\n"
"ds_read_b64_tr_b16 v[216:217], v245 offset:18944\n"
"v_mfma_f32_16x16x32_bf16 a[112:115], v[236:239], v[128:131], a[112:115]\n"
"s_add_u32 s62, s62, 16384\n"
"ds_read_b64_tr_b16 v[218:219], v245 offset:27136\n"
"v_mfma_f32_16x16x32_bf16 a[116:119], v[236:239], v[136:139], a[116:119]\n"
"s_and_b32 s62, s62, 524287\n"
"v_mfma_f32_16x16x32_bf16 a[100:103], v[232:235], v[136:139], a[100:103]\n"
"s_add_u32 s45, s45, 32768\n"
"v_mfma_f32_16x16x32_bf16 a[104:107], v[232:235], v[144:147], a[104:107]\n"
"s_cmp_eq_u32 s45, 163840\n"
"ds_read_b64_tr_b16 v[220:221], v246 offset:18944\n"
"v_mfma_f32_16x16x32_bf16 a[120:123], v[236:239], v[144:147], a[120:123]\n"
"s_cselect_b32 s45, 0, s45\n"
"ds_read_b64_tr_b16 v[222:223], v246 offset:27136\n"
"v_mfma_f32_16x16x32_bf16 a[124:127], v[236:239], v[152:155], a[124:127]\n"
"s_sub_u32 s61, s61, 1\n"
"v_exp_f32 v168, v168\n"
"v_mfma_f32_16x16x32_bf16 a[108:111], v[232:235], v[152:155], a[108:111]\n"
"s_waitcnt lgkmcnt(4)\n"
"v_exp_f32 v169, v169\n"
"v_mfma_f32_16x16x32_bf16 a[140:143], v[208:211], v[152:155], a[140:143]\n"
"s_cmp_eq_u32 s61, 0\n"
"ds_read_b64_tr_b16 v[224:225], v245 offset:19456\n"
"v_add_f32 v241, v168, v241\n"
"v_mfma_f32_16x16x32_bf16 a[156:159], v[212:215], v[152:155], a[156:159]\n"
"s_cselect_b32 s47, 1, 0\n"
"ds_read_b64_tr_b16 v[226:227], v245 offset:27648\n"
"v_add_f32 v241, v169, v241\n"
"v_mfma_f32_16x16x32_bf16 a[152:155], v[212:215], v[144:147], a[152:155]\n"
"s_cselect_b32 s61, 12, s61\n"
"v_exp_f32 v170, v170\n"
"v_mfma_f32_16x16x32_bf16 a[136:139], v[208:211], v[144:147], a[136:139]\n"
"s_cmp_lt_u32 s44, 2\n"
"v_exp_f32 v171, v171\n"
"v_mfma_f32_16x16x32_bf16 a[132:135], v[208:211], v[136:139], a[132:135]\n"
"s_cselect_b32 s47, s47, 0\n"
"ds_read_b64_tr_b16 v[228:229], v246 offset:19456\n"
"v_add_f32 v241, v170, v241\n"
"v_mfma_f32_16x16x32_bf16 a[148:151], v[212:215], v[136:139], a[148:151]\n"
"s_add_u32 s18, s18, s35\n"
"ds_read_b64_tr_b16 v[230:231], v246 offset:27648\n"
"v_add_f32 v241, v171, v241\n"
"v_mfma_f32_16x16x32_bf16 a[144:147], v[212:215], v[128:131], a[144:147]\n"
"s_add_u32 s71, s71, 1\n"
"v_cvt_pk_bf16_f32 v168, v168, v169\n"
"v_cvt_pk_bf16_f32 v169, v170, v171\n"
"v_mfma_f32_16x16x32_bf16 a[128:131], v[208:211], v[128:131], a[128:131]\n"
"s_waitcnt lgkmcnt(4)\n"
"v_mfma_f32_16x16x32_bf16 a[160:163], v[216:219], v[128:131], a[160:163]\n"
"s_memrealtime s[16:17]\n"
"ds_read_b64_tr_b16 v[232:233], v245 offset:19968\n"
"v_mfma_f32_16x16x32_bf16 a[176:179], v[220:223], v[128:131], a[176:179]\n"
"ds_read_b64_tr_b16 v[234:235], v245 offset:28160\n"
"v_mfma_f32_16x16x32_bf16 a[180:183], v[220:223], v[136:139], a[180:183]\n"
"v_exp_f32 v160, v160\n"
"v_mfma_f32_16x16x32_bf16 a[164:167], v[216:219], v[136:139], a[164:167]\n"
"v_exp_f32 v161, v161\n"
"v_mfma_f32_16x16x32_bf16 a[168:171], v[216:219], v[144:147], a[168:171]\n"
"ds_read_b64_tr_b16 v[236:237], v246 offset:19968\n"
"v_add_f32 v240, v160, v240\n"
"v_mfma_f32_16x16x32_bf16 a[184:187], v[220:223], v[144:147], a[184:187]\n"
"ds_read_b64_tr_b16 v[238:239], v246 offset:28160\n"
"v_add_f32 v240, v161, v240\n"
"v_mfma_f32_16x16x32_bf16 a[188:191], v[220:223], v[152:155], a[188:191]\n"
"v_exp_f32 v162, v162\n"
"v_mfma_f32_16x16x32_bf16 a[172:175], v[216:219], v[152:155], a[172:175]\n"
"s_waitcnt lgkmcnt(4)\n"
"v_exp_f32 v163, v163\n"
"v_mfma_f32_16x16x32_bf16 a[204:207], v[224:227], v[152:155], a[204:207]\n"
"v_add_f32 v240, v162, v240\n"
"v_add_f32 v240, v163, v240\n"
"v_mfma_f32_16x16x32_bf16 a[220:223], v[228:231], v[152:155], a[220:223]\n"
"s_getreg_b32 s46, hwreg(HW_REG_IB_STS, 0, 4)\n"
"v_cvt_pk_bf16_f32 v160, v160, v161\n"
"v_cvt_pk_bf16_f32 v161, v162, v163\n"
"v_mfma_f32_16x16x32_bf16 a[216:219], v[228:231], v[144:147], a[216:219]\n"
"s_cmp_ge_u32 s46, 8\n"
"v_mfma_f32_16x16x32_bf16 a[200:203], v[224:227], v[144:147], a[200:203]\n"
"s_cselect_b32 s49, 1, s47\n"
"v_mfma_f32_16x16x32_bf16 a[196:199], v[224:227], v[136:139], a[196:199]\n"
"s_or_b32 s48, s48, s49\n"
"s_cbranch_scc1 RSL0_%=\n"
"RRL0_%=:\n"
"v_mfma_f32_16x16x32_bf16 a[212:215], v[228:231], v[136:139], a[212:215]\n"
"s_waitcnt vmcnt(8) lgkmcnt(0)\n"
"s_barrier\n"
"v_mfma_f32_16x16x32_bf16 a[208:211], v[228:231], v[128:131], a[208:211]\n"
"s_lshl_b32 s16, s16, 2\n"
"ds_read_b128 v[208:211], v244 offset:0\n"
"v_mfma_f32_16x16x32_bf16 a[192:195], v[224:227], v[128:131], a[192:195]\n"
"s_sub_u32 s16, s18, s16\n"
"ds_read_b128 v[212:215], v244 offset:512\n"
"v_mfma_f32_16x16x32_bf16 a[224:227], v[232:235], v[128:131], a[224:227]\n"
"s_min_i32 s19, s16, 0\n"
"ds_read_b128 v[216:219], v244 offset:1024\n"
"v_mfma_f32_16x16x32_bf16 a[240:243], v[236:239], v[128:131], a[240:243]\n"
"s_ashr_i32 s16, s16, 3\n"
"v_exp_f32 v188, v188\n"
"v_mfma_f32_16x16x32_bf16 a[244:247], v[236:239], v[136:139], a[244:247]\n"
"s_min_i32 s16, s16, 32\n"
"v_exp_f32 v189, v189\n"
"v_mfma_f32_16x16x32_bf16 a[228:231], v[232:235], v[136:139], a[228:231]\n"
"s_sub_u32 s16, s16, 1\n"
"s_cmp_gt_i32 s16, 0\n"
"s_cbranch_scc1 65533\n"
"v_mfma_f32_16x16x32_bf16 a[232:235], v[232:235], v[144:147], a[232:235]\n"
"s_and_b32 s19, s19, s43\n"
"ds_read_b128 v[220:223], v244 offset:1536\n"
"v_add_f32 v243, v188, v243\n"
"v_mfma_f32_16x16x32_bf16 a[248:251], v[236:239], v[144:147], a[248:251]\n"
"s_sub_u32 s18, s18, s19\n"
"v_exp_f32 v190, v190\n"
"v_mfma_f32_16x16x32_bf16 a[252:255], v[236:239], v[152:155], a[252:255]\n"
"v_exp_f32 v191, v191\n"
"v_mfma_f32_16x16x32_bf16 a[236:239], v[232:235], v[152:155], a[236:239]\n"
"s_waitcnt lgkmcnt(2)\n"
"v_add_f32 v243, v189, v243\n"
"v_add_f32 v243, v190, v243\n"
"v_mfma_f32_16x16x32_bf16 v[128:131], v[208:211], v[0:3], v[192:195]\n"
"s_cmp_lt_u32 s71, 14\n"
"s_cbranch_scc1 LP_%=\n"
"v_mfma_f32_16x16x32_bf16 v[128:131], v[212:215], v[4:7], v[128:131]\n"
"s_add_u32 m0, s45, s52\n"
"ds_read_b128 v[224:227], v244 offset:2048\n"
"v_add_f32 v243, v191, v243\n"
"v_mfma_f32_16x16x32_bf16 v[136:139], v[212:215], v[36:39], v[196:199]\n"
"buffer_load_dwordx4 v247, s[8:11], s62 offen nt lds\n"
"v_mfma_f32_16x16x32_bf16 v[136:139], v[208:211], v[32:35], v[136:139]\n"
"v_cvt_pk_bf16_f32 v186, v188, v189\n"
"v_cvt_pk_bf16_f32 v187, v190, v191\n"
"v_mfma_f32_16x16x32_bf16 v[144:147], v[208:211], v[64:67], v[200:203]\n"
"ds_read_b128 v[228:231], v244 offset:2560\n"
"v_mfma_f32_16x16x32_bf16 v[144:147], v[212:215], v[68:71], v[144:147]\n"
"v_mfma_f32_16x16x32_bf16 v[152:155], v[212:215], v[100:103], v[204:207]\n"
"v_exp_f32 v180, v180\n"
"v_mfma_f32_16x16x32_bf16 v[152:155], v[208:211], v[96:99], v[152:155]\n"
"s_waitcnt lgkmcnt(2)\n"
"v_exp_f32 v181, v181\n"
"v_mfma_f32_16x16x32_bf16 v[152:155], v[216:219], v[104:107], v[152:155]\n"
"ds_read_b128 v[232:235], v244 offset:3072\n"
"v_add_u32 v245, s63, v251\n"
"v_mfma_f32_16x16x32_bf16 v[152:155], v[220:223], v[108:111], v[152:155]\n"
"v_xor_b32 v246, 32, v245\n"
"v_add_f32 v242, v180, v242\n"
"v_mfma_f32_16x16x32_bf16 v[144:147], v[220:223], v[76:79], v[144:147]\n"
"s_add_u32 m0, s45, s53\n"
"v_exp_f32 v182, v182\n"
"v_mfma_f32_16x16x32_bf16 v[144:147], v[216:219], v[72:75], v[144:147]\n"
"buffer_load_dwordx4 v247, s[8:11], s62 offen offset:128 nt lds\n"
"v_mfma_f32_16x16x32_bf16 v[136:139], v[216:219], v[40:43], v[136:139]\n"
"ds_read_b128 v[236:239], v244 offset:3584\n"
"v_add_f32 v242, v181, v242\n"
"v_mfma_f32_16x16x32_bf16 v[136:139], v[220:223], v[44:47], v[136:139]\n"
"v_exp_f32 v183, v183\n"
"v_mfma_f32_16x16x32_bf16 v[128:131], v[220:223], v[12:15], v[128:131]\n"
"v_add_f32 v242, v182, v242\n"
"v_add_f32 v242, v183, v242\n"
"v_mfma_f32_16x16x32_bf16 v[128:131], v[216:219], v[8:11], v[128:131]\n"
"s_waitcnt lgkmcnt(2)\n"
"v_cvt_pk_bf16_f32 v178, v180, v181\n"
"v_cvt_pk_bf16_f32 v179, v182, v183\n"
"v_mfma_f32_16x16x32_bf16 v[128:131], v[224:227], v[16:19], v[128:131]\n"
"ds_read_b128 v[208:211], v244 offset:8192\n"
"v_mfma_f32_16x16x32_bf16 v[128:131], v[228:231], v[20:23], v[128:131]\n"
"v_mfma_f32_16x16x32_bf16 v[136:139], v[228:231], v[52:55], v[136:139]\n"
"v_mfma_f32_16x16x32_bf16 v[136:139], v[224:227], v[48:51], v[136:139]\n"
"v_mfma_f32_16x16x32_bf16 v[144:147], v[224:227], v[80:83], v[144:147]\n"
"s_add_u32 m0, s45, s54\n"
"ds_read_b128 v[212:215], v244 offset:8704\n"
"v_mfma_f32_16x16x32_bf16 v[144:147], v[228:231], v[84:87], v[144:147]\n"
"buffer_load_dwordx4 v247, s[8:11], s62 offen offset:256 nt lds\n"
"v_mfma_f32_16x16x32_bf16 v[152:155], v[228:231], v[116:119], v[152:155]\n"
"v_exp_f32 v172, v172\n"
"v_mfma_f32_16x16x32_bf16 v[152:155], v[224:227], v[112:115], v[152:155]\n"
"s_waitcnt lgkmcnt(2)\n"
"v_exp_f32 v173, v173\n"
"v_mfma_f32_16x16x32_bf16 v[152:155], v[232:235], v[120:123], v[152:155]\n"
"ds_read_b128 v[216:219], v244 offset:9216\n"
"v_add_f32 v241, v172, v241\n"
"v_mfma_f32_16x16x32_bf16 v[152:155], v[236:239], v[124:127], v[152:155]\n"
"v_exp_f32 v174, v174\n"
"v_mfma_f32_16x16x32_bf16 v[144:147], v[236:239], v[92:95], v[144:147]\n"
"v_exp_f32 v175, v175\n"
"v_mfma_f32_16x16x32_bf16 v[144:147], v[232:235], v[88:91], v[144:147]\n"
"v_add_f32 v241, v173, v241\n"
"v_add_f32 v241, v174, v241\n"
"v_mfma_f32_16x16x32_bf16 v[136:139], v[232:235], v[56:59], v[136:139]\n"
"ds_read_b128 v[220:223], v244 offset:9728\n"
"v_add_f32 v241, v175, v241\n"
"v_mfma_f32_16x16x32_bf16 v[136:139], v[236:239], v[60:63], v[136:139]\n"
"v_cvt_pk_bf16_f32 v170, v172, v173\n"
"v_cvt_pk_bf16_f32 v171, v174, v175\n"
"v_mfma_f32_16x16x32_bf16 v[128:131], v[236:239], v[28:31], v[128:131]\n"
"s_add_u32 m0, s45, s55\n"
"v_mfma_f32_16x16x32_bf16 v[128:131], v[232:235], v[24:27], v[128:131]\n"
"s_waitcnt lgkmcnt(2)\n"
"buffer_load_dwordx4 v247, s[8:11], s62 offen offset:384 nt lds\n"
"v_mfma_f32_16x16x32_bf16 v[132:135], v[208:211], v[0:3], v[192:195]\n"
"ds_read_b128 v[224:227], v244 offset:10240\n"
"v_mfma_f32_16x16x32_bf16 v[132:135], v[212:215], v[4:7], v[132:135]\n"
"v_mfma_f32_16x16x32_bf16 v[140:143], v[212:215], v[36:39], v[196:199]\n"
"v_exp_f32 v164, v164\n"
"v_mfma_f32_16x16x32_bf16 v[140:143], v[208:211], v[32:35], v[140:143]\n"
"v_exp_f32 v165, v165\n"
"v_mfma_f32_16x16x32_bf16 v[148:151], v[208:211], v[64:67], v[200:203]\n"
"ds_read_b128 v[228:231], v244 offset:10752\n"
"v_add_f32 v240, v164, v240\n"
"v_mfma_f32_16x16x32_bf16 v[148:151], v[212:215], v[68:71], v[148:151]\n"
"v_exp_f32 v166, v166\n"
"v_mfma_f32_16x16x32_bf16 v[156:159], v[212:215], v[100:103], v[204:207]\n"
"v_exp_f32 v167, v167\n"
"v_mfma_f32_16x16x32_bf16 v[156:159], v[208:211], v[96:99], v[156:159]\n"
"s_waitcnt lgkmcnt(2)\n"
"v_add_f32 v240, v165, v240\n"
"v_add_f32 v240, v166, v240\n"
"v_mfma_f32_16x16x32_bf16 v[156:159], v[216:219], v[104:107], v[156:159]\n"
"ds_read_b128 v[232:235], v244 offset:11264\n"
"v_add_f32 v240, v167, v240\n"
"v_mfma_f32_16x16x32_bf16 v[156:159], v[220:223], v[108:111], v[156:159]\n"
"s_add_u32 m0, s45, s56\n"
"v_cvt_pk_bf16_f32 v162, v164, v165\n"
"v_cvt_pk_bf16_f32 v163, v166, v167\n"
"v_mfma_f32_16x16x32_bf16 v[148:151], v[220:223], v[76:79], v[148:151]\n"
"buffer_load_dwordx4 v248, s[12:15], s34 offen lds\n"
"v_mfma_f32_16x16x32_bf16 v[148:151], v[216:219], v[72:75], v[148:151]\n"
"v_mfma_f32_16x16x32_bf16 v[140:143], v[216:219], v[40:43], v[140:143]\n"
"ds_read_b128 v[236:239], v244 offset:11776\n"
"v_mfma_f32_16x16x32_bf16 v[140:143], v[220:223], v[44:47], v[140:143]\n"
"v_mfma_f32_16x16x32_bf16 v[132:135], v[220:223], v[12:15], v[132:135]\n"
"v_mfma_f32_16x16x32_bf16 v[132:135], v[216:219], v[8:11], v[132:135]\n"
"s_waitcnt lgkmcnt(2)\n"
"v_mfma_f32_16x16x32_bf16 v[132:135], v[224:227], v[16:19], v[132:135]\n"
"ds_read_b64_tr_b16 v[208:209], v245 offset:16384\n"
"v_mfma_f32_16x16x32_bf16 v[132:135], v[228:231], v[20:23], v[132:135]\n"
"ds_read_b64_tr_b16 v[210:211], v245 offset:24576\n"
"v_mfma_f32_16x16x32_bf16 v[140:143], v[228:231], v[52:55], v[140:143]\n"
"v_mfma_f32_16x16x32_bf16 v[140:143], v[224:227], v[48:51], v[140:143]\n"
"s_add_u32 m0, s45, s57\n"
"v_mfma_f32_16x16x32_bf16 v[148:151], v[224:227], v[80:83], v[148:151]\n"
"buffer_load_dwordx4 v248, s[12:15], s34 offen offset:128 lds\n"
"v_mfma_f32_16x16x32_bf16 v[148:151], v[228:231], v[84:87], v[148:151]\n"
"ds_read_b64_tr_b16 v[212:213], v246 offset:16384\n"
"v_mfma_f32_16x16x32_bf16 v[156:159], v[228:231], v[116:119], v[156:159]\n"
"ds_read_b64_tr_b16 v[214:215], v246 offset:24576\n"
"v_mfma_f32_16x16x32_bf16 v[156:159], v[224:227], v[112:115], v[156:159]\n"
"s_waitcnt lgkmcnt(4)\n"
"v_mfma_f32_16x16x32_bf16 v[156:159], v[232:235], v[120:123], v[156:159]\n"
"ds_read_b64_tr_b16 v[216:217], v245 offset:16896\n"
"v_mfma_f32_16x16x32_bf16 v[156:159], v[236:239], v[124:127], v[156:159]\n"
"ds_read_b64_tr_b16 v[218:219], v245 offset:25088\n"
"v_mfma_f32_16x16x32_bf16 v[148:151], v[236:239], v[92:95], v[148:151]\n"
"v_mfma_f32_16x16x32_bf16 v[148:151], v[232:235], v[88:91], v[148:151]\n"
"v_mfma_f32_16x16x32_bf16 v[140:143], v[232:235], v[56:59], v[140:143]\n"
"ds_read_b64_tr_b16 v[220:221], v246 offset:16896\n"
"v_mfma_f32_16x16x32_bf16 v[140:143], v[236:239], v[60:63], v[140:143]\n"
"s_add_u32 m0, s45, s58\n"
"ds_read_b64_tr_b16 v[222:223], v246 offset:25088\n"
"v_mfma_f32_16x16x32_bf16 v[132:135], v[236:239], v[28:31], v[132:135]\n"
"buffer_load_dwordx4 v248, s[12:15], s34 offen offset:256 lds\n"
"v_mfma_f32_16x16x32_bf16 v[132:135], v[232:235], v[24:27], v[132:135]\n"
"s_waitcnt lgkmcnt(4)\n"
"v_mfma_f32_16x16x32_bf16 a[12:15], v[208:211], v[184:187], a[12:15]\n"
"ds_read_b64_tr_b16 v[224:225], v245 offset:17408\n"
"v_mfma_f32_16x16x32_bf16 a[28:31], v[212:215], v[184:187], a[28:31]\n"
"ds_read_b64_tr_b16 v[226:227], v245 offset:25600\n"
"v_mfma_f32_16x16x32_bf16 a[24:27], v[212:215], v[176:179], a[24:27]\n"
"v_exp_f32 v152, v152\n"
"v_mfma_f32_16x16x32_bf16 a[8:11], v[208:211], v[176:179], a[8:11]\n"
"v_exp_f32 v153, v153\n"
"v_mfma_f32_16x16x32_bf16 a[4:7], v[208:211], v[168:171], a[4:7]\n"
"ds_read_b64_tr_b16 v[228:229], v246 offset:17408\n"
"v_add_f32 v243, v152, v243\n"
"v_mfma_f32_16x16x32_bf16 a[20:23], v[212:215], v[168:171], a[20:23]\n"
"ds_read_b64_tr_b16 v[230:231], v246 offset:25600\n"
"v_add_f32 v243, v153, v243\n"
"v_mfma_f32_16x16x32_bf16 a[16:19], v[212:215], v[160:163], a[16:19]\n"
"v_exp_f32 v154, v154\n"
"v_mfma_f32_16x16x32_bf16 a[0:3], v[208:211], v[160:163], a[0:3]\n"
"s_waitcnt lgkmcnt(4)\n"
"v_exp_f32 v155, v155\n"
"v_mfma_f32_16x16x32_bf16 a[32:35], v[216:219], v[160:163], a[32:35]\n"
"s_add_u32 m0, s45, s59\n"
"ds_read_b64_tr_b16 v[232:233], v245 offset:17920\n"
"v_add_f32 v243, v154, v243\n"
"v_mfma_f32_16x16x32_bf16 a[48:51], v[220:223], v[160:163], a[48:51]\n"
"buffer_load_dwordx4 v248, s[12:15], s34 offen offset:384 lds\n"
"v_mfma_f32_16x16x32_bf16 a[52:55], v[220:223], v[168:171], a[52:55]\n"
"ds_read_b64_tr_b16 v[234:235], v245 offset:26112\n"
"v_add_f32 v243, v155, v243\n"
"v_mfma_f32_16x16x32_bf16 a[36:39], v[216:219], v[168:171], a[36:39]\n"
"v_cvt_pk_bf16_f32 v152, v152, v153\n"
"v_cvt_pk_bf16_f32 v153, v154, v155\n"
"v_mfma_f32_16x16x32_bf16 a[40:43], v[216:219], v[176:179], a[40:43]\n"
"ds_read_b64_tr_b16 v[236:237], v246 offset:17920\n"
"v_mfma_f32_16x16x32_bf16 a[56:59], v[220:223], v[176:179], a[56:59]\n"
"ds_read_b64_tr_b16 v[238:239], v246 offset:26112\n"
"v_mfma_f32_16x16x32_bf16 a[60:63], v[220:223], v[184:187], a[60:63]\n"
"v_exp_f32 v144, v144\n"
"v_mfma_f32_16x16x32_bf16 a[44:47], v[216:219], v[184:187], a[44:47]\n"
"s_waitcnt lgkmcnt(4)\n"
"v_exp_f32 v145, v145\n"
"v_mfma_f32_16x16x32_bf16 a[76:79], v[224:227], v[184:187], a[76:79]\n"
"ds_read_b64_tr_b16 v[208:209], v245 offset:18432\n"
"v_add_f32 v242, v144, v242\n"
"v_mfma_f32_16x16x32_bf16 a[92:95], v[228:231], v[184:187], a[92:95]\n"
"ds_read_b64_tr_b16 v[210:211], v245 offset:26624\n"
"v_add_f32 v242, v145, v242\n"
"v_mfma_f32_16x16x32_bf16 a[88:91], v[228:231], v[176:179], a[88:91]\n"
"v_exp_f32 v146, v146\n"
"v_mfma_f32_16x16x32_bf16 a[72:75], v[224:227], v[176:179], a[72:75]\n"
"v_exp_f32 v147, v147\n"
"v_mfma_f32_16x16x32_bf16 a[68:71], v[224:227], v[168:171], a[68:71]\n"
"s_add_u32 s64, s63, 32768\n"
"ds_read_b64_tr_b16 v[212:213], v246 offset:18432\n"
"v_add_f32 v242, v146, v242\n"
"v_mfma_f32_16x16x32_bf16 a[84:87], v[228:231], v[168:171], a[84:87]\n"
"s_cmp_eq_u32 s64, 163840\n"
"ds_read_b64_tr_b16 v[214:215], v246 offset:26624\n"
"v_add_f32 v242, v147, v242\n"
"v_mfma_f32_16x16x32_bf16 a[80:83], v[228:231], v[160:163], a[80:83]\n"
"IWC3_%=:\n"
"s_cbranch_execz IRC_%=\n"
"s_cselect_b32 s64, 0, s64\n"
"v_cvt_pk_bf16_f32 v144, v144, v145\n"
"v_cvt_pk_bf16_f32 v145, v146, v147\n"
"v_mfma_f32_16x16x32_bf16 a[64:67], v[224:227], v[160:163], a[64:67]\n"
"s_waitcnt lgkmcnt(4)\n"
"v_add_u32 v244, s64, v250\n"
"v_mfma_f32_16x16x32_bf16 a[96:99], v[232:235], v[160:163], a[96:99]\n"
"s_mov_b32 s34, s62\n"
"ds_read_b64_tr_b16 v[216:217], v245 offset:18944\n"
"v_mfma_f32_16x16x32_bf16 a[112:115], v[236:239], v[160:163], a[112:115]\n"
"s_add_u32 s62, s62, 16384\n"
"ds_read_b64_tr_b16 v[218:219], v245 offset:27136\n"
"v_mfma_f32_16x16x32_bf16 a[116:119], v[236:239], v[168:171], a[116:119]\n"
"s_and_b32 s62, s62, 524287\n"
"v_mfma_f32_16x16x32_bf16 a[100:103], v[232:235], v[168:171], a[100:103]\n"
"s_add_u32 s45, s45, 32768\n"
"v_mfma_f32_16x16x32_bf16 a[104:107], v[232:235], v[176:179], a[104:107]\n"
"s_cmp_eq_u32 s45, 163840\n"
"ds_read_b64_tr_b16 v[220:221], v246 offset:18944\n"
"v_mfma_f32_16x16x32_bf16 a[120:123], v[236:239], v[176:179], a[120:123]\n"
"s_cselect_b32 s45, 0, s45\n"
"ds_read_b64_tr_b16 v[222:223], v246 offset:27136\n"
"v_mfma_f32_16x16x32_bf16 a[124:127], v[236:239], v[184:187], a[124:127]\n"
"s_sub_u32 s61, s61, 1\n"
"v_exp_f32 v136, v136\n"
"v_mfma_f32_16x16x32_bf16 a[108:111], v[232:235], v[184:187], a[108:111]\n"
"s_waitcnt lgkmcnt(4)\n"
"v_exp_f32 v137, v137\n"
"v_mfma_f32_16x16x32_bf16 a[140:143], v[208:211], v[184:187], a[140:143]\n"
"s_cmp_eq_u32 s61, 0\n"
"ds_read_b64_tr_b16 v[224:225], v245 offset:19456\n"
"v_add_f32 v241, v136, v241\n"
"v_mfma_f32_16x16x32_bf16 a[156:159], v[212:215], v[184:187], a[156:159]\n"
"s_cselect_b32 s47, 1, 0\n"
"ds_read_b64_tr_b16 v[226:227], v245 offset:27648\n"
"v_add_f32 v241, v137, v241\n"
"v_mfma_f32_16x16x32_bf16 a[152:155], v[212:215], v[176:179], a[152:155]\n"
"s_cselect_b32 s61, 12, s61\n"
"v_exp_f32 v138, v138\n"
"v_mfma_f32_16x16x32_bf16 a[136:139], v[208:211], v[176:179], a[136:139]\n"
"s_cmp_lt_u32 s44, 2\n"
"v_exp_f32 v139, v139\n"
"v_mfma_f32_16x16x32_bf16 a[132:135], v[208:211], v[168:171], a[132:135]\n"
"s_cselect_b32 s47, s47, 0\n"
"ds_read_b64_tr_b16 v[228:229], v246 offset:19456\n"
"v_add_f32 v241, v138, v241\n"
"v_mfma_f32_16x16x32_bf16 a[148:151], v[212:215], v[168:171], a[148:151]\n"
"s_add_u32 s18, s18, s35\n"
"ds_read_b64_tr_b16 v[230:231], v246 offset:27648\n"
"v_add_f32 v241, v139, v241\n"
"v_mfma_f32_16x16x32_bf16 a[144:147], v[212:215], v[160:163], a[144:147]\n"
"v_cvt_pk_bf16_f32 v136, v136, v137\n"
"v_cvt_pk_bf16_f32 v137, v138, v139\n"
"v_mfma_f32_16x16x32_bf16 a[128:131], v[208:211], v[160:163], a[128:131]\n"
"s_waitcnt lgkmcnt(4)\n"
"v_mfma_f32_16x16x32_bf16 a[160:163], v[216:219], v[160:163], a[160:163]\n"
"s_memrealtime s[16:17]\n"
"ds_read_b64_tr_b16 v[232:233], v245 offset:19968\n"
"v_mfma_f32_16x16x32_bf16 a[176:179], v[220:223], v[160:163], a[176:179]\n"
"ds_read_b64_tr_b16 v[234:235], v245 offset:28160\n"
"v_mfma_f32_16x16x32_bf16 a[180:183], v[220:223], v[168:171], a[180:183]\n"
"v_exp_f32 v128, v128\n"
"v_mfma_f32_16x16x32_bf16 a[164:167], v[216:219], v[168:171], a[164:167]\n"
"v_exp_f32 v129, v129\n"
"v_mfma_f32_16x16x32_bf16 a[168:171], v[216:219], v[176:179], a[168:171]\n"
"ds_read_b64_tr_b16 v[236:237], v246 offset:19968\n"
"v_add_f32 v240, v128, v240\n"
"v_mfma_f32_16x16x32_bf16 a[184:187], v[220:223], v[176:179], a[184:187]\n"
"ds_read_b64_tr_b16 v[238:239], v246 offset:28160\n"
"v_add_f32 v240, v129, v240\n"
"v_mfma_f32_16x16x32_bf16 a[188:191], v[220:223], v[184:187], a[188:191]\n"
"v_exp_f32 v130, v130\n"
"v_mfma_f32_16x16x32_bf16 a[172:175], v[216:219], v[184:187], a[172:175]\n"
"s_waitcnt lgkmcnt(4)\n"
"v_exp_f32 v131, v131\n"
"v_mfma_f32_16x16x32_bf16 a[204:207], v[224:227], v[184:187], a[204:207]\n"
"v_add_f32 v240, v130, v240\n"
"v_add_f32 v240, v131, v240\n"
"v_mfma_f32_16x16x32_bf16 a[220:223], v[228:231], v[184:187], a[220:223]\n"
"v_cvt_pk_bf16_f32 v128, v128, v129\n"
"v_cvt_pk_bf16_f32 v129, v130, v131\n"
"v_mfma_f32_16x16x32_bf16 a[216:219], v[228:231], v[176:179], a[216:219]\n"
"v_mfma_f32_16x16x32_bf16 a[200:203], v[224:227], v[176:179], a[200:203]\n"
"s_mov_b32 s49, 0\n"
"v_mfma_f32_16x16x32_bf16 a[196:199], v[224:227], v[168:171], a[196:199]\n"
"s_or_b32 s48, s48, s49\n"
"s_cbranch_scc1 RSP29_%=\n"
"RRP29_%=:\n"
"v_mfma_f32_16x16x32_bf16 a[212:215], v[228:231], v[168:171], a[212:215]\n"
"s_waitcnt vmcnt(9) lgkmcnt(0)\n"
"s_barrier\n"
"v_mfma_f32_16x16x32_bf16 a[208:211], v[228:231], v[160:163], a[208:211]\n"
"s_lshl_b32 s16, s16, 2\n"
"ds_read_b128 v[208:211], v244 offset:0\n"
"v_mfma_f32_16x16x32_bf16 a[192:195], v[224:227], v[160:163], a[192:195]\n"
"s_sub_u32 s16, s18, s16\n"
"ds_read_b128 v[212:215], v244 offset:512\n"
"v_mfma_f32_16x16x32_bf16 a[224:227], v[232:235], v[160:163], a[224:227]\n"
"s_min_i32 s19, s16, 0\n"
"ds_read_b128 v[216:219], v244 offset:1024\n"
"v_mfma_f32_16x16x32_bf16 a[240:243], v[236:239], v[160:163], a[240:243]\n"
"s_ashr_i32 s16, s16, 3\n"
"v_exp_f32 v156, v156\n"
"v_mfma_f32_16x16x32_bf16 a[244:247], v[236:239], v[168:171], a[244:247]\n"
"s_min_i32 s16, s16, 32\n"
"v_exp_f32 v157, v157\n"
"v_mfma_f32_16x16x32_bf16 a[228:231], v[232:235], v[168:171], a[228:231]\n"
"s_sub_u32 s16, s16, 1\n"
"s_cmp_gt_i32 s16, 0\n"
"s_cbranch_scc1 65533\n"
"v_mfma_f32_16x16x32_bf16 a[232:235], v[232:235], v[176:179], a[232:235]\n"
"s_and_b32 s19, s19, s43\n"
"ds_read_b128 v[220:223], v244 offset:1536\n"
"v_add_f32 v243, v156, v243\n"
"v_mfma_f32_16x16x32_bf16 a[248:251], v[236:239], v[176:179], a[248:251]\n"
"s_sub_u32 s18, s18, s19\n"
"v_exp_f32 v158, v158\n"
"v_mfma_f32_16x16x32_bf16 a[252:255], v[236:239], v[184:187], a[252:255]\n"
"v_exp_f32 v159, v159\n"
"v_mfma_f32_16x16x32_bf16 a[236:239], v[232:235], v[184:187], a[236:239]\n"
"s_waitcnt lgkmcnt(2)\n"
"v_add_f32 v243, v157, v243\n"
"v_add_f32 v243, v158, v243\n"
"v_mfma_f32_16x16x32_bf16 v[160:163], v[208:211], v[0:3], v[192:195]\n"
"s_add_u32 m0, s45, s52\n"
"ds_read_b128 v[224:227], v244 offset:2048\n"
"v_add_f32 v243, v159, v243\n"
"v_mfma_f32_16x16x32_bf16 v[160:163], v[212:215], v[4:7], v[160:163]\n"
"buffer_load_dwordx4 v247, s[8:11], s62 offen nt lds\n"
"v_mfma_f32_16x16x32_bf16 v[168:171], v[212:215], v[36:39], v[196:199]\n"
"v_cvt_pk_bf16_f32 v154, v156, v157\n"
"v_cvt_pk_bf16_f32 v155, v158, v159\n"
"v_mfma_f32_16x16x32_bf16 v[168:171], v[208:211], v[32:35], v[168:171]\n"
"v_mfma_f32_16x16x32_bf16 v[176:179], v[208:211], v[64:67], v[200:203]\n"
"ds_read_b128 v[228:231], v244 offset:2560\n"
"v_mfma_f32_16x16x32_bf16 v[176:179], v[212:215], v[68:71], v[176:179]\n"
"v_mfma_f32_16x16x32_bf16 v[184:187], v[212:215], v[100:103], v[204:207]\n"
"v_exp_f32 v148, v148\n"
"v_mfma_f32_16x16x32_bf16 v[184:187], v[208:211], v[96:99], v[184:187]\n"
"s_waitcnt lgkmcnt(2)\n"
"v_exp_f32 v149, v149\n"
"v_mfma_f32_16x16x32_bf16 v[184:187], v[216:219], v[104:107], v[184:187]\n"
"ds_read_b128 v[232:235], v244 offset:3072\n"
"v_add_u32 v245, s64, v251\n"
"v_mfma_f32_16x16x32_bf16 v[184:187], v[220:223], v[108:111], v[184:187]\n"
"v_xor_b32 v246, 32, v245\n"
"v_add_f32 v242, v148, v242\n"
"v_mfma_f32_16x16x32_bf16 v[176:179], v[220:223], v[76:79], v[176:179]\n"
"s_add_u32 m0, s45, s53\n"
"v_exp_f32 v150, v150\n"
"v_mfma_f32_16x16x32_bf16 v[176:179], v[216:219], v[72:75], v[176:179]\n"
"buffer_load_dwordx4 v247, s[8:11], s62 offen offset:128 nt lds\n"
"v_mfma_f32_16x16x32_bf16 v[168:171], v[216:219], v[40:43], v[168:171]\n"
"ds_read_b128 v[236:239], v244 offset:3584\n"
"v_add_f32 v242, v149, v242\n"
"v_mfma_f32_16x16x32_bf16 v[168:171], v[220:223], v[44:47], v[168:171]\n"
"v_exp_f32 v151, v151\n"
"v_mfma_f32_16x16x32_bf16 v[160:163], v[220:223], v[12:15], v[160:163]\n"
"v_add_f32 v242, v150, v242\n"
"v_add_f32 v242, v151, v242\n"
"v_mfma_f32_16x16x32_bf16 v[160:163], v[216:219], v[8:11], v[160:163]\n"
"s_waitcnt lgkmcnt(2)\n"
"v_cvt_pk_bf16_f32 v146, v148, v149\n"
"v_cvt_pk_bf16_f32 v147, v150, v151\n"
"v_mfma_f32_16x16x32_bf16 v[160:163], v[224:227], v[16:19], v[160:163]\n"
"ds_read_b128 v[208:211], v244 offset:8192\n"
"v_mfma_f32_16x16x32_bf16 v[160:163], v[228:231], v[20:23], v[160:163]\n"
"v_mfma_f32_16x16x32_bf16 v[168:171], v[228:231], v[52:55], v[168:171]\n"
"v_mfma_f32_16x16x32_bf16 v[168:171], v[224:227], v[48:51], v[168:171]\n"
"v_mfma_f32_16x16x32_bf16 v[176:179], v[224:227], v[80:83], v[176:179]\n"
"s_add_u32 m0, s45, s54\n"
"ds_read_b128 v[212:215], v244 offset:8704\n"
"v_mfma_f32_16x16x32_bf16 v[176:179], v[228:231], v[84:87], v[176:179]\n"
"buffer_load_dwordx4 v247, s[8:11], s62 offen offset:256 nt lds\n"
"v_mfma_f32_16x16x32_bf16 v[184:187], v[228:231], v[116:119], v[184:187]\n"
"v_exp_f32 v140, v140\n"
"v_mfma_f32_16x16x32_bf16 v[184:187], v[224:227], v[112:115], v[184:187]\n"
"s_waitcnt lgkmcnt(2)\n"
"v_exp_f32 v141, v141\n"
"v_mfma_f32_16x16x32_bf16 v[184:187], v[232:235], v[120:123], v[184:187]\n"
"ds_read_b128 v[216:219], v244 offset:9216\n"
"v_add_f32 v241, v140, v241\n"
"v_mfma_f32_16x16x32_bf16 v[184:187], v[236:239], v[124:127], v[184:187]\n"
"v_exp_f32 v142, v142\n"
"v_mfma_f32_16x16x32_bf16 v[176:179], v[236:239], v[92:95], v[176:179]\n"
"v_exp_f32 v143, v143\n"
"v_mfma_f32_16x16x32_bf16 v[176:179], v[232:235], v[88:91], v[176:179]\n"
"v_add_f32 v241, v141, v241\n"
"v_add_f32 v241, v142, v241\n"
"v_mfma_f32_16x16x32_bf16 v[168:171], v[232:235], v[56:59], v[168:171]\n"
"ds_read_b128 v[220:223], v244 offset:9728\n"
"v_add_f32 v241, v143, v241\n"
"v_mfma_f32_16x16x32_bf16 v[168:171], v[236:239], v[60:63], v[168:171]\n"
"v_cvt_pk_bf16_f32 v138, v140, v141\n"
"v_cvt_pk_bf16_f32 v139, v142, v143\n"
"v_mfma_f32_16x16x32_bf16 v[160:163], v[236:239], v[28:31], v[160:163]\n"
"s_add_u32 m0, s45, s55\n"
"v_mfma_f32_16x16x32_bf16 v[160:163], v[232:235], v[24:27], v[160:163]\n"
"s_waitcnt lgkmcnt(2)\n"
"buffer_load_dwordx4 v247, s[8:11], s62 offen offset:384 nt lds\n"
"v_mfma_f32_16x16x32_bf16 v[164:167], v[208:211], v[0:3], v[192:195]\n"
"ds_read_b128 v[224:227], v244 offset:10240\n"
"v_mfma_f32_16x16x32_bf16 v[164:167], v[212:215], v[4:7], v[164:167]\n"
"v_mfma_f32_16x16x32_bf16 v[172:175], v[212:215], v[36:39], v[196:199]\n"
"v_exp_f32 v132, v132\n"
"v_mfma_f32_16x16x32_bf16 v[172:175], v[208:211], v[32:35], v[172:175]\n"
"v_exp_f32 v133, v133\n"
"v_mfma_f32_16x16x32_bf16 v[180:183], v[208:211], v[64:67], v[200:203]\n"
"ds_read_b128 v[228:231], v244 offset:10752\n"
"v_add_f32 v240, v132, v240\n"
"v_mfma_f32_16x16x32_bf16 v[180:183], v[212:215], v[68:71], v[180:183]\n"
"v_exp_f32 v134, v134\n"
"v_mfma_f32_16x16x32_bf16 v[188:191], v[212:215], v[100:103], v[204:207]\n"
"v_exp_f32 v135, v135\n"
"v_mfma_f32_16x16x32_bf16 v[188:191], v[208:211], v[96:99], v[188:191]\n"
"s_waitcnt lgkmcnt(2)\n"
"v_add_f32 v240, v133, v240\n"
"v_add_f32 v240, v134, v240\n"
"v_mfma_f32_16x16x32_bf16 v[188:191], v[216:219], v[104:107], v[188:191]\n"
"ds_read_b128 v[232:235], v244 offset:11264\n"
"v_add_f32 v240, v135, v240\n"
"v_mfma_f32_16x16x32_bf16 v[188:191], v[220:223], v[108:111], v[188:191]\n"
"s_add_u32 m0, s45, s56\n"
"v_cvt_pk_bf16_f32 v130, v132, v133\n"
"v_cvt_pk_bf16_f32 v131, v134, v135\n"
"v_mfma_f32_16x16x32_bf16 v[180:183], v[220:223], v[76:79], v[180:183]\n"
"buffer_load_dwordx4 v248, s[12:15], s34 offen lds\n"
"v_mfma_f32_16x16x32_bf16 v[180:183], v[216:219], v[72:75], v[180:183]\n"
"v_mfma_f32_16x16x32_bf16 v[172:175], v[216:219], v[40:43], v[172:175]\n"
"ds_read_b128 v[236:239], v244 offset:11776\n"
"v_mfma_f32_16x16x32_bf16 v[172:175], v[220:223], v[44:47], v[172:175]\n"
"v_mfma_f32_16x16x32_bf16 v[164:167], v[220:223], v[12:15], v[164:167]\n"
"v_mfma_f32_16x16x32_bf16 v[164:167], v[216:219], v[8:11], v[164:167]\n"
"s_waitcnt lgkmcnt(2)\n"
"v_mfma_f32_16x16x32_bf16 v[164:167], v[224:227], v[16:19], v[164:167]\n"
"ds_read_b64_tr_b16 v[208:209], v245 offset:16384\n"
"v_mfma_f32_16x16x32_bf16 v[164:167], v[228:231], v[20:23], v[164:167]\n"
"ds_read_b64_tr_b16 v[210:211], v245 offset:24576\n"
"v_mfma_f32_16x16x32_bf16 v[172:175], v[228:231], v[52:55], v[172:175]\n"
"v_mfma_f32_16x16x32_bf16 v[172:175], v[224:227], v[48:51], v[172:175]\n"
"s_add_u32 m0, s45, s57\n"
"v_mfma_f32_16x16x32_bf16 v[180:183], v[224:227], v[80:83], v[180:183]\n"
"buffer_load_dwordx4 v248, s[12:15], s34 offen offset:128 lds\n"
"v_mfma_f32_16x16x32_bf16 v[180:183], v[228:231], v[84:87], v[180:183]\n"
"ds_read_b64_tr_b16 v[212:213], v246 offset:16384\n"
"v_mfma_f32_16x16x32_bf16 v[188:191], v[228:231], v[116:119], v[188:191]\n"
"ds_read_b64_tr_b16 v[214:215], v246 offset:24576\n"
"v_mfma_f32_16x16x32_bf16 v[188:191], v[224:227], v[112:115], v[188:191]\n"
"s_waitcnt lgkmcnt(4)\n"
"v_mfma_f32_16x16x32_bf16 v[188:191], v[232:235], v[120:123], v[188:191]\n"
"ds_read_b64_tr_b16 v[216:217], v245 offset:16896\n"
"v_mfma_f32_16x16x32_bf16 v[188:191], v[236:239], v[124:127], v[188:191]\n"
"ds_read_b64_tr_b16 v[218:219], v245 offset:25088\n"
"v_mfma_f32_16x16x32_bf16 v[180:183], v[236:239], v[92:95], v[180:183]\n"
"v_mfma_f32_16x16x32_bf16 v[180:183], v[232:235], v[88:91], v[180:183]\n"
"v_mfma_f32_16x16x32_bf16 v[172:175], v[232:235], v[56:59], v[172:175]\n"
"ds_read_b64_tr_b16 v[220:221], v246 offset:16896\n"
"v_mfma_f32_16x16x32_bf16 v[172:175], v[236:239], v[60:63], v[172:175]\n"
"s_add_u32 m0, s45, s58\n"
"ds_read_b64_tr_b16 v[222:223], v246 offset:25088\n"
"v_mfma_f32_16x16x32_bf16 v[164:167], v[236:239], v[28:31], v[164:167]\n"
"buffer_load_dwordx4 v248, s[12:15], s34 offen offset:256 lds\n"
"v_mfma_f32_16x16x32_bf16 v[164:167], v[232:235], v[24:27], v[164:167]\n"
"s_waitcnt lgkmcnt(4)\n"
"v_mfma_f32_16x16x32_bf16 a[12:15], v[208:211], v[152:155], a[12:15]\n"
"ds_read_b64_tr_b16 v[224:225], v245 offset:17408\n"
"v_mfma_f32_16x16x32_bf16 a[28:31], v[212:215], v[152:155], a[28:31]\n"
"ds_read_b64_tr_b16 v[226:227], v245 offset:25600\n"
"v_mfma_f32_16x16x32_bf16 a[24:27], v[212:215], v[144:147], a[24:27]\n"
"v_exp_f32 v184, v184\n"
"v_mfma_f32_16x16x32_bf16 a[8:11], v[208:211], v[144:147], a[8:11]\n"
"v_exp_f32 v185, v185\n"
"v_mfma_f32_16x16x32_bf16 a[4:7], v[208:211], v[136:139], a[4:7]\n"
"ds_read_b64_tr_b16 v[228:229], v246 offset:17408\n"
"v_add_f32 v243, v184, v243\n"
"v_mfma_f32_16x16x32_bf16 a[20:23], v[212:215], v[136:139], a[20:23]\n"
"ds_read_b64_tr_b16 v[230:231], v246 offset:25600\n"
"v_add_f32 v243, v185, v243\n"
"v_mfma_f32_16x16x32_bf16 a[16:19], v[212:215], v[128:131], a[16:19]\n"
"v_exp_f32 v186, v186\n"
"v_mfma_f32_16x16x32_bf16 a[0:3], v[208:211], v[128:131], a[0:3]\n"
"s_waitcnt lgkmcnt(4)\n"
"v_exp_f32 v187, v187\n"
"v_mfma_f32_16x16x32_bf16 a[32:35], v[216:219], v[128:131], a[32:35]\n"
"s_add_u32 m0, s45, s59\n"
"ds_read_b64_tr_b16 v[232:233], v245 offset:17920\n"
"v_add_f32 v243, v186, v243\n"
"v_mfma_f32_16x16x32_bf16 a[48:51], v[220:223], v[128:131], a[48:51]\n"
"buffer_load_dwordx4 v248, s[12:15], s34 offen offset:384 lds\n"
"v_mfma_f32_16x16x32_bf16 a[52:55], v[220:223], v[136:139], a[52:55]\n"
"ds_read_b64_tr_b16 v[234:235], v245 offset:26112\n"
"v_add_f32 v243, v187, v243\n"
"v_mfma_f32_16x16x32_bf16 a[36:39], v[216:219], v[136:139], a[36:39]\n"
"v_cvt_pk_bf16_f32 v184, v184, v185\n"
"v_cvt_pk_bf16_f32 v185, v186, v187\n"
"v_mfma_f32_16x16x32_bf16 a[40:43], v[216:219], v[144:147], a[40:43]\n"
"ds_read_b64_tr_b16 v[236:237], v246 offset:17920\n"
"v_mfma_f32_16x16x32_bf16 a[56:59], v[220:223], v[144:147], a[56:59]\n"
"ds_read_b64_tr_b16 v[238:239], v246 offset:26112\n"
"v_mfma_f32_16x16x32_bf16 a[60:63], v[220:223], v[152:155], a[60:63]\n"
"v_exp_f32 v176, v176\n"
"v_mfma_f32_16x16x32_bf16 a[44:47], v[216:219], v[152:155], a[44:47]\n"
"s_waitcnt lgkmcnt(4)\n"
"v_exp_f32 v177, v177\n"
"v_mfma_f32_16x16x32_bf16 a[76:79], v[224:227], v[152:155], a[76:79]\n"
"ds_read_b64_tr_b16 v[208:209], v245 offset:18432\n"
"v_add_f32 v242, v176, v242\n"
"v_mfma_f32_16x16x32_bf16 a[92:95], v[228:231], v[152:155], a[92:95]\n"
"ds_read_b64_tr_b16 v[210:211], v245 offset:26624\n"
"v_add_f32 v242, v177, v242\n"
"v_mfma_f32_16x16x32_bf16 a[88:91], v[228:231], v[144:147], a[88:91]\n"
"v_exp_f32 v178, v178\n"
"v_mfma_f32_16x16x32_bf16 a[72:75], v[224:227], v[144:147], a[72:75]\n"
"v_exp_f32 v179, v179\n"
"v_mfma_f32_16x16x32_bf16 a[68:71], v[224:227], v[136:139], a[68:71]\n"
"s_add_u32 s63, s64, 32768\n"
"ds_read_b64_tr_b16 v[212:213], v246 offset:18432\n"
"v_add_f32 v242, v178, v242\n"
"v_mfma_f32_16x16x32_bf16 a[84:87], v[228:231], v[136:139], a[84:87]\n"
"s_cmp_eq_u32 s63, 163840\n"
"ds_read_b64_tr_b16 v[214:215], v246 offset:26624\n"
"v_add_f32 v242, v179, v242\n"
"v_mfma_f32_16x16x32_bf16 a[80:83], v[228:231], v[128:131], a[80:83]\n"
"s_cselect_b32 s63, 0, s63\n"
"v_cvt_pk_bf16_f32 v176, v176, v177\n"
"v_cvt_pk_bf16_f32 v177, v178, v179\n"
"v_mfma_f32_16x16x32_bf16 a[64:67], v[224:227], v[128:131], a[64:67]\n"
"s_waitcnt lgkmcnt(4)\n"
"v_add_u32 v244, s63, v250\n"
"v_mfma_f32_16x16x32_bf16 a[96:99], v[232:235], v[128:131], a[96:99]\n"
"s_mov_b32 s34, s62\n"
"ds_read_b64_tr_b16 v[216:217], v245 offset:18944\n"
"v_mfma_f32_16x16x32_bf16 a[112:115], v[236:239], v[128:131], a[112:115]\n"
"s_add_u32 s62, s62, 16384\n"
"ds_read_b64_tr_b16 v[218:219], v245 offset:27136\n"
"v_mfma_f32_16x16x32_bf16 a[116:119], v[236:239], v[136:139], a[116:119]\n"
"s_and_b32 s62, s62, 524287\n"
"v_mfma_f32_16x16x32_bf16 a[100:103], v[232:235], v[136:139], a[100:103]\n"
"s_add_u32 s45, s45, 32768\n"
"v_mfma_f32_16x16x32_bf16 a[104:107], v[232:235], v[144:147], a[104:107]\n"
"s_cmp_eq_u32 s45, 163840\n"
"ds_read_b64_tr_b16 v[220:221], v246 offset:18944\n"
"v_mfma_f32_16x16x32_bf16 a[120:123], v[236:239], v[144:147], a[120:123]\n"
"s_cselect_b32 s45, 0, s45\n"
"ds_read_b64_tr_b16 v[222:223], v246 offset:27136\n"
"v_mfma_f32_16x16x32_bf16 a[124:127], v[236:239], v[152:155], a[124:127]\n"
"s_add_u32 s18, s18, s35\n"
"v_exp_f32 v168, v168\n"
"v_mfma_f32_16x16x32_bf16 a[108:111], v[232:235], v[152:155], a[108:111]\n"
"s_waitcnt lgkmcnt(4)\n"
"v_exp_f32 v169, v169\n"
"v_mfma_f32_16x16x32_bf16 a[140:143], v[208:211], v[152:155], a[140:143]\n"
"s_add_u32 s71, s71, 1\n"
"ds_read_b64_tr_b16 v[224:225], v245 offset:19456\n"
"v_add_f32 v241, v168, v241\n"
"v_mfma_f32_16x16x32_bf16 a[156:159], v[212:215], v[152:155], a[156:159]\n"
"ds_read_b64_tr_b16 v[226:227], v245 offset:27648\n"
"v_add_f32 v241, v169, v241\n"
"v_mfma_f32_16x16x32_bf16 a[152:155], v[212:215], v[144:147], a[152:155]\n"
"v_exp_f32 v170, v170\n"
"v_mfma_f32_16x16x32_bf16 a[136:139], v[208:211], v[144:147], a[136:139]\n"
"v_exp_f32 v171, v171\n"
"v_mfma_f32_16x16x32_bf16 a[132:135], v[208:211], v[136:139], a[132:135]\n"
"ds_read_b64_tr_b16 v[228:229], v246 offset:19456\n"
"v_add_f32 v241, v170, v241\n"
"v_mfma_f32_16x16x32_bf16 a[148:151], v[212:215], v[136:139], a[148:151]\n"
"ds_read_b64_tr_b16 v[230:231], v246 offset:27648\n"
"v_add_f32 v241, v171, v241\n"
"v_mfma_f32_16x16x32_bf16 a[144:147], v[212:215], v[128:131], a[144:147]\n"
"v_cvt_pk_bf16_f32 v168, v168, v169\n"
"v_cvt_pk_bf16_f32 v169, v170, v171\n"
"v_mfma_f32_16x16x32_bf16 a[128:131], v[208:211], v[128:131], a[128:131]\n"
"s_waitcnt lgkmcnt(4)\n"
"v_mfma_f32_16x16x32_bf16 a[160:163], v[216:219], v[128:131], a[160:163]\n"
"s_memrealtime s[16:17]\n"
"ds_read_b64_tr_b16 v[232:233], v245 offset:19968\n"
"v_mfma_f32_16x16x32_bf16 a[176:179], v[220:223], v[128:131], a[176:179]\n"
"ds_read_b64_tr_b16 v[234:235], v245 offset:28160\n"
"v_mfma_f32_16x16x32_bf16 a[180:183], v[220:223], v[136:139], a[180:183]\n"
"v_exp_f32 v160, v160\n"
"v_mfma_f32_16x16x32_bf16 a[164:167], v[216:219], v[136:139], a[164:167]\n"
"v_exp_f32 v161, v161\n"
"v_mfma_f32_16x16x32_bf16 a[168:171], v[216:219], v[144:147], a[168:171]\n"
"ds_read_b64_tr_b16 v[236:237], v246 offset:19968\n"
"v_add_f32 v240, v160, v240\n"
"v_mfma_f32_16x16x32_bf16 a[184:187], v[220:223], v[144:147], a[184:187]\n"
"ds_read_b64_tr_b16 v[238:239], v246 offset:28160\n"
"v_add_f32 v240, v161, v240\n"
"v_mfma_f32_16x16x32_bf16 a[188:191], v[220:223], v[152:155], a[188:191]\n"
"v_exp_f32 v162, v162\n"
"v_mfma_f32_16x16x32_bf16 a[172:175], v[216:219], v[152:155], a[172:175]\n"
"s_waitcnt lgkmcnt(4)\n"
"v_exp_f32 v163, v163\n"
"v_mfma_f32_16x16x32_bf16 a[204:207], v[224:227], v[152:155], a[204:207]\n"
"v_add_f32 v240, v162, v240\n"
"v_add_f32 v240, v163, v240\n"
"v_mfma_f32_16x16x32_bf16 a[220:223], v[228:231], v[152:155], a[220:223]\n"
"v_cvt_pk_bf16_f32 v160, v160, v161\n"
"v_cvt_pk_bf16_f32 v161, v162, v163\n"
"v_mfma_f32_16x16x32_bf16 a[216:219], v[228:231], v[144:147], a[216:219]\n"
"v_mfma_f32_16x16x32_bf16 a[200:203], v[224:227], v[144:147], a[200:203]\n"
"s_mov_b32 s49, 0\n"
"v_mfma_f32_16x16x32_bf16 a[196:199], v[224:227], v[136:139], a[196:199]\n"
"s_or_b32 s48, s48, s49\n"
"s_cbranch_scc1 RSP30_%=\n"
"RRP30_%=:\n"
"v_mfma_f32_16x16x32_bf16 a[212:215], v[228:231], v[136:139], a[212:215]\n"
"s_waitcnt vmcnt(8) lgkmcnt(0)\n"
"s_barrier\n"
"v_mfma_f32_16x16x32_bf16 a[208:211], v[228:231], v[128:131], a[208:211]\n"
"s_lshl_b32 s16, s16, 2\n"
"ds_read_b128 v[208:211], v244 offset:0\n"
"v_mfma_f32_16x16x32_bf16 a[192:195], v[224:227], v[128:131], a[192:195]\n"
"s_sub_u32 s16, s18, s16\n"
"ds_read_b128 v[212:215], v244 offset:512\n"
"v_mfma_f32_16x16x32_bf16 a[224:227], v[232:235], v[128:131], a[224:227]\n"
"s_min_i32 s19, s16, 0\n"
"ds_read_b128 v[216:219], v244 offset:1024\n"
"v_mfma_f32_16x16x32_bf16 a[240:243], v[236:239], v[128:131], a[240:243]\n"
"s_ashr_i32 s16, s16, 3\n"
"v_exp_f32 v188, v188\n"
"v_mfma_f32_16x16x32_bf16 a[244:247], v[236:239], v[136:139], a[244:247]\n"
"s_min_i32 s16, s16, 32\n"
"v_exp_f32 v189, v189\n"
"v_mfma_f32_16x16x32_bf16 a[228:231], v[232:235], v[136:139], a[228:231]\n"
"s_sub_u32 s16, s16, 1\n"
"s_cmp_gt_i32 s16, 0\n"
"s_cbranch_scc1 65533\n"
"v_mfma_f32_16x16x32_bf16 a[232:235], v[232:235], v[144:147], a[232:235]\n"
"s_and_b32 s19, s19, s43\n"
"ds_read_b128 v[220:223], v244 offset:1536\n"
"v_add_f32 v243, v188, v243\n"
"v_mfma_f32_16x16x32_bf16 a[248:251], v[236:239], v[144:147], a[248:251]\n"
"s_sub_u32 s18, s18, s19\n"
"v_exp_f32 v190, v190\n"
"v_mfma_f32_16x16x32_bf16 a[252:255], v[236:239], v[152:155], a[252:255]\n"
"v_exp_f32 v191, v191\n"
"v_mfma_f32_16x16x32_bf16 a[236:239], v[232:235], v[152:155], a[236:239]\n"
"s_waitcnt lgkmcnt(2)\n"
"v_add_f32 v243, v189, v243\n"
"v_add_f32 v243, v190, v243\n"
"v_mfma_f32_16x16x32_bf16 v[128:131], v[208:211], v[0:3], v[192:195]\n"
"s_add_u32 m0, s45, s56\n"
"ds_read_b128 v[224:227], v244 offset:2048\n"
"v_add_f32 v243, v191, v243\n"
"v_mfma_f32_16x16x32_bf16 v[128:131], v[212:215], v[4:7], v[128:131]\n"
"buffer_load_dwordx4 v248, s[12:15], s34 offen lds\n"
"v_mfma_f32_16x16x32_bf16 v[136:139], v[212:215], v[36:39], v[196:199]\n"
"v_cvt_pk_bf16_f32 v186, v188, v189\n"
"v_cvt_pk_bf16_f32 v187, v190, v191\n"
"v_mfma_f32_16x16x32_bf16 v[136:139], v[208:211], v[32:35], v[136:139]\n"
"v_mfma_f32_16x16x32_bf16 v[144:147], v[208:211], v[64:67], v[200:203]\n"
"ds_read_b128 v[228:231], v244 offset:2560\n"
"v_mbcnt_lo_u32_b32 v254, -1, 0\n"
"v_mfma_f32_16x16x32_bf16 v[144:147], v[212:215], v[68:71], v[144:147]\n"
"v_mbcnt_hi_u32_b32 v254, -1, v254\n"
"v_mfma_f32_16x16x32_bf16 v[152:155], v[212:215], v[100:103], v[204:207]\n"
"v_lshlrev_b32 v254, 7, v254\n"
"v_mfma_f32_16x16x32_bf16 v[152:155], v[208:211], v[96:99], v[152:155]\n"
"s_waitcnt lgkmcnt(2)\n"
"v_exp_f32 v180, v180\n"
"v_mfma_f32_16x16x32_bf16 v[152:155], v[216:219], v[104:107], v[152:155]\n"
"ds_read_b128 v[232:235], v244 offset:3072\n"
"v_add_u32 v245, s63, v251\n"
"v_mfma_f32_16x16x32_bf16 v[152:155], v[220:223], v[108:111], v[152:155]\n"
"v_xor_b32 v246, 32, v245\n"
"v_add_f32 v242, v180, v242\n"
"v_mfma_f32_16x16x32_bf16 v[144:147], v[220:223], v[76:79], v[144:147]\n"
"v_exp_f32 v181, v181\n"
"v_mfma_f32_16x16x32_bf16 v[144:147], v[216:219], v[72:75], v[144:147]\n"
"v_exp_f32 v182, v182\n"
"v_mfma_f32_16x16x32_bf16 v[136:139], v[216:219], v[40:43], v[136:139]\n"
"ds_read_b128 v[236:239], v244 offset:3584\n"
"v_add_f32 v242, v181, v242\n"
"v_mfma_f32_16x16x32_bf16 v[136:139], v[220:223], v[44:47], v[136:139]\n"
"v_exp_f32 v183, v183\n"
"v_mfma_f32_16x16x32_bf16 v[128:131], v[220:223], v[12:15], v[128:131]\n"
"v_add_f32 v242, v182, v242\n"
"v_add_f32 v242, v183, v242\n"
"v_mfma_f32_16x16x32_bf16 v[128:131], v[216:219], v[8:11], v[128:131]\n"
"s_waitcnt lgkmcnt(2)\n"
"v_cvt_pk_bf16_f32 v178, v180, v181\n"
"v_cvt_pk_bf16_f32 v179, v182, v183\n"
"v_mfma_f32_16x16x32_bf16 v[128:131], v[224:227], v[16:19], v[128:131]\n"
"s_add_u32 m0, s45, s57\n"
"ds_read_b128 v[208:211], v244 offset:8192\n"
"v_mfma_f32_16x16x32_bf16 v[128:131], v[228:231], v[20:23], v[128:131]\n"
"buffer_load_dwordx4 v248, s[12:15], s34 offen offset:128 lds\n"
"v_mfma_f32_16x16x32_bf16 v[136:139], v[228:231], v[52:55], v[136:139]\n"
"v_mfma_f32_16x16x32_bf16 v[136:139], v[224:227], v[48:51], v[136:139]\n"
"v_mfma_f32_16x16x32_bf16 v[144:147], v[224:227], v[80:83], v[144:147]\n"
"ds_read_b128 v[212:215], v244 offset:8704\n"
"v_mfma_f32_16x16x32_bf16 v[144:147], v[228:231], v[84:87], v[144:147]\n"
"v_exp_f32 v172, v172\n"
"v_mfma_f32_16x16x32_bf16 v[152:155], v[228:231], v[116:119], v[152:155]\n"
"v_exp_f32 v173, v173\n"
"v_mfma_f32_16x16x32_bf16 v[152:155], v[224:227], v[112:115], v[152:155]\n"
"s_waitcnt lgkmcnt(2)\n"
"v_exp_f32 v174, v174\n"
"v_mfma_f32_16x16x32_bf16 v[152:155], v[232:235], v[120:123], v[152:155]\n"
"ds_read_b128 v[216:219], v244 offset:9216\n"
"v_add_f32 v241, v172, v241\n"
"v_mfma_f32_16x16x32_bf16 v[152:155], v[236:239], v[124:127], v[152:155]\n"
"v_exp_f32 v175, v175\n"
"v_mfma_f32_16x16x32_bf16 v[144:147], v[236:239], v[92:95], v[144:147]\n"
"v_add_f32 v241, v173, v241\n"
"v_add_f32 v241, v174, v241\n"
"v_mfma_f32_16x16x32_bf16 v[144:147], v[232:235], v[88:91], v[144:147]\n"
"v_add_f32 v241, v175, v241\n"
"v_cvt_pk_bf16_f32 v170, v172, v173\n"
"v_mfma_f32_16x16x32_bf16 v[136:139], v[232:235], v[56:59], v[136:139]\n"
"ds_read_b128 v[220:223], v244 offset:9728\n"
"v_cvt_pk_bf16_f32 v171, v174, v175\n"
"v_mfma_f32_16x16x32_bf16 v[136:139], v[236:239], v[60:63], v[136:139]\n"
"v_mfma_f32_16x16x32_bf16 v[128:131], v[236:239], v[28:31], v[128:131]\n"
"v_mfma_f32_16x16x32_bf16 v[128:131], v[232:235], v[24:27], v[128:131]\n"
"s_waitcnt lgkmcnt(2)\n"
"v_mfma_f32_16x16x32_bf16 v[132:135], v[208:211], v[0:3], v[192:195]\n"
"s_add_u32 m0, s45, s58\n"
"ds_read_b128 v[224:227], v244 offset:10240\n"
"v_mfma_f32_16x16x32_bf16 v[132:135], v[212:215], v[4:7], v[132:135]\n"
"buffer_load_dwordx4 v248, s[12:15], s34 offen offset:256 lds\n"
"v_mfma_f32_16x16x32_bf16 v[140:143], v[212:215], v[36:39], v[196:199]\n"
"v_exp_f32 v164, v164\n"
"v_mfma_f32_16x16x32_bf16 v[140:143], v[208:211], v[32:35], v[140:143]\n"
"v_exp_f32 v165, v165\n"
"v_mfma_f32_16x16x32_bf16 v[148:151], v[208:211], v[64:67], v[200:203]\n"
"ds_read_b128 v[228:231], v244 offset:10752\n"
"v_add_f32 v240, v164, v240\n"
"v_mfma_f32_16x16x32_bf16 v[148:151], v[212:215], v[68:71], v[148:151]\n"
"v_exp_f32 v166, v166\n"
"v_mfma_f32_16x16x32_bf16 v[156:159], v[212:215], v[100:103], v[204:207]\n"
"v_exp_f32 v167, v167\n"
"v_mfma_f32_16x16x32_bf16 v[156:159], v[208:211], v[96:99], v[156:159]\n"
"s_waitcnt lgkmcnt(2)\n"
"v_add_f32 v240, v165, v240\n"
"v_add_f32 v240, v166, v240\n"
"v_mfma_f32_16x16x32_bf16 v[156:159], v[216:219], v[104:107], v[156:159]\n"
"ds_read_b128 v[232:235], v244 offset:11264\n"
"v_add_f32 v240, v167, v240\n"
"v_mfma_f32_16x16x32_bf16 v[156:159], v[220:223], v[108:111], v[156:159]\n"
"v_cvt_pk_bf16_f32 v162, v164, v165\n"
"v_cvt_pk_bf16_f32 v163, v166, v167\n"
"v_mfma_f32_16x16x32_bf16 v[148:151], v[220:223], v[76:79], v[148:151]\n"
"v_mfma_f32_16x16x32_bf16 v[148:151], v[216:219], v[72:75], v[148:151]\n"
"v_mfma_f32_16x16x32_bf16 v[140:143], v[216:219], v[40:43], v[140:143]\n"
"ds_read_b128 v[236:239], v244 offset:11776\n"
"v_mfma_f32_16x16x32_bf16 v[140:143], v[220:223], v[44:47], v[140:143]\n"
"v_mfma_f32_16x16x32_bf16 v[132:135], v[220:223], v[12:15], v[132:135]\n"
"v_mfma_f32_16x16x32_bf16 v[132:135], v[216:219], v[8:11], v[132:135]\n"
"s_waitcnt lgkmcnt(2)\n"
"v_mfma_f32_16x16x32_bf16 v[132:135], v[224:227], v[16:19], v[132:135]\n"
"ds_read_b64_tr_b16 v[208:209], v245 offset:16384\n"
"v_mfma_f32_16x16x32_bf16 v[132:135], v[228:231], v[20:23], v[132:135]\n"
"s_add_u32 m0, s45, s59\n"
"ds_read_b64_tr_b16 v[210:211], v245 offset:24576\n"
"v_mfma_f32_16x16x32_bf16 v[140:143], v[228:231], v[52:55], v[140:143]\n"
"buffer_load_dwordx4 v248, s[12:15], s34 offen offset:384 lds\n"
"v_mfma_f32_16x16x32_bf16 v[140:143], v[224:227], v[48:51], v[140:143]\n"
"v_mfma_f32_16x16x32_bf16 v[148:151], v[224:227], v[80:83], v[148:151]\n"
"ds_read_b64_tr_b16 v[212:213], v246 offset:16384\n"
"v_mfma_f32_16x16x32_bf16 v[148:151], v[228:231], v[84:87], v[148:151]\n"
"ds_read_b64_tr_b16 v[214:215], v246 offset:24576\n"
"v_mfma_f32_16x16x32_bf16 v[156:159], v[228:231], v[116:119], v[156:159]\n"
"v_mfma_f32_16x16x32_bf16 v[156:159], v[224:227], v[112:115], v[156:159]\n"
"s_waitcnt lgkmcnt(4)\n"
"v_mfma_f32_16x16x32_bf16 v[156:159], v[232:235], v[120:123], v[156:159]\n"
"ds_read_b64_tr_b16 v[216:217], v245 offset:16896\n"
"v_mfma_f32_16x16x32_bf16 v[156:159], v[236:239], v[124:127], v[156:159]\n"
"ds_read_b64_tr_b16 v[218:219], v245 offset:25088\n"
"v_mfma_f32_16x16x32_bf16 v[148:151], v[236:239], v[92:95], v[148:151]\n"
"v_mfma_f32_16x16x32_bf16 v[148:151], v[232:235], v[88:91], v[148:151]\n"
"v_mfma_f32_16x16x32_bf16 v[140:143], v[232:235], v[56:59], v[140:143]\n"
"ds_read_b64_tr_b16 v[220:221], v246 offset:16896\n"
"v_mfma_f32_16x16x32_bf16 v[140:143], v[236:239], v[60:63], v[140:143]\n"
"ds_read_b64_tr_b16 v[222:223], v246 offset:25088\n"
"v_mfma_f32_16x16x32_bf16 v[132:135], v[236:239], v[28:31], v[132:135]\n"
"v_mfma_f32_16x16x32_bf16 v[132:135], v[232:235], v[24:27], v[132:135]\n"
"s_waitcnt lgkmcnt(4)\n"
"v_mfma_f32_16x16x32_bf16 a[12:15], v[208:211], v[184:187], a[12:15]\n"
"ds_read_b64_tr_b16 v[224:225], v245 offset:17408\n"
"v_mfma_f32_16x16x32_bf16 a[28:31], v[212:215], v[184:187], a[28:31]\n"
"s_mul_i32 s65, s5, 7168\n"
"ds_read_b64_tr_b16 v[226:227], v245 offset:25600\n"
"v_mfma_f32_16x16x32_bf16 a[24:27], v[212:215], v[176:179], a[24:27]\n"
"s_mov_b32 m0, 73728\n"
"v_exp_f32 v152, v152\n"
"v_mfma_f32_16x16x32_bf16 a[8:11], v[208:211], v[176:179], a[8:11]\n"
"buffer_load_dword v254, s[20:23], s65 offen lds\n"
"v_mfma_f32_16x16x32_bf16 a[4:7], v[208:211], v[168:171], a[4:7]\n"
"ds_read_b64_tr_b16 v[228:229], v246 offset:17408\n"
"v_add_f32 v243, v152, v243\n"
"v_mfma_f32_16x16x32_bf16 a[20:23], v[212:215], v[168:171], a[20:23]\n"
"ds_read_b64_tr_b16 v[230:231], v246 offset:25600\n"
"v_mfma_f32_16x16x32_bf16 a[16:19], v[212:215], v[160:163], a[16:19]\n"
"v_exp_f32 v153, v153\n"
"v_mfma_f32_16x16x32_bf16 a[0:3], v[208:211], v[160:163], a[0:3]\n"
"s_waitcnt lgkmcnt(4)\n"
"v_exp_f32 v154, v154\n"
"v_mfma_f32_16x16x32_bf16 a[32:35], v[216:219], v[160:163], a[32:35]\n"
"ds_read_b64_tr_b16 v[232:233], v245 offset:17920\n"
"v_add_f32 v243, v153, v243\n"
"v_mfma_f32_16x16x32_bf16 a[48:51], v[220:223], v[160:163], a[48:51]\n"
"ds_read_b64_tr_b16 v[234:235], v245 offset:26112\n"
"v_add_f32 v243, v154, v243\n"
"v_mfma_f32_16x16x32_bf16 a[52:55], v[220:223], v[168:171], a[52:55]\n"
"v_exp_f32 v155, v155\n"
"v_mfma_f32_16x16x32_bf16 a[36:39], v[216:219], v[168:171], a[36:39]\n"
"v_add_f32 v243, v155, v243\n"
"v_cvt_pk_bf16_f32 v152, v152, v153\n"
"v_mfma_f32_16x16x32_bf16 a[40:43], v[216:219], v[176:179], a[40:43]\n"
"ds_read_b64_tr_b16 v[236:237], v246 offset:17920\n"
"v_cvt_pk_bf16_f32 v153, v154, v155\n"
"v_mfma_f32_16x16x32_bf16 a[56:59], v[220:223], v[176:179], a[56:59]\n"
"ds_read_b64_tr_b16 v[238:239], v246 offset:26112\n"
"v_mfma_f32_16x16x32_bf16 a[60:63], v[220:223], v[184:187], a[60:63]\n"
"v_exp_f32 v144, v144\n"
"v_mfma_f32_16x16x32_bf16 a[44:47], v[216:219], v[184:187], a[44:47]\n"
"s_waitcnt lgkmcnt(4)\n"
"v_exp_f32 v145, v145\n"
"v_mfma_f32_16x16x32_bf16 a[76:79], v[224:227], v[184:187], a[76:79]\n"
"ds_read_b64_tr_b16 v[208:209], v245 offset:18432\n"
"v_add_f32 v242, v144, v242\n"
"v_mfma_f32_16x16x32_bf16 a[92:95], v[228:231], v[184:187], a[92:95]\n"
"IWB0_%=:\n"
"s_cbranch_execz IWB4_%=\n"
"ds_read_b64_tr_b16 v[210:211], v245 offset:26624\n"
"v_add_f32 v242, v145, v242\n"
"v_mfma_f32_16x16x32_bf16 a[88:91], v[228:231], v[176:179], a[88:91]\n"
"v_exp_f32 v146, v146\n"
"v_mfma_f32_16x16x32_bf16 a[72:75], v[224:227], v[176:179], a[72:75]\n"
"v_exp_f32 v147, v147\n"
"v_mfma_f32_16x16x32_bf16 a[68:71], v[224:227], v[168:171], a[68:71]\n"
"s_add_u32 s64, s63, 32768\n"
"ds_read_b64_tr_b16 v[212:213], v246 offset:18432\n"
"v_add_f32 v242, v146, v242\n"
"v_mfma_f32_16x16x32_bf16 a[84:87], v[228:231], v[168:171], a[84:87]\n"
"s_cmp_eq_u32 s64, 163840\n"
"ds_read_b64_tr_b16 v[214:215], v246 offset:26624\n"
"v_add_f32 v242, v147, v242\n"
"v_mfma_f32_16x16x32_bf16 a[80:83], v[228:231], v[160:163], a[80:83]\n"
"s_cselect_b32 s64, 0, s64\n"
"v_cvt_pk_bf16_f32 v144, v144, v145\n"
"v_cvt_pk_bf16_f32 v145, v146, v147\n"
"v_mfma_f32_16x16x32_bf16 a[64:67], v[224:227], v[160:163], a[64:67]\n"
"s_waitcnt lgkmcnt(4)\n"
"v_add_u32 v244, s64, v250\n"
"v_mfma_f32_16x16x32_bf16 a[96:99], v[232:235], v[160:163], a[96:99]\n"
"s_mov_b32 s34, s62\n"
"ds_read_b64_tr_b16 v[216:217], v245 offset:18944\n"
"v_mfma_f32_16x16x32_bf16 a[112:115], v[236:239], v[160:163], a[112:115]\n"
"s_add_u32 s62, s62, 16384\n"
"ds_read_b64_tr_b16 v[218:219], v245 offset:27136\n"
"v_mfma_f32_16x16x32_bf16 a[116:119], v[236:239], v[168:171], a[116:119]\n"
"s_and_b32 s62, s62, 524287\n"
"v_mfma_f32_16x16x32_bf16 a[100:103], v[232:235], v[168:171], a[100:103]\n"
"s_add_u32 s45, s45, 32768\n"
"v_mfma_f32_16x16x32_bf16 a[104:107], v[232:235], v[176:179], a[104:107]\n"
"s_cmp_eq_u32 s45, 163840\n"
"ds_read_b64_tr_b16 v[220:221], v246 offset:18944\n"
"v_mfma_f32_16x16x32_bf16 a[120:123], v[236:239], v[176:179], a[120:123]\n"
"s_cselect_b32 s45, 0, s45\n"
"ds_read_b64_tr_b16 v[222:223], v246 offset:27136\n"
"v_mfma_f32_16x16x32_bf16 a[124:127], v[236:239], v[184:187], a[124:127]\n"
"s_add_u32 s18, s18, s35\n"
"v_exp_f32 v136, v136\n"
"v_mfma_f32_16x16x32_bf16 a[108:111], v[232:235], v[184:187], a[108:111]\n"
"s_waitcnt lgkmcnt(4)\n"
"v_exp_f32 v137, v137\n"
"v_mfma_f32_16x16x32_bf16 a[140:143], v[208:211], v[184:187], a[140:143]\n"
"ds_read_b64_tr_b16 v[224:225], v245 offset:19456\n"
"v_add_f32 v241, v136, v241\n"
"v_mfma_f32_16x16x32_bf16 a[156:159], v[212:215], v[184:187], a[156:159]\n"
"ds_read_b64_tr_b16 v[226:227], v245 offset:27648\n"
"v_add_f32 v241, v137, v241\n"
"v_mfma_f32_16x16x32_bf16 a[152:155], v[212:215], v[176:179], a[152:155]\n"
"v_exp_f32 v138, v138\n"
"v_mfma_f32_16x16x32_bf16 a[136:139], v[208:211], v[176:179], a[136:139]\n"
"v_exp_f32 v139, v139\n"
"v_mfma_f32_16x16x32_bf16 a[132:135], v[208:211], v[168:171], a[132:135]\n"
"ds_read_b64_tr_b16 v[228:229], v246 offset:19456\n"
"v_add_f32 v241, v138, v241\n"
"v_mfma_f32_16x16x32_bf16 a[148:151], v[212:215], v[168:171], a[148:151]\n"
"ds_read_b64_tr_b16 v[230:231], v246 offset:27648\n"
"v_add_f32 v241, v139, v241\n"
"v_mfma_f32_16x16x32_bf16 a[144:147], v[212:215], v[160:163], a[144:147]\n"
"v_cvt_pk_bf16_f32 v136, v136, v137\n"
"v_cvt_pk_bf16_f32 v137, v138, v139\n"
"v_mfma_f32_16x16x32_bf16 a[128:131], v[208:211], v[160:163], a[128:131]\n"
"s_waitcnt lgkmcnt(4)\n"
"v_mfma_f32_16x16x32_bf16 a[160:163], v[216:219], v[160:163], a[160:163]\n"
"ds_read_b64_tr_b16 v[232:233], v245 offset:19968\n"
"v_mfma_f32_16x16x32_bf16 a[176:179], v[220:223], v[160:163], a[176:179]\n"
"ds_read_b64_tr_b16 v[234:235], v245 offset:28160\n"
"v_mfma_f32_16x16x32_bf16 a[180:183], v[220:223], v[168:171], a[180:183]\n"
"v_exp_f32 v128, v128\n"
"v_mfma_f32_16x16x32_bf16 a[164:167], v[216:219], v[168:171], a[164:167]\n"
"v_exp_f32 v129, v129\n"
"v_mfma_f32_16x16x32_bf16 a[168:171], v[216:219], v[176:179], a[168:171]\n"
"ds_read_b64_tr_b16 v[236:237], v246 offset:19968\n"
"v_add_f32 v240, v128, v240\n"
"v_mfma_f32_16x16x32_bf16 a[184:187], v[220:223], v[176:179], a[184:187]\n"
"ds_read_b64_tr_b16 v[238:239], v246 offset:28160\n"
"v_add_f32 v240, v129, v240\n"
"v_mfma_f32_16x16x32_bf16 a[188:191], v[220:223], v[184:187], a[188:191]\n"
"v_exp_f32 v130, v130\n"
"v_mfma_f32_16x16x32_bf16 a[172:175], v[216:219], v[184:187], a[172:175]\n"
"s_waitcnt lgkmcnt(4)\n"
"v_exp_f32 v131, v131\n"
"v_mfma_f32_16x16x32_bf16 a[204:207], v[224:227], v[184:187], a[204:207]\n"
"v_add_f32 v240, v130, v240\n"
"v_add_f32 v240, v131, v240\n"
"v_mfma_f32_16x16x32_bf16 a[220:223], v[228:231], v[184:187], a[220:223]\n"
"v_cvt_pk_bf16_f32 v128, v128, v129\n"
"v_cvt_pk_bf16_f32 v129, v130, v131\n"
"v_mfma_f32_16x16x32_bf16 a[216:219], v[228:231], v[176:179], a[216:219]\n"
"v_mfma_f32_16x16x32_bf16 a[200:203], v[224:227], v[176:179], a[200:203]\n"
"s_mov_b32 s49, 0\n"
"v_mfma_f32_16x16x32_bf16 a[196:199], v[224:227], v[168:171], a[196:199]\n"
"s_or_b32 s48, s48, s49\n"
"s_cbranch_scc1 RSP31_%=\n"
"RRP31_%=:\n"
"v_mfma_f32_16x16x32_bf16 a[212:215], v[228:231], v[168:171], a[212:215]\n"
"s_waitcnt vmcnt(1) lgkmcnt(0)\n"
"s_barrier\n"
"v_mfma_f32_16x16x32_bf16 a[208:211], v[228:231], v[160:163], a[208:211]\n"
"ds_read_b128 v[208:211], v244 offset:0\n"
"v_mfma_f32_16x16x32_bf16 a[192:195], v[224:227], v[160:163], a[192:195]\n"
"ds_read_b128 v[212:215], v244 offset:512\n"
"v_mfma_f32_16x16x32_bf16 a[224:227], v[232:235], v[160:163], a[224:227]\n"
"ds_read_b128 v[216:219], v244 offset:1024\n"
"v_mfma_f32_16x16x32_bf16 a[240:243], v[236:239], v[160:163], a[240:243]\n"
"v_exp_f32 v156, v156\n"
"v_mfma_f32_16x16x32_bf16 a[244:247], v[236:239], v[168:171], a[244:247]\n"
"v_exp_f32 v157, v157\n"
"v_mfma_f32_16x16x32_bf16 a[228:231], v[232:235], v[168:171], a[228:231]\n"
"v_exp_f32 v158, v158\n"
"v_mfma_f32_16x16x32_bf16 a[232:235], v[232:235], v[176:179], a[232:235]\n"
"ds_read_b128 v[220:223], v244 offset:1536\n"
"v_add_f32 v243, v156, v243\n"
"v_mfma_f32_16x16x32_bf16 a[248:251], v[236:239], v[176:179], a[248:251]\n"
"v_exp_f32 v159, v159\n"
"v_mfma_f32_16x16x32_bf16 a[252:255], v[236:239], v[184:187], a[252:255]\n"
"v_add_f32 v243, v157, v243\n"
"v_add_f32 v243, v158, v243\n"
"v_mfma_f32_16x16x32_bf16 a[236:239], v[232:235], v[184:187], a[236:239]\n"
"s_waitcnt lgkmcnt(2)\n"
"v_add_f32 v243, v159, v243\n"
"v_cvt_pk_bf16_f32 v154, v156, v157\n"
"v_mfma_f32_16x16x32_bf16 v[160:163], v[208:211], v[0:3], v[192:195]\n"
"s_mov_b32 s65, 0\n"
"ds_read_b128 v[224:227], v244 offset:2048\n"
"v_cvt_pk_bf16_f32 v155, v158, v159\n"
"v_mfma_f32_16x16x32_bf16 v[160:163], v[212:215], v[4:7], v[160:163]\n"
"s_add_u32 m0, s60, 0\n"
"v_mfma_f32_16x16x32_bf16 v[168:171], v[212:215], v[36:39], v[196:199]\n"
"buffer_load_dwordx4 v249, s[20:23], s65 offen lds\n"
"v_mfma_f32_16x16x32_bf16 v[168:171], v[208:211], v[32:35], v[168:171]\n"
"v_mfma_f32_16x16x32_bf16 v[176:179], v[208:211], v[64:67], v[200:203]\n"
"ds_read_b128 v[228:231], v244 offset:2560\n"
"v_mfma_f32_16x16x32_bf16 v[176:179], v[212:215], v[68:71], v[176:179]\n"
"v_mfma_f32_16x16x32_bf16 v[184:187], v[212:215], v[100:103], v[204:207]\n"
"s_mov_b32 s65, 28672\n"
"v_exp_f32 v148, v148\n"
"v_mfma_f32_16x16x32_bf16 v[184:187], v[208:211], v[96:99], v[184:187]\n"
"s_waitcnt lgkmcnt(2)\n"
"v_exp_f32 v149, v149\n"
"v_mfma_f32_16x16x32_bf16 v[184:187], v[216:219], v[104:107], v[184:187]\n"
"s_add_u32 m0, s60, 1024\n"
"ds_read_b128 v[232:235], v244 offset:3072\n"
"v_add_u32 v245, s64, v251\n"
"v_mfma_f32_16x16x32_bf16 v[184:187], v[220:223], v[108:111], v[184:187]\n"
"buffer_load_dwordx4 v249, s[20:23], s65 offen lds\n"
"v_mfma_f32_16x16x32_bf16 v[176:179], v[220:223], v[76:79], v[176:179]\n"
"v_xor_b32 v246, 32, v245\n"
"v_add_f32 v242, v148, v242\n"
"v_mfma_f32_16x16x32_bf16 v[176:179], v[216:219], v[72:75], v[176:179]\n"
"v_exp_f32 v150, v150\n"
"v_mfma_f32_16x16x32_bf16 v[168:171], v[216:219], v[40:43], v[168:171]\n"
"ds_read_b128 v[236:239], v244 offset:3584\n"
"v_add_f32 v242, v149, v242\n"
"v_mfma_f32_16x16x32_bf16 v[168:171], v[220:223], v[44:47], v[168:171]\n"
"s_mov_b32 s65, 57344\n"
"v_exp_f32 v151, v151\n"
"v_mfma_f32_16x16x32_bf16 v[160:163], v[220:223], v[12:15], v[160:163]\n"
"s_add_u32 m0, s60, 2048\n"
"v_add_f32 v242, v150, v242\n"
"v_add_f32 v242, v151, v242\n"
"v_mfma_f32_16x16x32_bf16 v[160:163], v[216:219], v[8:11], v[160:163]\n"
"s_waitcnt lgkmcnt(2)\n"
"buffer_load_dwordx4 v249, s[20:23], s65 offen lds\n"
"v_mfma_f32_16x16x32_bf16 v[160:163], v[224:227], v[16:19], v[160:163]\n"
"ds_read_b128 v[208:211], v244 offset:8192\n"
"v_cvt_pk_bf16_f32 v146, v148, v149\n"
"v_mfma_f32_16x16x32_bf16 v[160:163], v[228:231], v[20:23], v[160:163]\n"
"v_cvt_pk_bf16_f32 v147, v150, v151\n"
"v_mfma_f32_16x16x32_bf16 v[168:171], v[228:231], v[52:55], v[168:171]\n"
"v_mfma_f32_16x16x32_bf16 v[168:171], v[224:227], v[48:51], v[168:171]\n"
"v_mfma_f32_16x16x32_bf16 v[176:179], v[224:227], v[80:83], v[176:179]\n"
"s_mov_b32 s65, 86016\n"
"ds_read_b128 v[212:215], v244 offset:8704\n"
"v_mfma_f32_16x16x32_bf16 v[176:179], v[228:231], v[84:87], v[176:179]\n"
"s_add_u32 m0, s60, 3072\n"
"v_exp_f32 v140, v140\n"
"v_mfma_f32_16x16x32_bf16 v[184:187], v[228:231], v[116:119], v[184:187]\n"
"buffer_load_dwordx4 v249, s[20:23], s65 offen lds\n"
"v_mfma_f32_16x16x32_bf16 v[184:187], v[224:227], v[112:115], v[184:187]\n"
"s_waitcnt lgkmcnt(2)\n"
"v_exp_f32 v141, v141\n"
"v_mfma_f32_16x16x32_bf16 v[184:187], v[232:235], v[120:123], v[184:187]\n"
"ds_read_b128 v[216:219], v244 offset:9216\n"
"v_add_f32 v241, v140, v241\n"
"v_mfma_f32_16x16x32_bf16 v[184:187], v[236:239], v[124:127], v[184:187]\n"
"v_exp_f32 v142, v142\n"
"v_mfma_f32_16x16x32_bf16 v[176:179], v[236:239], v[92:95], v[176:179]\n"
"v_exp_f32 v143, v143\n"
"v_mfma_f32_16x16x32_bf16 v[176:179], v[232:235], v[88:91], v[176:179]\n"
"s_mov_b32 s65, 114688\n"
"v_add_f32 v241, v141, v241\n"
"v_add_f32 v241, v142, v241\n"
"v_mfma_f32_16x16x32_bf16 v[168:171], v[232:235], v[56:59], v[168:171]\n"
"s_add_u32 m0, s60, 16384\n"
"ds_read_b128 v[220:223], v244 offset:9728\n"
"v_add_f32 v241, v143, v241\n"
"v_mfma_f32_16x16x32_bf16 v[168:171], v[236:239], v[60:63], v[168:171]\n"
"buffer_load_dwordx4 v249, s[20:23], s65 offen lds\n"
"v_mfma_f32_16x16x32_bf16 v[160:163], v[236:239], v[28:31], v[160:163]\n"
"v_cvt_pk_bf16_f32 v138, v140, v141\n"
"v_cvt_pk_bf16_f32 v139, v142, v143\n"
"v_mfma_f32_16x16x32_bf16 v[160:163], v[232:235], v[24:27], v[160:163]\n"
"s_waitcnt lgkmcnt(2)\n"
"v_mfma_f32_16x16x32_bf16 v[164:167], v[208:211], v[0:3], v[192:195]\n"
"ds_read_b128 v[224:227], v244 offset:10240\n"
"v_mfma_f32_16x16x32_bf16 v[164:167], v[212:215], v[4:7], v[164:167]\n"
"v_mfma_f32_16x16x32_bf16 v[172:175], v[212:215], v[36:39], v[196:199]\n"
"s_mov_b32 s65, 143360\n"
"v_exp_f32 v132, v132\n"
"v_mfma_f32_16x16x32_bf16 v[172:175], v[208:211], v[32:35], v[172:175]\n"
"s_add_u32 m0, s60, 17408\n"
"v_exp_f32 v133, v133\n"
"v_mfma_f32_16x16x32_bf16 v[180:183], v[208:211], v[64:67], v[200:203]\n"
"buffer_load_dwordx4 v249, s[20:23], s65 offen lds\n"
"v_mfma_f32_16x16x32_bf16 v[180:183], v[212:215], v[68:71], v[180:183]\n"
"ds_read_b128 v[228:231], v244 offset:10752\n"
"v_add_f32 v240, v132, v240\n"
"v_mfma_f32_16x16x32_bf16 v[188:191], v[212:215], v[100:103], v[204:207]\n"
"v_exp_f32 v134, v134\n"
"v_mfma_f32_16x16x32_bf16 v[188:191], v[208:211], v[96:99], v[188:191]\n"
"s_waitcnt lgkmcnt(2)\n"
"v_exp_f32 v135, v135\n"
"v_mfma_f32_16x16x32_bf16 v[188:191], v[216:219], v[104:107], v[188:191]\n"
"ds_read_b128 v[232:235], v244 offset:11264\n"
"v_add_f32 v240, v133, v240\n"
"v_mfma_f32_16x16x32_bf16 v[188:191], v[220:223], v[108:111], v[188:191]\n"
"s_mov_b32 s65, 172032\n"
"v_exp_f32 v184, v184\n"
"v_mfma_f32_16x16x32_bf16 v[180:183], v[220:223], v[76:79], v[180:183]\n"
"s_add_u32 m0, s60, 18432\n"
"v_exp_f32 v185, v185\n"
"v_mfma_f32_16x16x32_bf16 v[180:183], v[216:219], v[72:75], v[180:183]\n"
"buffer_load_dwordx4 v249, s[20:23], s65 offen lds\n"
"v_mfma_f32_16x16x32_bf16 v[172:175], v[216:219], v[40:43], v[172:175]\n"
"ds_read_b128 v[236:239], v244 offset:11776\n"
"v_add_f32 v240, v134, v240\n"
"v_mfma_f32_16x16x32_bf16 v[172:175], v[220:223], v[44:47], v[172:175]\n"
"v_exp_f32 v186, v186\n"
"v_mfma_f32_16x16x32_bf16 v[164:167], v[220:223], v[12:15], v[164:167]\n"
"v_exp_f32 v187, v187\n"
"v_mfma_f32_16x16x32_bf16 v[164:167], v[216:219], v[8:11], v[164:167]\n"
"s_waitcnt lgkmcnt(2)\n"
"v_exp_f32 v176, v176\n"
"v_mfma_f32_16x16x32_bf16 v[164:167], v[224:227], v[16:19], v[164:167]\n"
"s_mov_b32 s65, 200704\n"
"ds_read_b64_tr_b16 v[208:209], v245 offset:16384\n"
"v_add_f32 v240, v135, v240\n"
"v_mfma_f32_16x16x32_bf16 v[164:167], v[228:231], v[20:23], v[164:167]\n"
"s_add_u32 m0, s60, 19456\n"
"ds_read_b64_tr_b16 v[210:211], v245 offset:24576\n"
"v_add_f32 v243, v184, v243\n"
"v_mfma_f32_16x16x32_bf16 v[172:175], v[228:231], v[52:55], v[172:175]\n"
"buffer_load_dwordx4 v249, s[20:23], s65 offen lds\n"
"v_mfma_f32_16x16x32_bf16 v[172:175], v[224:227], v[48:51], v[172:175]\n"
"v_exp_f32 v177, v177\n"
"v_mfma_f32_16x16x32_bf16 v[180:183], v[224:227], v[80:83], v[180:183]\n"
"ds_read_b64_tr_b16 v[212:213], v246 offset:16384\n"
"v_cvt_pk_bf16_f32 v130, v132, v133\n"
"v_mfma_f32_16x16x32_bf16 v[180:183], v[228:231], v[84:87], v[180:183]\n"
"ds_read_b64_tr_b16 v[214:215], v246 offset:24576\n"
"v_cvt_pk_bf16_f32 v131, v134, v135\n"
"v_mfma_f32_16x16x32_bf16 v[188:191], v[228:231], v[116:119], v[188:191]\n"
"s_mov_b32 s65, 229376\n"
"v_exp_f32 v178, v178\n"
"v_mfma_f32_16x16x32_bf16 v[188:191], v[224:227], v[112:115], v[188:191]\n"
"s_waitcnt lgkmcnt(4)\n"
"v_exp_f32 v179, v179\n"
"v_mfma_f32_16x16x32_bf16 v[188:191], v[232:235], v[120:123], v[188:191]\n"
"s_add_u32 m0, s60, 98304\n"
"ds_read_b64_tr_b16 v[216:217], v245 offset:16896\n"
"v_add_f32 v243, v185, v243\n"
"v_mfma_f32_16x16x32_bf16 v[188:191], v[236:239], v[124:127], v[188:191]\n"
"buffer_load_dwordx4 v249, s[20:23], s65 offen lds\n"
"v_mfma_f32_16x16x32_bf16 v[180:183], v[236:239], v[92:95], v[180:183]\n"
"ds_read_b64_tr_b16 v[218:219], v245 offset:25088\n"
"v_add_f32 v243, v186, v243\n"
"v_mfma_f32_16x16x32_bf16 v[180:183], v[232:235], v[88:91], v[180:183]\n"
"v_exp_f32 v168, v168\n"
"v_mfma_f32_16x16x32_bf16 v[172:175], v[232:235], v[56:59], v[172:175]\n"
"ds_read_b64_tr_b16 v[220:221], v246 offset:16896\n"
"v_add_f32 v243, v187, v243\n"
"v_mfma_f32_16x16x32_bf16 v[172:175], v[236:239], v[60:63], v[172:175]\n"
"s_mov_b32 s65, 258048\n"
"ds_read_b64_tr_b16 v[222:223], v246 offset:25088\n"
"v_add_f32 v242, v176, v242\n"
"v_mfma_f32_16x16x32_bf16 v[164:167], v[236:239], v[28:31], v[164:167]\n"
"s_add_u32 m0, s60, 99328\n"
"v_exp_f32 v169, v169\n"
"v_mfma_f32_16x16x32_bf16 v[164:167], v[232:235], v[24:27], v[164:167]\n"
"s_waitcnt lgkmcnt(4)\n"
"buffer_load_dwordx4 v249, s[20:23], s65 offen lds\n"
"v_mfma_f32_16x16x32_bf16 a[12:15], v[208:211], v[152:155], a[12:15]\n"
"ds_read_b64_tr_b16 v[224:225], v245 offset:17408\n"
"v_cvt_pk_bf16_f32 v184, v184, v185\n"
"v_mfma_f32_16x16x32_bf16 a[28:31], v[212:215], v[152:155], a[28:31]\n"
"ds_read_b64_tr_b16 v[226:227], v245 offset:25600\n"
"v_cvt_pk_bf16_f32 v185, v186, v187\n"
"v_mfma_f32_16x16x32_bf16 a[24:27], v[212:215], v[144:147], a[24:27]\n"
"v_add_f32 v242, v177, v242\n"
"v_add_f32 v242, v178, v242\n"
"v_mfma_f32_16x16x32_bf16 a[8:11], v[208:211], v[144:147], a[8:11]\n"
"v_add_f32 v242, v179, v242\n"
"v_cvt_pk_bf16_f32 v176, v176, v177\n"
"v_mfma_f32_16x16x32_bf16 a[4:7], v[208:211], v[136:139], a[4:7]\n"
"s_mov_b32 s65, 286720\n"
"ds_read_b64_tr_b16 v[228:229], v246 offset:17408\n"
"v_add_f32 v241, v168, v241\n"
"v_mfma_f32_16x16x32_bf16 a[20:23], v[212:215], v[136:139], a[20:23]\n"
"s_add_u32 m0, s60, 100352\n"
"ds_read_b64_tr_b16 v[230:231], v246 offset:25600\n"
"v_add_f32 v241, v169, v241\n"
"v_mfma_f32_16x16x32_bf16 a[16:19], v[212:215], v[128:131], a[16:19]\n"
"buffer_load_dwordx4 v249, s[20:23], s65 offen lds\n"
"v_mfma_f32_16x16x32_bf16 a[0:3], v[208:211], v[128:131], a[0:3]\n"
"s_waitcnt lgkmcnt(4)\n"
"v_cvt_pk_bf16_f32 v177, v178, v179\n"
"v_cvt_pk_bf16_f32 v168, v168, v169\n"
"v_mfma_f32_16x16x32_bf16 a[32:35], v[216:219], v[128:131], a[32:35]\n"
"ds_read_b64_tr_b16 v[232:233], v245 offset:17920\n"
"v_add_u32 v192, 0, v251\n"
"v_mfma_f32_16x16x32_bf16 a[48:51], v[220:223], v[128:131], a[48:51]\n"
"ds_read_b64_tr_b16 v[234:235], v245 offset:26112\n"
"v_xor_b32 v193, 32, v192\n"
"v_mfma_f32_16x16x32_bf16 a[52:55], v[220:223], v[136:139], a[52:55]\n"
"v_exp_f32 v170, v170\n"
"v_mfma_f32_16x16x32_bf16 a[36:39], v[216:219], v[136:139], a[36:39]\n"
"s_mov_b32 s65, 315392\n"
"v_exp_f32 v171, v171\n"
"v_mfma_f32_16x16x32_bf16 a[40:43], v[216:219], v[144:147], a[40:43]\n"
"s_add_u32 m0, s60, 101376\n"
"ds_read_b64_tr_b16 v[236:237], v246 offset:17920\n"
"v_add_f32 v241, v170, v241\n"
"v_mfma_f32_16x16x32_bf16 a[56:59], v[220:223], v[144:147], a[56:59]\n"
"buffer_load_dwordx4 v249, s[20:23], s65 offen lds\n"
"v_mfma_f32_16x16x32_bf16 a[60:63], v[220:223], v[152:155], a[60:63]\n"
"ds_read_b64_tr_b16 v[238:239], v246 offset:26112\n"
"v_add_f32 v241, v171, v241\n"
"v_mfma_f32_16x16x32_bf16 a[44:47], v[216:219], v[152:155], a[44:47]\n"
"s_waitcnt lgkmcnt(4)\n"
"v_exp_f32 v160, v160\n"
"v_mfma_f32_16x16x32_bf16 a[76:79], v[224:227], v[152:155], a[76:79]\n"
"ds_read_b64_tr_b16 v[208:209], v245 offset:18432\n"
"v_cvt_pk_bf16_f32 v169, v170, v171\n"
"v_mfma_f32_16x16x32_bf16 a[92:95], v[228:231], v[152:155], a[92:95]\n"
"ds_read_b64_tr_b16 v[210:211], v245 offset:26624\n"
"v_add_f32 v240, v160, v240\n"
"v_mfma_f32_16x16x32_bf16 a[88:91], v[228:231], v[144:147], a[88:91]\n"
"v_exp_f32 v161, v161\n"
"v_mfma_f32_16x16x32_bf16 a[72:75], v[224:227], v[144:147], a[72:75]\n"
"v_exp_f32 v162, v162\n"
"v_mfma_f32_16x16x32_bf16 a[68:71], v[224:227], v[136:139], a[68:71]\n"
"s_add_u32 s63, s64, 32768\n"
"ds_read_b64_tr_b16 v[212:213], v246 offset:18432\n"
"v_add_f32 v240, v161, v240\n"
"v_mfma_f32_16x16x32_bf16 a[84:87], v[228:231], v[136:139], a[84:87]\n"
"s_cmp_eq_u32 s63, 163840\n"
"ds_read_b64_tr_b16 v[214:215], v246 offset:26624\n"
"v_add_f32 v240, v162, v240\n"
"v_mfma_f32_16x16x32_bf16 a[80:83], v[228:231], v[128:131], a[80:83]\n"
"s_cselect_b32 s63, 0, s63\n"
"v_exp_f32 v163, v163\n"
"v_mfma_f32_16x16x32_bf16 a[64:67], v[224:227], v[128:131], a[64:67]\n"
"s_waitcnt lgkmcnt(4)\n"
"v_exp_f32 v188, v188\n"
"v_mfma_f32_16x16x32_bf16 a[96:99], v[232:235], v[128:131], a[96:99]\n"
"s_add_u32 s71, s71, 1\n"
"ds_read_b64_tr_b16 v[216:217], v245 offset:18944\n"
"v_add_f32 v240, v163, v240\n"
"v_mfma_f32_16x16x32_bf16 a[112:115], v[236:239], v[128:131], a[112:115]\n"
"ds_read_b64_tr_b16 v[218:219], v245 offset:27136\n"
"v_cvt_pk_bf16_f32 v160, v160, v161\n"
"v_mfma_f32_16x16x32_bf16 a[116:119], v[236:239], v[136:139], a[116:119]\n"
"v_exp_f32 v189, v189\n"
"v_mfma_f32_16x16x32_bf16 a[100:103], v[232:235], v[136:139], a[100:103]\n"
"v_cvt_pk_bf16_f32 v161, v162, v163\n"
"v_add_f32 v243, v188, v243\n"
"v_mfma_f32_16x16x32_bf16 a[104:107], v[232:235], v[144:147], a[104:107]\n"
"ds_read_b64_tr_b16 v[220:221], v246 offset:18944\n"
"v_add_f32 v243, v189, v243\n"
"v_mfma_f32_16x16x32_bf16 a[120:123], v[236:239], v[144:147], a[120:123]\n"
"ds_read_b64_tr_b16 v[222:223], v246 offset:27136\n"
"v_cvt_pk_bf16_f32 v186, v188, v189\n"
"v_mfma_f32_16x16x32_bf16 a[124:127], v[236:239], v[152:155], a[124:127]\n"
"v_exp_f32 v190, v190\n"
"v_mfma_f32_16x16x32_bf16 a[108:111], v[232:235], v[152:155], a[108:111]\n"
"s_waitcnt lgkmcnt(4)\n"
"v_exp_f32 v191, v191\n"
"v_mfma_f32_16x16x32_bf16 a[140:143], v[208:211], v[152:155], a[140:143]\n"
"ds_read_b64_tr_b16 v[224:225], v245 offset:19456\n"
"v_add_f32 v243, v190, v243\n"
"v_mfma_f32_16x16x32_bf16 a[156:159], v[212:215], v[152:155], a[156:159]\n"
"ds_read_b64_tr_b16 v[226:227], v245 offset:27648\n"
"v_add_f32 v243, v191, v243\n"
"v_mfma_f32_16x16x32_bf16 a[152:155], v[212:215], v[144:147], a[152:155]\n"
"v_exp_f32 v180, v180\n"
"v_mfma_f32_16x16x32_bf16 a[136:139], v[208:211], v[144:147], a[136:139]\n"
"v_cvt_pk_bf16_f32 v187, v190, v191\n"
"v_add_f32 v242, v180, v242\n"
"v_mfma_f32_16x16x32_bf16 a[132:135], v[208:211], v[136:139], a[132:135]\n"
"ds_read_b64_tr_b16 v[228:229], v246 offset:19456\n"
"v_mov_b32 v255, v243\n"
"v_mfma_f32_16x16x32_bf16 a[148:151], v[212:215], v[136:139], a[148:151]\n"
"ds_read_b64_tr_b16 v[230:231], v246 offset:27648\n"
"v_add_u32 v194, 65536, v251\n"
"v_mfma_f32_16x16x32_bf16 a[144:147], v[212:215], v[128:131], a[144:147]\n"
"v_exp_f32 v181, v181\n"
"v_mfma_f32_16x16x32_bf16 a[128:131], v[208:211], v[128:131], a[128:131]\n"
"s_waitcnt lgkmcnt(4)\n"
"v_exp_f32 v182, v182\n"
"v_mfma_f32_16x16x32_bf16 a[160:163], v[216:219], v[128:131], a[160:163]\n"
"ds_read_b64_tr_b16 v[232:233], v245 offset:19968\n"
"v_add_f32 v242, v181, v242\n"
"v_mfma_f32_16x16x32_bf16 a[176:179], v[220:223], v[128:131], a[176:179]\n"
"ds_read_b64_tr_b16 v[234:235], v245 offset:28160\n"
"v_add_f32 v242, v182, v242\n"
"v_mfma_f32_16x16x32_bf16 a[180:183], v[220:223], v[136:139], a[180:183]\n"
"v_exp_f32 v183, v183\n"
"v_mfma_f32_16x16x32_bf16 a[164:167], v[216:219], v[136:139], a[164:167]\n"
"v_add_f32 v242, v183, v242\n"
"v_cvt_pk_bf16_f32 v178, v180, v181\n"
"v_mfma_f32_16x16x32_bf16 a[168:171], v[216:219], v[144:147], a[168:171]\n"
"ds_read_b64_tr_b16 v[236:237], v246 offset:19968\n"
"v_cvt_pk_bf16_f32 v179, v182, v183\n"
"v_mfma_f32_16x16x32_bf16 a[184:187], v[220:223], v[144:147], a[184:187]\n"
"ds_read_b64_tr_b16 v[238:239], v246 offset:28160\n"
"v_add_u32 v245, s63, v251\n"
"v_mfma_f32_16x16x32_bf16 a[188:191], v[220:223], v[152:155], a[188:191]\n"
"v_xor_b32 v246, 32, v245\n"
"v_mov_b32 v254, v242\n"
"v_mfma_f32_16x16x32_bf16 a[172:175], v[216:219], v[152:155], a[172:175]\n"
"s_waitcnt lgkmcnt(4)\n"
"v_exp_f32 v172, v172\n"
"v_mfma_f32_16x16x32_bf16 a[204:207], v[224:227], v[152:155], a[204:207]\n"
"ds_read_b64_tr_b16 v[208:209], v245 offset:16384\n"
"v_add_f32 v241, v172, v241\n"
"v_mfma_f32_16x16x32_bf16 a[220:223], v[228:231], v[152:155], a[220:223]\n"
"ds_read_b64_tr_b16 v[210:211], v245 offset:24576\n"
"v_xor_b32 v195, 32, v194\n"
"v_mfma_f32_16x16x32_bf16 a[216:219], v[228:231], v[144:147], a[216:219]\n"
"v_exp_f32 v173, v173\n"
"v_mfma_f32_16x16x32_bf16 a[200:203], v[224:227], v[144:147], a[200:203]\n"
"v_exp_f32 v174, v174\n"
"v_mfma_f32_16x16x32_bf16 a[196:199], v[224:227], v[136:139], a[196:199]\n"
"ds_read_b64_tr_b16 v[212:213], v246 offset:16384\n"
"v_add_f32 v241, v173, v241\n"
"v_mfma_f32_16x16x32_bf16 a[212:215], v[228:231], v[136:139], a[212:215]\n"
"ds_read_b64_tr_b16 v[214:215], v246 offset:24576\n"
"v_add_f32 v241, v174, v241\n"
"v_mfma_f32_16x16x32_bf16 a[208:211], v[228:231], v[128:131], a[208:211]\n"
"v_exp_f32 v175, v175\n"
"v_mfma_f32_16x16x32_bf16 a[192:195], v[224:227], v[128:131], a[192:195]\n"
"s_waitcnt lgkmcnt(4)\n"
"v_exp_f32 v164, v164\n"
"v_mfma_f32_16x16x32_bf16 a[224:227], v[232:235], v[128:131], a[224:227]\n"
"ds_read_b64_tr_b16 v[216:217], v245 offset:16896\n"
"v_add_f32 v241, v175, v241\n"
"v_mfma_f32_16x16x32_bf16 a[240:243], v[236:239], v[128:131], a[240:243]\n"
"ds_read_b64_tr_b16 v[218:219], v245 offset:25088\n"
"v_cvt_pk_bf16_f32 v170, v172, v173\n"
"v_mfma_f32_16x16x32_bf16 a[244:247], v[236:239], v[136:139], a[244:247]\n"
"v_exp_f32 v165, v165\n"
"v_mfma_f32_16x16x32_bf16 a[228:231], v[232:235], v[136:139], a[228:231]\n"
"v_cvt_pk_bf16_f32 v171, v174, v175\n"
"v_add_f32 v240, v164, v240\n"
"v_mfma_f32_16x16x32_bf16 a[232:235], v[232:235], v[144:147], a[232:235]\n"
"ds_read_b64_tr_b16 v[220:221], v246 offset:16896\n"
"v_exp_f32 v166, v166\n"
"v_exp_f32 v167, v167\n"
"v_add_f32 v240, v165, v240\n"
"v_add_f32 v240, v166, v240\n"
"v_add_f32 v240, v167, v240\n"
"v_cvt_pk_bf16_f32 v162, v164, v165\n"
"v_cvt_pk_bf16_f32 v163, v166, v167\n"
"v_mov_b32 v252, v240\n"
"v_mov_b32 v253, v241\n"
"v_permlane16_swap_b32 v254, v242\n"
"v_permlane16_swap_b32 v252, v240\n"
"v_permlane16_swap_b32 v253, v241\n"
"v_permlane16_swap_b32 v255, v243\n"
"v_add_f32 v240, v240, v252\n"
"v_add_f32 v241, v241, v253\n"
"v_add_f32 v242, v242, v254\n"
"v_add_f32 v243, v243, v255\n"
"v_mov_b32 v252, v240\n"
"v_mov_b32 v253, v241\n"
"v_mov_b32 v254, v242\n"
"v_mov_b32 v255, v243\n"
"v_permlane32_swap_b32 v252, v240\n"
"v_permlane32_swap_b32 v253, v241\n"
"v_permlane32_swap_b32 v254, v242\n"
"v_permlane32_swap_b32 v255, v243\n"
"v_add_f32 v240, v240, v252\n"
"v_add_f32 v241, v241, v253\n"
"v_add_f32 v242, v242, v254\n"
"v_add_f32 v243, v243, v255\n"
"v_add_f32 v252, v240, v241\n"
"v_add_f32 v253, v242, v243\n"
"v_add_f32 v252, v252, v253\n"
"v_cmp_ngt_f32 vcc, 0x6f800000, v252\n"
"v_rcp_f32 v240, v240\n"
"v_rcp_f32 v241, v241\n"
"v_rcp_f32 v242, v242\n"
"v_rcp_f32 v243, v243\n"
"s_nop 0\n"
"s_andn2_b64 s[6:7], vcc, s[68:69]\n"
"s_or_b32 s6, s6, s7\n"
"v_mov_b32 v252, s6\n"
"v_mov_b32 v253, s70\n"
"v_lshlrev_b32 v253, 2, v253\n"
"v_add_u32 v253, 65536, v253\n"
"ds_write_b32 v253, v252\n"
"v_mov_b32 v254, 65536\n"
"v_add_u32 v196, 131072, v251\n"
"v_xor_b32 v197, 32, v196\n"
"v_mbcnt_lo_u32_b32 v206, -1, 0\n"
"v_mbcnt_hi_u32_b32 v206, -1, v206\n"
"v_lshlrev_b32 v206, 4, v206\n"
"s_mul_i32 s6, s70, 28672\n"
"v_add_u32 v206, s6, v206\n"
"s_mul_i32 s6, s4, 114688\n"
"v_add_u32 v206, s6, v206\n"
"v_mfma_f32_16x16x32_bf16 a[248:251], v[236:239], v[144:147], a[248:251]\n"
"ds_read_b64_tr_b16 v[222:223], v246 offset:25088\n"
"v_mfma_f32_16x16x32_bf16 a[252:255], v[236:239], v[152:155], a[252:255]\n"
"s_waitcnt vmcnt(8) lgkmcnt(1)\n"
"s_barrier\n"
"v_mfma_f32_16x16x32_bf16 a[236:239], v[232:235], v[152:155], a[236:239]\n"
"v_mfma_f32_16x16x32_bf16 a[12:15], v[208:211], v[184:187], a[12:15]\n"
"s_mov_b32 s65, 344064\n"
"s_add_u32 m0, s60, 32768\n"
"ds_read_b128 v[198:201], v254\n"
"v_mfma_f32_16x16x32_bf16 a[28:31], v[212:215], v[184:187], a[28:31]\n"
"buffer_load_dwordx4 v249, s[20:23], s65 offen lds\n"
"v_mfma_f32_16x16x32_bf16 a[24:27], v[212:215], v[176:179], a[24:27]\n"
"s_mov_b32 s65, 372736\n"
"s_add_u32 m0, s60, 33792\n"
"ds_read_b64_tr_b16 v[224:225], v245 offset:17408\n"
"v_mfma_f32_16x16x32_bf16 a[8:11], v[208:211], v[176:179], a[8:11]\n"
"buffer_load_dwordx4 v249, s[20:23], s65 offen lds\n"
"v_mfma_f32_16x16x32_bf16 a[4:7], v[208:211], v[168:171], a[4:7]\n"
"s_mov_b32 s65, 401408\n"
"s_add_u32 m0, s60, 34816\n"
"ds_read_b64_tr_b16 v[226:227], v245 offset:25600\n"
"v_mfma_f32_16x16x32_bf16 a[20:23], v[212:215], v[168:171], a[20:23]\n"
"buffer_load_dwordx4 v249, s[20:23], s65 offen lds\n"
"v_mfma_f32_16x16x32_bf16 a[16:19], v[212:215], v[160:163], a[16:19]\n"
"s_mov_b32 s65, 430080\n"
"s_add_u32 m0, s60, 35840\n"
"ds_read_b64_tr_b16 v[228:229], v246 offset:17408\n"
"v_mfma_f32_16x16x32_bf16 a[0:3], v[208:211], v[160:163], a[0:3]\n"
"buffer_load_dwordx4 v249, s[20:23], s65 offen lds\n"
"s_waitcnt lgkmcnt(3)\n"
"v_or3_b32 v198, v198, v199, v200\n"
"v_or_b32 v198, v198, v201\n"
"v_mfma_f32_16x16x32_bf16 a[32:35], v[216:219], v[160:163], a[32:35]\n"
"ds_read_b64_tr_b16 v[230:231], v246 offset:25600\n"
"v_cmp_ne_u32 vcc, 0, v198\n"
"v_mfma_f32_16x16x32_bf16 a[48:51], v[220:223], v[160:163], a[48:51]\n"
"s_mov_b32 s65, 128\n"
"s_add_u32 m0, s60, 49152\n"
"ds_read_b64_tr_b16 v[232:233], v245 offset:17920\n"
"v_mfma_f32_16x16x32_bf16 a[52:55], v[220:223], v[168:171], a[52:55]\n"
"buffer_load_dwordx4 v249, s[20:23], s65 offen lds\n"
"v_mfma_f32_16x16x32_bf16 a[36:39], v[216:219], v[168:171], a[36:39]\n"
"s_mov_b32 s65, 28800\n"
"s_add_u32 m0, s60, 50176\n"
"ds_read_b64_tr_b16 v[234:235], v245 offset:26112\n"
"v_mfma_f32_16x16x32_bf16 a[40:43], v[216:219], v[176:179], a[40:43]\n"
"buffer_load_dwordx4 v249, s[20:23], s65 offen lds\n"
"v_mfma_f32_16x16x32_bf16 a[56:59], v[220:223], v[176:179], a[56:59]\n"
"s_mov_b32 s65, 57472\n"
"s_add_u32 m0, s60, 51200\n"
"ds_read_b64_tr_b16 v[236:237], v246 offset:17920\n"
"v_mfma_f32_16x16x32_bf16 a[60:63], v[220:223], v[184:187], a[60:63]\n"
"buffer_load_dwordx4 v249, s[20:23], s65 offen lds\n"
"v_mfma_f32_16x16x32_bf16 a[44:47], v[216:219], v[184:187], a[44:47]\n"
"s_waitcnt lgkmcnt(3)\n"
"s_mov_b32 s65, 86144\n"
"ds_read_b64_tr_b16 v[238:239], v246 offset:26112\n"
"v_mfma_f32_16x16x32_bf16 a[76:79], v[224:227], v[184:187], a[76:79]\n"
"s_add_u32 m0, s60, 52224\n"
"ds_read_b64_tr_b16 v[208:209], v245 offset:18432\n"
"v_mfma_f32_16x16x32_bf16 a[92:95], v[228:231], v[184:187], a[92:95]\n"
"buffer_load_dwordx4 v249, s[20:23], s65 offen lds\n"
"v_mfma_f32_16x16x32_bf16 a[88:91], v[228:231], v[176:179], a[88:91]\n"
"s_cbranch_vccnz FB_%=\n"
"ds_read_b64_tr_b16 v[210:211], v245 offset:26624\n"
"v_mfma_f32_16x16x32_bf16 a[72:75], v[224:227], v[176:179], a[72:75]\n"
"v_accvgpr_read_b32 v198, a0\n"
"v_accvgpr_read_b32 v199, a1\n"
"v_mfma_f32_16x16x32_bf16 a[68:71], v[224:227], v[168:171], a[68:71]\n"
"ds_read_b64_tr_b16 v[212:213], v246 offset:18432\n"
"v_accvgpr_read_b32 v200, a2\n"
"v_mfma_f32_16x16x32_bf16 a[84:87], v[228:231], v[168:171], a[84:87]\n"
"ds_read_b64_tr_b16 v[214:215], v246 offset:26624\n"
"v_accvgpr_read_b32 v201, a3\n"
"v_mfma_f32_16x16x32_bf16 a[80:83], v[228:231], v[160:163], a[80:83]\n"
"v_mul_f32 v198, v240, v198\n"
"v_mul_f32 v199, v240, v199\n"
"v_mfma_f32_16x16x32_bf16 a[64:67], v[224:227], v[160:163], a[64:67]\n"
"s_waitcnt lgkmcnt(4)\n"
"v_mul_f32 v200, v240, v200\n"
"v_mul_f32 v201, v240, v201\n"
"v_mfma_f32_16x16x32_bf16 a[96:99], v[232:235], v[160:163], a[96:99]\n"
"ds_read_b64_tr_b16 v[216:217], v245 offset:18944\n"
"v_cvt_pk_bf16_f32 v0, v198, v199\n"
"v_mfma_f32_16x16x32_bf16 a[112:115], v[236:239], v[160:163], a[112:115]\n"
"ds_read_b64_tr_b16 v[218:219], v245 offset:27136\n"
"v_cvt_pk_bf16_f32 v1, v200, v201\n"
"v_mfma_f32_16x16x32_bf16 a[116:119], v[236:239], v[168:171], a[116:119]\n"
"v_accvgpr_read_b32 v202, a16\n"
"v_accvgpr_read_b32 v203, a17\n"
"v_mfma_f32_16x16x32_bf16 a[100:103], v[232:235], v[168:171], a[100:103]\n"
"v_accvgpr_read_b32 v204, a18\n"
"v_accvgpr_read_b32 v205, a19\n"
"v_mfma_f32_16x16x32_bf16 a[104:107], v[232:235], v[176:179], a[104:107]\n"
"IWB1_%=:\n"
"s_cbranch_execz IWB5_%=\n"
"ds_read_b64_tr_b16 v[220:221], v246 offset:18944\n"
"v_mul_f32 v202, v240, v202\n"
"v_mfma_f32_16x16x32_bf16 a[120:123], v[236:239], v[176:179], a[120:123]\n"
"ds_read_b64_tr_b16 v[222:223], v246 offset:27136\n"
"v_mul_f32 v203, v240, v203\n"
"v_mfma_f32_16x16x32_bf16 a[124:127], v[236:239], v[184:187], a[124:127]\n"
"v_mul_f32 v204, v240, v204\n"
"v_mul_f32 v205, v240, v205\n"
"v_mfma_f32_16x16x32_bf16 a[108:111], v[232:235], v[184:187], a[108:111]\n"
"s_waitcnt lgkmcnt(4)\n"
"v_cvt_pk_bf16_f32 v2, v202, v203\n"
"v_cvt_pk_bf16_f32 v3, v204, v205\n"
"v_mfma_f32_16x16x32_bf16 a[140:143], v[208:211], v[184:187], a[140:143]\n"
"ds_read_b64_tr_b16 v[224:225], v245 offset:19456\n"
"v_accvgpr_read_b32 v198, a4\n"
"v_mfma_f32_16x16x32_bf16 a[156:159], v[212:215], v[184:187], a[156:159]\n"
"ds_read_b64_tr_b16 v[226:227], v245 offset:27648\n"
"v_accvgpr_read_b32 v199, a5\n"
"v_mfma_f32_16x16x32_bf16 a[152:155], v[212:215], v[176:179], a[152:155]\n"
"v_accvgpr_read_b32 v200, a6\n"
"v_accvgpr_read_b32 v201, a7\n"
"v_mfma_f32_16x16x32_bf16 a[136:139], v[208:211], v[176:179], a[136:139]\n"
"v_mul_f32 v198, v241, v198\n"
"v_mul_f32 v199, v241, v199\n"
"v_mfma_f32_16x16x32_bf16 a[132:135], v[208:211], v[168:171], a[132:135]\n"
"ds_read_b64_tr_b16 v[228:229], v246 offset:19456\n"
"v_mul_f32 v200, v241, v200\n"
"v_mfma_f32_16x16x32_bf16 a[148:151], v[212:215], v[168:171], a[148:151]\n"
"ds_read_b64_tr_b16 v[230:231], v246 offset:27648\n"
"v_mul_f32 v201, v241, v201\n"
"v_mfma_f32_16x16x32_bf16 a[144:147], v[212:215], v[160:163], a[144:147]\n"
"v_cvt_pk_bf16_f32 v4, v198, v199\n"
"v_cvt_pk_bf16_f32 v5, v200, v201\n"
"v_mfma_f32_16x16x32_bf16 a[128:131], v[208:211], v[160:163], a[128:131]\n"
"s_waitcnt lgkmcnt(4)\n"
"v_accvgpr_read_b32 v202, a20\n"
"v_accvgpr_read_b32 v203, a21\n"
"v_mfma_f32_16x16x32_bf16 a[160:163], v[216:219], v[160:163], a[160:163]\n"
"ds_read_b64_tr_b16 v[232:233], v245 offset:19968\n"
"v_accvgpr_read_b32 v204, a22\n"
"v_mfma_f32_16x16x32_bf16 a[176:179], v[220:223], v[160:163], a[176:179]\n"
"ds_read_b64_tr_b16 v[234:235], v245 offset:28160\n"
"v_accvgpr_read_b32 v205, a23\n"
"v_mfma_f32_16x16x32_bf16 a[180:183], v[220:223], v[168:171], a[180:183]\n"
"v_mul_f32 v202, v241, v202\n"
"v_mul_f32 v203, v241, v203\n"
"v_mfma_f32_16x16x32_bf16 a[164:167], v[216:219], v[168:171], a[164:167]\n"
"v_mul_f32 v204, v241, v204\n"
"v_mul_f32 v205, v241, v205\n"
"v_mfma_f32_16x16x32_bf16 a[168:171], v[216:219], v[176:179], a[168:171]\n"
"ds_read_b64_tr_b16 v[236:237], v246 offset:19968\n"
"v_cvt_pk_bf16_f32 v6, v202, v203\n"
"v_mfma_f32_16x16x32_bf16 a[184:187], v[220:223], v[176:179], a[184:187]\n"
"ds_read_b64_tr_b16 v[238:239], v246 offset:28160\n"
"v_cvt_pk_bf16_f32 v7, v204, v205\n"
"v_mfma_f32_16x16x32_bf16 a[188:191], v[220:223], v[184:187], a[188:191]\n"
"v_accvgpr_read_b32 v198, a32\n"
"v_accvgpr_read_b32 v199, a33\n"
"v_mfma_f32_16x16x32_bf16 a[172:175], v[216:219], v[184:187], a[172:175]\n"
"s_waitcnt lgkmcnt(4)\n"
"v_accvgpr_read_b32 v200, a34\n"
"v_accvgpr_read_b32 v201, a35\n"
"v_mfma_f32_16x16x32_bf16 a[204:207], v[224:227], v[184:187], a[204:207]\n"
"v_mul_f32 v198, v240, v198\n"
"v_mul_f32 v199, v240, v199\n"
"v_mfma_f32_16x16x32_bf16 a[220:223], v[228:231], v[184:187], a[220:223]\n"
"v_mul_f32 v200, v240, v200\n"
"v_mul_f32 v201, v240, v201\n"
"v_mfma_f32_16x16x32_bf16 a[216:219], v[228:231], v[176:179], a[216:219]\n"
"v_cvt_pk_bf16_f32 v8, v198, v199\n"
"v_cvt_pk_bf16_f32 v9, v200, v201\n"
"v_mfma_f32_16x16x32_bf16 a[200:203], v[224:227], v[176:179], a[200:203]\n"
"v_accvgpr_read_b32 v202, a48\n"
"v_accvgpr_read_b32 v203, a49\n"
"v_mfma_f32_16x16x32_bf16 a[196:199], v[224:227], v[168:171], a[196:199]\n"
"ds_read_b64_tr_b16 v[208:209], v192 offset:0\n"
"v_accvgpr_read_b32 v204, a50\n"
"v_mfma_f32_16x16x32_bf16 a[212:215], v[228:231], v[168:171], a[212:215]\n"
"ds_read_b64_tr_b16 v[210:211], v192 offset:8192\n"
"v_accvgpr_read_b32 v205, a51\n"
"v_mfma_f32_16x16x32_bf16 a[208:211], v[228:231], v[160:163], a[208:211]\n"
"ds_read_b64_tr_b16 v[212:213], v192 offset:1024\n"
"v_mul_f32 v202, v240, v202\n"
"v_mfma_f32_16x16x32_bf16 a[192:195], v[224:227], v[160:163], a[192:195]\n"
"s_waitcnt lgkmcnt(3)\n"
"ds_read_b64_tr_b16 v[214:215], v192 offset:9216\n"
"v_mul_f32 v203, v240, v203\n"
"v_mfma_f32_16x16x32_bf16 a[224:227], v[232:235], v[160:163], a[224:227]\n"
"ds_read_b64_tr_b16 v[216:217], v193 offset:0\n"
"v_mul_f32 v204, v240, v204\n"
"v_mfma_f32_16x16x32_bf16 a[240:243], v[236:239], v[160:163], a[240:243]\n"
"ds_read_b64_tr_b16 v[218:219], v193 offset:8192\n"
"v_mul_f32 v205, v240, v205\n"
"v_mfma_f32_16x16x32_bf16 a[244:247], v[236:239], v[168:171], a[244:247]\n"
"ds_read_b64_tr_b16 v[220:221], v193 offset:1024\n"
"v_cvt_pk_bf16_f32 v10, v202, v203\n"
"v_mfma_f32_16x16x32_bf16 a[228:231], v[232:235], v[168:171], a[228:231]\n"
"ds_read_b64_tr_b16 v[222:223], v193 offset:9216\n"
"v_cvt_pk_bf16_f32 v11, v204, v205\n"
"v_mfma_f32_16x16x32_bf16 a[232:235], v[232:235], v[176:179], a[232:235]\n"
"ds_read_b64_tr_b16 v[224:225], v192 offset:512\n"
"v_accvgpr_read_b32 v198, a36\n"
"v_mfma_f32_16x16x32_bf16 a[248:251], v[236:239], v[176:179], a[248:251]\n"
"ds_read_b64_tr_b16 v[226:227], v192 offset:8704\n"
"v_accvgpr_read_b32 v199, a37\n"
"v_mfma_f32_16x16x32_bf16 a[252:255], v[236:239], v[184:187], a[252:255]\n"
"s_waitcnt vmcnt(8)\n"
"s_barrier\n"
"v_mfma_f32_16x16x32_bf16 a[236:239], v[232:235], v[184:187], a[236:239]\n"
"s_waitcnt lgkmcnt(6)\n"
"ds_read_b64_tr_b16 v[228:229], v192 offset:1536\n"
"ds_read_b64_tr_b16 v[230:231], v192 offset:9728\n"
"v_mfma_f32_16x16x32_bf16 v[128:131], v[208:211], v[0:3], 0\n"
"s_mov_b32 s65, 114816\n"
"s_add_u32 m0, s60, 65536\n"
"ds_read_b64_tr_b16 v[232:233], v193 offset:512\n"
"v_accvgpr_read_b32 v200, a38\n"
"v_accvgpr_read_b32 v201, a39\n"
"v_mul_f32 v198, v241, v198\n"
"v_mul_f32 v199, v241, v199\n"
"v_mul_f32 v200, v241, v200\n"
"v_mul_f32 v201, v241, v201\n"
"v_cvt_pk_bf16_f32 v12, v198, v199\n"
"v_cvt_pk_bf16_f32 v13, v200, v201\n"
"v_accvgpr_read_b32 v202, a52\n"
"v_accvgpr_read_b32 v203, a53\n"
"v_accvgpr_read_b32 v204, a54\n"
"v_accvgpr_read_b32 v205, a55\n"
"v_mul_f32 v202, v241, v202\n"
"v_mul_f32 v203, v241, v203\n"
"v_mul_f32 v204, v241, v204\n"
"v_mul_f32 v205, v241, v205\n"
"v_cvt_pk_bf16_f32 v14, v202, v203\n"
"v_cvt_pk_bf16_f32 v15, v204, v205\n"
"v_mfma_f32_16x16x32_bf16 v[128:131], v[212:215], v[8:11], v[128:131]\n"
"buffer_load_dwordx4 v249, s[20:23], s65 offen lds\n"
"v_mfma_f32_16x16x32_bf16 v[144:147], v[212:215], v[12:15], 0\n"
"ds_read_b64_tr_b16 v[234:235], v193 offset:8704\n"
"ds_read_b64_tr_b16 v[236:237], v193 offset:1536\n"
"v_mfma_f32_16x16x32_bf16 v[144:147], v[208:211], v[4:7], v[144:147]\n"
"s_waitcnt lgkmcnt(7)\n"
"s_mov_b32 s65, 143488\n"
"ds_read_b64_tr_b16 v[238:239], v193 offset:9728\n"
"v_mfma_f32_16x16x32_bf16 v[148:151], v[216:219], v[4:7], 0\n"
"s_add_u32 m0, s60, 66560\n"
"ds_read_b64_tr_b16 v[208:209], v192 offset:2048\n"
"v_mfma_f32_16x16x32_bf16 v[148:151], v[220:223], v[12:15], v[148:151]\n"
"buffer_load_dwordx4 v249, s[20:23], s65 offen lds\n"
"v_mfma_f32_16x16x32_bf16 v[132:135], v[220:223], v[8:11], 0\n"
"ds_read_b64_tr_b16 v[210:211], v192 offset:10240\n"
"ds_read_b64_tr_b16 v[212:213], v192 offset:3072\n"
"v_mfma_f32_16x16x32_bf16 v[132:135], v[216:219], v[0:3], v[132:135]\n"
"s_waitcnt lgkmcnt(7)\n"
"s_mov_b32 s65, 172160\n"
"ds_read_b64_tr_b16 v[214:215], v192 offset:11264\n"
"v_mfma_f32_16x16x32_bf16 v[136:139], v[224:227], v[0:3], 0\n"
"s_mov_b32 s74, 0\n"
"s_add_u32 m0, s60, 67584\n"
"ds_read_b64_tr_b16 v[216:217], v193 offset:2048\n"
"v_mfma_f32_16x16x32_bf16 v[136:139], v[228:231], v[8:11], v[136:139]\n"
"buffer_load_dwordx4 v249, s[20:23], s65 offen lds\n"
"v_mfma_f32_16x16x32_bf16 v[152:155], v[228:231], v[12:15], 0\n"
"s_mov_b32 s65, 200832\n"
"s_add_u32 m0, s60, 68608\n"
"ds_read_b64_tr_b16 v[218:219], v193 offset:10240\n"
"ds_read_b64_tr_b16 v[220:221], v193 offset:3072\n"
"v_mfma_f32_16x16x32_bf16 v[152:155], v[224:227], v[4:7], v[152:155]\n"
"s_waitcnt lgkmcnt(7)\n"
"buffer_load_dwordx4 v249, s[20:23], s65 offen lds\n"
"v_mfma_f32_16x16x32_bf16 v[156:159], v[232:235], v[4:7], 0\n"
"ds_read_b64_tr_b16 v[222:223], v193 offset:11264\n"
"ds_read_b64_tr_b16 v[224:225], v192 offset:2560\n"
"v_mfma_f32_16x16x32_bf16 v[156:159], v[236:239], v[12:15], v[156:159]\n"
"ds_read_b64_tr_b16 v[226:227], v192 offset:10752\n"
"v_accvgpr_read_b32 v198, a64\n"
"v_mfma_f32_16x16x32_bf16 v[140:143], v[236:239], v[8:11], 0\n"
"s_mov_b32 s65, 229504\n"
"s_add_u32 m0, s60, 81920\n"
"ds_read_b64_tr_b16 v[228:229], v192 offset:3584\n"
"v_accvgpr_read_b32 v199, a65\n"
"v_accvgpr_read_b32 v200, a66\n"
"v_accvgpr_read_b32 v201, a67\n"
"v_mul_f32 v198, v240, v198\n"
"v_mul_f32 v199, v240, v199\n"
"v_mul_f32 v200, v240, v200\n"
"v_mul_f32 v201, v240, v201\n"
"v_cvt_pk_bf16_f32 v16, v198, v199\n"
"v_cvt_pk_bf16_f32 v17, v200, v201\n"
"v_accvgpr_read_b32 v202, a80\n"
"v_accvgpr_read_b32 v203, a81\n"
"v_accvgpr_read_b32 v204, a82\n"
"v_accvgpr_read_b32 v205, a83\n"
"v_mul_f32 v202, v240, v202\n"
"v_mul_f32 v203, v240, v203\n"
"v_mul_f32 v204, v240, v204\n"
"v_mul_f32 v205, v240, v205\n"
"v_cvt_pk_bf16_f32 v18, v202, v203\n"
"v_cvt_pk_bf16_f32 v19, v204, v205\n"
"v_mfma_f32_16x16x32_bf16 v[140:143], v[232:235], v[0:3], v[140:143]\n"
"s_waitcnt lgkmcnt(7)\n"
"buffer_load_dwordx4 v249, s[20:23], s65 offen lds\n"
"v_accvgpr_read_b32 v198, a68\n"
"v_accvgpr_read_b32 v199, a69\n"
"v_accvgpr_read_b32 v200, a70\n"
"v_accvgpr_read_b32 v201, a71\n"
"v_mul_f32 v198, v241, v198\n"
"v_mul_f32 v199, v241, v199\n"
"v_mul_f32 v200, v241, v200\n"
"v_mul_f32 v201, v241, v201\n"
"v_cvt_pk_bf16_f32 v20, v198, v199\n"
"v_cvt_pk_bf16_f32 v21, v200, v201\n"
"v_accvgpr_read_b32 v202, a84\n"
"v_accvgpr_read_b32 v203, a85\n"
"v_accvgpr_read_b32 v204, a86\n"
"v_accvgpr_read_b32 v205, a87\n"
"v_mul_f32 v202, v241, v202\n"
"v_mul_f32 v203, v241, v203\n"
"v_mul_f32 v204, v241, v204\n"
"v_mul_f32 v205, v241, v205\n"
"v_cvt_pk_bf16_f32 v22, v202, v203\n"
"v_cvt_pk_bf16_f32 v23, v204, v205\n"
"v_accvgpr_read_b32 v198, a96\n"
"v_accvgpr_read_b32 v199, a97\n"
"v_accvgpr_read_b32 v200, a98\n"
"v_accvgpr_read_b32 v201, a99\n"
"v_mul_f32 v198, v240, v198\n"
"v_mul_f32 v199, v240, v199\n"
"v_mul_f32 v200, v240, v200\n"
"v_mul_f32 v201, v240, v201\n"
"v_cvt_pk_bf16_f32 v24, v198, v199\n"
"v_cvt_pk_bf16_f32 v25, v200, v201\n"
"v_accvgpr_read_b32 v202, a112\n"
"v_accvgpr_read_b32 v203, a113\n"
"v_accvgpr_read_b32 v204, a114\n"
"v_accvgpr_read_b32 v205, a115\n"
"v_mul_f32 v202, v240, v202\n"
"v_mul_f32 v203, v240, v203\n"
"v_mul_f32 v204, v240, v204\n"
"v_mul_f32 v205, v240, v205\n"
"v_cvt_pk_bf16_f32 v26, v202, v203\n"
"v_cvt_pk_bf16_f32 v27, v204, v205\n"
"v_mfma_f32_16x16x32_bf16 v[128:131], v[208:211], v[16:19], v[128:131]\n"
"ds_read_b64_tr_b16 v[230:231], v192 offset:11776\n"
"ds_read_b64_tr_b16 v[232:233], v193 offset:2560\n"
"v_accvgpr_read_b32 v198, a100\n"
"v_accvgpr_read_b32 v199, a101\n"
"v_accvgpr_read_b32 v200, a102\n"
"v_accvgpr_read_b32 v201, a103\n"
"v_mul_f32 v198, v241, v198\n"
"v_mul_f32 v199, v241, v199\n"
"v_mul_f32 v200, v241, v200\n"
"v_mul_f32 v201, v241, v201\n"
"v_cvt_pk_bf16_f32 v28, v198, v199\n"
"v_cvt_pk_bf16_f32 v29, v200, v201\n"
"v_accvgpr_read_b32 v202, a116\n"
"v_accvgpr_read_b32 v203, a117\n"
"v_accvgpr_read_b32 v204, a118\n"
"v_accvgpr_read_b32 v205, a119\n"
"v_mul_f32 v202, v241, v202\n"
"v_mul_f32 v203, v241, v203\n"
"v_mul_f32 v204, v241, v204\n"
"v_mul_f32 v205, v241, v205\n"
"v_cvt_pk_bf16_f32 v30, v202, v203\n"
"v_cvt_pk_bf16_f32 v31, v204, v205\n"
"v_mfma_f32_16x16x32_bf16 v[128:131], v[212:215], v[24:27], v[128:131]\n"
"s_mov_b32 s65, 258176\n"
"s_add_u32 m0, s60, 82944\n"
"ds_read_b64_tr_b16 v[234:235], v193 offset:10752\n"
"v_mfma_f32_16x16x32_bf16 v[144:147], v[212:215], v[28:31], v[144:147]\n"
"buffer_load_dwordx4 v249, s[20:23], s65 offen lds\n"
"v_mfma_f32_16x16x32_bf16 v[144:147], v[208:211], v[20:23], v[144:147]\n"
"s_waitcnt lgkmcnt(6)\n"
"ds_read_b64_tr_b16 v[236:237], v193 offset:3584\n"
"ds_read_b64_tr_b16 v[238:239], v193 offset:11776\n"
"v_mfma_f32_16x16x32_bf16 v[148:151], v[216:219], v[20:23], v[148:151]\n"
"ds_read_b64_tr_b16 v[208:209], v192 offset:16384\n"
"v_accvgpr_read_b32 v198, a128\n"
"v_mfma_f32_16x16x32_bf16 v[148:151], v[220:223], v[28:31], v[148:151]\n"
"s_mov_b32 s65, 286848\n"
"s_add_u32 m0, s60, 83968\n"
"ds_read_b64_tr_b16 v[210:211], v192 offset:24576\n"
"v_mfma_f32_16x16x32_bf16 v[132:135], v[220:223], v[24:27], v[132:135]\n"
"buffer_load_dwordx4 v249, s[20:23], s65 offen lds\n"
"v_mfma_f32_16x16x32_bf16 v[132:135], v[216:219], v[16:19], v[132:135]\n"
"s_waitcnt lgkmcnt(6)\n"
"ds_read_b64_tr_b16 v[212:213], v192 offset:17408\n"
"ds_read_b64_tr_b16 v[214:215], v192 offset:25600\n"
"v_mfma_f32_16x16x32_bf16 v[136:139], v[224:227], v[16:19], v[136:139]\n"
"s_mov_b32 s65, 315520\n"
"s_add_u32 m0, s60, 84992\n"
"ds_read_b64_tr_b16 v[216:217], v193 offset:16384\n"
"v_mfma_f32_16x16x32_bf16 v[136:139], v[228:231], v[24:27], v[136:139]\n"
"buffer_load_dwordx4 v249, s[20:23], s65 offen lds\n"
"v_mfma_f32_16x16x32_bf16 v[152:155], v[228:231], v[28:31], v[152:155]\n"
"ds_read_b64_tr_b16 v[218:219], v193 offset:24576\n"
"ds_read_b64_tr_b16 v[220:221], v193 offset:17408\n"
"v_mfma_f32_16x16x32_bf16 v[152:155], v[224:227], v[20:23], v[152:155]\n"
"s_waitcnt lgkmcnt(7)\n"
"ds_read_b64_tr_b16 v[222:223], v193 offset:25600\n"
"v_accvgpr_read_b32 v199, a129\n"
"v_mfma_f32_16x16x32_bf16 v[156:159], v[232:235], v[20:23], v[156:159]\n"
"ds_read_b64_tr_b16 v[224:225], v192 offset:16896\n"
"v_accvgpr_read_b32 v200, a130\n"
"v_mfma_f32_16x16x32_bf16 v[156:159], v[236:239], v[28:31], v[156:159]\n"
"ds_read_b64_tr_b16 v[226:227], v192 offset:25088\n"
"v_accvgpr_read_b32 v201, a131\n"
"v_mfma_f32_16x16x32_bf16 v[140:143], v[236:239], v[24:27], v[140:143]\n"
"ds_read_b64_tr_b16 v[228:229], v192 offset:17920\n"
"v_mul_f32 v198, v240, v198\n"
"v_mul_f32 v199, v240, v199\n"
"v_mul_f32 v200, v240, v200\n"
"v_mul_f32 v201, v240, v201\n"
"v_cvt_pk_bf16_f32 v32, v198, v199\n"
"v_cvt_pk_bf16_f32 v33, v200, v201\n"
"v_accvgpr_read_b32 v202, a144\n"
"v_accvgpr_read_b32 v203, a145\n"
"v_accvgpr_read_b32 v204, a146\n"
"v_accvgpr_read_b32 v205, a147\n"
"v_mul_f32 v202, v240, v202\n"
"v_mul_f32 v203, v240, v203\n"
"v_mul_f32 v204, v240, v204\n"
"v_mul_f32 v205, v240, v205\n"
"v_cvt_pk_bf16_f32 v34, v202, v203\n"
"v_cvt_pk_bf16_f32 v35, v204, v205\n"
"v_mfma_f32_16x16x32_bf16 v[140:143], v[232:235], v[16:19], v[140:143]\n"
"s_waitcnt lgkmcnt(7)\n"
"ds_read_b64_tr_b16 v[230:231], v192 offset:26112\n"
"v_accvgpr_read_b32 v198, a132\n"
"v_accvgpr_read_b32 v199, a133\n"
"v_accvgpr_read_b32 v200, a134\n"
"v_accvgpr_read_b32 v201, a135\n"
"v_mul_f32 v198, v241, v198\n"
"v_mul_f32 v199, v241, v199\n"
"v_mul_f32 v200, v241, v200\n"
"v_mul_f32 v201, v241, v201\n"
"v_cvt_pk_bf16_f32 v36, v198, v199\n"
"v_cvt_pk_bf16_f32 v37, v200, v201\n"
"v_accvgpr_read_b32 v202, a148\n"
"v_accvgpr_read_b32 v203, a149\n"
"v_accvgpr_read_b32 v204, a150\n"
"v_accvgpr_read_b32 v205, a151\n"
"v_mul_f32 v202, v241, v202\n"
"v_mul_f32 v203, v241, v203\n"
"v_mul_f32 v204, v241, v204\n"
"v_mul_f32 v205, v241, v205\n"
"v_cvt_pk_bf16_f32 v38, v202, v203\n"
"v_cvt_pk_bf16_f32 v39, v204, v205\n"
"v_accvgpr_read_b32 v198, a160\n"
"v_accvgpr_read_b32 v199, a161\n"
"v_accvgpr_read_b32 v200, a162\n"
"v_accvgpr_read_b32 v201, a163\n"
"v_mul_f32 v198, v240, v198\n"
"v_mul_f32 v199, v240, v199\n"
"v_mul_f32 v200, v240, v200\n"
"v_mul_f32 v201, v240, v201\n"
"v_cvt_pk_bf16_f32 v40, v198, v199\n"
"v_cvt_pk_bf16_f32 v41, v200, v201\n"
"v_accvgpr_read_b32 v202, a176\n"
"v_accvgpr_read_b32 v203, a177\n"
"v_accvgpr_read_b32 v204, a178\n"
"v_accvgpr_read_b32 v205, a179\n"
"v_mul_f32 v202, v240, v202\n"
"v_mul_f32 v203, v240, v203\n"
"v_mul_f32 v204, v240, v204\n"
"v_mul_f32 v205, v240, v205\n"
"v_cvt_pk_bf16_f32 v42, v202, v203\n"
"v_cvt_pk_bf16_f32 v43, v204, v205\n"
"v_mfma_f32_16x16x32_bf16 v[128:131], v[208:211], v[32:35], v[128:131]\n"
"s_mov_b32 s65, 344192\n"
"s_add_u32 m0, s60, 114688\n"
"ds_read_b64_tr_b16 v[232:233], v193 offset:16896\n"
"v_accvgpr_read_b32 v198, a164\n"
"v_accvgpr_read_b32 v199, a165\n"
"v_accvgpr_read_b32 v200, a166\n"
"v_accvgpr_read_b32 v201, a167\n"
"v_mul_f32 v198, v241, v198\n"
"v_mul_f32 v199, v241, v199\n"
"v_mul_f32 v200, v241, v200\n"
"v_mul_f32 v201, v241, v201\n"
"v_cvt_pk_bf16_f32 v44, v198, v199\n"
"v_cvt_pk_bf16_f32 v45, v200, v201\n"
"v_accvgpr_read_b32 v202, a180\n"
"v_accvgpr_read_b32 v203, a181\n"
"v_accvgpr_read_b32 v204, a182\n"
"v_accvgpr_read_b32 v205, a183\n"
"v_mul_f32 v202, v241, v202\n"
"v_mul_f32 v203, v241, v203\n"
"v_mul_f32 v204, v241, v204\n"
"v_mul_f32 v205, v241, v205\n"
"v_cvt_pk_bf16_f32 v46, v202, v203\n"
"v_cvt_pk_bf16_f32 v47, v204, v205\n"
"v_mfma_f32_16x16x32_bf16 v[128:131], v[212:215], v[40:43], v[128:131]\n"
"buffer_load_dwordx4 v249, s[20:23], s65 offen lds\n"
"v_mfma_f32_16x16x32_bf16 v[144:147], v[212:215], v[44:47], v[144:147]\n"
"ds_read_b64_tr_b16 v[234:235], v193 offset:25088\n"
"ds_read_b64_tr_b16 v[236:237], v193 offset:17920\n"
"v_mfma_f32_16x16x32_bf16 v[144:147], v[208:211], v[36:39], v[144:147]\n"
"s_waitcnt lgkmcnt(7)\n"
"s_mov_b32 s65, 372864\n"
"ds_read_b64_tr_b16 v[238:239], v193 offset:26112\n"
"v_mfma_f32_16x16x32_bf16 v[148:151], v[216:219], v[36:39], v[148:151]\n"
"s_add_u32 m0, s60, 115712\n"
"ds_read_b64_tr_b16 v[208:209], v192 offset:18432\n"
"s_lshl_b32 s6, s4, 11\n"
"v_mfma_f32_16x16x32_bf16 v[148:151], v[220:223], v[44:47], v[148:151]\n"
"buffer_load_dwordx4 v249, s[20:23], s65 offen lds\n"
"v_mfma_f32_16x16x32_bf16 v[132:135], v[220:223], v[40:43], v[132:135]\n"
"ds_read_b64_tr_b16 v[210:211], v192 offset:26624\n"
"ds_read_b64_tr_b16 v[212:213], v192 offset:19456\n"
"v_mfma_f32_16x16x32_bf16 v[132:135], v[216:219], v[32:35], v[132:135]\n"
"s_waitcnt lgkmcnt(7)\n"
"s_mov_b32 s65, 401536\n"
"ds_read_b64_tr_b16 v[214:215], v192 offset:27648\n"
"v_mfma_f32_16x16x32_bf16 v[136:139], v[224:227], v[32:35], v[136:139]\n"
"s_add_u32 m0, s60, 116736\n"
"ds_read_b64_tr_b16 v[216:217], v193 offset:18432\n"
"s_lshl_b32 s7, s4, 8\n"
"v_mfma_f32_16x16x32_bf16 v[136:139], v[228:231], v[40:43], v[136:139]\n"
"buffer_load_dwordx4 v249, s[20:23], s65 offen lds\n"
"v_mfma_f32_16x16x32_bf16 v[152:155], v[228:231], v[44:47], v[152:155]\n"
"s_mov_b32 s65, 430208\n"
"s_add_u32 m0, s60, 117760\n"
"ds_read_b64_tr_b16 v[218:219], v193 offset:26624\n"
"ds_read_b64_tr_b16 v[220:221], v193 offset:19456\n"
"v_mfma_f32_16x16x32_bf16 v[152:155], v[224:227], v[36:39], v[152:155]\n"
"s_waitcnt lgkmcnt(7)\n"
"buffer_load_dwordx4 v249, s[20:23], s65 offen lds\n"
"v_mfma_f32_16x16x32_bf16 v[156:159], v[232:235], v[36:39], v[156:159]\n"
"ds_read_b64_tr_b16 v[222:223], v193 offset:27648\n"
"ds_read_b64_tr_b16 v[224:225], v192 offset:18944\n"
"v_mfma_f32_16x16x32_bf16 v[156:159], v[236:239], v[44:47], v[156:159]\n"
"ds_read_b64_tr_b16 v[226:227], v192 offset:27136\n"
"v_accvgpr_read_b32 v198, a192\n"
"v_mfma_f32_16x16x32_bf16 v[140:143], v[236:239], v[40:43], v[140:143]\n"
"s_mov_b32 s65, 256\n"
"s_add_u32 m0, s60, 131072\n"
"ds_read_b64_tr_b16 v[228:229], v192 offset:19968\n"
"v_accvgpr_read_b32 v199, a193\n"
"v_accvgpr_read_b32 v200, a194\n"
"v_accvgpr_read_b32 v201, a195\n"
"v_mul_f32 v198, v240, v198\n"
"v_mul_f32 v199, v240, v199\n"
"v_mul_f32 v200, v240, v200\n"
"v_mul_f32 v201, v240, v201\n"
"v_cvt_pk_bf16_f32 v48, v198, v199\n"
"v_cvt_pk_bf16_f32 v49, v200, v201\n"
"v_accvgpr_read_b32 v202, a208\n"
"v_accvgpr_read_b32 v203, a209\n"
"v_accvgpr_read_b32 v204, a210\n"
"v_accvgpr_read_b32 v205, a211\n"
"v_mul_f32 v202, v240, v202\n"
"v_mul_f32 v203, v240, v203\n"
"v_mul_f32 v204, v240, v204\n"
"v_mul_f32 v205, v240, v205\n"
"v_cvt_pk_bf16_f32 v50, v202, v203\n"
"v_cvt_pk_bf16_f32 v51, v204, v205\n"
"v_mfma_f32_16x16x32_bf16 v[140:143], v[232:235], v[32:35], v[140:143]\n"
"s_waitcnt lgkmcnt(7)\n"
"buffer_load_dwordx4 v249, s[20:23], s65 offen lds\n"
"v_accvgpr_read_b32 v198, a196\n"
"v_accvgpr_read_b32 v199, a197\n"
"v_accvgpr_read_b32 v200, a198\n"
"v_accvgpr_read_b32 v201, a199\n"
"v_mul_f32 v198, v241, v198\n"
"v_mul_f32 v199, v241, v199\n"
"v_mul_f32 v200, v241, v200\n"
"v_mul_f32 v201, v241, v201\n"
"v_cvt_pk_bf16_f32 v52, v198, v199\n"
"v_cvt_pk_bf16_f32 v53, v200, v201\n"
"v_accvgpr_read_b32 v202, a212\n"
"v_accvgpr_read_b32 v203, a213\n"
"v_accvgpr_read_b32 v204, a214\n"
"v_accvgpr_read_b32 v205, a215\n"
"v_mul_f32 v202, v241, v202\n"
"v_mul_f32 v203, v241, v203\n"
"v_mul_f32 v204, v241, v204\n"
"v_mul_f32 v205, v241, v205\n"
"v_cvt_pk_bf16_f32 v54, v202, v203\n"
"v_cvt_pk_bf16_f32 v55, v204, v205\n"
"v_accvgpr_read_b32 v198, a224\n"
"v_accvgpr_read_b32 v199, a225\n"
"v_accvgpr_read_b32 v200, a226\n"
"v_accvgpr_read_b32 v201, a227\n"
"v_mul_f32 v198, v240, v198\n"
"v_mul_f32 v199, v240, v199\n"
"v_mul_f32 v200, v240, v200\n"
"v_mul_f32 v201, v240, v201\n"
"v_cvt_pk_bf16_f32 v56, v198, v199\n"
"v_cvt_pk_bf16_f32 v57, v200, v201\n"
"v_accvgpr_read_b32 v202, a240\n"
"v_accvgpr_read_b32 v203, a241\n"
"v_accvgpr_read_b32 v204, a242\n"
"v_accvgpr_read_b32 v205, a243\n"
"v_mul_f32 v202, v240, v202\n"
"v_mul_f32 v203, v240, v203\n"
"v_mul_f32 v204, v240, v204\n"
"v_mul_f32 v205, v240, v205\n"
"v_cvt_pk_bf16_f32 v58, v202, v203\n"
"v_cvt_pk_bf16_f32 v59, v204, v205\n"
"v_mfma_f32_16x16x32_bf16 v[128:131], v[208:211], v[48:51], v[128:131]\n"
"ds_read_b64_tr_b16 v[230:231], v192 offset:28160\n"
"ds_read_b64_tr_b16 v[232:233], v193 offset:18944\n"
"v_accvgpr_read_b32 v198, a228\n"
"v_accvgpr_read_b32 v199, a229\n"
"v_accvgpr_read_b32 v200, a230\n"
"v_accvgpr_read_b32 v201, a231\n"
"v_mul_f32 v198, v241, v198\n"
"v_mul_f32 v199, v241, v199\n"
"v_mul_f32 v200, v241, v200\n"
"v_mul_f32 v201, v241, v201\n"
"v_cvt_pk_bf16_f32 v60, v198, v199\n"
"v_cvt_pk_bf16_f32 v61, v200, v201\n"
"v_accvgpr_read_b32 v202, a244\n"
"v_accvgpr_read_b32 v203, a245\n"
"v_accvgpr_read_b32 v204, a246\n"
"v_accvgpr_read_b32 v205, a247\n"
"v_mul_f32 v202, v241, v202\n"
"v_mul_f32 v203, v241, v203\n"
"v_mul_f32 v204, v241, v204\n"
"v_mul_f32 v205, v241, v205\n"
"v_cvt_pk_bf16_f32 v62, v202, v203\n"
"v_cvt_pk_bf16_f32 v63, v204, v205\n"
"v_mfma_f32_16x16x32_bf16 v[128:131], v[212:215], v[56:59], v[128:131]\n"
"s_mov_b32 s65, 28928\n"
"s_add_u32 m0, s60, 132096\n"
"ds_read_b64_tr_b16 v[234:235], v193 offset:27136\n"
"v_mfma_f32_16x16x32_bf16 v[144:147], v[212:215], v[60:63], v[144:147]\n"
"buffer_load_dwordx4 v249, s[20:23], s65 offen lds\n"
"v_mfma_f32_16x16x32_bf16 v[144:147], v[208:211], v[52:55], v[144:147]\n"
"s_waitcnt lgkmcnt(6)\n"
"ds_read_b64_tr_b16 v[236:237], v193 offset:19968\n"
"ds_read_b64_tr_b16 v[238:239], v193 offset:28160\n"
"v_mfma_f32_16x16x32_bf16 v[148:151], v[216:219], v[52:55], v[148:151]\n"
"ds_read_b64_tr_b16 v[208:209], v194 offset:32768\n"
"v_mbcnt_lo_u32_b32 v207, -1, 0\n"
"v_mfma_f32_16x16x32_bf16 v[148:151], v[220:223], v[60:63], v[148:151]\n"
"s_mov_b32 s65, 57600\n"
"s_add_u32 m0, s60, 133120\n"
"ds_read_b64_tr_b16 v[210:211], v194 offset:40960\n"
"v_mfma_f32_16x16x32_bf16 v[132:135], v[220:223], v[56:59], v[132:135]\n"
"buffer_load_dwordx4 v249, s[20:23], s65 offen lds\n"
"v_mfma_f32_16x16x32_bf16 v[132:135], v[216:219], v[48:51], v[132:135]\n"
"s_waitcnt lgkmcnt(6)\n"
"ds_read_b64_tr_b16 v[212:213], v194 offset:33792\n"
"ds_read_b64_tr_b16 v[214:215], v194 offset:41984\n"
"v_mfma_f32_16x16x32_bf16 v[136:139], v[224:227], v[48:51], v[136:139]\n"
"s_mov_b32 s65, 86272\n"
"s_add_u32 m0, s60, 134144\n"
"ds_read_b64_tr_b16 v[216:217], v195 offset:32768\n"
"v_mfma_f32_16x16x32_bf16 v[136:139], v[228:231], v[56:59], v[136:139]\n"
"buffer_load_dwordx4 v249, s[20:23], s65 offen lds\n"
"v_mfma_f32_16x16x32_bf16 v[152:155], v[228:231], v[60:63], v[152:155]\n"
"ds_read_b64_tr_b16 v[218:219], v195 offset:40960\n"
"ds_read_b64_tr_b16 v[220:221], v195 offset:33792\n"
"v_mfma_f32_16x16x32_bf16 v[152:155], v[224:227], v[52:55], v[152:155]\n"
"s_waitcnt lgkmcnt(7)\n"
"ds_read_b64_tr_b16 v[222:223], v195 offset:41984\n"
"v_mbcnt_hi_u32_b32 v207, -1, v207\n"
"v_mfma_f32_16x16x32_bf16 v[156:159], v[232:235], v[52:55], v[156:159]\n"
"ds_read_b64_tr_b16 v[224:225], v194 offset:33280\n"
"v_and_b32 v251, 7, v207\n"
"v_mfma_f32_16x16x32_bf16 v[156:159], v[236:239], v[60:63], v[156:159]\n"
"ds_read_b64_tr_b16 v[226:227], v194 offset:41472\n"
"s_add_u32 s6, s6, 917504\n"
"v_lshl_add_u32 v250, v251, 8, s6\n"
"s_add_u32 s7, s7, 917504\n"
"v_lshl_add_u32 v251, v251, 11, s7\n"
"v_mfma_f32_16x16x32_bf16 v[140:143], v[236:239], v[56:59], v[140:143]\n"
"s_waitcnt vmcnt(16)\n"
"s_barrier\n"
"v_accvgpr_read_b32 v198, a8\n"
"v_accvgpr_read_b32 v199, a9\n"
"v_accvgpr_read_b32 v200, a10\n"
"v_accvgpr_read_b32 v201, a11\n"
"v_mul_f32 v198, v242, v198\n"
"v_mul_f32 v199, v242, v199\n"
"v_mul_f32 v200, v242, v200\n"
"v_mul_f32 v201, v242, v201\n"
"v_cvt_pk_bf16_f32 v64, v198, v199\n"
"v_cvt_pk_bf16_f32 v65, v200, v201\n"
"v_accvgpr_read_b32 v202, a24\n"
"v_accvgpr_read_b32 v203, a25\n"
"v_accvgpr_read_b32 v204, a26\n"
"v_accvgpr_read_b32 v205, a27\n"
"v_mul_f32 v202, v242, v202\n"
"v_mul_f32 v203, v242, v203\n"
"v_mul_f32 v204, v242, v204\n"
"v_mul_f32 v205, v242, v205\n"
"v_cvt_pk_bf16_f32 v66, v202, v203\n"
"v_cvt_pk_bf16_f32 v67, v204, v205\n"
"v_mfma_f32_16x16x32_bf16 v[140:143], v[232:235], v[48:51], v[140:143]\n"
"IWB2_%=:\n"
"s_cbranch_execz IWB6_%=\n"
"s_waitcnt lgkmcnt(6)\n"
"ds_read_b64_tr_b16 v[228:229], v194 offset:34304\n"
"ds_read_b64_tr_b16 v[230:231], v194 offset:42496\n"
"v_accvgpr_read_b32 v198, a12\n"
"v_accvgpr_read_b32 v199, a13\n"
"v_accvgpr_read_b32 v200, a14\n"
"v_accvgpr_read_b32 v201, a15\n"
"v_mul_f32 v198, v243, v198\n"
"v_mul_f32 v199, v243, v199\n"
"v_mul_f32 v200, v243, v200\n"
"v_mul_f32 v201, v243, v201\n"
"v_cvt_pk_bf16_f32 v68, v198, v199\n"
"v_cvt_pk_bf16_f32 v69, v200, v201\n"
"v_accvgpr_read_b32 v202, a28\n"
"v_accvgpr_read_b32 v203, a29\n"
"v_accvgpr_read_b32 v204, a30\n"
"v_accvgpr_read_b32 v205, a31\n"
"v_mul_f32 v202, v243, v202\n"
"v_mul_f32 v203, v243, v203\n"
"v_mul_f32 v204, v243, v204\n"
"v_mul_f32 v205, v243, v205\n"
"v_cvt_pk_bf16_f32 v70, v202, v203\n"
"v_cvt_pk_bf16_f32 v71, v204, v205\n"
"v_accvgpr_read_b32 v198, a40\n"
"v_accvgpr_read_b32 v199, a41\n"
"v_accvgpr_read_b32 v200, a42\n"
"v_accvgpr_read_b32 v201, a43\n"
"v_mul_f32 v198, v242, v198\n"
"v_mul_f32 v199, v242, v199\n"
"v_mul_f32 v200, v242, v200\n"
"v_mul_f32 v201, v242, v201\n"
"v_cvt_pk_bf16_f32 v72, v198, v199\n"
"v_cvt_pk_bf16_f32 v73, v200, v201\n"
"v_accvgpr_read_b32 v202, a56\n"
"v_accvgpr_read_b32 v203, a57\n"
"v_accvgpr_read_b32 v204, a58\n"
"v_accvgpr_read_b32 v205, a59\n"
"v_mul_f32 v202, v242, v202\n"
"v_mul_f32 v203, v242, v203\n"
"v_mul_f32 v204, v242, v204\n"
"v_mul_f32 v205, v242, v205\n"
"v_cvt_pk_bf16_f32 v74, v202, v203\n"
"v_cvt_pk_bf16_f32 v75, v204, v205\n"
"v_mfma_f32_16x16x32_bf16 v[128:131], v[208:211], v[64:67], v[128:131]\n"
"s_mov_b32 s65, 114944\n"
"s_add_u32 m0, s60, 0\n"
"ds_read_b64_tr_b16 v[232:233], v195 offset:33280\n"
"v_accvgpr_read_b32 v198, a44\n"
"v_accvgpr_read_b32 v199, a45\n"
"v_accvgpr_read_b32 v200, a46\n"
"v_accvgpr_read_b32 v201, a47\n"
"v_mul_f32 v198, v243, v198\n"
"v_mul_f32 v199, v243, v199\n"
"v_mul_f32 v200, v243, v200\n"
"v_mul_f32 v201, v243, v201\n"
"v_cvt_pk_bf16_f32 v76, v198, v199\n"
"v_cvt_pk_bf16_f32 v77, v200, v201\n"
"v_accvgpr_read_b32 v202, a60\n"
"v_accvgpr_read_b32 v203, a61\n"
"v_accvgpr_read_b32 v204, a62\n"
"v_accvgpr_read_b32 v205, a63\n"
"v_mul_f32 v202, v243, v202\n"
"v_mul_f32 v203, v243, v203\n"
"v_mul_f32 v204, v243, v204\n"
"v_mul_f32 v205, v243, v205\n"
"v_cvt_pk_bf16_f32 v78, v202, v203\n"
"v_cvt_pk_bf16_f32 v79, v204, v205\n"
"v_mfma_f32_16x16x32_bf16 v[128:131], v[212:215], v[72:75], v[128:131]\n"
"buffer_load_dwordx4 v249, s[20:23], s65 offen lds\n"
"v_mfma_f32_16x16x32_bf16 v[144:147], v[212:215], v[76:79], v[144:147]\n"
"s_mov_b32 s65, 143616\n"
"s_add_u32 m0, s60, 1024\n"
"ds_read_b64_tr_b16 v[234:235], v195 offset:41472\n"
"ds_read_b64_tr_b16 v[236:237], v195 offset:34304\n"
"v_mfma_f32_16x16x32_bf16 v[144:147], v[208:211], v[68:71], v[144:147]\n"
"s_waitcnt lgkmcnt(7)\n"
"buffer_load_dwordx4 v249, s[20:23], s65 offen lds\n"
"v_mfma_f32_16x16x32_bf16 v[148:151], v[216:219], v[68:71], v[148:151]\n"
"ds_read_b64_tr_b16 v[238:239], v195 offset:42496\n"
"ds_read_b64_tr_b16 v[208:209], v194 offset:34816\n"
"v_mfma_f32_16x16x32_bf16 v[148:151], v[220:223], v[76:79], v[148:151]\n"
"s_mov_b32 s65, 172288\n"
"s_add_u32 m0, s60, 2048\n"
"ds_read_b64_tr_b16 v[210:211], v194 offset:43008\n"
"v_mfma_f32_16x16x32_bf16 v[132:135], v[220:223], v[72:75], v[132:135]\n"
"buffer_load_dwordx4 v249, s[20:23], s65 offen lds\n"
"v_mfma_f32_16x16x32_bf16 v[132:135], v[216:219], v[64:67], v[132:135]\n"
"s_waitcnt lgkmcnt(6)\n"
"ds_read_b64_tr_b16 v[212:213], v194 offset:35840\n"
"ds_read_b64_tr_b16 v[214:215], v194 offset:44032\n"
"v_mfma_f32_16x16x32_bf16 v[136:139], v[224:227], v[64:67], v[136:139]\n"
"s_mov_b32 s65, 200960\n"
"s_add_u32 m0, s60, 3072\n"
"ds_read_b64_tr_b16 v[216:217], v195 offset:34816\n"
"v_mfma_f32_16x16x32_bf16 v[136:139], v[228:231], v[72:75], v[136:139]\n"
"buffer_load_dwordx4 v249, s[20:23], s65 offen lds\n"
"v_mfma_f32_16x16x32_bf16 v[152:155], v[228:231], v[76:79], v[152:155]\n"
"ds_read_b64_tr_b16 v[218:219], v195 offset:43008\n"
"ds_read_b64_tr_b16 v[220:221], v195 offset:35840\n"
"v_mfma_f32_16x16x32_bf16 v[152:155], v[224:227], v[68:71], v[152:155]\n"
"s_waitcnt lgkmcnt(7)\n"
"s_mov_b32 s65, 229632\n"
"ds_read_b64_tr_b16 v[222:223], v195 offset:44032\n"
"v_mfma_f32_16x16x32_bf16 v[156:159], v[232:235], v[68:71], v[156:159]\n"
"s_add_u32 m0, s60, 16384\n"
"ds_read_b64_tr_b16 v[224:225], v194 offset:35328\n"
"v_mfma_f32_16x16x32_bf16 v[156:159], v[236:239], v[76:79], v[156:159]\n"
"buffer_load_dwordx4 v249, s[20:23], s65 offen lds\n"
"v_mfma_f32_16x16x32_bf16 v[140:143], v[236:239], v[72:75], v[140:143]\n"
"s_mov_b32 s65, 258304\n"
"s_add_u32 m0, s60, 17408\n"
"ds_read_b64_tr_b16 v[226:227], v194 offset:43520\n"
"ds_read_b64_tr_b16 v[228:229], v194 offset:36352\n"
"v_accvgpr_read_b32 v198, a72\n"
"v_accvgpr_read_b32 v199, a73\n"
"v_accvgpr_read_b32 v200, a74\n"
"v_accvgpr_read_b32 v201, a75\n"
"v_mul_f32 v198, v242, v198\n"
"v_mul_f32 v199, v242, v199\n"
"v_mul_f32 v200, v242, v200\n"
"v_mul_f32 v201, v242, v201\n"
"v_cvt_pk_bf16_f32 v80, v198, v199\n"
"v_cvt_pk_bf16_f32 v81, v200, v201\n"
"v_accvgpr_read_b32 v202, a88\n"
"v_accvgpr_read_b32 v203, a89\n"
"v_accvgpr_read_b32 v204, a90\n"
"v_accvgpr_read_b32 v205, a91\n"
"v_mul_f32 v202, v242, v202\n"
"v_mul_f32 v203, v242, v203\n"
"v_mul_f32 v204, v242, v204\n"
"v_mul_f32 v205, v242, v205\n"
"v_cvt_pk_bf16_f32 v82, v202, v203\n"
"v_cvt_pk_bf16_f32 v83, v204, v205\n"
"v_mfma_f32_16x16x32_bf16 v[140:143], v[232:235], v[64:67], v[140:143]\n"
"s_waitcnt lgkmcnt(7)\n"
"buffer_load_dwordx4 v249, s[20:23], s65 offen lds\n"
"v_accvgpr_read_b32 v198, a76\n"
"v_accvgpr_read_b32 v199, a77\n"
"v_accvgpr_read_b32 v200, a78\n"
"v_accvgpr_read_b32 v201, a79\n"
"v_mul_f32 v198, v243, v198\n"
"v_mul_f32 v199, v243, v199\n"
"v_mul_f32 v200, v243, v200\n"
"v_mul_f32 v201, v243, v201\n"
"v_cvt_pk_bf16_f32 v84, v198, v199\n"
"v_cvt_pk_bf16_f32 v85, v200, v201\n"
"v_accvgpr_read_b32 v202, a92\n"
"v_accvgpr_read_b32 v203, a93\n"
"v_accvgpr_read_b32 v204, a94\n"
"v_accvgpr_read_b32 v205, a95\n"
"v_mul_f32 v202, v243, v202\n"
"v_mul_f32 v203, v243, v203\n"
"v_mul_f32 v204, v243, v204\n"
"v_mul_f32 v205, v243, v205\n"
"v_cvt_pk_bf16_f32 v86, v202, v203\n"
"v_cvt_pk_bf16_f32 v87, v204, v205\n"
"v_accvgpr_read_b32 v198, a104\n"
"v_accvgpr_read_b32 v199, a105\n"
"v_accvgpr_read_b32 v200, a106\n"
"v_accvgpr_read_b32 v201, a107\n"
"v_mul_f32 v198, v242, v198\n"
"v_mul_f32 v199, v242, v199\n"
"v_mul_f32 v200, v242, v200\n"
"v_mul_f32 v201, v242, v201\n"
"v_cvt_pk_bf16_f32 v88, v198, v199\n"
"v_cvt_pk_bf16_f32 v89, v200, v201\n"
"v_accvgpr_read_b32 v202, a120\n"
"v_accvgpr_read_b32 v203, a121\n"
"v_accvgpr_read_b32 v204, a122\n"
"v_accvgpr_read_b32 v205, a123\n"
"v_mul_f32 v202, v242, v202\n"
"v_mul_f32 v203, v242, v203\n"
"v_mul_f32 v204, v242, v204\n"
"v_mul_f32 v205, v242, v205\n"
"v_cvt_pk_bf16_f32 v90, v202, v203\n"
"v_cvt_pk_bf16_f32 v91, v204, v205\n"
"v_mfma_f32_16x16x32_bf16 v[128:131], v[208:211], v[80:83], v[128:131]\n"
"s_mov_b32 s65, 286976\n"
"s_add_u32 m0, s60, 18432\n"
"ds_read_b64_tr_b16 v[230:231], v194 offset:44544\n"
"ds_read_b64_tr_b16 v[232:233], v195 offset:35328\n"
"v_accvgpr_read_b32 v198, a108\n"
"v_accvgpr_read_b32 v199, a109\n"
"v_accvgpr_read_b32 v200, a110\n"
"v_accvgpr_read_b32 v201, a111\n"
"v_mul_f32 v198, v243, v198\n"
"v_mul_f32 v199, v243, v199\n"
"v_mul_f32 v200, v243, v200\n"
"v_mul_f32 v201, v243, v201\n"
"v_cvt_pk_bf16_f32 v92, v198, v199\n"
"v_cvt_pk_bf16_f32 v93, v200, v201\n"
"v_accvgpr_read_b32 v202, a124\n"
"v_accvgpr_read_b32 v203, a125\n"
"v_accvgpr_read_b32 v204, a126\n"
"v_accvgpr_read_b32 v205, a127\n"
"v_mul_f32 v202, v243, v202\n"
"v_mul_f32 v203, v243, v203\n"
"v_mul_f32 v204, v243, v204\n"
"v_mul_f32 v205, v243, v205\n"
"v_cvt_pk_bf16_f32 v94, v202, v203\n"
"v_cvt_pk_bf16_f32 v95, v204, v205\n"
"v_mfma_f32_16x16x32_bf16 v[128:131], v[212:215], v[88:91], v[128:131]\n"
"buffer_load_dwordx4 v249, s[20:23], s65 offen lds\n"
"v_mfma_f32_16x16x32_bf16 v[144:147], v[212:215], v[92:95], v[144:147]\n"
"ds_read_b64_tr_b16 v[234:235], v195 offset:43520\n"
"ds_read_b64_tr_b16 v[236:237], v195 offset:36352\n"
"v_mfma_f32_16x16x32_bf16 v[144:147], v[208:211], v[84:87], v[144:147]\n"
"s_waitcnt lgkmcnt(7)\n"
"s_mov_b32 s65, 315648\n"
"ds_read_b64_tr_b16 v[238:239], v195 offset:44544\n"
"v_mfma_f32_16x16x32_bf16 v[148:151], v[216:219], v[84:87], v[148:151]\n"
"s_add_u32 m0, s60, 19456\n"
"ds_read_b64_tr_b16 v[208:209], v192 offset:32768\n"
"v_mfma_f32_16x16x32_bf16 v[148:151], v[220:223], v[92:95], v[148:151]\n"
"buffer_load_dwordx4 v249, s[20:23], s65 offen lds\n"
"v_mfma_f32_16x16x32_bf16 v[132:135], v[220:223], v[88:91], v[132:135]\n"
"buffer_load_dwordx4 v[252:255], v250, s[28:31], 0 offen offset:0 sc1\n"
"v_mfma_f32_16x16x32_bf16 v[132:135], v[216:219], v[80:83], v[132:135]\n"
"s_waitcnt lgkmcnt(5)\n"
"ds_read_b64_tr_b16 v[210:211], v192 offset:40960\n"
"ds_read_b64_tr_b16 v[212:213], v192 offset:33792\n"
"v_mfma_f32_16x16x32_bf16 v[136:139], v[224:227], v[80:83], v[136:139]\n"
"ds_read_b64_tr_b16 v[214:215], v192 offset:41984\n"
"ds_read_b64_tr_b16 v[216:217], v193 offset:32768\n"
"v_mfma_f32_16x16x32_bf16 v[136:139], v[228:231], v[88:91], v[136:139]\n"
"buffer_load_dwordx4 v[244:247], v251, s[28:31], 0 offen offset:0 sc1\n"
"v_mfma_f32_16x16x32_bf16 v[152:155], v[228:231], v[92:95], v[152:155]\n"
"ds_read_b64_tr_b16 v[218:219], v193 offset:40960\n"
"ds_read_b64_tr_b16 v[220:221], v193 offset:33792\n"
"v_mfma_f32_16x16x32_bf16 v[152:155], v[224:227], v[84:87], v[152:155]\n"
"s_waitcnt lgkmcnt(7)\n"
"ds_read_b64_tr_b16 v[222:223], v193 offset:41984\n"
"v_accvgpr_read_b32 v198, a136\n"
"v_mfma_f32_16x16x32_bf16 v[156:159], v[232:235], v[84:87], v[156:159]\n"
"ds_read_b64_tr_b16 v[224:225], v192 offset:33280\n"
"v_accvgpr_read_b32 v199, a137\n"
"v_mfma_f32_16x16x32_bf16 v[156:159], v[236:239], v[92:95], v[156:159]\n"
"ds_read_b64_tr_b16 v[226:227], v192 offset:41472\n"
"v_accvgpr_read_b32 v200, a138\n"
"v_mfma_f32_16x16x32_bf16 v[140:143], v[236:239], v[88:91], v[140:143]\n"
"ds_read_b64_tr_b16 v[228:229], v192 offset:34304\n"
"v_accvgpr_read_b32 v201, a139\n"
"v_mul_f32 v198, v242, v198\n"
"v_mul_f32 v199, v242, v199\n"
"v_mul_f32 v200, v242, v200\n"
"v_mul_f32 v201, v242, v201\n"
"v_cvt_pk_bf16_f32 v96, v198, v199\n"
"v_cvt_pk_bf16_f32 v97, v200, v201\n"
"v_accvgpr_read_b32 v202, a152\n"
"v_accvgpr_read_b32 v203, a153\n"
"v_accvgpr_read_b32 v204, a154\n"
"v_accvgpr_read_b32 v205, a155\n"
"v_mul_f32 v202, v242, v202\n"
"v_mul_f32 v203, v242, v203\n"
"v_mul_f32 v204, v242, v204\n"
"v_mul_f32 v205, v242, v205\n"
"v_cvt_pk_bf16_f32 v98, v202, v203\n"
"v_cvt_pk_bf16_f32 v99, v204, v205\n"
"v_mfma_f32_16x16x32_bf16 v[140:143], v[232:235], v[80:83], v[140:143]\n"
"s_waitcnt lgkmcnt(7)\n"
"ds_read_b64_tr_b16 v[230:231], v192 offset:42496\n"
"v_accvgpr_read_b32 v198, a140\n"
"v_accvgpr_read_b32 v199, a141\n"
"v_accvgpr_read_b32 v200, a142\n"
"v_accvgpr_read_b32 v201, a143\n"
"v_mul_f32 v198, v243, v198\n"
"v_mul_f32 v199, v243, v199\n"
"v_mul_f32 v200, v243, v200\n"
"v_mul_f32 v201, v243, v201\n"
"v_cvt_pk_bf16_f32 v100, v198, v199\n"
"v_cvt_pk_bf16_f32 v101, v200, v201\n"
"v_accvgpr_read_b32 v202, a156\n"
"v_accvgpr_read_b32 v203, a157\n"
"v_accvgpr_read_b32 v204, a158\n"
"v_accvgpr_read_b32 v205, a159\n"
"v_mul_f32 v202, v243, v202\n"
"v_mul_f32 v203, v243, v203\n"
"v_mul_f32 v204, v243, v204\n"
"v_mul_f32 v205, v243, v205\n"
"v_cvt_pk_bf16_f32 v102, v202, v203\n"
"v_cvt_pk_bf16_f32 v103, v204, v205\n"
"v_accvgpr_read_b32 v198, a168\n"
"v_accvgpr_read_b32 v199, a169\n"
"v_accvgpr_read_b32 v200, a170\n"
"v_accvgpr_read_b32 v201, a171\n"
"v_mul_f32 v198, v242, v198\n"
"v_mul_f32 v199, v242, v199\n"
"v_mul_f32 v200, v242, v200\n"
"v_mul_f32 v201, v242, v201\n"
"v_cvt_pk_bf16_f32 v104, v198, v199\n"
"v_cvt_pk_bf16_f32 v105, v200, v201\n"
"v_accvgpr_read_b32 v202, a184\n"
"v_accvgpr_read_b32 v203, a185\n"
"v_accvgpr_read_b32 v204, a186\n"
"v_accvgpr_read_b32 v205, a187\n"
"v_mul_f32 v202, v242, v202\n"
"v_mul_f32 v203, v242, v203\n"
"v_mul_f32 v204, v242, v204\n"
"v_mul_f32 v205, v242, v205\n"
"v_cvt_pk_bf16_f32 v106, v202, v203\n"
"v_cvt_pk_bf16_f32 v107, v204, v205\n"
"v_mfma_f32_16x16x32_bf16 v[128:131], v[208:211], v[96:99], v[128:131]\n"
"s_mov_b32 s65, 344320\n"
"ds_read_b64_tr_b16 v[232:233], v193 offset:33280\n"
"v_accvgpr_read_b32 v198, a172\n"
"v_accvgpr_read_b32 v199, a173\n"
"v_accvgpr_read_b32 v200, a174\n"
"v_accvgpr_read_b32 v201, a175\n"
"v_mul_f32 v198, v243, v198\n"
"v_mul_f32 v199, v243, v199\n"
"v_mul_f32 v200, v243, v200\n"
"v_mul_f32 v201, v243, v201\n"
"v_cvt_pk_bf16_f32 v108, v198, v199\n"
"v_cvt_pk_bf16_f32 v109, v200, v201\n"
"v_accvgpr_read_b32 v202, a188\n"
"v_accvgpr_read_b32 v203, a189\n"
"v_accvgpr_read_b32 v204, a190\n"
"v_accvgpr_read_b32 v205, a191\n"
"v_mul_f32 v202, v243, v202\n"
"v_mul_f32 v203, v243, v203\n"
"v_mul_f32 v204, v243, v204\n"
"v_mul_f32 v205, v243, v205\n"
"v_cvt_pk_bf16_f32 v110, v202, v203\n"
"v_cvt_pk_bf16_f32 v111, v204, v205\n"
"v_mfma_f32_16x16x32_bf16 v[128:131], v[212:215], v[104:107], v[128:131]\n"
"s_add_u32 m0, s60, 147456\n"
"ds_read_b64_tr_b16 v[234:235], v193 offset:41472\n"
"v_mfma_f32_16x16x32_bf16 v[144:147], v[212:215], v[108:111], v[144:147]\n"
"buffer_load_dwordx4 v249, s[20:23], s65 offen lds\n"
"v_mfma_f32_16x16x32_bf16 v[144:147], v[208:211], v[100:103], v[144:147]\n"
"s_waitcnt lgkmcnt(6)\n"
"ds_read_b64_tr_b16 v[236:237], v193 offset:34304\n"
"ds_read_b64_tr_b16 v[238:239], v193 offset:42496\n"
"v_mfma_f32_16x16x32_bf16 v[148:151], v[216:219], v[100:103], v[148:151]\n"
"ds_read_b64_tr_b16 v[208:209], v192 offset:34816\n"
"v_accvgpr_read_b32 v198, a200\n"
"v_mfma_f32_16x16x32_bf16 v[148:151], v[220:223], v[108:111], v[148:151]\n"
"ds_read_b64_tr_b16 v[210:211], v192 offset:43008\n"
"v_accvgpr_read_b32 v199, a201\n"
"v_mfma_f32_16x16x32_bf16 v[132:135], v[220:223], v[104:107], v[132:135]\n"
"ds_read_b64_tr_b16 v[212:213], v192 offset:35840\n"
"v_accvgpr_read_b32 v200, a202\n"
"v_mfma_f32_16x16x32_bf16 v[132:135], v[216:219], v[96:99], v[132:135]\n"
"s_waitcnt lgkmcnt(7)\n"
"ds_read_b64_tr_b16 v[214:215], v192 offset:44032\n"
"v_accvgpr_read_b32 v201, a203\n"
"v_mfma_f32_16x16x32_bf16 v[136:139], v[224:227], v[96:99], v[136:139]\n"
"s_mov_b32 s65, 372992\n"
"ds_read_b64_tr_b16 v[216:217], v193 offset:34816\n"
"v_mul_f32 v198, v242, v198\n"
"v_mfma_f32_16x16x32_bf16 v[136:139], v[228:231], v[104:107], v[136:139]\n"
"s_add_u32 m0, s60, 148480\n"
"ds_read_b64_tr_b16 v[218:219], v193 offset:43008\n"
"v_mul_f32 v199, v242, v199\n"
"v_mfma_f32_16x16x32_bf16 v[152:155], v[228:231], v[108:111], v[152:155]\n"
"buffer_load_dwordx4 v249, s[20:23], s65 offen lds\n"
"v_mfma_f32_16x16x32_bf16 v[152:155], v[224:227], v[100:103], v[152:155]\n"
"s_waitcnt lgkmcnt(6)\n"
"ds_read_b64_tr_b16 v[220:221], v193 offset:35840\n"
"ds_read_b64_tr_b16 v[222:223], v193 offset:44032\n"
"v_mfma_f32_16x16x32_bf16 v[156:159], v[232:235], v[100:103], v[156:159]\n"
"ds_read_b64_tr_b16 v[224:225], v192 offset:35328\n"
"v_mul_f32 v200, v242, v200\n"
"v_mfma_f32_16x16x32_bf16 v[156:159], v[236:239], v[108:111], v[156:159]\n"
"ds_read_b64_tr_b16 v[226:227], v192 offset:43520\n"
"v_mul_f32 v201, v242, v201\n"
"v_mfma_f32_16x16x32_bf16 v[140:143], v[236:239], v[104:107], v[140:143]\n"
"s_mov_b32 s65, 401664\n"
"ds_read_b64_tr_b16 v[228:229], v192 offset:36352\n"
"v_cvt_pk_bf16_f32 v112, v198, v199\n"
"v_cvt_pk_bf16_f32 v113, v200, v201\n"
"v_accvgpr_read_b32 v202, a216\n"
"v_accvgpr_read_b32 v203, a217\n"
"v_accvgpr_read_b32 v204, a218\n"
"v_accvgpr_read_b32 v205, a219\n"
"v_mul_f32 v202, v242, v202\n"
"v_mul_f32 v203, v242, v203\n"
"v_mul_f32 v204, v242, v204\n"
"v_mul_f32 v205, v242, v205\n"
"v_cvt_pk_bf16_f32 v114, v202, v203\n"
"v_cvt_pk_bf16_f32 v115, v204, v205\n"
"v_mfma_f32_16x16x32_bf16 v[140:143], v[232:235], v[96:99], v[140:143]\n"
"s_waitcnt lgkmcnt(7)\n"
"ds_read_b64_tr_b16 v[230:231], v192 offset:44544\n"
"v_accvgpr_read_b32 v198, a204\n"
"v_accvgpr_read_b32 v199, a205\n"
"v_accvgpr_read_b32 v200, a206\n"
"v_accvgpr_read_b32 v201, a207\n"
"v_mul_f32 v198, v243, v198\n"
"v_mul_f32 v199, v243, v199\n"
"v_mul_f32 v200, v243, v200\n"
"v_mul_f32 v201, v243, v201\n"
"v_cvt_pk_bf16_f32 v116, v198, v199\n"
"v_cvt_pk_bf16_f32 v117, v200, v201\n"
"v_accvgpr_read_b32 v202, a220\n"
"v_accvgpr_read_b32 v203, a221\n"
"v_accvgpr_read_b32 v204, a222\n"
"v_accvgpr_read_b32 v205, a223\n"
"v_mul_f32 v202, v243, v202\n"
"v_mul_f32 v203, v243, v203\n"
"v_mul_f32 v204, v243, v204\n"
"v_mul_f32 v205, v243, v205\n"
"v_cvt_pk_bf16_f32 v118, v202, v203\n"
"v_cvt_pk_bf16_f32 v119, v204, v205\n"
"v_accvgpr_read_b32 v198, a232\n"
"v_accvgpr_read_b32 v199, a233\n"
"v_accvgpr_read_b32 v200, a234\n"
"v_accvgpr_read_b32 v201, a235\n"
"v_mul_f32 v198, v242, v198\n"
"v_mul_f32 v199, v242, v199\n"
"v_mul_f32 v200, v242, v200\n"
"v_mul_f32 v201, v242, v201\n"
"v_cvt_pk_bf16_f32 v120, v198, v199\n"
"v_cvt_pk_bf16_f32 v121, v200, v201\n"
"v_accvgpr_read_b32 v202, a248\n"
"v_accvgpr_read_b32 v203, a249\n"
"v_accvgpr_read_b32 v204, a250\n"
"v_accvgpr_read_b32 v205, a251\n"
"v_mul_f32 v202, v242, v202\n"
"v_mul_f32 v203, v242, v203\n"
"v_mul_f32 v204, v242, v204\n"
"v_mul_f32 v205, v242, v205\n"
"v_cvt_pk_bf16_f32 v122, v202, v203\n"
"v_cvt_pk_bf16_f32 v123, v204, v205\n"
"v_mfma_f32_16x16x32_bf16 v[128:131], v[208:211], v[112:115], v[128:131]\n"
"s_add_u32 m0, s60, 149504\n"
"ds_read_b64_tr_b16 v[232:233], v193 offset:35328\n"
"v_accvgpr_read_b32 v198, a236\n"
"v_accvgpr_read_b32 v199, a237\n"
"v_accvgpr_read_b32 v200, a238\n"
"v_accvgpr_read_b32 v201, a239\n"
"v_mul_f32 v198, v243, v198\n"
"v_mul_f32 v199, v243, v199\n"
"v_mul_f32 v200, v243, v200\n"
"v_mul_f32 v201, v243, v201\n"
"v_cvt_pk_bf16_f32 v124, v198, v199\n"
"v_cvt_pk_bf16_f32 v125, v200, v201\n"
"v_accvgpr_read_b32 v202, a252\n"
"v_accvgpr_read_b32 v203, a253\n"
"v_accvgpr_read_b32 v204, a254\n"
"v_accvgpr_read_b32 v205, a255\n"
"v_mul_f32 v202, v243, v202\n"
"v_mul_f32 v203, v243, v203\n"
"v_mul_f32 v204, v243, v204\n"
"v_mul_f32 v205, v243, v205\n"
"v_cvt_pk_bf16_f32 v126, v202, v203\n"
"v_cvt_pk_bf16_f32 v127, v204, v205\n"
"v_mfma_f32_16x16x32_bf16 v[128:131], v[212:215], v[120:123], v[128:131]\n"
"buffer_load_dwordx4 v249, s[20:23], s65 offen lds\n"
"v_mfma_f32_16x16x32_bf16 v[144:147], v[212:215], v[124:127], v[144:147]\n"
"ds_read_b64_tr_b16 v[234:235], v193 offset:43520\n"
"ds_read_b64_tr_b16 v[236:237], v193 offset:36352\n"
"v_mfma_f32_16x16x32_bf16 v[144:147], v[208:211], v[116:119], v[144:147]\n"
"s_waitcnt lgkmcnt(7)\n"
"ds_read_b64_tr_b16 v[238:239], v193 offset:44544\n"
"v_mfma_f32_16x16x32_bf16 v[148:151], v[216:219], v[116:119], v[148:151]\n"
"ds_read_b64_tr_b16 v[208:209], v192 offset:49152\n"
"v_mfma_f32_16x16x32_bf16 v[148:151], v[220:223], v[124:127], v[148:151]\n"
"s_mov_b32 s65, 430336\n"
"ds_read_b64_tr_b16 v[210:211], v192 offset:57344\n"
"v_mfma_f32_16x16x32_bf16 v[132:135], v[220:223], v[120:123], v[132:135]\n"
"s_add_u32 m0, s60, 150528\n"
"ds_read_b64_tr_b16 v[212:213], v192 offset:50176\n"
"v_mfma_f32_16x16x32_bf16 v[132:135], v[216:219], v[112:115], v[132:135]\n"
"s_waitcnt lgkmcnt(7)\n"
"buffer_load_dwordx4 v249, s[20:23], s65 offen lds\n"
"v_mfma_f32_16x16x32_bf16 v[136:139], v[224:227], v[112:115], v[136:139]\n"
"ds_read_b64_tr_b16 v[214:215], v192 offset:58368\n"
"ds_read_b64_tr_b16 v[216:217], v192 offset:51200\n"
"v_mfma_f32_16x16x32_bf16 v[136:139], v[228:231], v[120:123], v[136:139]\n"
"ds_read_b64_tr_b16 v[218:219], v192 offset:59392\n"
"v_cvt_pk_bf16_f32 v144, v144, v145\n"
"v_mfma_f32_16x16x32_bf16 v[152:155], v[228:231], v[124:127], v[152:155]\n"
"ds_read_b64_tr_b16 v[220:221], v192 offset:52224\n"
"v_cvt_pk_bf16_f32 v128, v128, v129\n"
"v_mfma_f32_16x16x32_bf16 v[152:155], v[224:227], v[116:119], v[152:155]\n"
"s_waitcnt lgkmcnt(7)\n"
"ds_read_b64_tr_b16 v[222:223], v192 offset:60416\n"
"v_cvt_pk_bf16_f32 v129, v130, v131\n"
"v_mfma_f32_16x16x32_bf16 v[156:159], v[232:235], v[116:119], v[156:159]\n"
"ds_read_b64_tr_b16 v[224:225], v193 offset:49152\n"
"v_cvt_pk_bf16_f32 v130, v132, v133\n"
"v_mfma_f32_16x16x32_bf16 v[156:159], v[236:239], v[124:127], v[156:159]\n"
"ds_read_b64_tr_b16 v[226:227], v193 offset:57344\n"
"v_cvt_pk_bf16_f32 v131, v134, v135\n"
"v_mfma_f32_16x16x32_bf16 v[140:143], v[236:239], v[120:123], v[140:143]\n"
"s_waitcnt vmcnt(22)\n"
"s_barrier\n"
"v_mfma_f32_16x16x32_bf16 v[140:143], v[232:235], v[112:115], v[140:143]\n"
"s_waitcnt lgkmcnt(6)\n"
"ds_read_b64_tr_b16 v[228:229], v193 offset:50176\n"
"ds_read_b64_tr_b16 v[230:231], v193 offset:58368\n"
"v_mfma_f32_16x16x32_bf16 v[160:163], v[208:211], v[0:3], 0\n"
"s_mov_b32 s65, 384\n"
"ds_read_b64_tr_b16 v[232:233], v193 offset:51200\n"
"v_mfma_f32_16x16x32_bf16 v[160:163], v[212:215], v[8:11], v[160:163]\n"
"s_add_u32 m0, s60, 32768\n"
"ds_read_b64_tr_b16 v[234:235], v193 offset:59392\n"
"v_mfma_f32_16x16x32_bf16 v[176:179], v[212:215], v[12:15], 0\n"
"buffer_load_dwordx4 v249, s[20:23], s65 offen lds\n"
"v_mfma_f32_16x16x32_bf16 v[176:179], v[208:211], v[4:7], v[176:179]\n"
"s_waitcnt lgkmcnt(6)\n"
"ds_read_b64_tr_b16 v[236:237], v193 offset:52224\n"
"ds_read_b64_tr_b16 v[238:239], v193 offset:60416\n"
"v_mfma_f32_16x16x32_bf16 v[176:179], v[216:219], v[20:23], v[176:179]\n"
"ds_read_b64_tr_b16 v[208:209], v192 offset:49664\n"
"v_cvt_pk_bf16_f32 v145, v146, v147\n"
"v_mfma_f32_16x16x32_bf16 v[176:179], v[220:223], v[28:31], v[176:179]\n"
"ds_read_b64_tr_b16 v[210:211], v192 offset:57856\n"
"v_cvt_pk_bf16_f32 v146, v148, v149\n"
"v_mfma_f32_16x16x32_bf16 v[160:163], v[220:223], v[24:27], v[160:163]\n"
"ds_read_b64_tr_b16 v[212:213], v192 offset:50688\n"
"v_cvt_pk_bf16_f32 v147, v150, v151\n"
"v_mfma_f32_16x16x32_bf16 v[160:163], v[216:219], v[16:19], v[160:163]\n"
"s_waitcnt lgkmcnt(7)\n"
"ds_read_b64_tr_b16 v[214:215], v192 offset:58880\n"
"v_cvt_pk_bf16_f32 v136, v136, v137\n"
"v_mfma_f32_16x16x32_bf16 v[164:167], v[224:227], v[0:3], 0\n"
"s_mov_b32 s75, 4096\n"
"ds_read_b64_tr_b16 v[216:217], v192 offset:51712\n"
"v_mfma_f32_16x16x32_bf16 v[164:167], v[228:231], v[8:11], v[164:167]\n"
"s_mov_b32 s65, 29056\n"
"ds_read_b64_tr_b16 v[218:219], v192 offset:59904\n"
"v_mfma_f32_16x16x32_bf16 v[180:183], v[228:231], v[12:15], 0\n"
"s_add_u32 m0, s60, 33792\n"
"ds_read_b64_tr_b16 v[220:221], v192 offset:52736\n"
"v_mfma_f32_16x16x32_bf16 v[180:183], v[224:227], v[4:7], v[180:183]\n"
"s_waitcnt lgkmcnt(7)\n"
"buffer_load_dwordx4 v249, s[20:23], s65 offen lds\n"
"v_mfma_f32_16x16x32_bf16 v[180:183], v[232:235], v[20:23], v[180:183]\n"
"buffer_store_dwordx4 v[128:131], v206, s[28:31], s74 offen offset:0 sc1\n"
"v_mfma_f32_16x16x32_bf16 v[180:183], v[236:239], v[28:31], v[180:183]\n"
"ds_read_b64_tr_b16 v[222:223], v192 offset:60928\n"
"ds_read_b64_tr_b16 v[224:225], v193 offset:49664\n"
"v_mfma_f32_16x16x32_bf16 v[164:167], v[236:239], v[24:27], v[164:167]\n"
"s_mov_b32 s65, 57728\n"
"ds_read_b64_tr_b16 v[226:227], v193 offset:57856\n"
"ds_read_b64_tr_b16 v[228:229], v193 offset:50688\n"
"v_mfma_f32_16x16x32_bf16 v[164:167], v[232:235], v[16:19], v[164:167]\n"
"s_waitcnt lgkmcnt(7)\n"
"ds_read_b64_tr_b16 v[230:231], v193 offset:58880\n"
"v_cvt_pk_bf16_f32 v137, v138, v139\n"
"v_mfma_f32_16x16x32_bf16 v[168:171], v[208:211], v[0:3], 0\n"
"s_add_u32 m0, s60, 34816\n"
"ds_read_b64_tr_b16 v[232:233], v193 offset:51712\n"
"v_mfma_f32_16x16x32_bf16 v[168:171], v[212:215], v[8:11], v[168:171]\n"
"buffer_load_dwordx4 v249, s[20:23], s65 offen lds\n"
"v_mfma_f32_16x16x32_bf16 v[184:187], v[212:215], v[12:15], 0\n"
"ds_read_b64_tr_b16 v[234:235], v193 offset:59904\n"
"ds_read_b64_tr_b16 v[236:237], v193 offset:52736\n"
"v_mfma_f32_16x16x32_bf16 v[184:187], v[208:211], v[4:7], v[184:187]\n"
"s_waitcnt lgkmcnt(7)\n"
"ds_read_b64_tr_b16 v[238:239], v193 offset:60928\n"
"v_cvt_pk_bf16_f32 v138, v140, v141\n"
"v_mfma_f32_16x16x32_bf16 v[184:187], v[216:219], v[20:23], v[184:187]\n"
"buffer_store_dwordx4 v[144:147], v206, s[28:31], s74 offen offset:2048 sc1\n"
"v_mfma_f32_16x16x32_bf16 v[184:187], v[220:223], v[28:31], v[184:187]\n"
"s_mov_b32 s65, 86400\n"
"ds_read_b64_tr_b16 v[208:209], v194 offset:0\n"
"ds_read_b64_tr_b16 v[210:211], v194 offset:8192\n"
"v_mfma_f32_16x16x32_bf16 v[168:171], v[220:223], v[24:27], v[168:171]\n"
"s_add_u32 m0, s60, 35840\n"
"ds_read_b64_tr_b16 v[212:213], v194 offset:1024\n"
"v_mfma_f32_16x16x32_bf16 v[168:171], v[216:219], v[16:19], v[168:171]\n"
"s_waitcnt lgkmcnt(7)\n"
"buffer_load_dwordx4 v249, s[20:23], s65 offen lds\n"
"v_mfma_f32_16x16x32_bf16 v[172:175], v[224:227], v[0:3], 0\n"
"ds_read_b64_tr_b16 v[214:215], v194 offset:9216\n"
"ds_read_b64_tr_b16 v[216:217], v194 offset:2048\n"
"v_mfma_f32_16x16x32_bf16 v[172:175], v[228:231], v[8:11], v[172:175]\n"
"ds_read_b64_tr_b16 v[218:219], v194 offset:10240\n"
"v_cvt_pk_bf16_f32 v139, v142, v143\n"
"v_mfma_f32_16x16x32_bf16 v[188:191], v[228:231], v[12:15], 0\n"
"ds_read_b64_tr_b16 v[220:221], v194 offset:3072\n"
"v_cvt_pk_bf16_f32 v152, v152, v153\n"
"v_mfma_f32_16x16x32_bf16 v[188:191], v[224:227], v[4:7], v[188:191]\n"
"s_waitcnt lgkmcnt(7)\n"
"ds_read_b64_tr_b16 v[222:223], v194 offset:11264\n"
"v_cvt_pk_bf16_f32 v153, v154, v155\n"
"v_mfma_f32_16x16x32_bf16 v[188:191], v[232:235], v[20:23], v[188:191]\n"
"buffer_store_dwordx4 v[136:139], v206, s[28:31], s74 offen offset:1024 sc1\n"
"v_mfma_f32_16x16x32_bf16 v[188:191], v[236:239], v[28:31], v[188:191]\n"
"ds_read_b64_tr_b16 v[224:225], v195 offset:0\n"
"ds_read_b64_tr_b16 v[226:227], v195 offset:8192\n"
"v_mfma_f32_16x16x32_bf16 v[172:175], v[236:239], v[24:27], v[172:175]\n"
"IWB3_%=:\n"
"s_cbranch_execz IWB7_%=\n"
"ds_read_b64_tr_b16 v[228:229], v195 offset:1024\n"
"v_cvt_pk_bf16_f32 v154, v156, v157\n"
"v_mfma_f32_16x16x32_bf16 v[172:175], v[232:235], v[16:19], v[172:175]\n"
"s_waitcnt lgkmcnt(7)\n"
"ds_read_b64_tr_b16 v[230:231], v195 offset:9216\n"
"v_cvt_pk_bf16_f32 v155, v158, v159\n"
"v_mfma_f32_16x16x32_bf16 v[160:163], v[208:211], v[32:35], v[160:163]\n"
"s_mov_b32 s65, 115072\n"
"ds_read_b64_tr_b16 v[232:233], v195 offset:2048\n"
"v_mfma_f32_16x16x32_bf16 v[160:163], v[212:215], v[40:43], v[160:163]\n"
"s_add_u32 m0, s60, 98304\n"
"ds_read_b64_tr_b16 v[234:235], v195 offset:10240\n"
"v_mfma_f32_16x16x32_bf16 v[176:179], v[212:215], v[44:47], v[176:179]\n"
"buffer_load_dwordx4 v249, s[20:23], s65 offen lds\n"
"v_mfma_f32_16x16x32_bf16 v[176:179], v[208:211], v[36:39], v[176:179]\n"
"s_waitcnt lgkmcnt(6)\n"
"ds_read_b64_tr_b16 v[236:237], v195 offset:3072\n"
"ds_read_b64_tr_b16 v[238:239], v195 offset:11264\n"
"v_mfma_f32_16x16x32_bf16 v[176:179], v[216:219], v[52:55], v[176:179]\n"
"buffer_store_dwordx4 v[152:155], v206, s[28:31], s74 offen offset:3072 sc1\n"
"v_mfma_f32_16x16x32_bf16 v[176:179], v[220:223], v[60:63], v[176:179]\n"
"ds_read_b64_tr_b16 v[208:209], v194 offset:512\n"
"ds_read_b64_tr_b16 v[210:211], v194 offset:8704\n"
"v_mfma_f32_16x16x32_bf16 v[160:163], v[220:223], v[56:59], v[160:163]\n"
"ds_read_b64_tr_b16 v[212:213], v194 offset:1536\n"
"v_mfma_f32_16x16x32_bf16 v[160:163], v[216:219], v[48:51], v[160:163]\n"
"s_waitcnt lgkmcnt(7)\n"
"ds_read_b64_tr_b16 v[214:215], v194 offset:9728\n"
"v_mfma_f32_16x16x32_bf16 v[164:167], v[224:227], v[32:35], v[164:167]\n"
"s_mov_b32 s65, 143744\n"
"ds_read_b64_tr_b16 v[216:217], v194 offset:2560\n"
"v_mfma_f32_16x16x32_bf16 v[164:167], v[228:231], v[40:43], v[164:167]\n"
"s_add_u32 m0, s60, 99328\n"
"ds_read_b64_tr_b16 v[218:219], v194 offset:10752\n"
"v_mfma_f32_16x16x32_bf16 v[180:183], v[228:231], v[44:47], v[180:183]\n"
"buffer_load_dwordx4 v249, s[20:23], s65 offen lds\n"
"v_mfma_f32_16x16x32_bf16 v[180:183], v[224:227], v[36:39], v[180:183]\n"
"s_waitcnt lgkmcnt(6)\n"
"ds_read_b64_tr_b16 v[220:221], v194 offset:3584\n"
"ds_read_b64_tr_b16 v[222:223], v194 offset:11776\n"
"v_mfma_f32_16x16x32_bf16 v[180:183], v[232:235], v[52:55], v[180:183]\n"
"ds_read_b64_tr_b16 v[224:225], v195 offset:512\n"
"v_mfma_f32_16x16x32_bf16 v[180:183], v[236:239], v[60:63], v[180:183]\n"
"ds_read_b64_tr_b16 v[226:227], v195 offset:8704\n"
"v_mfma_f32_16x16x32_bf16 v[164:167], v[236:239], v[56:59], v[164:167]\n"
"s_mov_b32 s65, 172416\n"
"ds_read_b64_tr_b16 v[228:229], v195 offset:1536\n"
"v_mfma_f32_16x16x32_bf16 v[164:167], v[232:235], v[48:51], v[164:167]\n"
"s_waitcnt lgkmcnt(7)\n"
"ds_read_b64_tr_b16 v[230:231], v195 offset:9728\n"
"v_mfma_f32_16x16x32_bf16 v[168:171], v[208:211], v[32:35], v[168:171]\n"
"s_add_u32 m0, s60, 100352\n"
"ds_read_b64_tr_b16 v[232:233], v195 offset:2560\n"
"v_mfma_f32_16x16x32_bf16 v[168:171], v[212:215], v[40:43], v[168:171]\n"
"buffer_load_dwordx4 v249, s[20:23], s65 offen lds\n"
"v_mfma_f32_16x16x32_bf16 v[184:187], v[212:215], v[44:47], v[184:187]\n"
"ds_read_b64_tr_b16 v[234:235], v195 offset:10752\n"
"ds_read_b64_tr_b16 v[236:237], v195 offset:3584\n"
"v_mfma_f32_16x16x32_bf16 v[184:187], v[208:211], v[36:39], v[184:187]\n"
"s_waitcnt lgkmcnt(7)\n"
"ds_read_b64_tr_b16 v[238:239], v195 offset:11776\n"
"v_mfma_f32_16x16x32_bf16 v[184:187], v[216:219], v[52:55], v[184:187]\n"
"ds_read_b64_tr_b16 v[208:209], v194 offset:16384\n"
"v_mfma_f32_16x16x32_bf16 v[184:187], v[220:223], v[60:63], v[184:187]\n"
"s_mov_b32 s65, 201088\n"
"ds_read_b64_tr_b16 v[210:211], v194 offset:24576\n"
"v_mfma_f32_16x16x32_bf16 v[168:171], v[220:223], v[56:59], v[168:171]\n"
"s_add_u32 m0, s60, 101376\n"
"ds_read_b64_tr_b16 v[212:213], v194 offset:17408\n"
"v_mfma_f32_16x16x32_bf16 v[168:171], v[216:219], v[48:51], v[168:171]\n"
"s_waitcnt lgkmcnt(7)\n"
"buffer_load_dwordx4 v249, s[20:23], s65 offen lds\n"
"v_mfma_f32_16x16x32_bf16 v[172:175], v[224:227], v[32:35], v[172:175]\n"
"ds_read_b64_tr_b16 v[214:215], v194 offset:25600\n"
"ds_read_b64_tr_b16 v[216:217], v194 offset:18432\n"
"v_mfma_f32_16x16x32_bf16 v[172:175], v[228:231], v[40:43], v[172:175]\n"
"ds_read_b64_tr_b16 v[218:219], v194 offset:26624\n"
"v_mfma_f32_16x16x32_bf16 v[188:191], v[228:231], v[44:47], v[188:191]\n"
"ds_read_b64_tr_b16 v[220:221], v194 offset:19456\n"
"v_mfma_f32_16x16x32_bf16 v[188:191], v[224:227], v[36:39], v[188:191]\n"
"s_waitcnt lgkmcnt(7)\n"
"ds_read_b64_tr_b16 v[222:223], v194 offset:27648\n"
"v_mfma_f32_16x16x32_bf16 v[188:191], v[232:235], v[52:55], v[188:191]\n"
"ds_read_b64_tr_b16 v[224:225], v195 offset:16384\n"
"v_mfma_f32_16x16x32_bf16 v[188:191], v[236:239], v[60:63], v[188:191]\n"
"ds_read_b64_tr_b16 v[226:227], v195 offset:24576\n"
"v_mfma_f32_16x16x32_bf16 v[172:175], v[236:239], v[56:59], v[172:175]\n"
"s_waitcnt vmcnt(26)\n"
"s_barrier\n"
"v_mfma_f32_16x16x32_bf16 v[172:175], v[232:235], v[48:51], v[172:175]\n"
"s_waitcnt lgkmcnt(6)\n"
"ds_read_b64_tr_b16 v[228:229], v195 offset:17408\n"
"ds_read_b64_tr_b16 v[230:231], v195 offset:25600\n"
"v_mfma_f32_16x16x32_bf16 v[160:163], v[208:211], v[64:67], v[160:163]\n"
"s_mov_b32 s65, 229760\n"
"ds_read_b64_tr_b16 v[232:233], v195 offset:18432\n"
"v_mfma_f32_16x16x32_bf16 v[160:163], v[212:215], v[72:75], v[160:163]\n"
"s_add_u32 m0, s60, 49152\n"
"ds_read_b64_tr_b16 v[234:235], v195 offset:26624\n"
"v_mfma_f32_16x16x32_bf16 v[176:179], v[212:215], v[76:79], v[176:179]\n"
"buffer_load_dwordx4 v249, s[20:23], s65 offen lds\n"
"v_mfma_f32_16x16x32_bf16 v[176:179], v[208:211], v[68:71], v[176:179]\n"
"s_waitcnt lgkmcnt(6)\n"
"ds_read_b64_tr_b16 v[236:237], v195 offset:19456\n"
"ds_read_b64_tr_b16 v[238:239], v195 offset:27648\n"
"v_mfma_f32_16x16x32_bf16 v[176:179], v[216:219], v[84:87], v[176:179]\n"
"ds_read_b64_tr_b16 v[208:209], v194 offset:16896\n"
"v_mfma_f32_16x16x32_bf16 v[176:179], v[220:223], v[92:95], v[176:179]\n"
"ds_read_b64_tr_b16 v[210:211], v194 offset:25088\n"
"v_mfma_f32_16x16x32_bf16 v[160:163], v[220:223], v[88:91], v[160:163]\n"
"ds_read_b64_tr_b16 v[212:213], v194 offset:17920\n"
"v_mfma_f32_16x16x32_bf16 v[160:163], v[216:219], v[80:83], v[160:163]\n"
"s_waitcnt lgkmcnt(7)\n"
"ds_read_b64_tr_b16 v[214:215], v194 offset:26112\n"
"v_mfma_f32_16x16x32_bf16 v[164:167], v[224:227], v[64:67], v[164:167]\n"
"s_mov_b32 s65, 258432\n"
"ds_read_b64_tr_b16 v[216:217], v194 offset:18944\n"
"v_mfma_f32_16x16x32_bf16 v[164:167], v[228:231], v[72:75], v[164:167]\n"
"s_add_u32 m0, s60, 50176\n"
"ds_read_b64_tr_b16 v[218:219], v194 offset:27136\n"
"v_mfma_f32_16x16x32_bf16 v[180:183], v[228:231], v[76:79], v[180:183]\n"
"buffer_load_dwordx4 v249, s[20:23], s65 offen lds\n"
"v_mfma_f32_16x16x32_bf16 v[180:183], v[224:227], v[68:71], v[180:183]\n"
"s_waitcnt lgkmcnt(6)\n"
"ds_read_b64_tr_b16 v[220:221], v194 offset:19968\n"
"ds_read_b64_tr_b16 v[222:223], v194 offset:28160\n"
"v_mfma_f32_16x16x32_bf16 v[180:183], v[232:235], v[84:87], v[180:183]\n"
"ds_read_b64_tr_b16 v[224:225], v195 offset:16896\n"
"v_mfma_f32_16x16x32_bf16 v[180:183], v[236:239], v[92:95], v[180:183]\n"
"ds_read_b64_tr_b16 v[226:227], v195 offset:25088\n"
"v_mfma_f32_16x16x32_bf16 v[164:167], v[236:239], v[88:91], v[164:167]\n"
"s_mov_b32 s65, 287104\n"
"ds_read_b64_tr_b16 v[228:229], v195 offset:17920\n"
"v_mfma_f32_16x16x32_bf16 v[164:167], v[232:235], v[80:83], v[164:167]\n"
"s_waitcnt lgkmcnt(7)\n"
"ds_read_b64_tr_b16 v[230:231], v195 offset:26112\n"
"v_mfma_f32_16x16x32_bf16 v[168:171], v[208:211], v[64:67], v[168:171]\n"
"s_add_u32 m0, s60, 51200\n"
"ds_read_b64_tr_b16 v[232:233], v195 offset:18944\n"
"v_mfma_f32_16x16x32_bf16 v[168:171], v[212:215], v[72:75], v[168:171]\n"
"buffer_load_dwordx4 v249, s[20:23], s65 offen lds\n"
"v_mfma_f32_16x16x32_bf16 v[184:187], v[212:215], v[76:79], v[184:187]\n"
"ds_read_b64_tr_b16 v[234:235], v195 offset:27136\n"
"ds_read_b64_tr_b16 v[236:237], v195 offset:19968\n"
"v_mfma_f32_16x16x32_bf16 v[184:187], v[208:211], v[68:71], v[184:187]\n"
"s_waitcnt lgkmcnt(7)\n"
"ds_read_b64_tr_b16 v[238:239], v195 offset:28160\n"
"v_mfma_f32_16x16x32_bf16 v[184:187], v[216:219], v[84:87], v[184:187]\n"
"ds_read_b64_tr_b16 v[208:209], v194 offset:49152\n"
"v_mfma_f32_16x16x32_bf16 v[184:187], v[220:223], v[92:95], v[184:187]\n"
"s_mov_b32 s65, 315776\n"
"ds_read_b64_tr_b16 v[210:211], v194 offset:57344\n"
"v_mfma_f32_16x16x32_bf16 v[168:171], v[220:223], v[88:91], v[168:171]\n"
"s_add_u32 m0, s60, 52224\n"
"ds_read_b64_tr_b16 v[212:213], v194 offset:50176\n"
"v_mfma_f32_16x16x32_bf16 v[168:171], v[216:219], v[80:83], v[168:171]\n"
"s_waitcnt lgkmcnt(7)\n"
"buffer_load_dwordx4 v249, s[20:23], s65 offen lds\n"
"v_mfma_f32_16x16x32_bf16 v[172:175], v[224:227], v[64:67], v[172:175]\n"
"ds_read_b64_tr_b16 v[214:215], v194 offset:58368\n"
"ds_read_b64_tr_b16 v[216:217], v194 offset:51200\n"
"v_mfma_f32_16x16x32_bf16 v[172:175], v[228:231], v[72:75], v[172:175]\n"
"ds_read_b64_tr_b16 v[218:219], v194 offset:59392\n"
"v_mfma_f32_16x16x32_bf16 v[188:191], v[228:231], v[76:79], v[188:191]\n"
"ds_read_b64_tr_b16 v[220:221], v194 offset:52224\n"
"v_mfma_f32_16x16x32_bf16 v[188:191], v[224:227], v[68:71], v[188:191]\n"
"s_waitcnt lgkmcnt(7)\n"
"ds_read_b64_tr_b16 v[222:223], v194 offset:60416\n"
"v_mfma_f32_16x16x32_bf16 v[188:191], v[232:235], v[84:87], v[188:191]\n"
"ds_read_b64_tr_b16 v[224:225], v195 offset:49152\n"
"v_mfma_f32_16x16x32_bf16 v[188:191], v[236:239], v[92:95], v[188:191]\n"
"ds_read_b64_tr_b16 v[226:227], v195 offset:57344\n"
"v_mfma_f32_16x16x32_bf16 v[172:175], v[236:239], v[88:91], v[172:175]\n"
"ds_read_b64_tr_b16 v[228:229], v195 offset:50176\n"
"v_mfma_f32_16x16x32_bf16 v[172:175], v[232:235], v[80:83], v[172:175]\n"
"s_waitcnt lgkmcnt(7)\n"
"ds_read_b64_tr_b16 v[230:231], v195 offset:58368\n"
"v_mfma_f32_16x16x32_bf16 v[160:163], v[208:211], v[96:99], v[160:163]\n"
"s_mov_b32 s65, 344448\n"
"ds_read_b64_tr_b16 v[232:233], v195 offset:51200\n"
"v_lshrrev_b32 v240, 3, v207\n"
"v_mfma_f32_16x16x32_bf16 v[160:163], v[212:215], v[104:107], v[160:163]\n"
"s_add_u32 m0, s60, 65536\n"
"ds_read_b64_tr_b16 v[234:235], v195 offset:59392\n"
"v_add_u32 v240, s70, v240\n"
"v_mfma_f32_16x16x32_bf16 v[176:179], v[212:215], v[108:111], v[176:179]\n"
"buffer_load_dwordx4 v249, s[20:23], s65 offen lds\n"
"v_mfma_f32_16x16x32_bf16 v[176:179], v[208:211], v[100:103], v[176:179]\n"
"s_waitcnt lgkmcnt(6)\n"
"ds_read_b64_tr_b16 v[236:237], v195 offset:52224\n"
"ds_read_b64_tr_b16 v[238:239], v195 offset:60416\n"
"v_mfma_f32_16x16x32_bf16 v[176:179], v[216:219], v[116:119], v[176:179]\n"
"ds_read_b64_tr_b16 v[208:209], v194 offset:49664\n"
"v_lshl_add_u32 v251, v240, 21, v251\n"
"v_mfma_f32_16x16x32_bf16 v[176:179], v[220:223], v[124:127], v[176:179]\n"
"ds_read_b64_tr_b16 v[210:211], v194 offset:57856\n"
"s_lshr_b32 s6, s4, 1\n"
"v_mfma_f32_16x16x32_bf16 v[160:163], v[220:223], v[120:123], v[160:163]\n"
"ds_read_b64_tr_b16 v[212:213], v194 offset:50688\n"
"s_mul_i32 s6, s6, 28672\n"
"v_mfma_f32_16x16x32_bf16 v[160:163], v[216:219], v[112:115], v[160:163]\n"
"s_waitcnt lgkmcnt(7)\n"
"ds_read_b64_tr_b16 v[214:215], v194 offset:58880\n"
"s_and_b32 s7, s4, 1\n"
"v_mfma_f32_16x16x32_bf16 v[164:167], v[224:227], v[96:99], v[164:167]\n"
"s_mov_b32 s65, 373120\n"
"ds_read_b64_tr_b16 v[216:217], v194 offset:51712\n"
"s_lshl_b32 s7, s7, 11\n"
"v_mfma_f32_16x16x32_bf16 v[164:167], v[228:231], v[104:107], v[164:167]\n"
"s_add_u32 m0, s60, 66560\n"
"ds_read_b64_tr_b16 v[218:219], v194 offset:59904\n"
"s_add_u32 s6, s6, s7\n"
"v_mfma_f32_16x16x32_bf16 v[180:183], v[228:231], v[108:111], v[180:183]\n"
"buffer_load_dwordx4 v249, s[20:23], s65 offen lds\n"
"v_mfma_f32_16x16x32_bf16 v[180:183], v[224:227], v[100:103], v[180:183]\n"
"s_waitcnt lgkmcnt(6)\n"
"ds_read_b64_tr_b16 v[220:221], v194 offset:52736\n"
"ds_read_b64_tr_b16 v[222:223], v194 offset:60928\n"
"v_mfma_f32_16x16x32_bf16 v[180:183], v[232:235], v[116:119], v[180:183]\n"
"ds_read_b64_tr_b16 v[224:225], v195 offset:49664\n"
"s_lshr_b32 s7, s70, 1\n"
"v_mfma_f32_16x16x32_bf16 v[180:183], v[236:239], v[124:127], v[180:183]\n"
"ds_read_b64_tr_b16 v[226:227], v195 offset:57856\n"
"s_lshl_b32 s7, s7, 12\n"
"v_mfma_f32_16x16x32_bf16 v[164:167], v[236:239], v[120:123], v[164:167]\n"
"s_mov_b32 s65, 401792\n"
"ds_read_b64_tr_b16 v[228:229], v195 offset:50688\n"
"s_add_u32 s6, s6, s7\n"
"v_mfma_f32_16x16x32_bf16 v[164:167], v[232:235], v[112:115], v[164:167]\n"
"s_waitcnt lgkmcnt(7)\n"
"ds_read_b64_tr_b16 v[230:231], v195 offset:58880\n"
"s_and_b32 s7, s70, 1\n"
"v_mfma_f32_16x16x32_bf16 v[168:171], v[208:211], v[96:99], v[168:171]\n"
"s_add_u32 m0, s60, 67584\n"
"ds_read_b64_tr_b16 v[232:233], v195 offset:51712\n"
"s_lshl_b32 s7, s7, 10\n"
"v_mfma_f32_16x16x32_bf16 v[168:171], v[212:215], v[104:107], v[168:171]\n"
"buffer_load_dwordx4 v249, s[20:23], s65 offen lds\n"
"v_mfma_f32_16x16x32_bf16 v[184:187], v[212:215], v[108:111], v[184:187]\n"
"ds_read_b64_tr_b16 v[234:235], v195 offset:59904\n"
"ds_read_b64_tr_b16 v[236:237], v195 offset:52736\n"
"v_mfma_f32_16x16x32_bf16 v[184:187], v[208:211], v[100:103], v[184:187]\n"
"s_waitcnt lgkmcnt(7)\n"
"ds_read_b64_tr_b16 v[238:239], v195 offset:60928\n"
"v_cvt_pk_bf16_f32 v160, v160, v161\n"
"v_mfma_f32_16x16x32_bf16 v[184:187], v[216:219], v[116:119], v[184:187]\n"
"ds_read_b64_tr_b16 v[208:209], v196 offset:0\n"
"v_cvt_pk_bf16_f32 v161, v162, v163\n"
"v_mfma_f32_16x16x32_bf16 v[184:187], v[220:223], v[124:127], v[184:187]\n"
"s_mov_b32 s65, 430464\n"
"ds_read_b64_tr_b16 v[210:211], v196 offset:8192\n"
"s_add_u32 s6, s6, s7\n"
"v_mfma_f32_16x16x32_bf16 v[168:171], v[220:223], v[120:123], v[168:171]\n"
"s_add_u32 m0, s60, 68608\n"
"ds_read_b64_tr_b16 v[212:213], v196 offset:1024\n"
"v_mfma_f32_16x16x32_bf16 v[168:171], v[216:219], v[112:115], v[168:171]\n"
"s_waitcnt lgkmcnt(7)\n"
"buffer_load_dwordx4 v249, s[20:23], s65 offen lds\n"
"v_cvt_pk_bf16_f32 v162, v164, v165\n"
"v_cvt_pk_bf16_f32 v163, v166, v167\n"
"v_mfma_f32_16x16x32_bf16 v[172:175], v[224:227], v[96:99], v[172:175]\n"
"buffer_store_dwordx4 v[160:163], v206, s[28:31], s75 offen offset:0 sc1\n"
"v_mfma_f32_16x16x32_bf16 v[172:175], v[228:231], v[104:107], v[172:175]\n"
"ds_read_b64_tr_b16 v[214:215], v196 offset:9216\n"
"ds_read_b64_tr_b16 v[216:217], v196 offset:2048\n"
"v_mfma_f32_16x16x32_bf16 v[188:191], v[228:231], v[108:111], v[188:191]\n"
"ds_read_b64_tr_b16 v[218:219], v196 offset:10240\n"
"ds_read_b64_tr_b16 v[220:221], v196 offset:3072\n"
"v_mfma_f32_16x16x32_bf16 v[188:191], v[224:227], v[100:103], v[188:191]\n"
"s_waitcnt lgkmcnt(7)\n"
"ds_read_b64_tr_b16 v[222:223], v196 offset:11264\n"
"v_cvt_pk_bf16_f32 v176, v176, v177\n"
"v_cvt_pk_bf16_f32 v177, v178, v179\n"
"v_cvt_pk_bf16_f32 v178, v180, v181\n"
"v_cvt_pk_bf16_f32 v179, v182, v183\n"
"v_mfma_f32_16x16x32_bf16 v[188:191], v[232:235], v[116:119], v[188:191]\n"
"buffer_store_dwordx4 v[176:179], v206, s[28:31], s75 offen offset:2048 sc1\n"
"v_mfma_f32_16x16x32_bf16 v[188:191], v[236:239], v[124:127], v[188:191]\n"
"ds_read_b64_tr_b16 v[224:225], v197 offset:0\n"
"ds_read_b64_tr_b16 v[226:227], v197 offset:8192\n"
"v_mfma_f32_16x16x32_bf16 v[172:175], v[236:239], v[120:123], v[172:175]\n"
"s_waitcnt vmcnt(28)\n"
"s_barrier\n"
"v_mfma_f32_16x16x32_bf16 v[172:175], v[232:235], v[112:115], v[172:175]\n"
"s_waitcnt lgkmcnt(6)\n"
"ds_read_b64_tr_b16 v[228:229], v197 offset:1024\n"
"ds_read_b64_tr_b16 v[230:231], v197 offset:9216\n"
"v_mfma_f32_16x16x32_bf16 v[128:131], v[208:211], v[0:3], 0\n"
"s_mov_b32 s65, 512\n"
"ds_read_b64_tr_b16 v[232:233], v197 offset:2048\n"
"v_mfma_f32_16x16x32_bf16 v[128:131], v[212:215], v[8:11], v[128:131]\n"
"s_add_u32 m0, s60, 81920\n"
"ds_read_b64_tr_b16 v[234:235], v197 offset:10240\n"
"v_mfma_f32_16x16x32_bf16 v[144:147], v[212:215], v[12:15], 0\n"
"buffer_load_dwordx4 v249, s[20:23], s65 offen lds\n"
"v_mfma_f32_16x16x32_bf16 v[144:147], v[208:211], v[4:7], v[144:147]\n"
"s_waitcnt lgkmcnt(6)\n"
"ds_read_b64_tr_b16 v[236:237], v197 offset:3072\n"
"ds_read_b64_tr_b16 v[238:239], v197 offset:11264\n"
"v_mfma_f32_16x16x32_bf16 v[144:147], v[216:219], v[20:23], v[144:147]\n"
"ds_read_b64_tr_b16 v[208:209], v196 offset:512\n"
"v_cvt_pk_bf16_f32 v168, v168, v169\n"
"v_mfma_f32_16x16x32_bf16 v[144:147], v[220:223], v[28:31], v[144:147]\n"
"ds_read_b64_tr_b16 v[210:211], v196 offset:8704\n"
"v_cvt_pk_bf16_f32 v169, v170, v171\n"
"v_mfma_f32_16x16x32_bf16 v[128:131], v[220:223], v[24:27], v[128:131]\n"
"ds_read_b64_tr_b16 v[212:213], v196 offset:1536\n"
"v_cvt_pk_bf16_f32 v170, v172, v173\n"
"v_mfma_f32_16x16x32_bf16 v[128:131], v[216:219], v[16:19], v[128:131]\n"
"s_waitcnt lgkmcnt(7)\n"
"ds_read_b64_tr_b16 v[214:215], v196 offset:9728\n"
"v_cvt_pk_bf16_f32 v171, v174, v175\n"
"v_mfma_f32_16x16x32_bf16 v[132:135], v[224:227], v[0:3], 0\n"
"buffer_store_dwordx4 v[168:171], v206, s[28:31], s75 offen offset:1024 sc1\n"
"v_mfma_f32_16x16x32_bf16 v[132:135], v[228:231], v[8:11], v[132:135]\n"
"s_mov_b32 s74, 8192\n"
"ds_read_b64_tr_b16 v[216:217], v196 offset:2560\n"
"ds_read_b64_tr_b16 v[218:219], v196 offset:10752\n"
"v_mfma_f32_16x16x32_bf16 v[148:151], v[228:231], v[12:15], 0\n"
"s_mov_b32 s65, 29184\n"
"ds_read_b64_tr_b16 v[220:221], v196 offset:3584\n"
"v_mfma_f32_16x16x32_bf16 v[148:151], v[224:227], v[4:7], v[148:151]\n"
"s_waitcnt lgkmcnt(7)\n"
"ds_read_b64_tr_b16 v[222:223], v196 offset:11776\n"
"v_cvt_pk_bf16_f32 v184, v184, v185\n"
"v_mfma_f32_16x16x32_bf16 v[148:151], v[232:235], v[20:23], v[148:151]\n"
"s_add_u32 m0, s60, 82944\n"
"ds_read_b64_tr_b16 v[224:225], v197 offset:512\n"
"v_cvt_pk_bf16_f32 v185, v186, v187\n"
"v_cvt_pk_bf16_f32 v186, v188, v189\n"
"v_cvt_pk_bf16_f32 v187, v190, v191\n"
"v_mfma_f32_16x16x32_bf16 v[148:151], v[236:239], v[28:31], v[148:151]\n"
"buffer_store_dwordx4 v[184:187], v206, s[28:31], s75 offen offset:3072 sc1\n"
"v_mfma_f32_16x16x32_bf16 v[132:135], v[236:239], v[24:27], v[132:135]\n"
"buffer_load_dwordx4 v249, s[20:23], s65 offen lds\n"
"v_mfma_f32_16x16x32_bf16 v[132:135], v[232:235], v[16:19], v[132:135]\n"
"s_waitcnt lgkmcnt(5)\n"
"ds_read_b64_tr_b16 v[226:227], v197 offset:8704\n"
"ds_read_b64_tr_b16 v[228:229], v197 offset:1536\n"
"v_mfma_f32_16x16x32_bf16 v[136:139], v[208:211], v[0:3], 0\n"
"s_mov_b32 s65, 57856\n"
"ds_read_b64_tr_b16 v[230:231], v197 offset:9728\n"
"ds_read_b64_tr_b16 v[232:233], v197 offset:2560\n"
"v_mfma_f32_16x16x32_bf16 v[136:139], v[212:215], v[8:11], v[136:139]\n"
"s_add_u32 m0, s60, 83968\n"
"ds_read_b64_tr_b16 v[234:235], v197 offset:10752\n"
"v_mfma_f32_16x16x32_bf16 v[152:155], v[212:215], v[12:15], 0\n"
"buffer_load_dwordx4 v249, s[20:23], s65 offen lds\n"
"v_mfma_f32_16x16x32_bf16 v[152:155], v[208:211], v[4:7], v[152:155]\n"
"s_waitcnt lgkmcnt(6)\n"
"ds_read_b64_tr_b16 v[236:237], v197 offset:3584\n"
"ds_read_b64_tr_b16 v[238:239], v197 offset:11776\n"
"v_mfma_f32_16x16x32_bf16 v[152:155], v[216:219], v[20:23], v[152:155]\n"
"ds_read_b64_tr_b16 v[208:209], v192 offset:0\n"
"v_lshl_add_u32 v248, v207, 4, s6\n"
"v_mfma_f32_16x16x32_bf16 v[152:155], v[220:223], v[28:31], v[152:155]\n"
"s_mov_b32 s65, 86528\n"
"ds_read_b64_tr_b16 v[210:211], v192 offset:8192\n"
"v_mfma_f32_16x16x32_bf16 v[136:139], v[220:223], v[24:27], v[136:139]\n"
"s_add_u32 m0, s60, 84992\n"
"ds_read_b64_tr_b16 v[212:213], v192 offset:1024\n"
"v_mfma_f32_16x16x32_bf16 v[136:139], v[216:219], v[16:19], v[136:139]\n"
"s_waitcnt lgkmcnt(7)\n"
"buffer_load_dwordx4 v249, s[20:23], s65 offen lds\n"
"v_mfma_f32_16x16x32_bf16 v[140:143], v[224:227], v[0:3], 0\n"
"ds_read_b64_tr_b16 v[214:215], v192 offset:9216\n"
"ds_read_b64_tr_b16 v[216:217], v192 offset:2048\n"
"v_mfma_f32_16x16x32_bf16 v[140:143], v[228:231], v[8:11], v[140:143]\n"
"ds_read_b64_tr_b16 v[218:219], v192 offset:10240\n"
"v_mfma_f32_16x16x32_bf16 v[156:159], v[228:231], v[12:15], 0\n"
"ds_read_b64_tr_b16 v[220:221], v192 offset:3072\n"
"v_mfma_f32_16x16x32_bf16 v[156:159], v[224:227], v[4:7], v[156:159]\n"
"s_waitcnt lgkmcnt(7)\n"
"ds_read_b64_tr_b16 v[222:223], v192 offset:11264\n"
"v_mfma_f32_16x16x32_bf16 v[156:159], v[232:235], v[20:23], v[156:159]\n"
"ds_read_b64_tr_b16 v[224:225], v193 offset:0\n"
"v_mfma_f32_16x16x32_bf16 v[156:159], v[236:239], v[28:31], v[156:159]\n"
"ds_read_b64_tr_b16 v[226:227], v193 offset:8192\n"
"v_mfma_f32_16x16x32_bf16 v[140:143], v[236:239], v[24:27], v[140:143]\n"
"ds_read_b64_tr_b16 v[228:229], v193 offset:1024\n"
"v_mfma_f32_16x16x32_bf16 v[140:143], v[232:235], v[16:19], v[140:143]\n"
"s_waitcnt lgkmcnt(7)\n"
"ds_read_b64_tr_b16 v[230:231], v193 offset:9216\n"
"v_mfma_f32_16x16x32_bf16 v[128:131], v[208:211], v[32:35], v[128:131]\n"
"s_mov_b32 s65, 115200\n"
"ds_read_b64_tr_b16 v[232:233], v193 offset:2048\n"
"v_mfma_f32_16x16x32_bf16 v[128:131], v[212:215], v[40:43], v[128:131]\n"
"s_add_u32 m0, s60, 114688\n"
"ds_read_b64_tr_b16 v[234:235], v193 offset:10240\n"
"v_mfma_f32_16x16x32_bf16 v[144:147], v[212:215], v[44:47], v[144:147]\n"
"buffer_load_dwordx4 v249, s[20:23], s65 offen lds\n"
"v_mfma_f32_16x16x32_bf16 v[144:147], v[208:211], v[36:39], v[144:147]\n"
"s_waitcnt lgkmcnt(6)\n"
"ds_read_b64_tr_b16 v[236:237], v193 offset:3072\n"
"ds_read_b64_tr_b16 v[238:239], v193 offset:11264\n"
"v_mfma_f32_16x16x32_bf16 v[144:147], v[216:219], v[52:55], v[144:147]\n"
"ds_read_b64_tr_b16 v[208:209], v192 offset:512\n"
"v_mfma_f32_16x16x32_bf16 v[144:147], v[220:223], v[60:63], v[144:147]\n"
"ds_read_b64_tr_b16 v[210:211], v192 offset:8704\n"
"v_mfma_f32_16x16x32_bf16 v[128:131], v[220:223], v[56:59], v[128:131]\n"
"ds_read_b64_tr_b16 v[212:213], v192 offset:1536\n"
"v_mfma_f32_16x16x32_bf16 v[128:131], v[216:219], v[48:51], v[128:131]\n"
"s_waitcnt lgkmcnt(7)\n"
"ds_read_b64_tr_b16 v[214:215], v192 offset:9728\n"
"v_mfma_f32_16x16x32_bf16 v[132:135], v[224:227], v[32:35], v[132:135]\n"
"s_mov_b32 s65, 143872\n"
"ds_read_b64_tr_b16 v[216:217], v192 offset:2560\n"
"v_mfma_f32_16x16x32_bf16 v[132:135], v[228:231], v[40:43], v[132:135]\n"
"s_add_u32 m0, s60, 115712\n"
"ds_read_b64_tr_b16 v[218:219], v192 offset:10752\n"
"v_mfma_f32_16x16x32_bf16 v[148:151], v[228:231], v[44:47], v[148:151]\n"
"buffer_load_dwordx4 v249, s[20:23], s65 offen lds\n"
"v_mfma_f32_16x16x32_bf16 v[148:151], v[224:227], v[36:39], v[148:151]\n"
"s_waitcnt lgkmcnt(6)\n"
"ds_read_b64_tr_b16 v[220:221], v192 offset:3584\n"
"ds_read_b64_tr_b16 v[222:223], v192 offset:11776\n"
"v_mfma_f32_16x16x32_bf16 v[148:151], v[232:235], v[52:55], v[148:151]\n"
"ds_read_b64_tr_b16 v[224:225], v193 offset:512\n"
"v_mfma_f32_16x16x32_bf16 v[148:151], v[236:239], v[60:63], v[148:151]\n"
"ds_read_b64_tr_b16 v[226:227], v193 offset:8704\n"
"v_mfma_f32_16x16x32_bf16 v[132:135], v[236:239], v[56:59], v[132:135]\n"
"s_mov_b32 s65, 172544\n"
"ds_read_b64_tr_b16 v[228:229], v193 offset:1536\n"
"v_mfma_f32_16x16x32_bf16 v[132:135], v[232:235], v[48:51], v[132:135]\n"
"s_waitcnt lgkmcnt(7)\n"
"ds_read_b64_tr_b16 v[230:231], v193 offset:9728\n"
"v_mfma_f32_16x16x32_bf16 v[136:139], v[208:211], v[32:35], v[136:139]\n"
"s_add_u32 m0, s60, 116736\n"
"ds_read_b64_tr_b16 v[232:233], v193 offset:2560\n"
"v_mfma_f32_16x16x32_bf16 v[136:139], v[212:215], v[40:43], v[136:139]\n"
"buffer_load_dwordx4 v249, s[20:23], s65 offen lds\n"
"v_mfma_f32_16x16x32_bf16 v[152:155], v[212:215], v[44:47], v[152:155]\n"
"ds_read_b64_tr_b16 v[234:235], v193 offset:10752\n"
"ds_read_b64_tr_b16 v[236:237], v193 offset:3584\n"
"v_mfma_f32_16x16x32_bf16 v[152:155], v[208:211], v[36:39], v[152:155]\n"
"s_waitcnt lgkmcnt(7)\n"
"ds_read_b64_tr_b16 v[238:239], v193 offset:11776\n"
"v_mfma_f32_16x16x32_bf16 v[152:155], v[216:219], v[52:55], v[152:155]\n"
"ds_read_b64_tr_b16 v[208:209], v192 offset:16384\n"
"v_mfma_f32_16x16x32_bf16 v[152:155], v[220:223], v[60:63], v[152:155]\n"
"s_mov_b32 s65, 201216\n"
"ds_read_b64_tr_b16 v[210:211], v192 offset:24576\n"
"v_mfma_f32_16x16x32_bf16 v[136:139], v[220:223], v[56:59], v[136:139]\n"
"s_add_u32 m0, s60, 117760\n"
"ds_read_b64_tr_b16 v[212:213], v192 offset:17408\n"
"v_mfma_f32_16x16x32_bf16 v[136:139], v[216:219], v[48:51], v[136:139]\n"
"s_waitcnt lgkmcnt(7)\n"
"buffer_load_dwordx4 v249, s[20:23], s65 offen lds\n"
"v_mfma_f32_16x16x32_bf16 v[140:143], v[224:227], v[32:35], v[140:143]\n"
"ds_read_b64_tr_b16 v[214:215], v192 offset:25600\n"
"ds_read_b64_tr_b16 v[216:217], v192 offset:18432\n"
"v_mfma_f32_16x16x32_bf16 v[140:143], v[228:231], v[40:43], v[140:143]\n"
"ds_read_b64_tr_b16 v[218:219], v192 offset:26624\n"
"v_mfma_f32_16x16x32_bf16 v[156:159], v[228:231], v[44:47], v[156:159]\n"
"ds_read_b64_tr_b16 v[220:221], v192 offset:19456\n"
"v_mfma_f32_16x16x32_bf16 v[156:159], v[224:227], v[36:39], v[156:159]\n"
"s_waitcnt lgkmcnt(7)\n"
"ds_read_b64_tr_b16 v[222:223], v192 offset:27648\n"
"v_mfma_f32_16x16x32_bf16 v[156:159], v[232:235], v[52:55], v[156:159]\n"
"ds_read_b64_tr_b16 v[224:225], v193 offset:16384\n"
"v_mfma_f32_16x16x32_bf16 v[156:159], v[236:239], v[60:63], v[156:159]\n"
"ds_read_b64_tr_b16 v[226:227], v193 offset:24576\n"
"v_mfma_f32_16x16x32_bf16 v[140:143], v[236:239], v[56:59], v[140:143]\n"
"s_waitcnt vmcnt(7)\n"
"s_barrier\n"
"v_mfma_f32_16x16x32_bf16 v[140:143], v[232:235], v[48:51], v[140:143]\n"
"s_waitcnt lgkmcnt(6)\n"
"ds_read_b64_tr_b16 v[228:229], v193 offset:17408\n"
"ds_read_b64_tr_b16 v[230:231], v193 offset:25600\n"
"v_mfma_f32_16x16x32_bf16 v[128:131], v[208:211], v[64:67], v[128:131]\n"
"buffer_store_dword v252, v251, s[28:31], 0 offen offset:128 sc1\n"
"v_mfma_f32_16x16x32_bf16 v[128:131], v[212:215], v[72:75], v[128:131]\n"
"ds_read_b64_tr_b16 v[232:233], v193 offset:18432\n"
"ds_read_b64_tr_b16 v[234:235], v193 offset:26624\n"
"v_mfma_f32_16x16x32_bf16 v[144:147], v[212:215], v[76:79], v[144:147]\n"
"ds_read_b64_tr_b16 v[236:237], v193 offset:19456\n"
"v_mfma_f32_16x16x32_bf16 v[144:147], v[208:211], v[68:71], v[144:147]\n"
"s_waitcnt lgkmcnt(7)\n"
"ds_read_b64_tr_b16 v[238:239], v193 offset:27648\n"
"v_mfma_f32_16x16x32_bf16 v[144:147], v[216:219], v[84:87], v[144:147]\n"
"ds_read_b64_tr_b16 v[208:209], v192 offset:16896\n"
"v_mfma_f32_16x16x32_bf16 v[144:147], v[220:223], v[92:95], v[144:147]\n"
"s_mov_b32 s65, 229888\n"
"ds_read_b64_tr_b16 v[210:211], v192 offset:25088\n"
"v_mfma_f32_16x16x32_bf16 v[128:131], v[220:223], v[88:91], v[128:131]\n"
"s_add_u32 m0, s60, 0\n"
"ds_read_b64_tr_b16 v[212:213], v192 offset:17920\n"
"v_mfma_f32_16x16x32_bf16 v[128:131], v[216:219], v[80:83], v[128:131]\n"
"s_waitcnt lgkmcnt(7)\n"
"buffer_load_dwordx4 v249, s[20:23], s65 offen lds\n"
"v_mfma_f32_16x16x32_bf16 v[132:135], v[224:227], v[64:67], v[132:135]\n"
"ds_read_b64_tr_b16 v[214:215], v192 offset:26112\n"
"ds_read_b64_tr_b16 v[216:217], v192 offset:18944\n"
"v_mfma_f32_16x16x32_bf16 v[132:135], v[228:231], v[72:75], v[132:135]\n"
"ds_read_b64_tr_b16 v[218:219], v192 offset:27136\n"
"v_mfma_f32_16x16x32_bf16 v[148:151], v[228:231], v[76:79], v[148:151]\n"
"ds_read_b64_tr_b16 v[220:221], v192 offset:19968\n"
"v_mfma_f32_16x16x32_bf16 v[148:151], v[224:227], v[68:71], v[148:151]\n"
"s_waitcnt lgkmcnt(7)\n"
"ds_read_b64_tr_b16 v[222:223], v192 offset:28160\n"
"v_mfma_f32_16x16x32_bf16 v[148:151], v[232:235], v[84:87], v[148:151]\n"
"s_mov_b32 s65, 258560\n"
"ds_read_b64_tr_b16 v[224:225], v193 offset:16896\n"
"v_mfma_f32_16x16x32_bf16 v[148:151], v[236:239], v[92:95], v[148:151]\n"
"s_add_u32 m0, s60, 1024\n"
"ds_read_b64_tr_b16 v[226:227], v193 offset:25088\n"
"v_mfma_f32_16x16x32_bf16 v[132:135], v[236:239], v[88:91], v[132:135]\n"
"buffer_load_dwordx4 v249, s[20:23], s65 offen lds\n"
"v_mfma_f32_16x16x32_bf16 v[132:135], v[232:235], v[80:83], v[132:135]\n"
"s_waitcnt lgkmcnt(6)\n"
"ds_read_b64_tr_b16 v[228:229], v193 offset:17920\n"
"ds_read_b64_tr_b16 v[230:231], v193 offset:26112\n"
"v_mfma_f32_16x16x32_bf16 v[136:139], v[208:211], v[64:67], v[136:139]\n"
"s_mov_b32 s65, 287232\n"
"ds_read_b64_tr_b16 v[232:233], v193 offset:18944\n"
"v_mfma_f32_16x16x32_bf16 v[136:139], v[212:215], v[72:75], v[136:139]\n"
"s_add_u32 m0, s60, 2048\n"
"ds_read_b64_tr_b16 v[234:235], v193 offset:27136\n"
"v_mfma_f32_16x16x32_bf16 v[152:155], v[212:215], v[76:79], v[152:155]\n"
"buffer_load_dwordx4 v249, s[20:23], s65 offen lds\n"
"v_mfma_f32_16x16x32_bf16 v[152:155], v[208:211], v[68:71], v[152:155]\n"
"s_waitcnt lgkmcnt(6)\n"
"ds_read_b64_tr_b16 v[236:237], v193 offset:19968\n"
"ds_read_b64_tr_b16 v[238:239], v193 offset:28160\n"
"v_mfma_f32_16x16x32_bf16 v[152:155], v[216:219], v[84:87], v[152:155]\n"
"ds_read_b64_tr_b16 v[208:209], v196 offset:16384\n"
"v_mfma_f32_16x16x32_bf16 v[152:155], v[220:223], v[92:95], v[152:155]\n"
"ds_read_b64_tr_b16 v[210:211], v196 offset:24576\n"
"v_mfma_f32_16x16x32_bf16 v[136:139], v[220:223], v[88:91], v[136:139]\n"
"s_mov_b32 s65, 315904\n"
"ds_read_b64_tr_b16 v[212:213], v196 offset:17408\n"
"v_mfma_f32_16x16x32_bf16 v[136:139], v[216:219], v[80:83], v[136:139]\n"
"s_waitcnt lgkmcnt(7)\n"
"ds_read_b64_tr_b16 v[214:215], v196 offset:25600\n"
"v_mfma_f32_16x16x32_bf16 v[140:143], v[224:227], v[64:67], v[140:143]\n"
"s_add_u32 m0, s60, 3072\n"
"ds_read_b64_tr_b16 v[216:217], v196 offset:18432\n"
"v_mfma_f32_16x16x32_bf16 v[140:143], v[228:231], v[72:75], v[140:143]\n"
"buffer_load_dwordx4 v249, s[20:23], s65 offen lds\n"
"v_mfma_f32_16x16x32_bf16 v[156:159], v[228:231], v[76:79], v[156:159]\n"
"ds_read_b64_tr_b16 v[218:219], v196 offset:26624\n"
"ds_read_b64_tr_b16 v[220:221], v196 offset:19456\n"
"v_mfma_f32_16x16x32_bf16 v[156:159], v[224:227], v[68:71], v[156:159]\n"
"IWB4_%=:\n"
"s_cbranch_execz IWB8_%=\n"
"s_waitcnt lgkmcnt(7)\n"
"ds_read_b64_tr_b16 v[222:223], v196 offset:27648\n"
"v_mfma_f32_16x16x32_bf16 v[156:159], v[232:235], v[84:87], v[156:159]\n"
"ds_read_b64_tr_b16 v[224:225], v197 offset:16384\n"
"v_mfma_f32_16x16x32_bf16 v[156:159], v[236:239], v[92:95], v[156:159]\n"
"ds_read_b64_tr_b16 v[226:227], v197 offset:24576\n"
"v_mfma_f32_16x16x32_bf16 v[140:143], v[236:239], v[88:91], v[140:143]\n"
"ds_read_b64_tr_b16 v[228:229], v197 offset:17408\n"
"v_mfma_f32_16x16x32_bf16 v[140:143], v[232:235], v[80:83], v[140:143]\n"
"s_waitcnt lgkmcnt(7)\n"
"ds_read_b64_tr_b16 v[230:231], v197 offset:25600\n"
"v_mfma_f32_16x16x32_bf16 v[128:131], v[208:211], v[96:99], v[128:131]\n"
"s_mov_b32 s65, 344576\n"
"ds_read_b64_tr_b16 v[232:233], v197 offset:18432\n"
"v_mfma_f32_16x16x32_bf16 v[128:131], v[212:215], v[104:107], v[128:131]\n"
"s_add_u32 m0, s60, 131072\n"
"ds_read_b64_tr_b16 v[234:235], v197 offset:26624\n"
"v_mfma_f32_16x16x32_bf16 v[144:147], v[212:215], v[108:111], v[144:147]\n"
"buffer_load_dwordx4 v249, s[20:23], s65 offen lds\n"
"v_mfma_f32_16x16x32_bf16 v[144:147], v[208:211], v[100:103], v[144:147]\n"
"s_waitcnt lgkmcnt(6)\n"
"ds_read_b64_tr_b16 v[236:237], v197 offset:19456\n"
"ds_read_b64_tr_b16 v[238:239], v197 offset:27648\n"
"v_mfma_f32_16x16x32_bf16 v[144:147], v[216:219], v[116:119], v[144:147]\n"
"ds_read_b64_tr_b16 v[208:209], v196 offset:16896\n"
"v_mfma_f32_16x16x32_bf16 v[144:147], v[220:223], v[124:127], v[144:147]\n"
"ds_read_b64_tr_b16 v[210:211], v196 offset:25088\n"
"v_mfma_f32_16x16x32_bf16 v[128:131], v[220:223], v[120:123], v[128:131]\n"
"ds_read_b64_tr_b16 v[212:213], v196 offset:17920\n"
"v_mfma_f32_16x16x32_bf16 v[128:131], v[216:219], v[112:115], v[128:131]\n"
"s_waitcnt lgkmcnt(7)\n"
"ds_read_b64_tr_b16 v[214:215], v196 offset:26112\n"
"v_mfma_f32_16x16x32_bf16 v[132:135], v[224:227], v[96:99], v[132:135]\n"
"s_mov_b32 s65, 373248\n"
"ds_read_b64_tr_b16 v[216:217], v196 offset:18944\n"
"v_mfma_f32_16x16x32_bf16 v[132:135], v[228:231], v[104:107], v[132:135]\n"
"s_add_u32 m0, s60, 132096\n"
"ds_read_b64_tr_b16 v[218:219], v196 offset:27136\n"
"v_mfma_f32_16x16x32_bf16 v[148:151], v[228:231], v[108:111], v[148:151]\n"
"buffer_load_dwordx4 v249, s[20:23], s65 offen lds\n"
"v_mfma_f32_16x16x32_bf16 v[148:151], v[224:227], v[100:103], v[148:151]\n"
"s_waitcnt lgkmcnt(6)\n"
"ds_read_b64_tr_b16 v[220:221], v196 offset:19968\n"
"ds_read_b64_tr_b16 v[222:223], v196 offset:28160\n"
"v_mfma_f32_16x16x32_bf16 v[148:151], v[232:235], v[116:119], v[148:151]\n"
"ds_read_b64_tr_b16 v[224:225], v197 offset:16896\n"
"v_mfma_f32_16x16x32_bf16 v[148:151], v[236:239], v[124:127], v[148:151]\n"
"ds_read_b64_tr_b16 v[226:227], v197 offset:25088\n"
"v_mfma_f32_16x16x32_bf16 v[132:135], v[236:239], v[120:123], v[132:135]\n"
"s_mov_b32 s65, 401920\n"
"ds_read_b64_tr_b16 v[228:229], v197 offset:17920\n"
"v_mfma_f32_16x16x32_bf16 v[132:135], v[232:235], v[112:115], v[132:135]\n"
"s_waitcnt lgkmcnt(7)\n"
"ds_read_b64_tr_b16 v[230:231], v197 offset:26112\n"
"v_mfma_f32_16x16x32_bf16 v[136:139], v[208:211], v[96:99], v[136:139]\n"
"s_add_u32 m0, s60, 133120\n"
"ds_read_b64_tr_b16 v[232:233], v197 offset:18944\n"
"v_mfma_f32_16x16x32_bf16 v[136:139], v[212:215], v[104:107], v[136:139]\n"
"buffer_load_dwordx4 v249, s[20:23], s65 offen lds\n"
"v_mfma_f32_16x16x32_bf16 v[152:155], v[212:215], v[108:111], v[152:155]\n"
"ds_read_b64_tr_b16 v[234:235], v197 offset:27136\n"
"ds_read_b64_tr_b16 v[236:237], v197 offset:19968\n"
"v_mfma_f32_16x16x32_bf16 v[152:155], v[208:211], v[100:103], v[152:155]\n"
"s_waitcnt lgkmcnt(7)\n"
"ds_read_b64_tr_b16 v[238:239], v197 offset:28160\n"
"v_cvt_pk_bf16_f32 v128, v128, v129\n"
"v_mfma_f32_16x16x32_bf16 v[152:155], v[216:219], v[116:119], v[152:155]\n"
"ds_read_b64_tr_b16 v[208:209], v192 offset:32768\n"
"v_cvt_pk_bf16_f32 v129, v130, v131\n"
"v_mfma_f32_16x16x32_bf16 v[152:155], v[220:223], v[124:127], v[152:155]\n"
"s_mov_b32 s65, 430592\n"
"ds_read_b64_tr_b16 v[210:211], v192 offset:40960\n"
"v_mfma_f32_16x16x32_bf16 v[136:139], v[220:223], v[120:123], v[136:139]\n"
"s_add_u32 m0, s60, 134144\n"
"ds_read_b64_tr_b16 v[212:213], v192 offset:33792\n"
"v_mfma_f32_16x16x32_bf16 v[136:139], v[216:219], v[112:115], v[136:139]\n"
"s_waitcnt lgkmcnt(7)\n"
"buffer_load_dwordx4 v249, s[20:23], s65 offen lds\n"
"v_cvt_pk_bf16_f32 v130, v132, v133\n"
"v_cvt_pk_bf16_f32 v131, v134, v135\n"
"v_mfma_f32_16x16x32_bf16 v[140:143], v[224:227], v[96:99], v[140:143]\n"
"buffer_store_dwordx4 v[128:131], v206, s[28:31], s74 offen offset:0 sc1\n"
"v_mfma_f32_16x16x32_bf16 v[140:143], v[228:231], v[104:107], v[140:143]\n"
"ds_read_b64_tr_b16 v[214:215], v192 offset:41984\n"
"ds_read_b64_tr_b16 v[216:217], v192 offset:34816\n"
"v_mfma_f32_16x16x32_bf16 v[156:159], v[228:231], v[108:111], v[156:159]\n"
"ds_read_b64_tr_b16 v[218:219], v192 offset:43008\n"
"ds_read_b64_tr_b16 v[220:221], v192 offset:35840\n"
"v_mfma_f32_16x16x32_bf16 v[156:159], v[224:227], v[100:103], v[156:159]\n"
"s_waitcnt lgkmcnt(7)\n"
"ds_read_b64_tr_b16 v[222:223], v192 offset:44032\n"
"v_cvt_pk_bf16_f32 v144, v144, v145\n"
"v_cvt_pk_bf16_f32 v145, v146, v147\n"
"v_cvt_pk_bf16_f32 v146, v148, v149\n"
"v_cvt_pk_bf16_f32 v147, v150, v151\n"
"v_mfma_f32_16x16x32_bf16 v[156:159], v[232:235], v[116:119], v[156:159]\n"
"buffer_store_dwordx4 v[144:147], v206, s[28:31], s74 offen offset:2048 sc1\n"
"v_mfma_f32_16x16x32_bf16 v[156:159], v[236:239], v[124:127], v[156:159]\n"
"ds_read_b64_tr_b16 v[224:225], v193 offset:32768\n"
"ds_read_b64_tr_b16 v[226:227], v193 offset:40960\n"
"v_mfma_f32_16x16x32_bf16 v[140:143], v[236:239], v[120:123], v[140:143]\n"
"s_barrier\n"
"v_mfma_f32_16x16x32_bf16 v[140:143], v[232:235], v[112:115], v[140:143]\n"
"s_waitcnt lgkmcnt(6)\n"
"ds_read_b64_tr_b16 v[228:229], v193 offset:33792\n"
"ds_read_b64_tr_b16 v[230:231], v193 offset:41984\n"
"v_mfma_f32_16x16x32_bf16 v[160:163], v[208:211], v[0:3], 0\n"
"s_mov_b32 s65, 640\n"
"ds_read_b64_tr_b16 v[232:233], v193 offset:34816\n"
"v_mfma_f32_16x16x32_bf16 v[160:163], v[212:215], v[8:11], v[160:163]\n"
"s_add_u32 m0, s60, 16384\n"
"ds_read_b64_tr_b16 v[234:235], v193 offset:43008\n"
"v_mfma_f32_16x16x32_bf16 v[176:179], v[212:215], v[12:15], 0\n"
"buffer_load_dwordx4 v249, s[20:23], s65 offen lds\n"
"v_mfma_f32_16x16x32_bf16 v[176:179], v[208:211], v[4:7], v[176:179]\n"
"s_waitcnt lgkmcnt(6)\n"
"ds_read_b64_tr_b16 v[236:237], v193 offset:35840\n"
"ds_read_b64_tr_b16 v[238:239], v193 offset:44032\n"
"v_mfma_f32_16x16x32_bf16 v[176:179], v[216:219], v[20:23], v[176:179]\n"
"ds_read_b64_tr_b16 v[208:209], v192 offset:33280\n"
"v_cvt_pk_bf16_f32 v136, v136, v137\n"
"v_mfma_f32_16x16x32_bf16 v[176:179], v[220:223], v[28:31], v[176:179]\n"
"s_mov_b32 s65, 29312\n"
"ds_read_b64_tr_b16 v[210:211], v192 offset:41472\n"
"v_mfma_f32_16x16x32_bf16 v[160:163], v[220:223], v[24:27], v[160:163]\n"
"s_add_u32 m0, s60, 17408\n"
"ds_read_b64_tr_b16 v[212:213], v192 offset:34304\n"
"v_mfma_f32_16x16x32_bf16 v[160:163], v[216:219], v[16:19], v[160:163]\n"
"s_waitcnt lgkmcnt(7)\n"
"buffer_load_dwordx4 v249, s[20:23], s65 offen lds\n"
"v_cvt_pk_bf16_f32 v137, v138, v139\n"
"v_cvt_pk_bf16_f32 v138, v140, v141\n"
"v_cvt_pk_bf16_f32 v139, v142, v143\n"
"v_mfma_f32_16x16x32_bf16 v[164:167], v[224:227], v[0:3], 0\n"
"buffer_store_dwordx4 v[136:139], v206, s[28:31], s74 offen offset:1024 sc1\n"
"v_mfma_f32_16x16x32_bf16 v[164:167], v[228:231], v[8:11], v[164:167]\n"
"s_mov_b32 s75, 12288\n"
"ds_read_b64_tr_b16 v[214:215], v192 offset:42496\n"
"ds_read_b64_tr_b16 v[216:217], v192 offset:35328\n"
"v_mfma_f32_16x16x32_bf16 v[180:183], v[228:231], v[12:15], 0\n"
"ds_read_b64_tr_b16 v[218:219], v192 offset:43520\n"
"ds_read_b64_tr_b16 v[220:221], v192 offset:36352\n"
"v_mfma_f32_16x16x32_bf16 v[180:183], v[224:227], v[4:7], v[180:183]\n"
"s_waitcnt lgkmcnt(7)\n"
"ds_read_b64_tr_b16 v[222:223], v192 offset:44544\n"
"v_cvt_pk_bf16_f32 v152, v152, v153\n"
"v_mfma_f32_16x16x32_bf16 v[180:183], v[232:235], v[20:23], v[180:183]\n"
"s_mov_b32 s65, 57984\n"
"ds_read_b64_tr_b16 v[224:225], v193 offset:33280\n"
"v_cvt_pk_bf16_f32 v153, v154, v155\n"
"v_cvt_pk_bf16_f32 v154, v156, v157\n"
"v_cvt_pk_bf16_f32 v155, v158, v159\n"
"v_mfma_f32_16x16x32_bf16 v[180:183], v[236:239], v[28:31], v[180:183]\n"
"buffer_store_dwordx4 v[152:155], v206, s[28:31], s74 offen offset:3072 sc1\n"
"v_mfma_f32_16x16x32_bf16 v[164:167], v[236:239], v[24:27], v[164:167]\n"
"s_add_u32 m0, s60, 18432\n"
"ds_read_b64_tr_b16 v[226:227], v193 offset:41472\n"
"ds_read_b64_tr_b16 v[228:229], v193 offset:34304\n"
"v_mfma_f32_16x16x32_bf16 v[164:167], v[232:235], v[16:19], v[164:167]\n"
"s_waitcnt lgkmcnt(7)\n"
"buffer_load_dwordx4 v249, s[20:23], s65 offen lds\n"
"v_mfma_f32_16x16x32_bf16 v[168:171], v[208:211], v[0:3], 0\n"
"s_mov_b32 s65, 86656\n"
"ds_read_b64_tr_b16 v[230:231], v193 offset:42496\n"
"ds_read_b64_tr_b16 v[232:233], v193 offset:35328\n"
"v_mfma_f32_16x16x32_bf16 v[168:171], v[212:215], v[8:11], v[168:171]\n"
"s_add_u32 m0, s60, 19456\n"
"ds_read_b64_tr_b16 v[234:235], v193 offset:43520\n"
"v_mfma_f32_16x16x32_bf16 v[184:187], v[212:215], v[12:15], 0\n"
"buffer_load_dwordx4 v249, s[20:23], s65 offen lds\n"
"v_mfma_f32_16x16x32_bf16 v[184:187], v[208:211], v[4:7], v[184:187]\n"
"s_waitcnt lgkmcnt(6)\n"
"ds_read_b64_tr_b16 v[236:237], v193 offset:36352\n"
"ds_read_b64_tr_b16 v[238:239], v193 offset:44544\n"
"v_mfma_f32_16x16x32_bf16 v[184:187], v[216:219], v[20:23], v[184:187]\n"
"ds_read_b64_tr_b16 v[208:209], v194 offset:32768\n"
"v_mfma_f32_16x16x32_bf16 v[184:187], v[220:223], v[28:31], v[184:187]\n"
"ds_read_b64_tr_b16 v[210:211], v194 offset:40960\n"
"v_mfma_f32_16x16x32_bf16 v[168:171], v[220:223], v[24:27], v[168:171]\n"
"buffer_load_dword v240, v250, s[28:31], 0 offen offset:128 sc1\n"
"v_mfma_f32_16x16x32_bf16 v[168:171], v[216:219], v[16:19], v[168:171]\n"
"s_waitcnt lgkmcnt(6)\n"
"ds_read_b64_tr_b16 v[212:213], v194 offset:33792\n"
"ds_read_b64_tr_b16 v[214:215], v194 offset:41984\n"
"v_mfma_f32_16x16x32_bf16 v[172:175], v[224:227], v[0:3], 0\n"
"ds_read_b64_tr_b16 v[216:217], v194 offset:34816\n"
"v_mfma_f32_16x16x32_bf16 v[172:175], v[228:231], v[8:11], v[172:175]\n"
"ds_read_b64_tr_b16 v[218:219], v194 offset:43008\n"
"v_mfma_f32_16x16x32_bf16 v[188:191], v[228:231], v[12:15], 0\n"
"ds_read_b64_tr_b16 v[220:221], v194 offset:35840\n"
"v_mfma_f32_16x16x32_bf16 v[188:191], v[224:227], v[4:7], v[188:191]\n"
"s_waitcnt lgkmcnt(7)\n"
"ds_read_b64_tr_b16 v[222:223], v194 offset:44032\n"
"v_mfma_f32_16x16x32_bf16 v[188:191], v[232:235], v[20:23], v[188:191]\n"
"ds_read_b64_tr_b16 v[224:225], v195 offset:32768\n"
"v_mfma_f32_16x16x32_bf16 v[188:191], v[236:239], v[28:31], v[188:191]\n"
"ds_read_b64_tr_b16 v[226:227], v195 offset:40960\n"
"v_mfma_f32_16x16x32_bf16 v[172:175], v[236:239], v[24:27], v[172:175]\n"
"ds_read_b64_tr_b16 v[228:229], v195 offset:33792\n"
"v_mfma_f32_16x16x32_bf16 v[172:175], v[232:235], v[16:19], v[172:175]\n"
"s_waitcnt lgkmcnt(7)\n"
"ds_read_b64_tr_b16 v[230:231], v195 offset:41984\n"
"v_mfma_f32_16x16x32_bf16 v[160:163], v[208:211], v[32:35], v[160:163]\n"
"s_mov_b32 s65, 115328\n"
"ds_read_b64_tr_b16 v[232:233], v195 offset:34816\n"
"v_mfma_f32_16x16x32_bf16 v[160:163], v[212:215], v[40:43], v[160:163]\n"
"s_add_u32 m0, s60, 147456\n"
"ds_read_b64_tr_b16 v[234:235], v195 offset:43008\n"
"v_mfma_f32_16x16x32_bf16 v[176:179], v[212:215], v[44:47], v[176:179]\n"
"buffer_load_dwordx4 v249, s[20:23], s65 offen lds\n"
"v_mfma_f32_16x16x32_bf16 v[176:179], v[208:211], v[36:39], v[176:179]\n"
"s_waitcnt lgkmcnt(6)\n"
"ds_read_b64_tr_b16 v[236:237], v195 offset:35840\n"
"ds_read_b64_tr_b16 v[238:239], v195 offset:44032\n"
"v_mfma_f32_16x16x32_bf16 v[176:179], v[216:219], v[52:55], v[176:179]\n"
"ds_read_b64_tr_b16 v[208:209], v194 offset:33280\n"
"v_mfma_f32_16x16x32_bf16 v[176:179], v[220:223], v[60:63], v[176:179]\n"
"ds_read_b64_tr_b16 v[210:211], v194 offset:41472\n"
"v_mfma_f32_16x16x32_bf16 v[160:163], v[220:223], v[56:59], v[160:163]\n"
"ds_read_b64_tr_b16 v[212:213], v194 offset:34304\n"
"v_mfma_f32_16x16x32_bf16 v[160:163], v[216:219], v[48:51], v[160:163]\n"
"s_waitcnt lgkmcnt(7)\n"
"ds_read_b64_tr_b16 v[214:215], v194 offset:42496\n"
"v_mfma_f32_16x16x32_bf16 v[164:167], v[224:227], v[32:35], v[164:167]\n"
"s_mov_b32 s65, 144000\n"
"ds_read_b64_tr_b16 v[216:217], v194 offset:35328\n"
"v_mfma_f32_16x16x32_bf16 v[164:167], v[228:231], v[40:43], v[164:167]\n"
"s_add_u32 m0, s60, 148480\n"
"ds_read_b64_tr_b16 v[218:219], v194 offset:43520\n"
"v_mfma_f32_16x16x32_bf16 v[180:183], v[228:231], v[44:47], v[180:183]\n"
"buffer_load_dwordx4 v249, s[20:23], s65 offen lds\n"
"v_mfma_f32_16x16x32_bf16 v[180:183], v[224:227], v[36:39], v[180:183]\n"
"s_waitcnt lgkmcnt(6)\n"
"ds_read_b64_tr_b16 v[220:221], v194 offset:36352\n"
"ds_read_b64_tr_b16 v[222:223], v194 offset:44544\n"
"v_mfma_f32_16x16x32_bf16 v[180:183], v[232:235], v[52:55], v[180:183]\n"
"ds_read_b64_tr_b16 v[224:225], v195 offset:33280\n"
"v_mfma_f32_16x16x32_bf16 v[180:183], v[236:239], v[60:63], v[180:183]\n"
"ds_read_b64_tr_b16 v[226:227], v195 offset:41472\n"
"v_mfma_f32_16x16x32_bf16 v[164:167], v[236:239], v[56:59], v[164:167]\n"
"s_mov_b32 s65, 172672\n"
"ds_read_b64_tr_b16 v[228:229], v195 offset:34304\n"
"v_mfma_f32_16x16x32_bf16 v[164:167], v[232:235], v[48:51], v[164:167]\n"
"s_waitcnt lgkmcnt(7)\n"
"ds_read_b64_tr_b16 v[230:231], v195 offset:42496\n"
"v_mfma_f32_16x16x32_bf16 v[168:171], v[208:211], v[32:35], v[168:171]\n"
"s_add_u32 m0, s60, 149504\n"
"ds_read_b64_tr_b16 v[232:233], v195 offset:35328\n"
"v_mfma_f32_16x16x32_bf16 v[168:171], v[212:215], v[40:43], v[168:171]\n"
"buffer_load_dwordx4 v249, s[20:23], s65 offen lds\n"
"v_mfma_f32_16x16x32_bf16 v[184:187], v[212:215], v[44:47], v[184:187]\n"
"ds_read_b64_tr_b16 v[234:235], v195 offset:43520\n"
"ds_read_b64_tr_b16 v[236:237], v195 offset:36352\n"
"v_mfma_f32_16x16x32_bf16 v[184:187], v[208:211], v[36:39], v[184:187]\n"
"s_waitcnt lgkmcnt(7)\n"
"ds_read_b64_tr_b16 v[238:239], v195 offset:44544\n"
"v_mfma_f32_16x16x32_bf16 v[184:187], v[216:219], v[52:55], v[184:187]\n"
"ds_read_b64_tr_b16 v[208:209], v192 offset:49152\n"
"v_mfma_f32_16x16x32_bf16 v[184:187], v[220:223], v[60:63], v[184:187]\n"
"s_mov_b32 s65, 201344\n"
"ds_read_b64_tr_b16 v[210:211], v192 offset:57344\n"
"v_mfma_f32_16x16x32_bf16 v[168:171], v[220:223], v[56:59], v[168:171]\n"
"s_add_u32 m0, s60, 150528\n"
"ds_read_b64_tr_b16 v[212:213], v192 offset:50176\n"
"v_mfma_f32_16x16x32_bf16 v[168:171], v[216:219], v[48:51], v[168:171]\n"
"s_waitcnt lgkmcnt(7)\n"
"buffer_load_dwordx4 v249, s[20:23], s65 offen lds\n"
"v_mfma_f32_16x16x32_bf16 v[172:175], v[224:227], v[32:35], v[172:175]\n"
"ds_read_b64_tr_b16 v[214:215], v192 offset:58368\n"
"ds_read_b64_tr_b16 v[216:217], v192 offset:51200\n"
"v_mfma_f32_16x16x32_bf16 v[172:175], v[228:231], v[40:43], v[172:175]\n"
"ds_read_b64_tr_b16 v[218:219], v192 offset:59392\n"
"v_mfma_f32_16x16x32_bf16 v[188:191], v[228:231], v[44:47], v[188:191]\n"
"ds_read_b64_tr_b16 v[220:221], v192 offset:52224\n"
"v_mfma_f32_16x16x32_bf16 v[188:191], v[224:227], v[36:39], v[188:191]\n"
"s_waitcnt lgkmcnt(7)\n"
"ds_read_b64_tr_b16 v[222:223], v192 offset:60416\n"
"v_mfma_f32_16x16x32_bf16 v[188:191], v[232:235], v[52:55], v[188:191]\n"
"ds_read_b64_tr_b16 v[224:225], v193 offset:49152\n"
"v_mfma_f32_16x16x32_bf16 v[188:191], v[236:239], v[60:63], v[188:191]\n"
"ds_read_b64_tr_b16 v[226:227], v193 offset:57344\n"
"v_mfma_f32_16x16x32_bf16 v[172:175], v[236:239], v[56:59], v[172:175]\n"
"s_waitcnt vmcnt(26)\n"
"s_barrier\n"
"v_mfma_f32_16x16x32_bf16 v[172:175], v[232:235], v[48:51], v[172:175]\n"
"s_waitcnt lgkmcnt(6)\n"
"ds_read_b64_tr_b16 v[228:229], v193 offset:50176\n"
"ds_read_b64_tr_b16 v[230:231], v193 offset:58368\n"
"v_mfma_f32_16x16x32_bf16 v[160:163], v[208:211], v[64:67], v[160:163]\n"
"s_mov_b32 s65, 230016\n"
"ds_read_b64_tr_b16 v[232:233], v193 offset:51200\n"
"v_mfma_f32_16x16x32_bf16 v[160:163], v[212:215], v[72:75], v[160:163]\n"
"s_add_u32 m0, s60, 32768\n"
"ds_read_b64_tr_b16 v[234:235], v193 offset:59392\n"
"v_mfma_f32_16x16x32_bf16 v[176:179], v[212:215], v[76:79], v[176:179]\n"
"buffer_load_dwordx4 v249, s[20:23], s65 offen lds\n"
"v_mfma_f32_16x16x32_bf16 v[176:179], v[208:211], v[68:71], v[176:179]\n"
"s_waitcnt lgkmcnt(6)\n"
"ds_read_b64_tr_b16 v[236:237], v193 offset:52224\n"
"ds_read_b64_tr_b16 v[238:239], v193 offset:60416\n"
"v_mfma_f32_16x16x32_bf16 v[176:179], v[216:219], v[84:87], v[176:179]\n"
"ds_read_b64_tr_b16 v[208:209], v192 offset:49664\n"
"v_mfma_f32_16x16x32_bf16 v[176:179], v[220:223], v[92:95], v[176:179]\n"
"ds_read_b64_tr_b16 v[210:211], v192 offset:57856\n"
"v_mfma_f32_16x16x32_bf16 v[160:163], v[220:223], v[88:91], v[160:163]\n"
"ds_read_b64_tr_b16 v[212:213], v192 offset:50688\n"
"v_mfma_f32_16x16x32_bf16 v[160:163], v[216:219], v[80:83], v[160:163]\n"
"s_waitcnt lgkmcnt(7)\n"
"ds_read_b64_tr_b16 v[214:215], v192 offset:58880\n"
"v_mfma_f32_16x16x32_bf16 v[164:167], v[224:227], v[64:67], v[164:167]\n"
"s_mov_b32 s65, 258688\n"
"ds_read_b64_tr_b16 v[216:217], v192 offset:51712\n"
"v_mfma_f32_16x16x32_bf16 v[164:167], v[228:231], v[72:75], v[164:167]\n"
"s_add_u32 m0, s60, 33792\n"
"ds_read_b64_tr_b16 v[218:219], v192 offset:59904\n"
"v_mfma_f32_16x16x32_bf16 v[180:183], v[228:231], v[76:79], v[180:183]\n"
"buffer_load_dwordx4 v249, s[20:23], s65 offen lds\n"
"v_mfma_f32_16x16x32_bf16 v[180:183], v[224:227], v[68:71], v[180:183]\n"
"s_waitcnt lgkmcnt(6)\n"
"ds_read_b64_tr_b16 v[220:221], v192 offset:52736\n"
"ds_read_b64_tr_b16 v[222:223], v192 offset:60928\n"
"v_mfma_f32_16x16x32_bf16 v[180:183], v[232:235], v[84:87], v[180:183]\n"
"ds_read_b64_tr_b16 v[224:225], v193 offset:49664\n"
"v_mfma_f32_16x16x32_bf16 v[180:183], v[236:239], v[92:95], v[180:183]\n"
"ds_read_b64_tr_b16 v[226:227], v193 offset:57856\n"
"v_mfma_f32_16x16x32_bf16 v[164:167], v[236:239], v[88:91], v[164:167]\n"
"s_mov_b32 s65, 287360\n"
"ds_read_b64_tr_b16 v[228:229], v193 offset:50688\n"
"v_mfma_f32_16x16x32_bf16 v[164:167], v[232:235], v[80:83], v[164:167]\n"
"s_waitcnt lgkmcnt(7)\n"
"ds_read_b64_tr_b16 v[230:231], v193 offset:58880\n"
"v_mfma_f32_16x16x32_bf16 v[168:171], v[208:211], v[64:67], v[168:171]\n"
"s_add_u32 m0, s60, 34816\n"
"ds_read_b64_tr_b16 v[232:233], v193 offset:51712\n"
"v_mfma_f32_16x16x32_bf16 v[168:171], v[212:215], v[72:75], v[168:171]\n"
"buffer_load_dwordx4 v249, s[20:23], s65 offen lds\n"
"v_mfma_f32_16x16x32_bf16 v[184:187], v[212:215], v[76:79], v[184:187]\n"
"ds_read_b64_tr_b16 v[234:235], v193 offset:59904\n"
"ds_read_b64_tr_b16 v[236:237], v193 offset:52736\n"
"v_mfma_f32_16x16x32_bf16 v[184:187], v[208:211], v[68:71], v[184:187]\n"
"s_waitcnt lgkmcnt(7)\n"
"ds_read_b64_tr_b16 v[238:239], v193 offset:60928\n"
"v_mfma_f32_16x16x32_bf16 v[184:187], v[216:219], v[84:87], v[184:187]\n"
"ds_read_b64_tr_b16 v[208:209], v194 offset:0\n"
"v_mfma_f32_16x16x32_bf16 v[184:187], v[220:223], v[92:95], v[184:187]\n"
"s_mov_b32 s65, 316032\n"
"ds_read_b64_tr_b16 v[210:211], v194 offset:8192\n"
"v_mfma_f32_16x16x32_bf16 v[168:171], v[220:223], v[88:91], v[168:171]\n"
"s_add_u32 m0, s60, 35840\n"
"ds_read_b64_tr_b16 v[212:213], v194 offset:1024\n"
"v_mfma_f32_16x16x32_bf16 v[168:171], v[216:219], v[80:83], v[168:171]\n"
"s_waitcnt lgkmcnt(7)\n"
"buffer_load_dwordx4 v249, s[20:23], s65 offen lds\n"
"v_mfma_f32_16x16x32_bf16 v[172:175], v[224:227], v[64:67], v[172:175]\n"
"ds_read_b64_tr_b16 v[214:215], v194 offset:9216\n"
"ds_read_b64_tr_b16 v[216:217], v194 offset:2048\n"
"v_mfma_f32_16x16x32_bf16 v[172:175], v[228:231], v[72:75], v[172:175]\n"
"ds_read_b64_tr_b16 v[218:219], v194 offset:10240\n"
"v_mfma_f32_16x16x32_bf16 v[188:191], v[228:231], v[76:79], v[188:191]\n"
"ds_read_b64_tr_b16 v[220:221], v194 offset:3072\n"
"v_mfma_f32_16x16x32_bf16 v[188:191], v[224:227], v[68:71], v[188:191]\n"
"s_waitcnt lgkmcnt(7)\n"
"ds_read_b64_tr_b16 v[222:223], v194 offset:11264\n"
"v_mfma_f32_16x16x32_bf16 v[188:191], v[232:235], v[84:87], v[188:191]\n"
"ds_read_b64_tr_b16 v[224:225], v195 offset:0\n"
"v_mfma_f32_16x16x32_bf16 v[188:191], v[236:239], v[92:95], v[188:191]\n"
"ds_read_b64_tr_b16 v[226:227], v195 offset:8192\n"
"v_mfma_f32_16x16x32_bf16 v[172:175], v[236:239], v[88:91], v[172:175]\n"
"s_waitcnt vmcnt(8)\n"
"ds_read_b64_tr_b16 v[228:229], v195 offset:1024\n"
"v_mfma_f32_16x16x32_bf16 v[172:175], v[232:235], v[80:83], v[172:175]\n"
"s_waitcnt lgkmcnt(7)\n"
"ds_read_b64_tr_b16 v[230:231], v195 offset:9216\n"
"v_cmp_ne_u32 vcc, v240, v244\n"
"v_mfma_f32_16x16x32_bf16 v[160:163], v[208:211], v[96:99], v[160:163]\n"
"ds_read_b64_tr_b16 v[232:233], v195 offset:2048\n"
"v_mfma_f32_16x16x32_bf16 v[160:163], v[212:215], v[104:107], v[160:163]\n"
"ds_read_b64_tr_b16 v[234:235], v195 offset:10240\n"
"v_mfma_f32_16x16x32_bf16 v[176:179], v[212:215], v[108:111], v[176:179]\n"
"s_cbranch_vccnz SP0_%=\n"
"SR0_%=:\n"
"v_mfma_f32_16x16x32_bf16 v[176:179], v[208:211], v[100:103], v[176:179]\n"
"s_waitcnt lgkmcnt(6)\n"
"s_mov_b32 s65, 344704\n"
"ds_read_b64_tr_b16 v[236:237], v195 offset:3072\n"
"ds_read_b64_tr_b16 v[238:239], v195 offset:11264\n"
"v_mfma_f32_16x16x32_bf16 v[176:179], v[216:219], v[116:119], v[176:179]\n"
"s_add_u32 m0, s60, 98304\n"
"ds_read_b64_tr_b16 v[208:209], v194 offset:512\n"
"v_mfma_f32_16x16x32_bf16 v[176:179], v[220:223], v[124:127], v[176:179]\n"
"buffer_load_dwordx4 v249, s[20:23], s65 offen lds\n"
"v_mfma_f32_16x16x32_bf16 v[160:163], v[220:223], v[120:123], v[160:163]\n"
"s_mov_b32 s65, 373376\n"
"s_add_u32 m0, s60, 99328\n"
"ds_read_b64_tr_b16 v[210:211], v194 offset:8704\n"
"ds_read_b64_tr_b16 v[212:213], v194 offset:1536\n"
"v_mfma_f32_16x16x32_bf16 v[160:163], v[216:219], v[112:115], v[160:163]\n"
"s_waitcnt lgkmcnt(7)\n"
"buffer_load_dwordx4 v249, s[20:23], s65 offen lds\n"
"v_mfma_f32_16x16x32_bf16 v[164:167], v[224:227], v[96:99], v[164:167]\n"
"ds_read_b64_tr_b16 v[214:215], v194 offset:9728\n"
"ds_read_b64_tr_b16 v[216:217], v194 offset:2560\n"
"v_mfma_f32_16x16x32_bf16 v[164:167], v[228:231], v[104:107], v[164:167]\n"
"ds_read_b64_tr_b16 v[218:219], v194 offset:10752\n"
"v_mfma_f32_16x16x32_bf16 v[180:183], v[228:231], v[108:111], v[180:183]\n"
"s_mov_b32 s65, 402048\n"
"s_add_u32 m0, s60, 100352\n"
"ds_read_b64_tr_b16 v[220:221], v194 offset:3584\n"
"v_mfma_f32_16x16x32_bf16 v[180:183], v[224:227], v[100:103], v[180:183]\n"
"s_waitcnt lgkmcnt(7)\n"
"buffer_load_dwordx4 v249, s[20:23], s65 offen lds\n"
"v_mfma_f32_16x16x32_bf16 v[180:183], v[232:235], v[116:119], v[180:183]\n"
"ds_read_b64_tr_b16 v[222:223], v194 offset:11776\n"
"ds_read_b64_tr_b16 v[224:225], v195 offset:512\n"
"v_mfma_f32_16x16x32_bf16 v[180:183], v[236:239], v[124:127], v[180:183]\n"
"s_mov_b32 s65, 430720\n"
"s_add_u32 m0, s60, 101376\n"
"ds_read_b64_tr_b16 v[226:227], v195 offset:8704\n"
"v_mfma_f32_16x16x32_bf16 v[164:167], v[236:239], v[120:123], v[164:167]\n"
"buffer_load_dwordx4 v249, s[20:23], s65 offen lds\n"
"v_mfma_f32_16x16x32_bf16 v[164:167], v[232:235], v[112:115], v[164:167]\n"
"s_waitcnt lgkmcnt(6)\n"
"ds_read_b64_tr_b16 v[228:229], v195 offset:1536\n"
"ds_read_b64_tr_b16 v[230:231], v195 offset:9728\n"
"v_mfma_f32_16x16x32_bf16 v[168:171], v[208:211], v[96:99], v[168:171]\n"
"ds_read_b64_tr_b16 v[232:233], v195 offset:2560\n"
"v_cvt_pk_bf16_f32 v176, v176, v177\n"
"v_mfma_f32_16x16x32_bf16 v[168:171], v[212:215], v[104:107], v[168:171]\n"
"buffer_load_dwordx4 v[252:255], v250, s[28:31], 0 offen offset:0 sc1\n"
"v_mfma_f32_16x16x32_bf16 v[184:187], v[212:215], v[108:111], v[184:187]\n"
"ds_read_b64_tr_b16 v[234:235], v195 offset:10752\n"
"ds_read_b64_tr_b16 v[236:237], v195 offset:3584\n"
"v_mfma_f32_16x16x32_bf16 v[184:187], v[208:211], v[100:103], v[184:187]\n"
"s_waitcnt lgkmcnt(7)\n"
"ds_read_b64_tr_b16 v[238:239], v195 offset:11776\n"
"v_cvt_pk_bf16_f32 v160, v160, v161\n"
"v_mfma_f32_16x16x32_bf16 v[184:187], v[216:219], v[116:119], v[184:187]\n"
"s_mov_b32 s66, 0\n"
"ds_read_b64_tr_b16 v[208:209], v194 offset:16384\n"
"v_cvt_pk_bf16_f32 v161, v162, v163\n"
"v_mfma_f32_16x16x32_bf16 v[184:187], v[220:223], v[124:127], v[184:187]\n"
"buffer_load_dwordx4 a[0:3], v248, s[28:31], s66 offen sc1\n"
"v_mfma_f32_16x16x32_bf16 v[168:171], v[220:223], v[120:123], v[168:171]\n"
"ds_read_b64_tr_b16 v[210:211], v194 offset:24576\n"
"ds_read_b64_tr_b16 v[212:213], v194 offset:17408\n"
"v_mfma_f32_16x16x32_bf16 v[168:171], v[216:219], v[112:115], v[168:171]\n"
"s_waitcnt lgkmcnt(7)\n"
"ds_read_b64_tr_b16 v[214:215], v194 offset:25600\n"
"v_cvt_pk_bf16_f32 v162, v164, v165\n"
"v_cvt_pk_bf16_f32 v163, v166, v167\n"
"v_mfma_f32_16x16x32_bf16 v[172:175], v[224:227], v[96:99], v[172:175]\n"
"buffer_store_dwordx4 v[160:163], v206, s[28:31], s75 offen offset:0 sc1\n"
"v_mfma_f32_16x16x32_bf16 v[172:175], v[228:231], v[104:107], v[172:175]\n"
"s_mov_b32 s66, 114688\n"
"ds_read_b64_tr_b16 v[216:217], v194 offset:18432\n"
"ds_read_b64_tr_b16 v[218:219], v194 offset:26624\n"
"v_mfma_f32_16x16x32_bf16 v[188:191], v[228:231], v[108:111], v[188:191]\n"
"buffer_load_dwordx4 a[4:7], v248, s[28:31], s66 offen sc1\n"
"v_mfma_f32_16x16x32_bf16 v[188:191], v[224:227], v[100:103], v[188:191]\n"
"s_waitcnt lgkmcnt(6)\n"
"ds_read_b64_tr_b16 v[220:221], v194 offset:19456\n"
"ds_read_b64_tr_b16 v[222:223], v194 offset:27648\n"
"v_cvt_pk_bf16_f32 v177, v178, v179\n"
"v_cvt_pk_bf16_f32 v178, v180, v181\n"
"v_cvt_pk_bf16_f32 v179, v182, v183\n"
"v_mfma_f32_16x16x32_bf16 v[188:191], v[232:235], v[116:119], v[188:191]\n"
"buffer_store_dwordx4 v[176:179], v206, s[28:31], s75 offen offset:2048 sc1\n"
"v_mfma_f32_16x16x32_bf16 v[188:191], v[236:239], v[124:127], v[188:191]\n"
"ds_read_b64_tr_b16 v[224:225], v195 offset:16384\n"
"ds_read_b64_tr_b16 v[226:227], v195 offset:24576\n"
"v_mfma_f32_16x16x32_bf16 v[172:175], v[236:239], v[120:123], v[172:175]\n"
"s_barrier\n"
"v_mfma_f32_16x16x32_bf16 v[172:175], v[232:235], v[112:115], v[172:175]\n"
"s_waitcnt lgkmcnt(6)\n"
"ds_read_b64_tr_b16 v[228:229], v195 offset:17408\n"
"ds_read_b64_tr_b16 v[230:231], v195 offset:25600\n"
"v_mfma_f32_16x16x32_bf16 v[128:131], v[208:211], v[0:3], 0\n"
"s_mov_b32 s65, 768\n"
"ds_read_b64_tr_b16 v[232:233], v195 offset:18432\n"
"v_mfma_f32_16x16x32_bf16 v[128:131], v[212:215], v[8:11], v[128:131]\n"
"s_add_u32 m0, s60, 49152\n"
"ds_read_b64_tr_b16 v[234:235], v195 offset:26624\n"
"v_mfma_f32_16x16x32_bf16 v[144:147], v[212:215], v[12:15], 0\n"
"buffer_load_dwordx4 v249, s[20:23], s65 offen lds\n"
"v_mfma_f32_16x16x32_bf16 v[144:147], v[208:211], v[4:7], v[144:147]\n"
"s_waitcnt lgkmcnt(6)\n"
"ds_read_b64_tr_b16 v[236:237], v195 offset:19456\n"
"ds_read_b64_tr_b16 v[238:239], v195 offset:27648\n"
"v_mfma_f32_16x16x32_bf16 v[144:147], v[216:219], v[20:23], v[144:147]\n"
"s_mov_b32 s65, 29440\n"
"ds_read_b64_tr_b16 v[208:209], v194 offset:16896\n"
"v_mfma_f32_16x16x32_bf16 v[144:147], v[220:223], v[28:31], v[144:147]\n"
"s_add_u32 m0, s60, 50176\n"
"ds_read_b64_tr_b16 v[210:211], v194 offset:25088\n"
"v_mfma_f32_16x16x32_bf16 v[128:131], v[220:223], v[24:27], v[128:131]\n"
"buffer_load_dwordx4 v249, s[20:23], s65 offen lds\n"
"v_mfma_f32_16x16x32_bf16 v[128:131], v[216:219], v[16:19], v[128:131]\n"
"s_waitcnt lgkmcnt(6)\n"
"ds_read_b64_tr_b16 v[212:213], v194 offset:17920\n"
"ds_read_b64_tr_b16 v[214:215], v194 offset:26112\n"
"v_cvt_pk_bf16_f32 v168, v168, v169\n"
"v_cvt_pk_bf16_f32 v169, v170, v171\n"
"v_cvt_pk_bf16_f32 v170, v172, v173\n"
"v_cvt_pk_bf16_f32 v171, v174, v175\n"
"v_mfma_f32_16x16x32_bf16 v[132:135], v[224:227], v[0:3], 0\n"
"buffer_store_dwordx4 v[168:171], v206, s[28:31], s75 offen offset:1024 sc1\n"
"v_mfma_f32_16x16x32_bf16 v[132:135], v[228:231], v[8:11], v[132:135]\n"
"s_mov_b32 s74, 16384\n"
"ds_read_b64_tr_b16 v[216:217], v194 offset:18944\n"
"ds_read_b64_tr_b16 v[218:219], v194 offset:27136\n"
"v_mfma_f32_16x16x32_bf16 v[148:151], v[228:231], v[12:15], 0\n"
"s_mov_b32 s65, 58112\n"
"ds_read_b64_tr_b16 v[220:221], v194 offset:19968\n"
"v_mfma_f32_16x16x32_bf16 v[148:151], v[224:227], v[4:7], v[148:151]\n"
"s_waitcnt lgkmcnt(7)\n"
"ds_read_b64_tr_b16 v[222:223], v194 offset:28160\n"
"v_cvt_pk_bf16_f32 v184, v184, v185\n"
"v_mfma_f32_16x16x32_bf16 v[148:151], v[232:235], v[20:23], v[148:151]\n"
"s_add_u32 m0, s60, 51200\n"
"ds_read_b64_tr_b16 v[224:225], v195 offset:16896\n"
"v_cvt_pk_bf16_f32 v185, v186, v187\n"
"v_cvt_pk_bf16_f32 v186, v188, v189\n"
"v_cvt_pk_bf16_f32 v187, v190, v191\n"
"v_mfma_f32_16x16x32_bf16 v[148:151], v[236:239], v[28:31], v[148:151]\n"
"buffer_store_dwordx4 v[184:187], v206, s[28:31], s75 offen offset:3072 sc1\n"
"v_mfma_f32_16x16x32_bf16 v[132:135], v[236:239], v[24:27], v[132:135]\n"
"buffer_load_dwordx4 v249, s[20:23], s65 offen lds\n"
"v_mfma_f32_16x16x32_bf16 v[132:135], v[232:235], v[16:19], v[132:135]\n"
"s_waitcnt lgkmcnt(5)\n"
"ds_read_b64_tr_b16 v[226:227], v195 offset:25088\n"
"ds_read_b64_tr_b16 v[228:229], v195 offset:17920\n"
"v_mfma_f32_16x16x32_bf16 v[136:139], v[208:211], v[0:3], 0\n"
"s_mov_b32 s65, 86784\n"
"ds_read_b64_tr_b16 v[230:231], v195 offset:26112\n"
"ds_read_b64_tr_b16 v[232:233], v195 offset:18944\n"
"v_mfma_f32_16x16x32_bf16 v[136:139], v[212:215], v[8:11], v[136:139]\n"
"s_add_u32 m0, s60, 52224\n"
"ds_read_b64_tr_b16 v[234:235], v195 offset:27136\n"
"v_mfma_f32_16x16x32_bf16 v[152:155], v[212:215], v[12:15], 0\n"
"buffer_load_dwordx4 v249, s[20:23], s65 offen lds\n"
"v_mfma_f32_16x16x32_bf16 v[152:155], v[208:211], v[4:7], v[152:155]\n"
"IWB5_%=:\n"
"s_cbranch_execz IWB9_%=\n"
"s_waitcnt lgkmcnt(6)\n"
"ds_read_b64_tr_b16 v[236:237], v195 offset:19968\n"
"ds_read_b64_tr_b16 v[238:239], v195 offset:28160\n"
"v_mfma_f32_16x16x32_bf16 v[152:155], v[216:219], v[20:23], v[152:155]\n"
"s_mov_b32 s66, 229376\n"
"ds_read_b64_tr_b16 v[208:209], v194 offset:49152\n"
"v_mfma_f32_16x16x32_bf16 v[152:155], v[220:223], v[28:31], v[152:155]\n"
"buffer_load_dwordx4 a[8:11], v248, s[28:31], s66 offen sc1\n"
"v_mfma_f32_16x16x32_bf16 v[136:139], v[220:223], v[24:27], v[136:139]\n"
"ds_read_b64_tr_b16 v[210:211], v194 offset:57344\n"
"ds_read_b64_tr_b16 v[212:213], v194 offset:50176\n"
"v_mfma_f32_16x16x32_bf16 v[136:139], v[216:219], v[16:19], v[136:139]\n"
"s_waitcnt lgkmcnt(7)\n"
"ds_read_b64_tr_b16 v[214:215], v194 offset:58368\n"
"v_mfma_f32_16x16x32_bf16 v[140:143], v[224:227], v[0:3], 0\n"
"s_mov_b32 s66, 344064\n"
"ds_read_b64_tr_b16 v[216:217], v194 offset:51200\n"
"v_mfma_f32_16x16x32_bf16 v[140:143], v[228:231], v[8:11], v[140:143]\n"
"buffer_load_dwordx4 a[12:15], v248, s[28:31], s66 offen sc1\n"
"v_mfma_f32_16x16x32_bf16 v[156:159], v[228:231], v[12:15], 0\n"
"ds_read_b64_tr_b16 v[218:219], v194 offset:59392\n"
"ds_read_b64_tr_b16 v[220:221], v194 offset:52224\n"
"v_mfma_f32_16x16x32_bf16 v[156:159], v[224:227], v[4:7], v[156:159]\n"
"s_waitcnt lgkmcnt(7)\n"
"ds_read_b64_tr_b16 v[222:223], v194 offset:60416\n"
"v_mfma_f32_16x16x32_bf16 v[156:159], v[232:235], v[20:23], v[156:159]\n"
"ds_read_b64_tr_b16 v[224:225], v195 offset:49152\n"
"v_mfma_f32_16x16x32_bf16 v[156:159], v[236:239], v[28:31], v[156:159]\n"
"ds_read_b64_tr_b16 v[226:227], v195 offset:57344\n"
"v_mfma_f32_16x16x32_bf16 v[140:143], v[236:239], v[24:27], v[140:143]\n"
"ds_read_b64_tr_b16 v[228:229], v195 offset:50176\n"
"v_mfma_f32_16x16x32_bf16 v[140:143], v[232:235], v[16:19], v[140:143]\n"
"s_waitcnt lgkmcnt(7)\n"
"ds_read_b64_tr_b16 v[230:231], v195 offset:58368\n"
"v_mfma_f32_16x16x32_bf16 v[128:131], v[208:211], v[32:35], v[128:131]\n"
"s_mov_b32 s65, 115456\n"
"ds_read_b64_tr_b16 v[232:233], v195 offset:51200\n"
"v_mfma_f32_16x16x32_bf16 v[128:131], v[212:215], v[40:43], v[128:131]\n"
"s_add_u32 m0, s60, 65536\n"
"ds_read_b64_tr_b16 v[234:235], v195 offset:59392\n"
"v_mfma_f32_16x16x32_bf16 v[144:147], v[212:215], v[44:47], v[144:147]\n"
"buffer_load_dwordx4 v249, s[20:23], s65 offen lds\n"
"v_mfma_f32_16x16x32_bf16 v[144:147], v[208:211], v[36:39], v[144:147]\n"
"s_waitcnt lgkmcnt(6)\n"
"ds_read_b64_tr_b16 v[236:237], v195 offset:52224\n"
"ds_read_b64_tr_b16 v[238:239], v195 offset:60416\n"
"v_mfma_f32_16x16x32_bf16 v[144:147], v[216:219], v[52:55], v[144:147]\n"
"s_mov_b32 s65, 144128\n"
"ds_read_b64_tr_b16 v[208:209], v194 offset:49664\n"
"v_mfma_f32_16x16x32_bf16 v[144:147], v[220:223], v[60:63], v[144:147]\n"
"s_add_u32 m0, s60, 66560\n"
"ds_read_b64_tr_b16 v[210:211], v194 offset:57856\n"
"v_mfma_f32_16x16x32_bf16 v[128:131], v[220:223], v[56:59], v[128:131]\n"
"buffer_load_dwordx4 v249, s[20:23], s65 offen lds\n"
"v_mfma_f32_16x16x32_bf16 v[128:131], v[216:219], v[48:51], v[128:131]\n"
"s_waitcnt lgkmcnt(6)\n"
"ds_read_b64_tr_b16 v[212:213], v194 offset:50688\n"
"ds_read_b64_tr_b16 v[214:215], v194 offset:58880\n"
"v_mfma_f32_16x16x32_bf16 v[132:135], v[224:227], v[32:35], v[132:135]\n"
"ds_read_b64_tr_b16 v[216:217], v194 offset:51712\n"
"v_mfma_f32_16x16x32_bf16 v[132:135], v[228:231], v[40:43], v[132:135]\n"
"s_mov_b32 s65, 172800\n"
"ds_read_b64_tr_b16 v[218:219], v194 offset:59904\n"
"v_mfma_f32_16x16x32_bf16 v[148:151], v[228:231], v[44:47], v[148:151]\n"
"s_add_u32 m0, s60, 67584\n"
"ds_read_b64_tr_b16 v[220:221], v194 offset:52736\n"
"v_mfma_f32_16x16x32_bf16 v[148:151], v[224:227], v[36:39], v[148:151]\n"
"s_waitcnt lgkmcnt(7)\n"
"buffer_load_dwordx4 v249, s[20:23], s65 offen lds\n"
"v_mfma_f32_16x16x32_bf16 v[148:151], v[232:235], v[52:55], v[148:151]\n"
"ds_read_b64_tr_b16 v[222:223], v194 offset:60928\n"
"ds_read_b64_tr_b16 v[224:225], v195 offset:49664\n"
"v_mfma_f32_16x16x32_bf16 v[148:151], v[236:239], v[60:63], v[148:151]\n"
"ds_read_b64_tr_b16 v[226:227], v195 offset:57856\n"
"v_mfma_f32_16x16x32_bf16 v[132:135], v[236:239], v[56:59], v[132:135]\n"
"s_mov_b32 s65, 201472\n"
"ds_read_b64_tr_b16 v[228:229], v195 offset:50688\n"
"v_mfma_f32_16x16x32_bf16 v[132:135], v[232:235], v[48:51], v[132:135]\n"
"s_waitcnt lgkmcnt(7)\n"
"ds_read_b64_tr_b16 v[230:231], v195 offset:58880\n"
"v_mfma_f32_16x16x32_bf16 v[136:139], v[208:211], v[32:35], v[136:139]\n"
"s_add_u32 m0, s60, 68608\n"
"ds_read_b64_tr_b16 v[232:233], v195 offset:51712\n"
"v_mfma_f32_16x16x32_bf16 v[136:139], v[212:215], v[40:43], v[136:139]\n"
"buffer_load_dwordx4 v249, s[20:23], s65 offen lds\n"
"v_mfma_f32_16x16x32_bf16 v[152:155], v[212:215], v[44:47], v[152:155]\n"
"s_mov_b32 s66, 458752\n"
"ds_read_b64_tr_b16 v[234:235], v195 offset:59904\n"
"ds_read_b64_tr_b16 v[236:237], v195 offset:52736\n"
"v_mfma_f32_16x16x32_bf16 v[152:155], v[208:211], v[36:39], v[152:155]\n"
"s_waitcnt lgkmcnt(7)\n"
"buffer_load_dwordx4 a[16:19], v248, s[28:31], s66 offen sc1\n"
"v_mfma_f32_16x16x32_bf16 v[152:155], v[216:219], v[52:55], v[152:155]\n"
"ds_read_b64_tr_b16 v[238:239], v195 offset:60928\n"
"ds_read_b64_tr_b16 v[208:209], v192 offset:0\n"
"v_mfma_f32_16x16x32_bf16 v[152:155], v[220:223], v[60:63], v[152:155]\n"
"ds_read_b64_tr_b16 v[210:211], v192 offset:8192\n"
"v_mfma_f32_16x16x32_bf16 v[136:139], v[220:223], v[56:59], v[136:139]\n"
"ds_read_b64_tr_b16 v[212:213], v192 offset:1024\n"
"v_mfma_f32_16x16x32_bf16 v[136:139], v[216:219], v[48:51], v[136:139]\n"
"s_waitcnt lgkmcnt(7)\n"
"ds_read_b64_tr_b16 v[214:215], v192 offset:9216\n"
"v_mfma_f32_16x16x32_bf16 v[140:143], v[224:227], v[32:35], v[140:143]\n"
"s_mov_b32 s66, 573440\n"
"ds_read_b64_tr_b16 v[216:217], v192 offset:2048\n"
"v_mfma_f32_16x16x32_bf16 v[140:143], v[228:231], v[40:43], v[140:143]\n"
"buffer_load_dwordx4 a[20:23], v248, s[28:31], s66 offen sc1\n"
"v_mfma_f32_16x16x32_bf16 v[156:159], v[228:231], v[44:47], v[156:159]\n"
"ds_read_b64_tr_b16 v[218:219], v192 offset:10240\n"
"ds_read_b64_tr_b16 v[220:221], v192 offset:3072\n"
"v_mfma_f32_16x16x32_bf16 v[156:159], v[224:227], v[36:39], v[156:159]\n"
"s_waitcnt lgkmcnt(7)\n"
"ds_read_b64_tr_b16 v[222:223], v192 offset:11264\n"
"v_mfma_f32_16x16x32_bf16 v[156:159], v[232:235], v[52:55], v[156:159]\n"
"ds_read_b64_tr_b16 v[224:225], v193 offset:0\n"
"v_mfma_f32_16x16x32_bf16 v[156:159], v[236:239], v[60:63], v[156:159]\n"
"ds_read_b64_tr_b16 v[226:227], v193 offset:8192\n"
"v_mfma_f32_16x16x32_bf16 v[140:143], v[236:239], v[56:59], v[140:143]\n"
"s_waitcnt vmcnt(10)\n"
"s_barrier\n"
"v_mfma_f32_16x16x32_bf16 v[140:143], v[232:235], v[48:51], v[140:143]\n"
"s_waitcnt lgkmcnt(6)\n"
"ds_read_b64_tr_b16 v[228:229], v193 offset:1024\n"
"ds_read_b64_tr_b16 v[230:231], v193 offset:9216\n"
"v_mfma_f32_16x16x32_bf16 v[128:131], v[208:211], v[64:67], v[128:131]\n"
"buffer_store_dwordx2 v[252:253], v251, s[28:31], 0 offen offset:128 sc1\n"
"v_mfma_f32_16x16x32_bf16 v[128:131], v[212:215], v[72:75], v[128:131]\n"
"ds_read_b64_tr_b16 v[232:233], v193 offset:2048\n"
"ds_read_b64_tr_b16 v[234:235], v193 offset:10240\n"
"v_mfma_f32_16x16x32_bf16 v[144:147], v[212:215], v[76:79], v[144:147]\n"
"ds_read_b64_tr_b16 v[236:237], v193 offset:3072\n"
"v_mfma_f32_16x16x32_bf16 v[144:147], v[208:211], v[68:71], v[144:147]\n"
"s_waitcnt lgkmcnt(7)\n"
"ds_read_b64_tr_b16 v[238:239], v193 offset:11264\n"
"v_mfma_f32_16x16x32_bf16 v[144:147], v[216:219], v[84:87], v[144:147]\n"
"s_mov_b32 s65, 230144\n"
"ds_read_b64_tr_b16 v[208:209], v192 offset:512\n"
"v_mfma_f32_16x16x32_bf16 v[144:147], v[220:223], v[92:95], v[144:147]\n"
"s_add_u32 m0, s60, 81920\n"
"ds_read_b64_tr_b16 v[210:211], v192 offset:8704\n"
"v_mfma_f32_16x16x32_bf16 v[128:131], v[220:223], v[88:91], v[128:131]\n"
"buffer_load_dwordx4 v249, s[20:23], s65 offen lds\n"
"v_mfma_f32_16x16x32_bf16 v[128:131], v[216:219], v[80:83], v[128:131]\n"
"s_waitcnt lgkmcnt(6)\n"
"ds_read_b64_tr_b16 v[212:213], v192 offset:1536\n"
"ds_read_b64_tr_b16 v[214:215], v192 offset:9728\n"
"v_mfma_f32_16x16x32_bf16 v[132:135], v[224:227], v[64:67], v[132:135]\n"
"s_mov_b32 s65, 258816\n"
"ds_read_b64_tr_b16 v[216:217], v192 offset:2560\n"
"v_mfma_f32_16x16x32_bf16 v[132:135], v[228:231], v[72:75], v[132:135]\n"
"s_add_u32 m0, s60, 82944\n"
"ds_read_b64_tr_b16 v[218:219], v192 offset:10752\n"
"v_mfma_f32_16x16x32_bf16 v[148:151], v[228:231], v[76:79], v[148:151]\n"
"buffer_load_dwordx4 v249, s[20:23], s65 offen lds\n"
"v_mfma_f32_16x16x32_bf16 v[148:151], v[224:227], v[68:71], v[148:151]\n"
"s_waitcnt lgkmcnt(6)\n"
"ds_read_b64_tr_b16 v[220:221], v192 offset:3584\n"
"ds_read_b64_tr_b16 v[222:223], v192 offset:11776\n"
"v_mfma_f32_16x16x32_bf16 v[148:151], v[232:235], v[84:87], v[148:151]\n"
"s_mov_b32 s65, 287488\n"
"ds_read_b64_tr_b16 v[224:225], v193 offset:512\n"
"v_mfma_f32_16x16x32_bf16 v[148:151], v[236:239], v[92:95], v[148:151]\n"
"s_add_u32 m0, s60, 83968\n"
"ds_read_b64_tr_b16 v[226:227], v193 offset:8704\n"
"v_mfma_f32_16x16x32_bf16 v[132:135], v[236:239], v[88:91], v[132:135]\n"
"buffer_load_dwordx4 v249, s[20:23], s65 offen lds\n"
"v_mfma_f32_16x16x32_bf16 v[132:135], v[232:235], v[80:83], v[132:135]\n"
"s_waitcnt lgkmcnt(6)\n"
"ds_read_b64_tr_b16 v[228:229], v193 offset:1536\n"
"ds_read_b64_tr_b16 v[230:231], v193 offset:9728\n"
"v_mfma_f32_16x16x32_bf16 v[136:139], v[208:211], v[64:67], v[136:139]\n"
"s_mov_b32 s65, 316160\n"
"ds_read_b64_tr_b16 v[232:233], v193 offset:2560\n"
"v_mfma_f32_16x16x32_bf16 v[136:139], v[212:215], v[72:75], v[136:139]\n"
"s_add_u32 m0, s60, 84992\n"
"ds_read_b64_tr_b16 v[234:235], v193 offset:10752\n"
"v_mfma_f32_16x16x32_bf16 v[152:155], v[212:215], v[76:79], v[152:155]\n"
"buffer_load_dwordx4 v249, s[20:23], s65 offen lds\n"
"v_mfma_f32_16x16x32_bf16 v[152:155], v[208:211], v[68:71], v[152:155]\n"
"s_waitcnt lgkmcnt(6)\n"
"ds_read_b64_tr_b16 v[236:237], v193 offset:3584\n"
"ds_read_b64_tr_b16 v[238:239], v193 offset:11776\n"
"v_mfma_f32_16x16x32_bf16 v[152:155], v[216:219], v[84:87], v[152:155]\n"
"s_mov_b32 s66, 688128\n"
"ds_read_b64_tr_b16 v[208:209], v196 offset:0\n"
"v_mfma_f32_16x16x32_bf16 v[152:155], v[220:223], v[92:95], v[152:155]\n"
"buffer_load_dwordx4 a[24:27], v248, s[28:31], s66 offen sc1\n"
"v_mfma_f32_16x16x32_bf16 v[136:139], v[220:223], v[88:91], v[136:139]\n"
"ds_read_b64_tr_b16 v[210:211], v196 offset:8192\n"
"ds_read_b64_tr_b16 v[212:213], v196 offset:1024\n"
"v_mfma_f32_16x16x32_bf16 v[136:139], v[216:219], v[80:83], v[136:139]\n"
"s_waitcnt lgkmcnt(7)\n"
"ds_read_b64_tr_b16 v[214:215], v196 offset:9216\n"
"v_mfma_f32_16x16x32_bf16 v[140:143], v[224:227], v[64:67], v[140:143]\n"
"s_mov_b32 s66, 802816\n"
"ds_read_b64_tr_b16 v[216:217], v196 offset:2048\n"
"v_mfma_f32_16x16x32_bf16 v[140:143], v[228:231], v[72:75], v[140:143]\n"
"buffer_load_dwordx4 a[28:31], v248, s[28:31], s66 offen sc1\n"
"v_mfma_f32_16x16x32_bf16 v[156:159], v[228:231], v[76:79], v[156:159]\n"
"ds_read_b64_tr_b16 v[218:219], v196 offset:10240\n"
"ds_read_b64_tr_b16 v[220:221], v196 offset:3072\n"
"v_mfma_f32_16x16x32_bf16 v[156:159], v[224:227], v[68:71], v[156:159]\n"
"s_waitcnt lgkmcnt(7)\n"
"ds_read_b64_tr_b16 v[222:223], v196 offset:11264\n"
"v_mfma_f32_16x16x32_bf16 v[156:159], v[232:235], v[84:87], v[156:159]\n"
"ds_read_b64_tr_b16 v[224:225], v197 offset:0\n"
"v_mfma_f32_16x16x32_bf16 v[156:159], v[236:239], v[92:95], v[156:159]\n"
"ds_read_b64_tr_b16 v[226:227], v197 offset:8192\n"
"v_mfma_f32_16x16x32_bf16 v[140:143], v[236:239], v[88:91], v[140:143]\n"
"ds_read_b64_tr_b16 v[228:229], v197 offset:1024\n"
"v_mfma_f32_16x16x32_bf16 v[140:143], v[232:235], v[80:83], v[140:143]\n"
"s_waitcnt lgkmcnt(7)\n"
"ds_read_b64_tr_b16 v[230:231], v197 offset:9216\n"
"v_mfma_f32_16x16x32_bf16 v[128:131], v[208:211], v[96:99], v[128:131]\n"
"s_mov_b32 s65, 344832\n"
"ds_read_b64_tr_b16 v[232:233], v197 offset:2048\n"
"v_mfma_f32_16x16x32_bf16 v[128:131], v[212:215], v[104:107], v[128:131]\n"
"s_add_u32 m0, s60, 114688\n"
"ds_read_b64_tr_b16 v[234:235], v197 offset:10240\n"
"v_mfma_f32_16x16x32_bf16 v[144:147], v[212:215], v[108:111], v[144:147]\n"
"buffer_load_dwordx4 v249, s[20:23], s65 offen lds\n"
"v_mfma_f32_16x16x32_bf16 v[144:147], v[208:211], v[100:103], v[144:147]\n"
"s_waitcnt lgkmcnt(6)\n"
"ds_read_b64_tr_b16 v[236:237], v197 offset:3072\n"
"ds_read_b64_tr_b16 v[238:239], v197 offset:11264\n"
"v_mfma_f32_16x16x32_bf16 v[144:147], v[216:219], v[116:119], v[144:147]\n"
"ds_read_b64_tr_b16 v[208:209], v196 offset:512\n"
"v_mfma_f32_16x16x32_bf16 v[144:147], v[220:223], v[124:127], v[144:147]\n"
"ds_read_b64_tr_b16 v[210:211], v196 offset:8704\n"
"v_mfma_f32_16x16x32_bf16 v[128:131], v[220:223], v[120:123], v[128:131]\n"
"ds_read_b64_tr_b16 v[212:213], v196 offset:1536\n"
"v_mfma_f32_16x16x32_bf16 v[128:131], v[216:219], v[112:115], v[128:131]\n"
"s_waitcnt lgkmcnt(7)\n"
"ds_read_b64_tr_b16 v[214:215], v196 offset:9728\n"
"v_mfma_f32_16x16x32_bf16 v[132:135], v[224:227], v[96:99], v[132:135]\n"
"s_mov_b32 s65, 373504\n"
"ds_read_b64_tr_b16 v[216:217], v196 offset:2560\n"
"v_mfma_f32_16x16x32_bf16 v[132:135], v[228:231], v[104:107], v[132:135]\n"
"s_add_u32 m0, s60, 115712\n"
"ds_read_b64_tr_b16 v[218:219], v196 offset:10752\n"
"v_mfma_f32_16x16x32_bf16 v[148:151], v[228:231], v[108:111], v[148:151]\n"
"buffer_load_dwordx4 v249, s[20:23], s65 offen lds\n"
"v_mfma_f32_16x16x32_bf16 v[148:151], v[224:227], v[100:103], v[148:151]\n"
"s_waitcnt lgkmcnt(6)\n"
"ds_read_b64_tr_b16 v[220:221], v196 offset:3584\n"
"ds_read_b64_tr_b16 v[222:223], v196 offset:11776\n"
"v_mfma_f32_16x16x32_bf16 v[148:151], v[232:235], v[116:119], v[148:151]\n"
"ds_read_b64_tr_b16 v[224:225], v197 offset:512\n"
"v_mfma_f32_16x16x32_bf16 v[148:151], v[236:239], v[124:127], v[148:151]\n"
"ds_read_b64_tr_b16 v[226:227], v197 offset:8704\n"
"v_mfma_f32_16x16x32_bf16 v[132:135], v[236:239], v[120:123], v[132:135]\n"
"s_mov_b32 s65, 402176\n"
"ds_read_b64_tr_b16 v[228:229], v197 offset:1536\n"
"v_mfma_f32_16x16x32_bf16 v[132:135], v[232:235], v[112:115], v[132:135]\n"
"s_waitcnt lgkmcnt(7)\n"
"ds_read_b64_tr_b16 v[230:231], v197 offset:9728\n"
"v_mfma_f32_16x16x32_bf16 v[136:139], v[208:211], v[96:99], v[136:139]\n"
"s_add_u32 m0, s60, 116736\n"
"ds_read_b64_tr_b16 v[232:233], v197 offset:2560\n"
"v_mfma_f32_16x16x32_bf16 v[136:139], v[212:215], v[104:107], v[136:139]\n"
"buffer_load_dwordx4 v249, s[20:23], s65 offen lds\n"
"v_mfma_f32_16x16x32_bf16 v[152:155], v[212:215], v[108:111], v[152:155]\n"
"ds_read_b64_tr_b16 v[234:235], v197 offset:10752\n"
"ds_read_b64_tr_b16 v[236:237], v197 offset:3584\n"
"v_mfma_f32_16x16x32_bf16 v[152:155], v[208:211], v[100:103], v[152:155]\n"
"s_waitcnt lgkmcnt(7)\n"
"ds_read_b64_tr_b16 v[238:239], v197 offset:11776\n"
"v_cvt_pk_bf16_f32 v128, v128, v129\n"
"v_mfma_f32_16x16x32_bf16 v[152:155], v[216:219], v[116:119], v[152:155]\n"
"ds_read_b64_tr_b16 v[208:209], v192 offset:16384\n"
"v_cvt_pk_bf16_f32 v129, v130, v131\n"
"v_mfma_f32_16x16x32_bf16 v[152:155], v[220:223], v[124:127], v[152:155]\n"
"s_mov_b32 s65, 430848\n"
"ds_read_b64_tr_b16 v[210:211], v192 offset:24576\n"
"v_mfma_f32_16x16x32_bf16 v[136:139], v[220:223], v[120:123], v[136:139]\n"
"s_add_u32 m0, s60, 117760\n"
"ds_read_b64_tr_b16 v[212:213], v192 offset:17408\n"
"v_mfma_f32_16x16x32_bf16 v[136:139], v[216:219], v[112:115], v[136:139]\n"
"s_waitcnt lgkmcnt(7)\n"
"buffer_load_dwordx4 v249, s[20:23], s65 offen lds\n"
"v_cvt_pk_bf16_f32 v130, v132, v133\n"
"v_cvt_pk_bf16_f32 v131, v134, v135\n"
"v_mfma_f32_16x16x32_bf16 v[140:143], v[224:227], v[96:99], v[140:143]\n"
"buffer_store_dwordx4 v[128:131], v206, s[28:31], s74 offen offset:0 sc1\n"
"v_mfma_f32_16x16x32_bf16 v[140:143], v[228:231], v[104:107], v[140:143]\n"
"ds_read_b64_tr_b16 v[214:215], v192 offset:25600\n"
"ds_read_b64_tr_b16 v[216:217], v192 offset:18432\n"
"v_mfma_f32_16x16x32_bf16 v[156:159], v[228:231], v[108:111], v[156:159]\n"
"ds_read_b64_tr_b16 v[218:219], v192 offset:26624\n"
"ds_read_b64_tr_b16 v[220:221], v192 offset:19456\n"
"v_mfma_f32_16x16x32_bf16 v[156:159], v[224:227], v[100:103], v[156:159]\n"
"s_waitcnt lgkmcnt(7)\n"
"ds_read_b64_tr_b16 v[222:223], v192 offset:27648\n"
"v_cvt_pk_bf16_f32 v144, v144, v145\n"
"v_cvt_pk_bf16_f32 v145, v146, v147\n"
"v_cvt_pk_bf16_f32 v146, v148, v149\n"
"v_cvt_pk_bf16_f32 v147, v150, v151\n"
"v_mfma_f32_16x16x32_bf16 v[156:159], v[232:235], v[116:119], v[156:159]\n"
"buffer_store_dwordx4 v[144:147], v206, s[28:31], s74 offen offset:2048 sc1\n"
"v_mfma_f32_16x16x32_bf16 v[156:159], v[236:239], v[124:127], v[156:159]\n"
"ds_read_b64_tr_b16 v[224:225], v193 offset:16384\n"
"ds_read_b64_tr_b16 v[226:227], v193 offset:24576\n"
"v_mfma_f32_16x16x32_bf16 v[140:143], v[236:239], v[120:123], v[140:143]\n"
"s_barrier\n"
"v_mfma_f32_16x16x32_bf16 v[140:143], v[232:235], v[112:115], v[140:143]\n"
"s_waitcnt lgkmcnt(6)\n"
"ds_read_b64_tr_b16 v[228:229], v193 offset:17408\n"
"ds_read_b64_tr_b16 v[230:231], v193 offset:25600\n"
"v_mfma_f32_16x16x32_bf16 v[160:163], v[208:211], v[0:3], 0\n"
"ds_read_b64_tr_b16 v[232:233], v193 offset:18432\n"
"v_cvt_pk_bf16_f32 v152, v152, v153\n"
"v_mfma_f32_16x16x32_bf16 v[160:163], v[212:215], v[8:11], v[160:163]\n"
"ds_read_b64_tr_b16 v[234:235], v193 offset:26624\n"
"v_cvt_pk_bf16_f32 v153, v154, v155\n"
"v_mfma_f32_16x16x32_bf16 v[176:179], v[212:215], v[12:15], 0\n"
"ds_read_b64_tr_b16 v[236:237], v193 offset:19456\n"
"v_cvt_pk_bf16_f32 v136, v136, v137\n"
"v_mfma_f32_16x16x32_bf16 v[176:179], v[208:211], v[4:7], v[176:179]\n"
"s_waitcnt lgkmcnt(7)\n"
"ds_read_b64_tr_b16 v[238:239], v193 offset:27648\n"
"v_cvt_pk_bf16_f32 v137, v138, v139\n"
"v_mfma_f32_16x16x32_bf16 v[176:179], v[216:219], v[20:23], v[176:179]\n"
"ds_read_b64_tr_b16 v[208:209], v192 offset:16896\n"
"v_cvt_pk_bf16_f32 v138, v140, v141\n"
"v_mfma_f32_16x16x32_bf16 v[176:179], v[220:223], v[28:31], v[176:179]\n"
"ds_read_b64_tr_b16 v[210:211], v192 offset:25088\n"
"v_cvt_pk_bf16_f32 v139, v142, v143\n"
"v_mfma_f32_16x16x32_bf16 v[160:163], v[220:223], v[24:27], v[160:163]\n"
"ds_read_b64_tr_b16 v[212:213], v192 offset:17920\n"
"v_cvt_pk_bf16_f32 v154, v156, v157\n"
"v_mfma_f32_16x16x32_bf16 v[160:163], v[216:219], v[16:19], v[160:163]\n"
"s_waitcnt lgkmcnt(7)\n"
"ds_read_b64_tr_b16 v[214:215], v192 offset:26112\n"
"v_cvt_pk_bf16_f32 v155, v158, v159\n"
"v_mfma_f32_16x16x32_bf16 v[164:167], v[224:227], v[0:3], 0\n"
"buffer_store_dwordx4 v[136:139], v206, s[28:31], s74 offen offset:1024 sc1\n"
"v_mfma_f32_16x16x32_bf16 v[164:167], v[228:231], v[8:11], v[164:167]\n"
"s_mov_b32 s75, 20480\n"
"ds_read_b64_tr_b16 v[216:217], v192 offset:18944\n"
"ds_read_b64_tr_b16 v[218:219], v192 offset:27136\n"
"v_mfma_f32_16x16x32_bf16 v[180:183], v[228:231], v[12:15], 0\n"
"ds_read_b64_tr_b16 v[220:221], v192 offset:19968\n"
"v_mfma_f32_16x16x32_bf16 v[180:183], v[224:227], v[4:7], v[180:183]\n"
"s_waitcnt lgkmcnt(7)\n"
"ds_read_b64_tr_b16 v[222:223], v192 offset:28160\n"
"v_mfma_f32_16x16x32_bf16 v[180:183], v[232:235], v[20:23], v[180:183]\n"
"ds_read_b64_tr_b16 v[224:225], v193 offset:16896\n"
"v_mfma_f32_16x16x32_bf16 v[180:183], v[236:239], v[28:31], v[180:183]\n"
"buffer_store_dwordx4 v[152:155], v206, s[28:31], s74 offen offset:3072 sc1\n"
"v_mfma_f32_16x16x32_bf16 v[164:167], v[236:239], v[24:27], v[164:167]\n"
"ds_read_b64_tr_b16 v[226:227], v193 offset:25088\n"
"ds_read_b64_tr_b16 v[228:229], v193 offset:17920\n"
"v_mfma_f32_16x16x32_bf16 v[164:167], v[232:235], v[16:19], v[164:167]\n"
"s_waitcnt lgkmcnt(7)\n"
"ds_read_b64_tr_b16 v[230:231], v193 offset:26112\n"
"v_mfma_f32_16x16x32_bf16 v[168:171], v[208:211], v[0:3], 0\n"
"ds_read_b64_tr_b16 v[232:233], v193 offset:18944\n"
"v_mfma_f32_16x16x32_bf16 v[168:171], v[212:215], v[8:11], v[168:171]\n"
"ds_read_b64_tr_b16 v[234:235], v193 offset:27136\n"
"v_mfma_f32_16x16x32_bf16 v[184:187], v[212:215], v[12:15], 0\n"
"ds_read_b64_tr_b16 v[236:237], v193 offset:19968\n"
"v_mfma_f32_16x16x32_bf16 v[184:187], v[208:211], v[4:7], v[184:187]\n"
"s_waitcnt lgkmcnt(7)\n"
"ds_read_b64_tr_b16 v[238:239], v193 offset:28160\n"
"v_mfma_f32_16x16x32_bf16 v[184:187], v[216:219], v[20:23], v[184:187]\n"
"ds_read_b64_tr_b16 v[208:209], v196 offset:16384\n"
"v_mfma_f32_16x16x32_bf16 v[184:187], v[220:223], v[28:31], v[184:187]\n"
"ds_read_b64_tr_b16 v[210:211], v196 offset:24576\n"
"v_mfma_f32_16x16x32_bf16 v[168:171], v[220:223], v[24:27], v[168:171]\n"
"ds_read_b64_tr_b16 v[212:213], v196 offset:17408\n"
"v_mfma_f32_16x16x32_bf16 v[168:171], v[216:219], v[16:19], v[168:171]\n"
"s_waitcnt lgkmcnt(7)\n"
"ds_read_b64_tr_b16 v[214:215], v196 offset:25600\n"
"v_mfma_f32_16x16x32_bf16 v[172:175], v[224:227], v[0:3], 0\n"
"ds_read_b64_tr_b16 v[216:217], v196 offset:18432\n"
"v_mfma_f32_16x16x32_bf16 v[172:175], v[228:231], v[8:11], v[172:175]\n"
"ds_read_b64_tr_b16 v[218:219], v196 offset:26624\n"
"v_mfma_f32_16x16x32_bf16 v[188:191], v[228:231], v[12:15], 0\n"
"ds_read_b64_tr_b16 v[220:221], v196 offset:19456\n"
"v_mfma_f32_16x16x32_bf16 v[188:191], v[224:227], v[4:7], v[188:191]\n"
"s_waitcnt lgkmcnt(7)\n"
"ds_read_b64_tr_b16 v[222:223], v196 offset:27648\n"
"v_mfma_f32_16x16x32_bf16 v[188:191], v[232:235], v[20:23], v[188:191]\n"
"ds_read_b64_tr_b16 v[224:225], v197 offset:16384\n"
"v_mfma_f32_16x16x32_bf16 v[188:191], v[236:239], v[28:31], v[188:191]\n"
"ds_read_b64_tr_b16 v[226:227], v197 offset:24576\n"
"v_mfma_f32_16x16x32_bf16 v[172:175], v[236:239], v[24:27], v[172:175]\n"
"ds_read_b64_tr_b16 v[228:229], v197 offset:17408\n"
"v_mfma_f32_16x16x32_bf16 v[172:175], v[232:235], v[16:19], v[172:175]\n"
"s_waitcnt lgkmcnt(7)\n"
"ds_read_b64_tr_b16 v[230:231], v197 offset:25600\n"
"v_mfma_f32_16x16x32_bf16 v[160:163], v[208:211], v[32:35], v[160:163]\n"
"ds_read_b64_tr_b16 v[232:233], v197 offset:18432\n"
"v_mfma_f32_16x16x32_bf16 v[160:163], v[212:215], v[40:43], v[160:163]\n"
"ds_read_b64_tr_b16 v[234:235], v197 offset:26624\n"
"v_mfma_f32_16x16x32_bf16 v[176:179], v[212:215], v[44:47], v[176:179]\n"
"ds_read_b64_tr_b16 v[236:237], v197 offset:19456\n"
"v_mfma_f32_16x16x32_bf16 v[176:179], v[208:211], v[36:39], v[176:179]\n"
"s_waitcnt lgkmcnt(7)\n"
"ds_read_b64_tr_b16 v[238:239], v197 offset:27648\n"
"v_mfma_f32_16x16x32_bf16 v[176:179], v[216:219], v[52:55], v[176:179]\n"
"ds_read_b64_tr_b16 v[208:209], v196 offset:16896\n"
"v_mfma_f32_16x16x32_bf16 v[176:179], v[220:223], v[60:63], v[176:179]\n"
"ds_read_b64_tr_b16 v[210:211], v196 offset:25088\n"
"v_mfma_f32_16x16x32_bf16 v[160:163], v[220:223], v[56:59], v[160:163]\n"
"ds_read_b64_tr_b16 v[212:213], v196 offset:17920\n"
"v_mfma_f32_16x16x32_bf16 v[160:163], v[216:219], v[48:51], v[160:163]\n"
"s_waitcnt lgkmcnt(7)\n"
"ds_read_b64_tr_b16 v[214:215], v196 offset:26112\n"
"v_mfma_f32_16x16x32_bf16 v[164:167], v[224:227], v[32:35], v[164:167]\n"
"ds_read_b64_tr_b16 v[216:217], v196 offset:18944\n"
"v_mfma_f32_16x16x32_bf16 v[164:167], v[228:231], v[40:43], v[164:167]\n"
"ds_read_b64_tr_b16 v[218:219], v196 offset:27136\n"
"v_mfma_f32_16x16x32_bf16 v[180:183], v[228:231], v[44:47], v[180:183]\n"
"ds_read_b64_tr_b16 v[220:221], v196 offset:19968\n"
"v_mfma_f32_16x16x32_bf16 v[180:183], v[224:227], v[36:39], v[180:183]\n"
"s_waitcnt lgkmcnt(7)\n"
"ds_read_b64_tr_b16 v[222:223], v196 offset:28160\n"
"v_mfma_f32_16x16x32_bf16 v[180:183], v[232:235], v[52:55], v[180:183]\n"
"buffer_load_dword v240, v250, s[28:31], 0 offen offset:132 sc1\n"
"v_mfma_f32_16x16x32_bf16 v[180:183], v[236:239], v[60:63], v[180:183]\n"
"ds_read_b64_tr_b16 v[224:225], v197 offset:16896\n"
"ds_read_b64_tr_b16 v[226:227], v197 offset:25088\n"
"v_mfma_f32_16x16x32_bf16 v[164:167], v[236:239], v[56:59], v[164:167]\n"
"ds_read_b64_tr_b16 v[228:229], v197 offset:17920\n"
"v_mfma_f32_16x16x32_bf16 v[164:167], v[232:235], v[48:51], v[164:167]\n"
"s_waitcnt lgkmcnt(7)\n"
"ds_read_b64_tr_b16 v[230:231], v197 offset:26112\n"
"v_mfma_f32_16x16x32_bf16 v[168:171], v[208:211], v[32:35], v[168:171]\n"
"ds_read_b64_tr_b16 v[232:233], v197 offset:18944\n"
"v_mfma_f32_16x16x32_bf16 v[168:171], v[212:215], v[40:43], v[168:171]\n"
"ds_read_b64_tr_b16 v[234:235], v197 offset:27136\n"
"v_mfma_f32_16x16x32_bf16 v[184:187], v[212:215], v[44:47], v[184:187]\n"
"ds_read_b64_tr_b16 v[236:237], v197 offset:19968\n"
"v_mfma_f32_16x16x32_bf16 v[184:187], v[208:211], v[36:39], v[184:187]\n"
"s_waitcnt lgkmcnt(7)\n"
"ds_read_b64_tr_b16 v[238:239], v197 offset:28160\n"
"v_mfma_f32_16x16x32_bf16 v[184:187], v[216:219], v[52:55], v[184:187]\n"
"ds_read_b64_tr_b16 v[208:209], v192 offset:32768\n"
"v_mfma_f32_16x16x32_bf16 v[184:187], v[220:223], v[60:63], v[184:187]\n"
"ds_read_b64_tr_b16 v[210:211], v192 offset:40960\n"
"v_mfma_f32_16x16x32_bf16 v[168:171], v[220:223], v[56:59], v[168:171]\n"
"ds_read_b64_tr_b16 v[212:213], v192 offset:33792\n"
"v_mfma_f32_16x16x32_bf16 v[168:171], v[216:219], v[48:51], v[168:171]\n"
"s_waitcnt lgkmcnt(7)\n"
"ds_read_b64_tr_b16 v[214:215], v192 offset:41984\n"
"v_mfma_f32_16x16x32_bf16 v[172:175], v[224:227], v[32:35], v[172:175]\n"
"ds_read_b64_tr_b16 v[216:217], v192 offset:34816\n"
"v_mfma_f32_16x16x32_bf16 v[172:175], v[228:231], v[40:43], v[172:175]\n"
"ds_read_b64_tr_b16 v[218:219], v192 offset:43008\n"
"v_mfma_f32_16x16x32_bf16 v[188:191], v[228:231], v[44:47], v[188:191]\n"
"ds_read_b64_tr_b16 v[220:221], v192 offset:35840\n"
"v_mfma_f32_16x16x32_bf16 v[188:191], v[224:227], v[36:39], v[188:191]\n"
"s_waitcnt lgkmcnt(7)\n"
"ds_read_b64_tr_b16 v[222:223], v192 offset:44032\n"
"v_mfma_f32_16x16x32_bf16 v[188:191], v[232:235], v[52:55], v[188:191]\n"
"ds_read_b64_tr_b16 v[224:225], v193 offset:32768\n"
"v_mfma_f32_16x16x32_bf16 v[188:191], v[236:239], v[60:63], v[188:191]\n"
"ds_read_b64_tr_b16 v[226:227], v193 offset:40960\n"
"v_mfma_f32_16x16x32_bf16 v[172:175], v[236:239], v[56:59], v[172:175]\n"
"s_waitcnt vmcnt(24)\n"
"s_barrier\n"
"v_mfma_f32_16x16x32_bf16 v[172:175], v[232:235], v[48:51], v[172:175]\n"
"s_waitcnt lgkmcnt(6)\n"
"ds_read_b64_tr_b16 v[228:229], v193 offset:33792\n"
"ds_read_b64_tr_b16 v[230:231], v193 offset:41984\n"
"v_mfma_f32_16x16x32_bf16 v[160:163], v[208:211], v[64:67], v[160:163]\n"
"ds_read_b64_tr_b16 v[232:233], v193 offset:34816\n"
"v_mfma_f32_16x16x32_bf16 v[160:163], v[212:215], v[72:75], v[160:163]\n"
"ds_read_b64_tr_b16 v[234:235], v193 offset:43008\n"
"v_mfma_f32_16x16x32_bf16 v[176:179], v[212:215], v[76:79], v[176:179]\n"
"ds_read_b64_tr_b16 v[236:237], v193 offset:35840\n"
"v_mfma_f32_16x16x32_bf16 v[176:179], v[208:211], v[68:71], v[176:179]\n"
"s_waitcnt lgkmcnt(7)\n"
"ds_read_b64_tr_b16 v[238:239], v193 offset:44032\n"
"v_mfma_f32_16x16x32_bf16 v[176:179], v[216:219], v[84:87], v[176:179]\n"
"ds_read_b64_tr_b16 v[208:209], v192 offset:33280\n"
"v_mfma_f32_16x16x32_bf16 v[176:179], v[220:223], v[92:95], v[176:179]\n"
"ds_read_b64_tr_b16 v[210:211], v192 offset:41472\n"
"v_mfma_f32_16x16x32_bf16 v[160:163], v[220:223], v[88:91], v[160:163]\n"
"ds_read_b64_tr_b16 v[212:213], v192 offset:34304\n"
"v_mfma_f32_16x16x32_bf16 v[160:163], v[216:219], v[80:83], v[160:163]\n"
"s_waitcnt lgkmcnt(7)\n"
"ds_read_b64_tr_b16 v[214:215], v192 offset:42496\n"
"v_mfma_f32_16x16x32_bf16 v[164:167], v[224:227], v[64:67], v[164:167]\n"
"ds_read_b64_tr_b16 v[216:217], v192 offset:35328\n"
"v_mfma_f32_16x16x32_bf16 v[164:167], v[228:231], v[72:75], v[164:167]\n"
"ds_read_b64_tr_b16 v[218:219], v192 offset:43520\n"
"v_mfma_f32_16x16x32_bf16 v[180:183], v[228:231], v[76:79], v[180:183]\n"
"ds_read_b64_tr_b16 v[220:221], v192 offset:36352\n"
"v_mfma_f32_16x16x32_bf16 v[180:183], v[224:227], v[68:71], v[180:183]\n"
"s_waitcnt lgkmcnt(7)\n"
"ds_read_b64_tr_b16 v[222:223], v192 offset:44544\n"
"v_mfma_f32_16x16x32_bf16 v[180:183], v[232:235], v[84:87], v[180:183]\n"
"ds_read_b64_tr_b16 v[224:225], v193 offset:33280\n"
"v_mfma_f32_16x16x32_bf16 v[180:183], v[236:239], v[92:95], v[180:183]\n"
"ds_read_b64_tr_b16 v[226:227], v193 offset:41472\n"
"v_mfma_f32_16x16x32_bf16 v[164:167], v[236:239], v[88:91], v[164:167]\n"
"ds_read_b64_tr_b16 v[228:229], v193 offset:34304\n"
"v_mfma_f32_16x16x32_bf16 v[164:167], v[232:235], v[80:83], v[164:167]\n"
"s_waitcnt lgkmcnt(7)\n"
"ds_read_b64_tr_b16 v[230:231], v193 offset:42496\n"
"v_mfma_f32_16x16x32_bf16 v[168:171], v[208:211], v[64:67], v[168:171]\n"
"ds_read_b64_tr_b16 v[232:233], v193 offset:35328\n"
"v_mfma_f32_16x16x32_bf16 v[168:171], v[212:215], v[72:75], v[168:171]\n"
"ds_read_b64_tr_b16 v[234:235], v193 offset:43520\n"
"v_mfma_f32_16x16x32_bf16 v[184:187], v[212:215], v[76:79], v[184:187]\n"
"ds_read_b64_tr_b16 v[236:237], v193 offset:36352\n"
"v_mfma_f32_16x16x32_bf16 v[184:187], v[208:211], v[68:71], v[184:187]\n"
"s_waitcnt lgkmcnt(7)\n"
"ds_read_b64_tr_b16 v[238:239], v193 offset:44544\n"
"v_mfma_f32_16x16x32_bf16 v[184:187], v[216:219], v[84:87], v[184:187]\n"
"ds_read_b64_tr_b16 v[208:209], v194 offset:32768\n"
"v_mfma_f32_16x16x32_bf16 v[184:187], v[220:223], v[92:95], v[184:187]\n"
"ds_read_b64_tr_b16 v[210:211], v194 offset:40960\n"
"v_mfma_f32_16x16x32_bf16 v[168:171], v[220:223], v[88:91], v[168:171]\n"
"ds_read_b64_tr_b16 v[212:213], v194 offset:33792\n"
"v_mfma_f32_16x16x32_bf16 v[168:171], v[216:219], v[80:83], v[168:171]\n"
"s_waitcnt lgkmcnt(7)\n"
"ds_read_b64_tr_b16 v[214:215], v194 offset:41984\n"
"v_mfma_f32_16x16x32_bf16 v[172:175], v[224:227], v[64:67], v[172:175]\n"
"ds_read_b64_tr_b16 v[216:217], v194 offset:34816\n"
"v_mfma_f32_16x16x32_bf16 v[172:175], v[228:231], v[72:75], v[172:175]\n"
"ds_read_b64_tr_b16 v[218:219], v194 offset:43008\n"
"v_mfma_f32_16x16x32_bf16 v[188:191], v[228:231], v[76:79], v[188:191]\n"
"ds_read_b64_tr_b16 v[220:221], v194 offset:35840\n"
"v_mfma_f32_16x16x32_bf16 v[188:191], v[224:227], v[68:71], v[188:191]\n"
"s_waitcnt lgkmcnt(7)\n"
"ds_read_b64_tr_b16 v[222:223], v194 offset:44032\n"
"v_mfma_f32_16x16x32_bf16 v[188:191], v[232:235], v[84:87], v[188:191]\n"
"ds_read_b64_tr_b16 v[224:225], v195 offset:32768\n"
"v_mfma_f32_16x16x32_bf16 v[188:191], v[236:239], v[92:95], v[188:191]\n"
"ds_read_b64_tr_b16 v[226:227], v195 offset:40960\n"
"v_mfma_f32_16x16x32_bf16 v[172:175], v[236:239], v[88:91], v[172:175]\n"
"s_waitcnt vmcnt(0)\n"
"ds_read_b64_tr_b16 v[228:229], v195 offset:33792\n"
"v_mfma_f32_16x16x32_bf16 v[172:175], v[232:235], v[80:83], v[172:175]\n"
"s_waitcnt lgkmcnt(7)\n"
"ds_read_b64_tr_b16 v[230:231], v195 offset:41984\n"
"v_cmp_ne_u32 vcc, v240, v245\n"
"v_mfma_f32_16x16x32_bf16 v[160:163], v[208:211], v[96:99], v[160:163]\n"
"IWB6_%=:\n"
"s_cbranch_execz IWB10_%=\n"
"ds_read_b64_tr_b16 v[232:233], v195 offset:34816\n"
"v_mfma_f32_16x16x32_bf16 v[160:163], v[212:215], v[104:107], v[160:163]\n"
"ds_read_b64_tr_b16 v[234:235], v195 offset:43008\n"
"v_mfma_f32_16x16x32_bf16 v[176:179], v[212:215], v[108:111], v[176:179]\n"
"s_cbranch_vccnz SP1_%=\n"
"SR1_%=:\n"
"v_mfma_f32_16x16x32_bf16 v[176:179], v[208:211], v[100:103], v[176:179]\n"
"s_waitcnt lgkmcnt(6)\n"
"ds_read_b64_tr_b16 v[236:237], v195 offset:35840\n"
"ds_read_b64_tr_b16 v[238:239], v195 offset:44032\n"
"v_mfma_f32_16x16x32_bf16 v[176:179], v[216:219], v[116:119], v[176:179]\n"
"s_mov_b32 s66, 8192\n"
"ds_read_b64_tr_b16 v[208:209], v194 offset:33280\n"
"v_mfma_f32_16x16x32_bf16 v[176:179], v[220:223], v[124:127], v[176:179]\n"
"buffer_load_dwordx4 a[32:35], v248, s[28:31], s66 offen sc1\n"
"v_mfma_f32_16x16x32_bf16 v[160:163], v[220:223], v[120:123], v[160:163]\n"
"ds_read_b64_tr_b16 v[210:211], v194 offset:41472\n"
"ds_read_b64_tr_b16 v[212:213], v194 offset:34304\n"
"v_mfma_f32_16x16x32_bf16 v[160:163], v[216:219], v[112:115], v[160:163]\n"
"s_waitcnt lgkmcnt(7)\n"
"ds_read_b64_tr_b16 v[214:215], v194 offset:42496\n"
"v_mfma_f32_16x16x32_bf16 v[164:167], v[224:227], v[96:99], v[164:167]\n"
"ds_read_b64_tr_b16 v[216:217], v194 offset:35328\n"
"v_mfma_f32_16x16x32_bf16 v[164:167], v[228:231], v[104:107], v[164:167]\n"
"ds_read_b64_tr_b16 v[218:219], v194 offset:43520\n"
"v_mfma_f32_16x16x32_bf16 v[180:183], v[228:231], v[108:111], v[180:183]\n"
"ds_read_b64_tr_b16 v[220:221], v194 offset:36352\n"
"v_mfma_f32_16x16x32_bf16 v[180:183], v[224:227], v[100:103], v[180:183]\n"
"s_waitcnt lgkmcnt(7)\n"
"ds_read_b64_tr_b16 v[222:223], v194 offset:44544\n"
"v_mfma_f32_16x16x32_bf16 v[180:183], v[232:235], v[116:119], v[180:183]\n"
"s_mov_b32 s66, 122880\n"
"ds_read_b64_tr_b16 v[224:225], v195 offset:33280\n"
"v_mfma_f32_16x16x32_bf16 v[180:183], v[236:239], v[124:127], v[180:183]\n"
"buffer_load_dwordx4 a[36:39], v248, s[28:31], s66 offen sc1\n"
"v_mfma_f32_16x16x32_bf16 v[164:167], v[236:239], v[120:123], v[164:167]\n"
"ds_read_b64_tr_b16 v[226:227], v195 offset:41472\n"
"ds_read_b64_tr_b16 v[228:229], v195 offset:34304\n"
"v_mfma_f32_16x16x32_bf16 v[164:167], v[232:235], v[112:115], v[164:167]\n"
"s_waitcnt lgkmcnt(7)\n"
"ds_read_b64_tr_b16 v[230:231], v195 offset:42496\n"
"v_mfma_f32_16x16x32_bf16 v[168:171], v[208:211], v[96:99], v[168:171]\n"
"ds_read_b64_tr_b16 v[232:233], v195 offset:35328\n"
"v_cvt_pk_bf16_f32 v176, v176, v177\n"
"v_mfma_f32_16x16x32_bf16 v[168:171], v[212:215], v[104:107], v[168:171]\n"
"ds_read_b64_tr_b16 v[234:235], v195 offset:43520\n"
"v_cvt_pk_bf16_f32 v177, v178, v179\n"
"v_mfma_f32_16x16x32_bf16 v[184:187], v[212:215], v[108:111], v[184:187]\n"
"ds_read_b64_tr_b16 v[236:237], v195 offset:36352\n"
"v_cvt_pk_bf16_f32 v160, v160, v161\n"
"v_mfma_f32_16x16x32_bf16 v[184:187], v[208:211], v[100:103], v[184:187]\n"
"s_waitcnt lgkmcnt(7)\n"
"ds_read_b64_tr_b16 v[238:239], v195 offset:44544\n"
"v_cvt_pk_bf16_f32 v161, v162, v163\n"
"v_mfma_f32_16x16x32_bf16 v[184:187], v[216:219], v[116:119], v[184:187]\n"
"s_mov_b32 s66, 237568\n"
"ds_read_b64_tr_b16 v[208:209], v192 offset:49152\n"
"v_mfma_f32_16x16x32_bf16 v[184:187], v[220:223], v[124:127], v[184:187]\n"
"buffer_load_dwordx4 a[40:43], v248, s[28:31], s66 offen sc1\n"
"v_mfma_f32_16x16x32_bf16 v[168:171], v[220:223], v[120:123], v[168:171]\n"
"ds_read_b64_tr_b16 v[210:211], v192 offset:57344\n"
"ds_read_b64_tr_b16 v[212:213], v192 offset:50176\n"
"v_mfma_f32_16x16x32_bf16 v[168:171], v[216:219], v[112:115], v[168:171]\n"
"s_waitcnt lgkmcnt(7)\n"
"ds_read_b64_tr_b16 v[214:215], v192 offset:58368\n"
"v_cvt_pk_bf16_f32 v162, v164, v165\n"
"v_cvt_pk_bf16_f32 v163, v166, v167\n"
"v_mfma_f32_16x16x32_bf16 v[172:175], v[224:227], v[96:99], v[172:175]\n"
"buffer_store_dwordx4 v[160:163], v206, s[28:31], s75 offen offset:0 sc1\n"
"v_mfma_f32_16x16x32_bf16 v[172:175], v[228:231], v[104:107], v[172:175]\n"
"ds_read_b64_tr_b16 v[216:217], v192 offset:51200\n"
"ds_read_b64_tr_b16 v[218:219], v192 offset:59392\n"
"v_mfma_f32_16x16x32_bf16 v[188:191], v[228:231], v[108:111], v[188:191]\n"
"ds_read_b64_tr_b16 v[220:221], v192 offset:52224\n"
"v_cvt_pk_bf16_f32 v178, v180, v181\n"
"v_mfma_f32_16x16x32_bf16 v[188:191], v[224:227], v[100:103], v[188:191]\n"
"s_waitcnt lgkmcnt(7)\n"
"ds_read_b64_tr_b16 v[222:223], v192 offset:60416\n"
"v_cvt_pk_bf16_f32 v179, v182, v183\n"
"v_mfma_f32_16x16x32_bf16 v[188:191], v[232:235], v[116:119], v[188:191]\n"
"buffer_store_dwordx4 v[176:179], v206, s[28:31], s75 offen offset:2048 sc1\n"
"v_mfma_f32_16x16x32_bf16 v[188:191], v[236:239], v[124:127], v[188:191]\n"
"ds_read_b64_tr_b16 v[224:225], v193 offset:49152\n"
"ds_read_b64_tr_b16 v[226:227], v193 offset:57344\n"
"v_mfma_f32_16x16x32_bf16 v[172:175], v[236:239], v[120:123], v[172:175]\n"
"s_barrier\n"
"v_mfma_f32_16x16x32_bf16 v[172:175], v[232:235], v[112:115], v[172:175]\n"
"s_waitcnt lgkmcnt(6)\n"
"ds_read_b64_tr_b16 v[228:229], v193 offset:50176\n"
"ds_read_b64_tr_b16 v[230:231], v193 offset:58368\n"
"v_mfma_f32_16x16x32_bf16 v[128:131], v[208:211], v[0:3], 0\n"
"s_mov_b32 s66, 352256\n"
"ds_read_b64_tr_b16 v[232:233], v193 offset:51200\n"
"v_mfma_f32_16x16x32_bf16 v[128:131], v[212:215], v[8:11], v[128:131]\n"
"buffer_load_dwordx4 a[44:47], v248, s[28:31], s66 offen sc1\n"
"v_mfma_f32_16x16x32_bf16 v[144:147], v[212:215], v[12:15], 0\n"
"ds_read_b64_tr_b16 v[234:235], v193 offset:59392\n"
"ds_read_b64_tr_b16 v[236:237], v193 offset:52224\n"
"v_mfma_f32_16x16x32_bf16 v[144:147], v[208:211], v[4:7], v[144:147]\n"
"s_waitcnt lgkmcnt(7)\n"
"ds_read_b64_tr_b16 v[238:239], v193 offset:60416\n"
"v_cvt_pk_bf16_f32 v168, v168, v169\n"
"v_mfma_f32_16x16x32_bf16 v[144:147], v[216:219], v[20:23], v[144:147]\n"
"ds_read_b64_tr_b16 v[208:209], v192 offset:49664\n"
"v_cvt_pk_bf16_f32 v169, v170, v171\n"
"v_mfma_f32_16x16x32_bf16 v[144:147], v[220:223], v[28:31], v[144:147]\n"
"ds_read_b64_tr_b16 v[210:211], v192 offset:57856\n"
"v_cvt_pk_bf16_f32 v170, v172, v173\n"
"v_mfma_f32_16x16x32_bf16 v[128:131], v[220:223], v[24:27], v[128:131]\n"
"ds_read_b64_tr_b16 v[212:213], v192 offset:50688\n"
"v_cvt_pk_bf16_f32 v171, v174, v175\n"
"v_mfma_f32_16x16x32_bf16 v[128:131], v[216:219], v[16:19], v[128:131]\n"
"s_waitcnt lgkmcnt(7)\n"
"ds_read_b64_tr_b16 v[214:215], v192 offset:58880\n"
"v_cvt_pk_bf16_f32 v184, v184, v185\n"
"v_mfma_f32_16x16x32_bf16 v[132:135], v[224:227], v[0:3], 0\n"
"buffer_store_dwordx4 v[168:171], v206, s[28:31], s75 offen offset:1024 sc1\n"
"v_mfma_f32_16x16x32_bf16 v[132:135], v[228:231], v[8:11], v[132:135]\n"
"s_mov_b32 s74, 24576\n"
"ds_read_b64_tr_b16 v[216:217], v192 offset:51712\n"
"ds_read_b64_tr_b16 v[218:219], v192 offset:59904\n"
"v_mfma_f32_16x16x32_bf16 v[148:151], v[228:231], v[12:15], 0\n"
"s_mov_b32 s66, 466944\n"
"ds_read_b64_tr_b16 v[220:221], v192 offset:52736\n"
"v_mfma_f32_16x16x32_bf16 v[148:151], v[224:227], v[4:7], v[148:151]\n"
"s_waitcnt lgkmcnt(7)\n"
"buffer_load_dwordx4 a[48:51], v248, s[28:31], s66 offen sc1\n"
"v_mfma_f32_16x16x32_bf16 v[148:151], v[232:235], v[20:23], v[148:151]\n"
"ds_read_b64_tr_b16 v[222:223], v192 offset:60928\n"
"ds_read_b64_tr_b16 v[224:225], v193 offset:49664\n"
"v_cvt_pk_bf16_f32 v185, v186, v187\n"
"v_cvt_pk_bf16_f32 v186, v188, v189\n"
"v_cvt_pk_bf16_f32 v187, v190, v191\n"
"v_mfma_f32_16x16x32_bf16 v[148:151], v[236:239], v[28:31], v[148:151]\n"
"buffer_store_dwordx4 v[184:187], v206, s[28:31], s75 offen offset:3072 sc1\n"
"v_mfma_f32_16x16x32_bf16 v[132:135], v[236:239], v[24:27], v[132:135]\n"
"ds_read_b64_tr_b16 v[226:227], v193 offset:57856\n"
"ds_read_b64_tr_b16 v[228:229], v193 offset:50688\n"
"v_mfma_f32_16x16x32_bf16 v[132:135], v[232:235], v[16:19], v[132:135]\n"
"s_waitcnt lgkmcnt(7)\n"
"ds_read_b64_tr_b16 v[230:231], v193 offset:58880\n"
"v_mfma_f32_16x16x32_bf16 v[136:139], v[208:211], v[0:3], 0\n"
"ds_read_b64_tr_b16 v[232:233], v193 offset:51712\n"
"v_mfma_f32_16x16x32_bf16 v[136:139], v[212:215], v[8:11], v[136:139]\n"
"ds_read_b64_tr_b16 v[234:235], v193 offset:59904\n"
"v_mfma_f32_16x16x32_bf16 v[152:155], v[212:215], v[12:15], 0\n"
"s_mov_b32 s66, 581632\n"
"ds_read_b64_tr_b16 v[236:237], v193 offset:52736\n"
"v_mfma_f32_16x16x32_bf16 v[152:155], v[208:211], v[4:7], v[152:155]\n"
"s_waitcnt lgkmcnt(7)\n"
"buffer_load_dwordx4 a[52:55], v248, s[28:31], s66 offen sc1\n"
"v_mfma_f32_16x16x32_bf16 v[152:155], v[216:219], v[20:23], v[152:155]\n"
"ds_read_b64_tr_b16 v[238:239], v193 offset:60928\n"
"ds_read_b64_tr_b16 v[208:209], v194 offset:0\n"
"v_mfma_f32_16x16x32_bf16 v[152:155], v[220:223], v[28:31], v[152:155]\n"
"ds_read_b64_tr_b16 v[210:211], v194 offset:8192\n"
"v_mfma_f32_16x16x32_bf16 v[136:139], v[220:223], v[24:27], v[136:139]\n"
"ds_read_b64_tr_b16 v[212:213], v194 offset:1024\n"
"v_mfma_f32_16x16x32_bf16 v[136:139], v[216:219], v[16:19], v[136:139]\n"
"s_waitcnt lgkmcnt(7)\n"
"ds_read_b64_tr_b16 v[214:215], v194 offset:9216\n"
"v_mfma_f32_16x16x32_bf16 v[140:143], v[224:227], v[0:3], 0\n"
"ds_read_b64_tr_b16 v[216:217], v194 offset:2048\n"
"v_mfma_f32_16x16x32_bf16 v[140:143], v[228:231], v[8:11], v[140:143]\n"
"ds_read_b64_tr_b16 v[218:219], v194 offset:10240\n"
"v_mfma_f32_16x16x32_bf16 v[156:159], v[228:231], v[12:15], 0\n"
"ds_read_b64_tr_b16 v[220:221], v194 offset:3072\n"
"v_mfma_f32_16x16x32_bf16 v[156:159], v[224:227], v[4:7], v[156:159]\n"
"s_waitcnt lgkmcnt(7)\n"
"ds_read_b64_tr_b16 v[222:223], v194 offset:11264\n"
"v_mfma_f32_16x16x32_bf16 v[156:159], v[232:235], v[20:23], v[156:159]\n"
"ds_read_b64_tr_b16 v[224:225], v195 offset:0\n"
"v_mfma_f32_16x16x32_bf16 v[156:159], v[236:239], v[28:31], v[156:159]\n"
"ds_read_b64_tr_b16 v[226:227], v195 offset:8192\n"
"v_mfma_f32_16x16x32_bf16 v[140:143], v[236:239], v[24:27], v[140:143]\n"
"ds_read_b64_tr_b16 v[228:229], v195 offset:1024\n"
"v_mfma_f32_16x16x32_bf16 v[140:143], v[232:235], v[16:19], v[140:143]\n"
"s_waitcnt lgkmcnt(7)\n"
"ds_read_b64_tr_b16 v[230:231], v195 offset:9216\n"
"v_mfma_f32_16x16x32_bf16 v[128:131], v[208:211], v[32:35], v[128:131]\n"
"s_mov_b32 s66, 696320\n"
"ds_read_b64_tr_b16 v[232:233], v195 offset:2048\n"
"v_mfma_f32_16x16x32_bf16 v[128:131], v[212:215], v[40:43], v[128:131]\n"
"buffer_load_dwordx4 a[56:59], v248, s[28:31], s66 offen sc1\n"
"v_mfma_f32_16x16x32_bf16 v[144:147], v[212:215], v[44:47], v[144:147]\n"
"ds_read_b64_tr_b16 v[234:235], v195 offset:10240\n"
"ds_read_b64_tr_b16 v[236:237], v195 offset:3072\n"
"v_mfma_f32_16x16x32_bf16 v[144:147], v[208:211], v[36:39], v[144:147]\n"
"s_waitcnt lgkmcnt(7)\n"
"ds_read_b64_tr_b16 v[238:239], v195 offset:11264\n"
"v_mfma_f32_16x16x32_bf16 v[144:147], v[216:219], v[52:55], v[144:147]\n"
"ds_read_b64_tr_b16 v[208:209], v194 offset:512\n"
"v_mfma_f32_16x16x32_bf16 v[144:147], v[220:223], v[60:63], v[144:147]\n"
"ds_read_b64_tr_b16 v[210:211], v194 offset:8704\n"
"v_mfma_f32_16x16x32_bf16 v[128:131], v[220:223], v[56:59], v[128:131]\n"
"ds_read_b64_tr_b16 v[212:213], v194 offset:1536\n"
"v_mfma_f32_16x16x32_bf16 v[128:131], v[216:219], v[48:51], v[128:131]\n"
"s_waitcnt lgkmcnt(7)\n"
"ds_read_b64_tr_b16 v[214:215], v194 offset:9728\n"
"v_mfma_f32_16x16x32_bf16 v[132:135], v[224:227], v[32:35], v[132:135]\n"
"ds_read_b64_tr_b16 v[216:217], v194 offset:2560\n"
"v_mfma_f32_16x16x32_bf16 v[132:135], v[228:231], v[40:43], v[132:135]\n"
"ds_read_b64_tr_b16 v[218:219], v194 offset:10752\n"
"v_mfma_f32_16x16x32_bf16 v[148:151], v[228:231], v[44:47], v[148:151]\n"
"ds_read_b64_tr_b16 v[220:221], v194 offset:3584\n"
"v_mfma_f32_16x16x32_bf16 v[148:151], v[224:227], v[36:39], v[148:151]\n"
"s_waitcnt lgkmcnt(7)\n"
"ds_read_b64_tr_b16 v[222:223], v194 offset:11776\n"
"v_mfma_f32_16x16x32_bf16 v[148:151], v[232:235], v[52:55], v[148:151]\n"
"ds_read_b64_tr_b16 v[224:225], v195 offset:512\n"
"v_mfma_f32_16x16x32_bf16 v[148:151], v[236:239], v[60:63], v[148:151]\n"
"ds_read_b64_tr_b16 v[226:227], v195 offset:8704\n"
"v_mfma_f32_16x16x32_bf16 v[132:135], v[236:239], v[56:59], v[132:135]\n"
"s_mov_b32 s66, 811008\n"
"ds_read_b64_tr_b16 v[228:229], v195 offset:1536\n"
"v_mfma_f32_16x16x32_bf16 v[132:135], v[232:235], v[48:51], v[132:135]\n"
"s_waitcnt lgkmcnt(7)\n"
"buffer_load_dwordx4 a[60:63], v248, s[28:31], s66 offen sc1\n"
"v_mfma_f32_16x16x32_bf16 v[136:139], v[208:211], v[32:35], v[136:139]\n"
"ds_read_b64_tr_b16 v[230:231], v195 offset:9728\n"
"ds_read_b64_tr_b16 v[232:233], v195 offset:2560\n"
"v_mfma_f32_16x16x32_bf16 v[136:139], v[212:215], v[40:43], v[136:139]\n"
"ds_read_b64_tr_b16 v[234:235], v195 offset:10752\n"
"v_mfma_f32_16x16x32_bf16 v[152:155], v[212:215], v[44:47], v[152:155]\n"
"ds_read_b64_tr_b16 v[236:237], v195 offset:3584\n"
"v_mfma_f32_16x16x32_bf16 v[152:155], v[208:211], v[36:39], v[152:155]\n"
"s_waitcnt lgkmcnt(7)\n"
"ds_read_b64_tr_b16 v[238:239], v195 offset:11776\n"
"v_mfma_f32_16x16x32_bf16 v[152:155], v[216:219], v[52:55], v[152:155]\n"
"ds_read_b64_tr_b16 v[208:209], v194 offset:16384\n"
"v_mfma_f32_16x16x32_bf16 v[152:155], v[220:223], v[60:63], v[152:155]\n"
"ds_read_b64_tr_b16 v[210:211], v194 offset:24576\n"
"v_mfma_f32_16x16x32_bf16 v[136:139], v[220:223], v[56:59], v[136:139]\n"
"ds_read_b64_tr_b16 v[212:213], v194 offset:17408\n"
"v_mfma_f32_16x16x32_bf16 v[136:139], v[216:219], v[48:51], v[136:139]\n"
"s_waitcnt lgkmcnt(7)\n"
"ds_read_b64_tr_b16 v[214:215], v194 offset:25600\n"
"v_mfma_f32_16x16x32_bf16 v[140:143], v[224:227], v[32:35], v[140:143]\n"
"ds_read_b64_tr_b16 v[216:217], v194 offset:18432\n"
"v_mfma_f32_16x16x32_bf16 v[140:143], v[228:231], v[40:43], v[140:143]\n"
"ds_read_b64_tr_b16 v[218:219], v194 offset:26624\n"
"v_mfma_f32_16x16x32_bf16 v[156:159], v[228:231], v[44:47], v[156:159]\n"
"ds_read_b64_tr_b16 v[220:221], v194 offset:19456\n"
"v_mfma_f32_16x16x32_bf16 v[156:159], v[224:227], v[36:39], v[156:159]\n"
"s_waitcnt lgkmcnt(7)\n"
"ds_read_b64_tr_b16 v[222:223], v194 offset:27648\n"
"v_mfma_f32_16x16x32_bf16 v[156:159], v[232:235], v[52:55], v[156:159]\n"
"ds_read_b64_tr_b16 v[224:225], v195 offset:16384\n"
"v_mfma_f32_16x16x32_bf16 v[156:159], v[236:239], v[60:63], v[156:159]\n"
"ds_read_b64_tr_b16 v[226:227], v195 offset:24576\n"
"v_mfma_f32_16x16x32_bf16 v[140:143], v[236:239], v[56:59], v[140:143]\n"
"s_waitcnt vmcnt(3)\n"
"s_barrier\n"
"v_mfma_f32_16x16x32_bf16 v[140:143], v[232:235], v[48:51], v[140:143]\n"
"s_waitcnt lgkmcnt(6)\n"
"ds_read_b64_tr_b16 v[228:229], v195 offset:17408\n"
"ds_read_b64_tr_b16 v[230:231], v195 offset:25600\n"
"v_mfma_f32_16x16x32_bf16 v[128:131], v[208:211], v[64:67], v[128:131]\n"
"buffer_store_dwordx3 v[252:254], v251, s[28:31], 0 offen offset:128 sc1\n"
"v_mfma_f32_16x16x32_bf16 v[128:131], v[212:215], v[72:75], v[128:131]\n"
"ds_read_b64_tr_b16 v[232:233], v195 offset:18432\n"
"ds_read_b64_tr_b16 v[234:235], v195 offset:26624\n"
"v_mfma_f32_16x16x32_bf16 v[144:147], v[212:215], v[76:79], v[144:147]\n"
"ds_read_b64_tr_b16 v[236:237], v195 offset:19456\n"
"v_mfma_f32_16x16x32_bf16 v[144:147], v[208:211], v[68:71], v[144:147]\n"
"s_waitcnt lgkmcnt(7)\n"
"ds_read_b64_tr_b16 v[238:239], v195 offset:27648\n"
"v_mfma_f32_16x16x32_bf16 v[144:147], v[216:219], v[84:87], v[144:147]\n"
"ds_read_b64_tr_b16 v[208:209], v194 offset:16896\n"
"v_mfma_f32_16x16x32_bf16 v[144:147], v[220:223], v[92:95], v[144:147]\n"
"ds_read_b64_tr_b16 v[210:211], v194 offset:25088\n"
"v_mfma_f32_16x16x32_bf16 v[128:131], v[220:223], v[88:91], v[128:131]\n"
"ds_read_b64_tr_b16 v[212:213], v194 offset:17920\n"
"v_mfma_f32_16x16x32_bf16 v[128:131], v[216:219], v[80:83], v[128:131]\n"
"s_waitcnt lgkmcnt(7)\n"
"ds_read_b64_tr_b16 v[214:215], v194 offset:26112\n"
"v_mfma_f32_16x16x32_bf16 v[132:135], v[224:227], v[64:67], v[132:135]\n"
"ds_read_b64_tr_b16 v[216:217], v194 offset:18944\n"
"v_mfma_f32_16x16x32_bf16 v[132:135], v[228:231], v[72:75], v[132:135]\n"
"ds_read_b64_tr_b16 v[218:219], v194 offset:27136\n"
"v_mfma_f32_16x16x32_bf16 v[148:151], v[228:231], v[76:79], v[148:151]\n"
"ds_read_b64_tr_b16 v[220:221], v194 offset:19968\n"
"v_mfma_f32_16x16x32_bf16 v[148:151], v[224:227], v[68:71], v[148:151]\n"
"s_waitcnt lgkmcnt(7)\n"
"ds_read_b64_tr_b16 v[222:223], v194 offset:28160\n"
"v_mfma_f32_16x16x32_bf16 v[148:151], v[232:235], v[84:87], v[148:151]\n"
"ds_read_b64_tr_b16 v[224:225], v195 offset:16896\n"
"v_mfma_f32_16x16x32_bf16 v[148:151], v[236:239], v[92:95], v[148:151]\n"
"ds_read_b64_tr_b16 v[226:227], v195 offset:25088\n"
"v_mfma_f32_16x16x32_bf16 v[132:135], v[236:239], v[88:91], v[132:135]\n"
"ds_read_b64_tr_b16 v[228:229], v195 offset:17920\n"
"v_mfma_f32_16x16x32_bf16 v[132:135], v[232:235], v[80:83], v[132:135]\n"
"s_waitcnt lgkmcnt(7)\n"
"ds_read_b64_tr_b16 v[230:231], v195 offset:26112\n"
"v_mfma_f32_16x16x32_bf16 v[136:139], v[208:211], v[64:67], v[136:139]\n"
"ds_read_b64_tr_b16 v[232:233], v195 offset:18944\n"
"v_mfma_f32_16x16x32_bf16 v[136:139], v[212:215], v[72:75], v[136:139]\n"
"ds_read_b64_tr_b16 v[234:235], v195 offset:27136\n"
"v_mfma_f32_16x16x32_bf16 v[152:155], v[212:215], v[76:79], v[152:155]\n"
"ds_read_b64_tr_b16 v[236:237], v195 offset:19968\n"
"v_mfma_f32_16x16x32_bf16 v[152:155], v[208:211], v[68:71], v[152:155]\n"
"s_waitcnt lgkmcnt(7)\n"
"ds_read_b64_tr_b16 v[238:239], v195 offset:28160\n"
"v_mfma_f32_16x16x32_bf16 v[152:155], v[216:219], v[84:87], v[152:155]\n"
"ds_read_b64_tr_b16 v[208:209], v194 offset:49152\n"
"v_mfma_f32_16x16x32_bf16 v[152:155], v[220:223], v[92:95], v[152:155]\n"
"ds_read_b64_tr_b16 v[210:211], v194 offset:57344\n"
"v_mfma_f32_16x16x32_bf16 v[136:139], v[220:223], v[88:91], v[136:139]\n"
"ds_read_b64_tr_b16 v[212:213], v194 offset:50176\n"
"v_mfma_f32_16x16x32_bf16 v[136:139], v[216:219], v[80:83], v[136:139]\n"
"s_waitcnt lgkmcnt(7)\n"
"ds_read_b64_tr_b16 v[214:215], v194 offset:58368\n"
"v_mfma_f32_16x16x32_bf16 v[140:143], v[224:227], v[64:67], v[140:143]\n"
"ds_read_b64_tr_b16 v[216:217], v194 offset:51200\n"
"v_mfma_f32_16x16x32_bf16 v[140:143], v[228:231], v[72:75], v[140:143]\n"
"ds_read_b64_tr_b16 v[218:219], v194 offset:59392\n"
"v_mfma_f32_16x16x32_bf16 v[156:159], v[228:231], v[76:79], v[156:159]\n"
"ds_read_b64_tr_b16 v[220:221], v194 offset:52224\n"
"v_mfma_f32_16x16x32_bf16 v[156:159], v[224:227], v[68:71], v[156:159]\n"
"s_waitcnt lgkmcnt(7)\n"
"ds_read_b64_tr_b16 v[222:223], v194 offset:60416\n"
"v_mfma_f32_16x16x32_bf16 v[156:159], v[232:235], v[84:87], v[156:159]\n"
"ds_read_b64_tr_b16 v[224:225], v195 offset:49152\n"
"v_mfma_f32_16x16x32_bf16 v[156:159], v[236:239], v[92:95], v[156:159]\n"
"ds_read_b64_tr_b16 v[226:227], v195 offset:57344\n"
"v_mfma_f32_16x16x32_bf16 v[140:143], v[236:239], v[88:91], v[140:143]\n"
"ds_read_b64_tr_b16 v[228:229], v195 offset:50176\n"
"v_mfma_f32_16x16x32_bf16 v[140:143], v[232:235], v[80:83], v[140:143]\n"
"s_waitcnt lgkmcnt(7)\n"
"ds_read_b64_tr_b16 v[230:231], v195 offset:58368\n"
"v_mfma_f32_16x16x32_bf16 v[128:131], v[208:211], v[96:99], v[128:131]\n"
"ds_read_b64_tr_b16 v[232:233], v195 offset:51200\n"
"v_mfma_f32_16x16x32_bf16 v[128:131], v[212:215], v[104:107], v[128:131]\n"
"ds_read_b64_tr_b16 v[234:235], v195 offset:59392\n"
"v_mfma_f32_16x16x32_bf16 v[144:147], v[212:215], v[108:111], v[144:147]\n"
"ds_read_b64_tr_b16 v[236:237], v195 offset:52224\n"
"v_mfma_f32_16x16x32_bf16 v[144:147], v[208:211], v[100:103], v[144:147]\n"
"s_waitcnt lgkmcnt(7)\n"
"ds_read_b64_tr_b16 v[238:239], v195 offset:60416\n"
"v_mfma_f32_16x16x32_bf16 v[144:147], v[216:219], v[116:119], v[144:147]\n"
"ds_read_b64_tr_b16 v[208:209], v194 offset:49664\n"
"v_mfma_f32_16x16x32_bf16 v[144:147], v[220:223], v[124:127], v[144:147]\n"
"ds_read_b64_tr_b16 v[210:211], v194 offset:57856\n"
"v_mfma_f32_16x16x32_bf16 v[128:131], v[220:223], v[120:123], v[128:131]\n"
"ds_read_b64_tr_b16 v[212:213], v194 offset:50688\n"
"v_mfma_f32_16x16x32_bf16 v[128:131], v[216:219], v[112:115], v[128:131]\n"
"s_waitcnt lgkmcnt(7)\n"
"ds_read_b64_tr_b16 v[214:215], v194 offset:58880\n"
"v_mfma_f32_16x16x32_bf16 v[132:135], v[224:227], v[96:99], v[132:135]\n"
"ds_read_b64_tr_b16 v[216:217], v194 offset:51712\n"
"v_mfma_f32_16x16x32_bf16 v[132:135], v[228:231], v[104:107], v[132:135]\n"
"ds_read_b64_tr_b16 v[218:219], v194 offset:59904\n"
"v_mfma_f32_16x16x32_bf16 v[148:151], v[228:231], v[108:111], v[148:151]\n"
"ds_read_b64_tr_b16 v[220:221], v194 offset:52736\n"
"v_mfma_f32_16x16x32_bf16 v[148:151], v[224:227], v[100:103], v[148:151]\n"
"s_waitcnt lgkmcnt(7)\n"
"ds_read_b64_tr_b16 v[222:223], v194 offset:60928\n"
"v_mfma_f32_16x16x32_bf16 v[148:151], v[232:235], v[116:119], v[148:151]\n"
"ds_read_b64_tr_b16 v[224:225], v195 offset:49664\n"
"v_mfma_f32_16x16x32_bf16 v[148:151], v[236:239], v[124:127], v[148:151]\n"
"ds_read_b64_tr_b16 v[226:227], v195 offset:57856\n"
"v_mfma_f32_16x16x32_bf16 v[132:135], v[236:239], v[120:123], v[132:135]\n"
"ds_read_b64_tr_b16 v[228:229], v195 offset:50688\n"
"v_mfma_f32_16x16x32_bf16 v[132:135], v[232:235], v[112:115], v[132:135]\n"
"s_waitcnt lgkmcnt(7)\n"
"ds_read_b64_tr_b16 v[230:231], v195 offset:58880\n"
"v_mfma_f32_16x16x32_bf16 v[136:139], v[208:211], v[96:99], v[136:139]\n"
"ds_read_b64_tr_b16 v[232:233], v195 offset:51712\n"
"v_cvt_pk_bf16_f32 v144, v144, v145\n"
"v_mfma_f32_16x16x32_bf16 v[136:139], v[212:215], v[104:107], v[136:139]\n"
"ds_read_b64_tr_b16 v[234:235], v195 offset:59904\n"
"v_cvt_pk_bf16_f32 v145, v146, v147\n"
"v_mfma_f32_16x16x32_bf16 v[152:155], v[212:215], v[108:111], v[152:155]\n"
"ds_read_b64_tr_b16 v[236:237], v195 offset:52736\n"
"v_cvt_pk_bf16_f32 v128, v128, v129\n"
"v_mfma_f32_16x16x32_bf16 v[152:155], v[208:211], v[100:103], v[152:155]\n"
"s_waitcnt lgkmcnt(7)\n"
"ds_read_b64_tr_b16 v[238:239], v195 offset:60928\n"
"v_cvt_pk_bf16_f32 v129, v130, v131\n"
"v_mfma_f32_16x16x32_bf16 v[152:155], v[216:219], v[116:119], v[152:155]\n"
"v_cvt_pk_bf16_f32 v130, v132, v133\n"
"v_cvt_pk_bf16_f32 v131, v134, v135\n"
"v_mfma_f32_16x16x32_bf16 v[152:155], v[220:223], v[124:127], v[152:155]\n"
"v_cvt_pk_bf16_f32 v146, v148, v149\n"
"v_cvt_pk_bf16_f32 v147, v150, v151\n"
"v_mfma_f32_16x16x32_bf16 v[136:139], v[220:223], v[120:123], v[136:139]\n"
"v_mfma_f32_16x16x32_bf16 v[136:139], v[216:219], v[112:115], v[136:139]\n"
"s_waitcnt lgkmcnt(4)\n"
"v_mfma_f32_16x16x32_bf16 v[140:143], v[224:227], v[96:99], v[140:143]\n"
"buffer_store_dwordx4 v[128:131], v206, s[28:31], s74 offen offset:0 sc1\n"
"v_mfma_f32_16x16x32_bf16 v[140:143], v[228:231], v[104:107], v[140:143]\n"
"v_mfma_f32_16x16x32_bf16 v[156:159], v[228:231], v[108:111], v[156:159]\n"
"v_mfma_f32_16x16x32_bf16 v[156:159], v[224:227], v[100:103], v[156:159]\n"
"s_waitcnt lgkmcnt(0)\n"
"v_mfma_f32_16x16x32_bf16 v[156:159], v[232:235], v[116:119], v[156:159]\n"
"buffer_store_dwordx4 v[144:147], v206, s[28:31], s74 offen offset:2048 sc1\n"
"v_mfma_f32_16x16x32_bf16 v[156:159], v[236:239], v[124:127], v[156:159]\n"
"v_mfma_f32_16x16x32_bf16 v[140:143], v[236:239], v[120:123], v[140:143]\n"
"v_mfma_f32_16x16x32_bf16 v[140:143], v[232:235], v[112:115], v[140:143]\n"
"v_cvt_pk_bf16_f32 v136, v136, v137\n"
"v_cvt_pk_bf16_f32 v137, v138, v139\n"
"s_nop 5\n"
"v_cvt_pk_bf16_f32 v138, v140, v141\n"
"v_cvt_pk_bf16_f32 v139, v142, v143\n"
"buffer_store_dwordx4 v[136:139], v206, s[28:31], s74 offen offset:1024 sc1\n"
"v_cvt_pk_bf16_f32 v152, v152, v153\n"
"v_cvt_pk_bf16_f32 v153, v154, v155\n"
"v_cvt_pk_bf16_f32 v154, v156, v157\n"
"v_cvt_pk_bf16_f32 v155, v158, v159\n"
"buffer_store_dwordx4 v[152:155], v206, s[28:31], s74 offen offset:3072 sc1\n"
"buffer_load_dword v240, v250, s[28:31], 0 offen offset:136 sc1\n"
"v_and_b32 v212, 15, v207\n"
"v_mul_u32_u24 v212, 1792, v212\n"
"v_bfe_u32 v213, v207, 4, 1\n"
"v_lshl_add_u32 v214, v213, 5, v212\n"
"v_lshrrev_b32 v213, 5, v207\n"
"v_lshl_add_u32 v214, v213, 4, v214\n"
"s_lshl_b32 s21, s70, 6\n"
"v_add_u32 v208, s21, v214\n"
"v_lshrrev_b32 v213, 4, v207\n"
"v_lshl_add_u32 v212, v213, 3, v212\n"
"s_and_b32 s21, s70, 1\n"
"s_lshl_b32 s21, s21, 6\n"
"s_lshr_b32 s22, s70, 1\n"
"s_lshl_b32 s23, s22, 5\n"
"s_add_u32 s21, s21, s23\n"
"v_add_u32 v209, s21, v212\n"
"s_mul_i32 s22, s22, 4088\n"
"v_subrev_u32 v210, s22, v248\n"
"s_mov_b32 s24, s99\n"
"s_and_b32 s25, s33, 0xffff\n"
"s_mov_b32 s26, 27776\n"
"s_mov_b32 s27, 0x20000\n"
"v_mov_b32 v211, 0xffff0000\n"
"s_waitcnt vmcnt(0)\n"
"s_barrier\n"
"buffer_store_dwordx4 v[252:255], v251, s[28:31], 0 offen offset:128 sc1\n"
"v_cmp_ne_u32 vcc, v240, v246\n"
"s_nop 4\n"
"s_cbranch_vccnz SP2_%=\n"
"SR2_%=:\n"
"s_mov_b32 s66, 16384\n"
"buffer_load_dwordx4 v[64:67], v248, s[28:31], s66 offen sc1\n"
"s_mov_b32 s66, 131072\n"
"buffer_load_dwordx4 v[68:71], v248, s[28:31], s66 offen sc1\n"
"s_mov_b32 s66, 245760\n"
"buffer_load_dwordx4 v[72:75], v248, s[28:31], s66 offen sc1\n"
"s_mov_b32 s66, 360448\n"
"buffer_load_dwordx4 v[76:79], v248, s[28:31], s66 offen sc1\n"
"s_mov_b32 s66, 475136\n"
"buffer_load_dwordx4 v[80:83], v248, s[28:31], s66 offen sc1\n"
"s_mov_b32 s66, 589824\n"
"buffer_load_dwordx4 v[84:87], v248, s[28:31], s66 offen sc1\n"
"s_mov_b32 s66, 704512\n"
"buffer_load_dwordx4 v[88:91], v248, s[28:31], s66 offen sc1\n"
"s_mov_b32 s66, 819200\n"
"buffer_load_dwordx4 v[92:95], v248, s[28:31], s66 offen sc1\n"
"v_accvgpr_read_b32 v0, a0\n"
"v_accvgpr_read_b32 v1, a1\n"
"v_accvgpr_read_b32 v2, a2\n"
"v_accvgpr_read_b32 v3, a3\n"
"v_accvgpr_read_b32 v4, a4\n"
"v_accvgpr_read_b32 v5, a5\n"
"v_accvgpr_read_b32 v6, a6\n"
"v_accvgpr_read_b32 v7, a7\n"
"v_accvgpr_read_b32 v8, a8\n"
"v_accvgpr_read_b32 v9, a9\n"
"v_accvgpr_read_b32 v10, a10\n"
"v_accvgpr_read_b32 v11, a11\n"
"v_accvgpr_read_b32 v12, a12\n"
"v_accvgpr_read_b32 v13, a13\n"
"v_accvgpr_read_b32 v14, a14\n"
"v_accvgpr_read_b32 v15, a15\n"
"v_accvgpr_read_b32 v16, a16\n"
"v_accvgpr_read_b32 v17, a17\n"
"v_accvgpr_read_b32 v18, a18\n"
"v_accvgpr_read_b32 v19, a19\n"
"v_accvgpr_read_b32 v20, a20\n"
"v_accvgpr_read_b32 v21, a21\n"
"v_accvgpr_read_b32 v22, a22\n"
"v_accvgpr_read_b32 v23, a23\n"
"v_accvgpr_read_b32 v24, a24\n"
"v_accvgpr_read_b32 v25, a25\n"
"v_accvgpr_read_b32 v26, a26\n"
"v_accvgpr_read_b32 v27, a27\n"
"v_accvgpr_read_b32 v28, a28\n"
"v_accvgpr_read_b32 v29, a29\n"
"v_accvgpr_read_b32 v30, a30\n"
"v_accvgpr_read_b32 v31, a31\n"
"v_accvgpr_read_b32 v32, a32\n"
"v_accvgpr_read_b32 v33, a33\n"
"v_accvgpr_read_b32 v34, a34\n"
"v_accvgpr_read_b32 v35, a35\n"
"v_accvgpr_read_b32 v36, a36\n"
"v_accvgpr_read_b32 v37, a37\n"
"v_accvgpr_read_b32 v38, a38\n"
"v_accvgpr_read_b32 v39, a39\n"
"v_accvgpr_read_b32 v40, a40\n"
"v_accvgpr_read_b32 v41, a41\n"
"v_accvgpr_read_b32 v42, a42\n"
"v_accvgpr_read_b32 v43, a43\n"
"v_accvgpr_read_b32 v44, a44\n"
"v_accvgpr_read_b32 v45, a45\n"
"v_accvgpr_read_b32 v46, a46\n"
"v_accvgpr_read_b32 v47, a47\n"
"v_accvgpr_read_b32 v48, a48\n"
"v_accvgpr_read_b32 v49, a49\n"
"v_accvgpr_read_b32 v50, a50\n"
"v_accvgpr_read_b32 v51, a51\n"
"v_accvgpr_read_b32 v52, a52\n"
"v_accvgpr_read_b32 v53, a53\n"
"v_accvgpr_read_b32 v54, a54\n"
"v_accvgpr_read_b32 v55, a55\n"
"v_accvgpr_read_b32 v56, a56\n"
"v_accvgpr_read_b32 v57, a57\n"
"v_accvgpr_read_b32 v58, a58\n"
"v_accvgpr_read_b32 v59, a59\n"
"v_accvgpr_read_b32 v60, a60\n"
"v_accvgpr_read_b32 v61, a61\n"
"v_accvgpr_read_b32 v62, a62\n"
"v_accvgpr_read_b32 v63, a63\n"
"v_lshlrev_b32 v128, 16, v0\n"
"v_and_b32 v129, v211, v0\n"
"v_lshlrev_b32 v130, 16, v1\n"
"v_and_b32 v131, v211, v1\n"
"v_lshlrev_b32 v132, 16, v2\n"
"v_and_b32 v133, v211, v2\n"
"v_lshlrev_b32 v134, 16, v3\n"
"v_and_b32 v135, v211, v3\n"
"v_lshlrev_b32 v136, 16, v4\n"
"v_and_b32 v137, v211, v4\n"
"v_lshlrev_b32 v138, 16, v5\n"
"v_and_b32 v139, v211, v5\n"
"v_lshlrev_b32 v140, 16, v6\n"
"v_and_b32 v141, v211, v6\n"
"v_lshlrev_b32 v142, 16, v7\n"
"v_and_b32 v143, v211, v7\n"
"v_pk_add_f32 v[128:129], v[128:129], v[136:137]\n"
"v_pk_add_f32 v[130:131], v[130:131], v[138:139]\n"
"v_pk_add_f32 v[132:133], v[132:133], v[140:141]\n"
"v_pk_add_f32 v[134:135], v[134:135], v[142:143]\n"
"v_lshlrev_b32 v136, 16, v8\n"
"v_and_b32 v137, v211, v8\n"
"v_lshlrev_b32 v138, 16, v9\n"
"v_and_b32 v139, v211, v9\n"
"v_lshlrev_b32 v140, 16, v10\n"
"v_and_b32 v141, v211, v10\n"
"v_lshlrev_b32 v142, 16, v11\n"
"v_and_b32 v143, v211, v11\n"
"v_pk_add_f32 v[128:129], v[128:129], v[136:137]\n"
"v_pk_add_f32 v[130:131], v[130:131], v[138:139]\n"
"v_pk_add_f32 v[132:133], v[132:133], v[140:141]\n"
"v_pk_add_f32 v[134:135], v[134:135], v[142:143]\n"
"v_lshlrev_b32 v136, 16, v12\n"
"v_and_b32 v137, v211, v12\n"
"v_lshlrev_b32 v138, 16, v13\n"
"v_and_b32 v139, v211, v13\n"
"v_lshlrev_b32 v140, 16, v14\n"
"v_and_b32 v141, v211, v14\n"
"v_lshlrev_b32 v142, 16, v15\n"
"v_and_b32 v143, v211, v15\n"
"v_pk_add_f32 v[128:129], v[128:129], v[136:137]\n"
"v_pk_add_f32 v[130:131], v[130:131], v[138:139]\n"
"v_pk_add_f32 v[132:133], v[132:133], v[140:141]\n"
"IWB7_%=:\n"
"s_cbranch_execz IWB11_%=\n"
"v_pk_add_f32 v[134:135], v[134:135], v[142:143]\n"
"v_lshlrev_b32 v136, 16, v16\n"
"v_and_b32 v137, v211, v16\n"
"v_lshlrev_b32 v138, 16, v17\n"
"v_and_b32 v139, v211, v17\n"
"v_lshlrev_b32 v140, 16, v18\n"
"v_and_b32 v141, v211, v18\n"
"v_lshlrev_b32 v142, 16, v19\n"
"v_and_b32 v143, v211, v19\n"
"v_pk_add_f32 v[128:129], v[128:129], v[136:137]\n"
"v_pk_add_f32 v[130:131], v[130:131], v[138:139]\n"
"v_pk_add_f32 v[132:133], v[132:133], v[140:141]\n"
"v_pk_add_f32 v[134:135], v[134:135], v[142:143]\n"
"v_lshlrev_b32 v136, 16, v20\n"
"v_and_b32 v137, v211, v20\n"
"v_lshlrev_b32 v138, 16, v21\n"
"v_and_b32 v139, v211, v21\n"
"v_lshlrev_b32 v140, 16, v22\n"
"v_and_b32 v141, v211, v22\n"
"v_lshlrev_b32 v142, 16, v23\n"
"v_and_b32 v143, v211, v23\n"
"v_pk_add_f32 v[128:129], v[128:129], v[136:137]\n"
"v_pk_add_f32 v[130:131], v[130:131], v[138:139]\n"
"v_pk_add_f32 v[132:133], v[132:133], v[140:141]\n"
"v_pk_add_f32 v[134:135], v[134:135], v[142:143]\n"
"v_lshlrev_b32 v136, 16, v24\n"
"v_and_b32 v137, v211, v24\n"
"v_lshlrev_b32 v138, 16, v25\n"
"v_and_b32 v139, v211, v25\n"
"v_lshlrev_b32 v140, 16, v26\n"
"v_and_b32 v141, v211, v26\n"
"v_lshlrev_b32 v142, 16, v27\n"
"v_and_b32 v143, v211, v27\n"
"v_pk_add_f32 v[128:129], v[128:129], v[136:137]\n"
"v_pk_add_f32 v[130:131], v[130:131], v[138:139]\n"
"v_pk_add_f32 v[132:133], v[132:133], v[140:141]\n"
"v_pk_add_f32 v[134:135], v[134:135], v[142:143]\n"
"v_lshlrev_b32 v136, 16, v28\n"
"v_and_b32 v137, v211, v28\n"
"v_lshlrev_b32 v138, 16, v29\n"
"v_and_b32 v139, v211, v29\n"
"v_lshlrev_b32 v140, 16, v30\n"
"v_and_b32 v141, v211, v30\n"
"v_lshlrev_b32 v142, 16, v31\n"
"v_and_b32 v143, v211, v31\n"
"v_pk_add_f32 v[128:129], v[128:129], v[136:137]\n"
"v_pk_add_f32 v[130:131], v[130:131], v[138:139]\n"
"v_pk_add_f32 v[132:133], v[132:133], v[140:141]\n"
"v_pk_add_f32 v[134:135], v[134:135], v[142:143]\n"
"v_cvt_pk_bf16_f32 v144, v128, v129\n"
"v_cvt_pk_bf16_f32 v145, v130, v131\n"
"v_cvt_pk_bf16_f32 v146, v132, v133\n"
"v_cvt_pk_bf16_f32 v147, v134, v135\n"
"s_nop 0\n"
"v_permlane16_swap_b32 v144, v146\n"
"v_permlane16_swap_b32 v145, v147\n"
"buffer_store_dwordx4 v[144:147], v208, s[24:27], 0 offen offset:0\n"
"buffer_load_dword v240, v250, s[28:31], 0 offen offset:140 sc1\n"
"v_lshlrev_b32 v128, 16, v32\n"
"v_and_b32 v129, v211, v32\n"
"v_lshlrev_b32 v130, 16, v33\n"
"v_and_b32 v131, v211, v33\n"
"v_lshlrev_b32 v132, 16, v34\n"
"v_and_b32 v133, v211, v34\n"
"v_lshlrev_b32 v134, 16, v35\n"
"v_and_b32 v135, v211, v35\n"
"v_lshlrev_b32 v136, 16, v36\n"
"v_and_b32 v137, v211, v36\n"
"v_lshlrev_b32 v138, 16, v37\n"
"v_and_b32 v139, v211, v37\n"
"v_lshlrev_b32 v140, 16, v38\n"
"v_and_b32 v141, v211, v38\n"
"v_lshlrev_b32 v142, 16, v39\n"
"v_and_b32 v143, v211, v39\n"
"v_pk_add_f32 v[128:129], v[128:129], v[136:137]\n"
"v_pk_add_f32 v[130:131], v[130:131], v[138:139]\n"
"v_pk_add_f32 v[132:133], v[132:133], v[140:141]\n"
"v_pk_add_f32 v[134:135], v[134:135], v[142:143]\n"
"v_lshlrev_b32 v136, 16, v40\n"
"v_and_b32 v137, v211, v40\n"
"v_lshlrev_b32 v138, 16, v41\n"
"v_and_b32 v139, v211, v41\n"
"v_lshlrev_b32 v140, 16, v42\n"
"v_and_b32 v141, v211, v42\n"
"v_lshlrev_b32 v142, 16, v43\n"
"v_and_b32 v143, v211, v43\n"
"v_pk_add_f32 v[128:129], v[128:129], v[136:137]\n"
"v_pk_add_f32 v[130:131], v[130:131], v[138:139]\n"
"v_pk_add_f32 v[132:133], v[132:133], v[140:141]\n"
"v_pk_add_f32 v[134:135], v[134:135], v[142:143]\n"
"v_lshlrev_b32 v136, 16, v44\n"
"v_and_b32 v137, v211, v44\n"
"v_lshlrev_b32 v138, 16, v45\n"
"v_and_b32 v139, v211, v45\n"
"v_lshlrev_b32 v140, 16, v46\n"
"v_and_b32 v141, v211, v46\n"
"v_lshlrev_b32 v142, 16, v47\n"
"v_and_b32 v143, v211, v47\n"
"v_pk_add_f32 v[128:129], v[128:129], v[136:137]\n"
"v_pk_add_f32 v[130:131], v[130:131], v[138:139]\n"
"v_pk_add_f32 v[132:133], v[132:133], v[140:141]\n"
"v_pk_add_f32 v[134:135], v[134:135], v[142:143]\n"
"v_lshlrev_b32 v136, 16, v48\n"
"v_and_b32 v137, v211, v48\n"
"v_lshlrev_b32 v138, 16, v49\n"
"v_and_b32 v139, v211, v49\n"
"v_lshlrev_b32 v140, 16, v50\n"
"v_and_b32 v141, v211, v50\n"
"v_lshlrev_b32 v142, 16, v51\n"
"v_and_b32 v143, v211, v51\n"
"v_pk_add_f32 v[128:129], v[128:129], v[136:137]\n"
"v_pk_add_f32 v[130:131], v[130:131], v[138:139]\n"
"v_pk_add_f32 v[132:133], v[132:133], v[140:141]\n"
"v_pk_add_f32 v[134:135], v[134:135], v[142:143]\n"
"v_lshlrev_b32 v136, 16, v52\n"
"v_and_b32 v137, v211, v52\n"
"v_lshlrev_b32 v138, 16, v53\n"
"v_and_b32 v139, v211, v53\n"
"v_lshlrev_b32 v140, 16, v54\n"
"v_and_b32 v141, v211, v54\n"
"v_lshlrev_b32 v142, 16, v55\n"
"v_and_b32 v143, v211, v55\n"
"v_pk_add_f32 v[128:129], v[128:129], v[136:137]\n"
"v_pk_add_f32 v[130:131], v[130:131], v[138:139]\n"
"v_pk_add_f32 v[132:133], v[132:133], v[140:141]\n"
"v_pk_add_f32 v[134:135], v[134:135], v[142:143]\n"
"v_lshlrev_b32 v136, 16, v56\n"
"v_and_b32 v137, v211, v56\n"
"v_lshlrev_b32 v138, 16, v57\n"
"v_and_b32 v139, v211, v57\n"
"v_lshlrev_b32 v140, 16, v58\n"
"v_and_b32 v141, v211, v58\n"
"v_lshlrev_b32 v142, 16, v59\n"
"v_and_b32 v143, v211, v59\n"
"v_pk_add_f32 v[128:129], v[128:129], v[136:137]\n"
"v_pk_add_f32 v[130:131], v[130:131], v[138:139]\n"
"v_pk_add_f32 v[132:133], v[132:133], v[140:141]\n"
"v_pk_add_f32 v[134:135], v[134:135], v[142:143]\n"
"v_lshlrev_b32 v136, 16, v60\n"
"v_and_b32 v137, v211, v60\n"
"v_lshlrev_b32 v138, 16, v61\n"
"v_and_b32 v139, v211, v61\n"
"v_lshlrev_b32 v140, 16, v62\n"
"v_and_b32 v141, v211, v62\n"
"v_lshlrev_b32 v142, 16, v63\n"
"v_and_b32 v143, v211, v63\n"
"v_pk_add_f32 v[128:129], v[128:129], v[136:137]\n"
"v_pk_add_f32 v[130:131], v[130:131], v[138:139]\n"
"v_pk_add_f32 v[132:133], v[132:133], v[140:141]\n"
"v_pk_add_f32 v[134:135], v[134:135], v[142:143]\n"
"s_waitcnt vmcnt(0)\n"
"v_cmp_ne_u32 vcc, v240, v247\n"
"v_cvt_pk_bf16_f32 v148, v128, v129\n"
"v_cvt_pk_bf16_f32 v149, v130, v131\n"
"v_cvt_pk_bf16_f32 v150, v132, v133\n"
"v_cvt_pk_bf16_f32 v151, v134, v135\n"
"s_nop 0\n"
"v_permlane16_swap_b32 v148, v150\n"
"v_permlane16_swap_b32 v149, v151\n"
"buffer_store_dwordx4 v[148:151], v208, s[24:27], 0 offen offset:256\n"
"s_cbranch_vccnz SP3_%=\n"
"SR3_%=:\n"
"s_mov_b32 s66, 24576\n"
"buffer_load_dwordx2 v[96:97], v210, s[28:31], s66 offen sc1\n"
"s_mov_b32 s66, 139264\n"
"buffer_load_dwordx2 v[98:99], v210, s[28:31], s66 offen sc1\n"
"s_mov_b32 s66, 253952\n"
"buffer_load_dwordx2 v[100:101], v210, s[28:31], s66 offen sc1\n"
"s_mov_b32 s66, 368640\n"
"buffer_load_dwordx2 v[102:103], v210, s[28:31], s66 offen sc1\n"
"s_mov_b32 s66, 483328\n"
"buffer_load_dwordx2 v[104:105], v210, s[28:31], s66 offen sc1\n"
"s_mov_b32 s66, 598016\n"
"buffer_load_dwordx2 v[106:107], v210, s[28:31], s66 offen sc1\n"
"s_mov_b32 s66, 712704\n"
"buffer_load_dwordx2 v[108:109], v210, s[28:31], s66 offen sc1\n"
"s_mov_b32 s66, 827392\n"
"buffer_load_dwordx2 v[110:111], v210, s[28:31], s66 offen sc1\n"
"v_lshlrev_b32 v128, 16, v64\n"
"v_and_b32 v129, v211, v64\n"
"v_lshlrev_b32 v130, 16, v65\n"
"v_and_b32 v131, v211, v65\n"
"v_lshlrev_b32 v132, 16, v66\n"
"v_and_b32 v133, v211, v66\n"
"v_lshlrev_b32 v134, 16, v67\n"
"v_and_b32 v135, v211, v67\n"
"v_lshlrev_b32 v136, 16, v68\n"
"v_and_b32 v137, v211, v68\n"
"v_lshlrev_b32 v138, 16, v69\n"
"v_and_b32 v139, v211, v69\n"
"v_lshlrev_b32 v140, 16, v70\n"
"v_and_b32 v141, v211, v70\n"
"v_lshlrev_b32 v142, 16, v71\n"
"v_and_b32 v143, v211, v71\n"
"v_pk_add_f32 v[128:129], v[128:129], v[136:137]\n"
"v_pk_add_f32 v[130:131], v[130:131], v[138:139]\n"
"v_pk_add_f32 v[132:133], v[132:133], v[140:141]\n"
"v_pk_add_f32 v[134:135], v[134:135], v[142:143]\n"
"v_lshlrev_b32 v136, 16, v72\n"
"v_and_b32 v137, v211, v72\n"
"v_lshlrev_b32 v138, 16, v73\n"
"v_and_b32 v139, v211, v73\n"
"v_lshlrev_b32 v140, 16, v74\n"
"v_and_b32 v141, v211, v74\n"
"v_lshlrev_b32 v142, 16, v75\n"
"v_and_b32 v143, v211, v75\n"
"v_pk_add_f32 v[128:129], v[128:129], v[136:137]\n"
"v_pk_add_f32 v[130:131], v[130:131], v[138:139]\n"
"v_pk_add_f32 v[132:133], v[132:133], v[140:141]\n"
"v_pk_add_f32 v[134:135], v[134:135], v[142:143]\n"
"v_lshlrev_b32 v136, 16, v76\n"
"v_and_b32 v137, v211, v76\n"
"v_lshlrev_b32 v138, 16, v77\n"
"v_and_b32 v139, v211, v77\n"
"v_lshlrev_b32 v140, 16, v78\n"
"v_and_b32 v141, v211, v78\n"
"v_lshlrev_b32 v142, 16, v79\n"
"v_and_b32 v143, v211, v79\n"
"v_pk_add_f32 v[128:129], v[128:129], v[136:137]\n"
"v_pk_add_f32 v[130:131], v[130:131], v[138:139]\n"
"v_pk_add_f32 v[132:133], v[132:133], v[140:141]\n"
"v_pk_add_f32 v[134:135], v[134:135], v[142:143]\n"
"v_lshlrev_b32 v136, 16, v80\n"
"v_and_b32 v137, v211, v80\n"
"v_lshlrev_b32 v138, 16, v81\n"
"v_and_b32 v139, v211, v81\n"
"v_lshlrev_b32 v140, 16, v82\n"
"v_and_b32 v141, v211, v82\n"
"v_lshlrev_b32 v142, 16, v83\n"
"v_and_b32 v143, v211, v83\n"
"v_pk_add_f32 v[128:129], v[128:129], v[136:137]\n"
"v_pk_add_f32 v[130:131], v[130:131], v[138:139]\n"
"v_pk_add_f32 v[132:133], v[132:133], v[140:141]\n"
"v_pk_add_f32 v[134:135], v[134:135], v[142:143]\n"
"v_lshlrev_b32 v136, 16, v84\n"
"v_and_b32 v137, v211, v84\n"
"v_lshlrev_b32 v138, 16, v85\n"
"v_and_b32 v139, v211, v85\n"
"v_lshlrev_b32 v140, 16, v86\n"
"v_and_b32 v141, v211, v86\n"
"v_lshlrev_b32 v142, 16, v87\n"
"v_and_b32 v143, v211, v87\n"
"v_pk_add_f32 v[128:129], v[128:129], v[136:137]\n"
"v_pk_add_f32 v[130:131], v[130:131], v[138:139]\n"
"v_pk_add_f32 v[132:133], v[132:133], v[140:141]\n"
"v_pk_add_f32 v[134:135], v[134:135], v[142:143]\n"
"v_lshlrev_b32 v136, 16, v88\n"
"v_and_b32 v137, v211, v88\n"
"v_lshlrev_b32 v138, 16, v89\n"
"v_and_b32 v139, v211, v89\n"
"v_lshlrev_b32 v140, 16, v90\n"
"v_and_b32 v141, v211, v90\n"
"v_lshlrev_b32 v142, 16, v91\n"
"v_and_b32 v143, v211, v91\n"
"v_pk_add_f32 v[128:129], v[128:129], v[136:137]\n"
"v_pk_add_f32 v[130:131], v[130:131], v[138:139]\n"
"v_pk_add_f32 v[132:133], v[132:133], v[140:141]\n"
"v_pk_add_f32 v[134:135], v[134:135], v[142:143]\n"
"v_lshlrev_b32 v136, 16, v92\n"
"v_and_b32 v137, v211, v92\n"
"v_lshlrev_b32 v138, 16, v93\n"
"v_and_b32 v139, v211, v93\n"
"v_lshlrev_b32 v140, 16, v94\n"
"v_and_b32 v141, v211, v94\n"
"v_lshlrev_b32 v142, 16, v95\n"
"v_and_b32 v143, v211, v95\n"
"v_pk_add_f32 v[128:129], v[128:129], v[136:137]\n"
"v_pk_add_f32 v[130:131], v[130:131], v[138:139]\n"
"v_pk_add_f32 v[132:133], v[132:133], v[140:141]\n"
"v_pk_add_f32 v[134:135], v[134:135], v[142:143]\n"
"v_cvt_pk_bf16_f32 v152, v128, v129\n"
"v_cvt_pk_bf16_f32 v153, v130, v131\n"
"v_cvt_pk_bf16_f32 v154, v132, v133\n"
"v_cvt_pk_bf16_f32 v155, v134, v135\n"
"s_nop 0\n"
"v_permlane16_swap_b32 v152, v154\n"
"v_permlane16_swap_b32 v153, v155\n"
"buffer_store_dwordx4 v[152:155], v208, s[24:27], 0 offen offset:512\n"
"s_waitcnt vmcnt(8)\n"
"v_lshlrev_b32 v128, 16, v96\n"
"v_and_b32 v129, v211, v96\n"
"v_lshlrev_b32 v130, 16, v97\n"
"v_and_b32 v131, v211, v97\n"
"s_waitcnt vmcnt(7)\n"
"v_lshlrev_b32 v136, 16, v98\n"
"v_and_b32 v137, v211, v98\n"
"v_lshlrev_b32 v138, 16, v99\n"
"v_and_b32 v139, v211, v99\n"
"v_pk_add_f32 v[128:129], v[128:129], v[136:137]\n"
"v_pk_add_f32 v[130:131], v[130:131], v[138:139]\n"
"s_waitcnt vmcnt(6)\n"
"v_lshlrev_b32 v136, 16, v100\n"
"v_and_b32 v137, v211, v100\n"
"v_lshlrev_b32 v138, 16, v101\n"
"v_and_b32 v139, v211, v101\n"
"v_pk_add_f32 v[128:129], v[128:129], v[136:137]\n"
"v_pk_add_f32 v[130:131], v[130:131], v[138:139]\n"
"s_waitcnt vmcnt(5)\n"
"v_lshlrev_b32 v136, 16, v102\n"
"v_and_b32 v137, v211, v102\n"
"v_lshlrev_b32 v138, 16, v103\n"
"v_and_b32 v139, v211, v103\n"
"v_pk_add_f32 v[128:129], v[128:129], v[136:137]\n"
"v_pk_add_f32 v[130:131], v[130:131], v[138:139]\n"
"s_waitcnt vmcnt(4)\n"
"v_lshlrev_b32 v136, 16, v104\n"
"v_and_b32 v137, v211, v104\n"
"v_lshlrev_b32 v138, 16, v105\n"
"v_and_b32 v139, v211, v105\n"
"v_pk_add_f32 v[128:129], v[128:129], v[136:137]\n"
"v_pk_add_f32 v[130:131], v[130:131], v[138:139]\n"
"s_waitcnt vmcnt(3)\n"
"v_lshlrev_b32 v136, 16, v106\n"
"v_and_b32 v137, v211, v106\n"
"v_lshlrev_b32 v138, 16, v107\n"
"v_and_b32 v139, v211, v107\n"
"v_pk_add_f32 v[128:129], v[128:129], v[136:137]\n"
"v_pk_add_f32 v[130:131], v[130:131], v[138:139]\n"
"s_waitcnt vmcnt(2)\n"
"v_lshlrev_b32 v136, 16, v108\n"
"v_and_b32 v137, v211, v108\n"
"v_lshlrev_b32 v138, 16, v109\n"
"v_and_b32 v139, v211, v109\n"
"v_pk_add_f32 v[128:129], v[128:129], v[136:137]\n"
"v_pk_add_f32 v[130:131], v[130:131], v[138:139]\n"
"s_waitcnt vmcnt(1)\n"
"v_lshlrev_b32 v136, 16, v110\n"
"v_and_b32 v137, v211, v110\n"
"v_lshlrev_b32 v138, 16, v111\n"
"v_and_b32 v139, v211, v111\n"
"v_pk_add_f32 v[128:129], v[128:129], v[136:137]\n"
"v_pk_add_f32 v[130:131], v[130:131], v[138:139]\n"
"v_cvt_pk_bf16_f32 v160, v128, v129\n"
"v_cvt_pk_bf16_f32 v161, v130, v131\n"
"buffer_store_dwordx2 v[160:161], v209, s[24:27], 0 offen offset:768\n"
"s_endpgm\n"
"IWB8_%=:\n"
"s_cbranch_execz IRB_%=\n"
"RSL0_%=:\n"
"s_mov_b32 s47, 0\n"
"s_cmp_lt_u32 s44, 3\n"
"s_cbranch_scc1 RAL0_%=\n"
"s_cmp_ge_u32 s46, 2\n"
"s_cselect_b32 s49, 1, 0\n"
"s_mov_b32 s50, 1\n"
"s_add_u32 m0, s45, s52\n"
"s_nop 0\n"
"buffer_load_dwordx4 v247, s[8:11], s62 offen nt lds\n"
"s_add_u32 m0, s45, s53\n"
"s_nop 0\n"
"buffer_load_dwordx4 v247, s[8:11], s62 offen offset:128 nt lds\n"
"s_add_u32 m0, s45, s54\n"
"s_nop 0\n"
"buffer_load_dwordx4 v247, s[8:11], s62 offen offset:256 nt lds\n"
"s_add_u32 m0, s45, s55\n"
"s_nop 0\n"
"buffer_load_dwordx4 v247, s[8:11], s62 offen offset:384 nt lds\n"
"s_add_u32 m0, s45, s56\n"
"s_nop 0\n"
"buffer_load_dwordx4 v248, s[12:15], s34 offen lds\n"
"s_add_u32 m0, s45, s57\n"
"s_nop 0\n"
"buffer_load_dwordx4 v248, s[12:15], s34 offen offset:128 lds\n"
"s_add_u32 m0, s45, s58\n"
"s_nop 0\n"
"buffer_load_dwordx4 v248, s[12:15], s34 offen offset:256 lds\n"
"s_add_u32 m0, s45, s59\n"
"s_nop 0\n"
"buffer_load_dwordx4 v248, s[12:15], s34 offen offset:384 lds\n"
"s_mov_b32 s34, s62\n"
"s_add_u32 s62, s62, 16384\n"
"s_and_b32 s62, s62, 524287\n"
"s_add_u32 s45, s45, 32768\n"
"s_cmp_eq_u32 s45, 163840\n"
"s_cselect_b32 s45, 0, s45\n"
"s_or_b32 s47, s49, s51\n"
"s_cmp_lt_u32 s71, 14\n"
"s_cselect_b32 s47, s47, 0\n"
"s_cmp_lg_u32 s47, 0\n"
"s_cselect_b32 s44, 3, 2\n"
"s_branch RBL0_%=\n"
"RAL0_%=:\n"
"s_cmp_lt_u32 s44, 2\n"
"s_cselect_b32 s47, s49, 0\n"
"s_cmp_lt_u32 s71, 14\n"
"s_cselect_b32 s47, s47, 0\n"
"s_add_u32 s44, s44, s47\n"
"s_cmp_ge_u32 s46, 8\n"
"s_cselect_b32 s49, 1, 0\n"
"RBL0_%=:\n"
"s_and_b32 s50, s50, s49\n"
"s_cselect_b32 s51, 7, s51\n"
"s_mov_b32 s50, s49\n"
"s_cmp_eq_u32 s44, 3\n"
"s_cselect_b32 s48, 1, s49\n"
"s_cmp_eq_u32 s51, 0\n"
"s_cbranch_scc1 REL0_%=\n"
"s_mov_b32 s43, -1\n"
"s_mov_b32 s35, 544\n"
"s_mov_b32 s48, s51\n"
"RLL0_%=:\n"
"s_sleep 2\n"
"s_sub_u32 s48, s48, 1\n"
"s_cmp_lg_u32 s48, 0\n"
"s_cbranch_scc1 RLL0_%=\n"
"s_sub_u32 s51, s51, 1\n"
"s_mov_b32 s48, 1\n"
"REL0_%=:\n"
"s_cmp_lg_u32 s47, 0\n"
"s_cbranch_scc1 ALL0_%=\n"
"s_branch RRL0_%=\n"
"IWB9_%=:\n"
"s_cbranch_execz IRB_%=\n"
"RSL1_%=:\n"
"s_mov_b32 s47, 0\n"
"s_cmp_lt_u32 s44, 3\n"
"s_cbranch_scc1 RAL1_%=\n"
"s_cmp_ge_u32 s46, 2\n"
"s_cselect_b32 s49, 1, 0\n"
"s_mov_b32 s50, 1\n"
"s_add_u32 m0, s45, s52\n"
"s_nop 0\n"
"buffer_load_dwordx4 v247, s[8:11], s62 offen nt lds\n"
"s_add_u32 m0, s45, s53\n"
"s_nop 0\n"
"buffer_load_dwordx4 v247, s[8:11], s62 offen offset:128 nt lds\n"
"s_add_u32 m0, s45, s54\n"
"s_nop 0\n"
"buffer_load_dwordx4 v247, s[8:11], s62 offen offset:256 nt lds\n"
"s_add_u32 m0, s45, s55\n"
"s_nop 0\n"
"buffer_load_dwordx4 v247, s[8:11], s62 offen offset:384 nt lds\n"
"s_add_u32 m0, s45, s56\n"
"s_nop 0\n"
"buffer_load_dwordx4 v248, s[12:15], s34 offen lds\n"
"s_add_u32 m0, s45, s57\n"
"s_nop 0\n"
"buffer_load_dwordx4 v248, s[12:15], s34 offen offset:128 lds\n"
"s_add_u32 m0, s45, s58\n"
"s_nop 0\n"
"buffer_load_dwordx4 v248, s[12:15], s34 offen offset:256 lds\n"
"s_add_u32 m0, s45, s59\n"
"s_nop 0\n"
"buffer_load_dwordx4 v248, s[12:15], s34 offen offset:384 lds\n"
"s_mov_b32 s34, s62\n"
"s_add_u32 s62, s62, 16384\n"
"s_and_b32 s62, s62, 524287\n"
"s_add_u32 s45, s45, 32768\n"
"s_cmp_eq_u32 s45, 163840\n"
"s_cselect_b32 s45, 0, s45\n"
"s_or_b32 s47, s49, s51\n"
"s_cmp_lg_u32 s47, 0\n"
"s_cselect_b32 s44, 3, 2\n"
"s_branch RBL1_%=\n"
"RAL1_%=:\n"
"s_cmp_lt_u32 s44, 2\n"
"s_cselect_b32 s47, s49, 0\n"
"s_add_u32 s44, s44, s47\n"
"s_cmp_ge_u32 s46, 8\n"
"s_cselect_b32 s49, 1, 0\n"
"RBL1_%=:\n"
"s_and_b32 s50, s50, s49\n"
"s_cselect_b32 s51, 7, s51\n"
"s_mov_b32 s50, s49\n"
"s_cmp_eq_u32 s44, 3\n"
"s_cselect_b32 s48, 1, s49\n"
"s_cmp_eq_u32 s51, 0\n"
"s_cbranch_scc1 REL1_%=\n"
"s_mov_b32 s43, -1\n"
"s_mov_b32 s35, 544\n"
"s_mov_b32 s48, s51\n"
"RLL1_%=:\n"
"s_sleep 2\n"
"s_sub_u32 s48, s48, 1\n"
"s_cmp_lg_u32 s48, 0\n"
"s_cbranch_scc1 RLL1_%=\n"
"s_sub_u32 s51, s51, 1\n"
"s_mov_b32 s48, 1\n"
"REL1_%=:\n"
"s_cmp_lg_u32 s47, 0\n"
"s_cbranch_scc1 ALL1_%=\n"
"s_branch RRL1_%=\n"
"RSP29_%=:\n"
"s_and_b32 s50, s50, s49\n"
"s_cselect_b32 s51, 7, s51\n"
"s_mov_b32 s50, s49\n"
"s_cmp_eq_u32 s44, 3\n"
"s_cselect_b32 s48, 1, s49\n"
"s_cmp_eq_u32 s51, 0\n"
"s_cbranch_scc1 REP29_%=\n"
"s_mov_b32 s43, -1\n"
"s_mov_b32 s35, 544\n"
"s_mov_b32 s48, s51\n"
"RLP29_%=:\n"
"s_sleep 2\n"
"s_sub_u32 s48, s48, 1\n"
"s_cmp_lg_u32 s48, 0\n"
"s_cbranch_scc1 RLP29_%=\n"
"s_sub_u32 s51, s51, 1\n"
"s_mov_b32 s48, 1\n"
"REP29_%=:\n"
"s_branch RRP29_%=\n"
"RSP30_%=:\n"
"s_and_b32 s50, s50, s49\n"
"s_cselect_b32 s51, 7, s51\n"
"s_mov_b32 s50, s49\n"
"s_cmp_eq_u32 s44, 3\n"
"s_cselect_b32 s48, 1, s49\n"
"s_cmp_eq_u32 s51, 0\n"
"s_cbranch_scc1 REP30_%=\n"
"s_mov_b32 s43, -1\n"
"s_mov_b32 s35, 544\n"
"s_mov_b32 s48, s51\n"
"RLP30_%=:\n"
"s_sleep 2\n"
"s_sub_u32 s48, s48, 1\n"
"s_cmp_lg_u32 s48, 0\n"
"s_cbranch_scc1 RLP30_%=\n"
"s_sub_u32 s51, s51, 1\n"
"s_mov_b32 s48, 1\n"
"REP30_%=:\n"
"s_branch RRP30_%=\n"
"RSP31_%=:\n"
"s_and_b32 s50, s50, s49\n"
"s_cselect_b32 s51, 7, s51\n"
"s_mov_b32 s50, s49\n"
"s_cmp_eq_u32 s44, 3\n"
"s_cselect_b32 s48, 1, s49\n"
"s_cmp_eq_u32 s51, 0\n"
"s_cbranch_scc1 REP31_%=\n"
"s_mov_b32 s43, -1\n"
"s_mov_b32 s35, 544\n"
"s_mov_b32 s48, s51\n"
"RLP31_%=:\n"
"s_sleep 2\n"
"s_sub_u32 s48, s48, 1\n"
"s_cmp_lg_u32 s48, 0\n"
"s_cbranch_scc1 RLP31_%=\n"
"s_sub_u32 s51, s51, 1\n"
"s_mov_b32 s48, 1\n"
"REP31_%=:\n"
"s_branch RRP31_%=\n"
"IWB10_%=:\n"
"s_cbranch_execz IRB_%=\n"
"ALL1_%=:\n"
"v_mfma_f32_16x16x32_bf16 a[212:215], v[228:231], v[168:171], a[212:215]\n"
"s_waitcnt vmcnt(8) lgkmcnt(0)\n"
"s_barrier\n"
"v_mfma_f32_16x16x32_bf16 a[208:211], v[228:231], v[160:163], a[208:211]\n"
"s_lshl_b32 s16, s16, 2\n"
"ds_read_b128 v[208:211], v244 offset:0\n"
"v_mfma_f32_16x16x32_bf16 a[192:195], v[224:227], v[160:163], a[192:195]\n"
"s_sub_u32 s16, s18, s16\n"
"ds_read_b128 v[212:215], v244 offset:512\n"
"v_mfma_f32_16x16x32_bf16 a[224:227], v[232:235], v[160:163], a[224:227]\n"
"s_min_i32 s19, s16, 0\n"
"ds_read_b128 v[216:219], v244 offset:1024\n"
"v_mfma_f32_16x16x32_bf16 a[240:243], v[236:239], v[160:163], a[240:243]\n"
"s_ashr_i32 s16, s16, 3\n"
"v_exp_f32 v156, v156\n"
"v_mfma_f32_16x16x32_bf16 a[244:247], v[236:239], v[168:171], a[244:247]\n"
"s_min_i32 s16, s16, 32\n"
"v_exp_f32 v157, v157\n"
"v_mfma_f32_16x16x32_bf16 a[228:231], v[232:235], v[168:171], a[228:231]\n"
"s_sub_u32 s16, s16, 1\n"
"s_cmp_gt_i32 s16, 0\n"
"s_cbranch_scc1 65533\n"
"v_mfma_f32_16x16x32_bf16 a[232:235], v[232:235], v[176:179], a[232:235]\n"
"s_and_b32 s19, s19, s43\n"
"ds_read_b128 v[220:223], v244 offset:1536\n"
"v_add_f32 v243, v156, v243\n"
"v_mfma_f32_16x16x32_bf16 a[248:251], v[236:239], v[176:179], a[248:251]\n"
"s_sub_u32 s18, s18, s19\n"
"v_exp_f32 v158, v158\n"
"v_mfma_f32_16x16x32_bf16 a[252:255], v[236:239], v[184:187], a[252:255]\n"
"v_exp_f32 v159, v159\n"
"v_mfma_f32_16x16x32_bf16 a[236:239], v[232:235], v[184:187], a[236:239]\n"
"s_waitcnt lgkmcnt(2)\n"
"v_add_f32 v243, v157, v243\n"
"v_add_f32 v243, v158, v243\n"
"v_mfma_f32_16x16x32_bf16 v[160:163], v[208:211], v[0:3], v[192:195]\n"
"s_nop 0\n"
"ds_read_b128 v[224:227], v244 offset:2048\n"
"v_add_f32 v243, v159, v243\n"
"v_mfma_f32_16x16x32_bf16 v[160:163], v[212:215], v[4:7], v[160:163]\n"
"s_nop 0\n"
"v_mfma_f32_16x16x32_bf16 v[168:171], v[212:215], v[36:39], v[196:199]\n"
"v_cvt_pk_bf16_f32 v154, v156, v157\n"
"v_cvt_pk_bf16_f32 v155, v158, v159\n"
"v_mfma_f32_16x16x32_bf16 v[168:171], v[208:211], v[32:35], v[168:171]\n"
"v_mfma_f32_16x16x32_bf16 v[176:179], v[208:211], v[64:67], v[200:203]\n"
"ds_read_b128 v[228:231], v244 offset:2560\n"
"v_mfma_f32_16x16x32_bf16 v[176:179], v[212:215], v[68:71], v[176:179]\n"
"v_mfma_f32_16x16x32_bf16 v[184:187], v[212:215], v[100:103], v[204:207]\n"
"v_exp_f32 v148, v148\n"
"v_mfma_f32_16x16x32_bf16 v[184:187], v[208:211], v[96:99], v[184:187]\n"
"s_waitcnt lgkmcnt(2)\n"
"v_exp_f32 v149, v149\n"
"v_mfma_f32_16x16x32_bf16 v[184:187], v[216:219], v[104:107], v[184:187]\n"
"ds_read_b128 v[232:235], v244 offset:3072\n"
"v_add_u32 v245, s64, v251\n"
"v_mfma_f32_16x16x32_bf16 v[184:187], v[220:223], v[108:111], v[184:187]\n"
"s_nop 0\n"
"v_xor_b32 v246, 32, v245\n"
"v_add_f32 v242, v148, v242\n"
"v_mfma_f32_16x16x32_bf16 v[176:179], v[220:223], v[76:79], v[176:179]\n"
"s_nop 0\n"
"v_mfma_f32_16x16x32_bf16 v[176:179], v[216:219], v[72:75], v[176:179]\n"
"v_exp_f32 v150, v150\n"
"v_mfma_f32_16x16x32_bf16 v[168:171], v[216:219], v[40:43], v[168:171]\n"
"ds_read_b128 v[236:239], v244 offset:3584\n"
"v_add_f32 v242, v149, v242\n"
"v_mfma_f32_16x16x32_bf16 v[168:171], v[220:223], v[44:47], v[168:171]\n"
"v_exp_f32 v151, v151\n"
"v_mfma_f32_16x16x32_bf16 v[160:163], v[220:223], v[12:15], v[160:163]\n"
"v_add_f32 v242, v150, v242\n"
"v_add_f32 v242, v151, v242\n"
"v_mfma_f32_16x16x32_bf16 v[160:163], v[216:219], v[8:11], v[160:163]\n"
"s_waitcnt lgkmcnt(2)\n"
"v_cvt_pk_bf16_f32 v146, v148, v149\n"
"v_cvt_pk_bf16_f32 v147, v150, v151\n"
"v_mfma_f32_16x16x32_bf16 v[160:163], v[224:227], v[16:19], v[160:163]\n"
"ds_read_b128 v[208:211], v244 offset:8192\n"
"v_mfma_f32_16x16x32_bf16 v[160:163], v[228:231], v[20:23], v[160:163]\n"
"v_mfma_f32_16x16x32_bf16 v[168:171], v[228:231], v[52:55], v[168:171]\n"
"s_nop 0\n"
"v_mfma_f32_16x16x32_bf16 v[168:171], v[224:227], v[48:51], v[168:171]\n"
"s_nop 0\n"
"v_mfma_f32_16x16x32_bf16 v[176:179], v[224:227], v[80:83], v[176:179]\n"
"ds_read_b128 v[212:215], v244 offset:8704\n"
"v_mfma_f32_16x16x32_bf16 v[176:179], v[228:231], v[84:87], v[176:179]\n"
"v_exp_f32 v140, v140\n"
"v_mfma_f32_16x16x32_bf16 v[184:187], v[228:231], v[116:119], v[184:187]\n"
"v_exp_f32 v141, v141\n"
"v_mfma_f32_16x16x32_bf16 v[184:187], v[224:227], v[112:115], v[184:187]\n"
"s_waitcnt lgkmcnt(2)\n"
"v_exp_f32 v142, v142\n"
"v_mfma_f32_16x16x32_bf16 v[184:187], v[232:235], v[120:123], v[184:187]\n"
"ds_read_b128 v[216:219], v244 offset:9216\n"
"v_add_f32 v241, v140, v241\n"
"v_mfma_f32_16x16x32_bf16 v[184:187], v[236:239], v[124:127], v[184:187]\n"
"v_exp_f32 v143, v143\n"
"v_mfma_f32_16x16x32_bf16 v[176:179], v[236:239], v[92:95], v[176:179]\n"
"v_add_f32 v241, v141, v241\n"
"v_add_f32 v241, v142, v241\n"
"v_mfma_f32_16x16x32_bf16 v[176:179], v[232:235], v[88:91], v[176:179]\n"
"s_nop 0\n"
"v_add_f32 v241, v143, v241\n"
"v_cvt_pk_bf16_f32 v138, v140, v141\n"
"v_mfma_f32_16x16x32_bf16 v[168:171], v[232:235], v[56:59], v[168:171]\n"
"s_nop 0\n"
"v_mfma_f32_16x16x32_bf16 v[168:171], v[236:239], v[60:63], v[168:171]\n"
"ds_read_b128 v[220:223], v244 offset:9728\n"
"v_cvt_pk_bf16_f32 v139, v142, v143\n"
"v_mfma_f32_16x16x32_bf16 v[160:163], v[236:239], v[28:31], v[160:163]\n"
"v_mfma_f32_16x16x32_bf16 v[160:163], v[232:235], v[24:27], v[160:163]\n"
"s_waitcnt lgkmcnt(2)\n"
"v_mfma_f32_16x16x32_bf16 v[164:167], v[208:211], v[0:3], v[192:195]\n"
"ds_read_b128 v[224:227], v244 offset:10240\n"
"v_mfma_f32_16x16x32_bf16 v[164:167], v[212:215], v[4:7], v[164:167]\n"
"v_mfma_f32_16x16x32_bf16 v[172:175], v[212:215], v[36:39], v[196:199]\n"
"v_exp_f32 v132, v132\n"
"v_mfma_f32_16x16x32_bf16 v[172:175], v[208:211], v[32:35], v[172:175]\n"
"v_exp_f32 v133, v133\n"
"v_mfma_f32_16x16x32_bf16 v[180:183], v[208:211], v[64:67], v[200:203]\n"
"s_nop 0\n"
"ds_read_b128 v[228:231], v244 offset:10752\n"
"v_add_f32 v240, v132, v240\n"
"v_mfma_f32_16x16x32_bf16 v[180:183], v[212:215], v[68:71], v[180:183]\n"
"s_nop 0\n"
"v_mfma_f32_16x16x32_bf16 v[188:191], v[212:215], v[100:103], v[204:207]\n"
"v_exp_f32 v134, v134\n"
"v_mfma_f32_16x16x32_bf16 v[188:191], v[208:211], v[96:99], v[188:191]\n"
"s_waitcnt lgkmcnt(2)\n"
"v_exp_f32 v135, v135\n"
"v_mfma_f32_16x16x32_bf16 v[188:191], v[216:219], v[104:107], v[188:191]\n"
"ds_read_b128 v[232:235], v244 offset:11264\n"
"v_add_f32 v240, v133, v240\n"
"v_mfma_f32_16x16x32_bf16 v[188:191], v[220:223], v[108:111], v[188:191]\n"
"v_add_f32 v240, v134, v240\n"
"v_add_f32 v240, v135, v240\n"
"v_mfma_f32_16x16x32_bf16 v[180:183], v[220:223], v[76:79], v[180:183]\n"
"v_cvt_pk_bf16_f32 v130, v132, v133\n"
"v_cvt_pk_bf16_f32 v131, v134, v135\n"
"v_mfma_f32_16x16x32_bf16 v[180:183], v[216:219], v[72:75], v[180:183]\n"
"v_mfma_f32_16x16x32_bf16 v[172:175], v[216:219], v[40:43], v[172:175]\n"
"ds_read_b128 v[236:239], v244 offset:11776\n"
"v_mfma_f32_16x16x32_bf16 v[172:175], v[220:223], v[44:47], v[172:175]\n"
"s_nop 0\n"
"v_mfma_f32_16x16x32_bf16 v[164:167], v[220:223], v[12:15], v[164:167]\n"
"s_nop 0\n"
"v_mfma_f32_16x16x32_bf16 v[164:167], v[216:219], v[8:11], v[164:167]\n"
"s_waitcnt lgkmcnt(2)\n"
"v_mfma_f32_16x16x32_bf16 v[164:167], v[224:227], v[16:19], v[164:167]\n"
"ds_read_b64_tr_b16 v[208:209], v245 offset:16384\n"
"v_mfma_f32_16x16x32_bf16 v[164:167], v[228:231], v[20:23], v[164:167]\n"
"ds_read_b64_tr_b16 v[210:211], v245 offset:24576\n"
"v_mfma_f32_16x16x32_bf16 v[172:175], v[228:231], v[52:55], v[172:175]\n"
"v_mfma_f32_16x16x32_bf16 v[172:175], v[224:227], v[48:51], v[172:175]\n"
"v_mfma_f32_16x16x32_bf16 v[180:183], v[224:227], v[80:83], v[180:183]\n"
"ds_read_b64_tr_b16 v[212:213], v246 offset:16384\n"
"v_mfma_f32_16x16x32_bf16 v[180:183], v[228:231], v[84:87], v[180:183]\n"
"ds_read_b64_tr_b16 v[214:215], v246 offset:24576\n"
"v_mfma_f32_16x16x32_bf16 v[188:191], v[228:231], v[116:119], v[188:191]\n"
"s_nop 0\n"
"v_mfma_f32_16x16x32_bf16 v[188:191], v[224:227], v[112:115], v[188:191]\n"
"s_waitcnt lgkmcnt(4)\n"
"s_nop 0\n"
"v_mfma_f32_16x16x32_bf16 v[188:191], v[232:235], v[120:123], v[188:191]\n"
"ds_read_b64_tr_b16 v[216:217], v245 offset:16896\n"
"v_mfma_f32_16x16x32_bf16 v[188:191], v[236:239], v[124:127], v[188:191]\n"
"ds_read_b64_tr_b16 v[218:219], v245 offset:25088\n"
"v_mfma_f32_16x16x32_bf16 v[180:183], v[236:239], v[92:95], v[180:183]\n"
"v_mfma_f32_16x16x32_bf16 v[180:183], v[232:235], v[88:91], v[180:183]\n"
"v_mfma_f32_16x16x32_bf16 v[172:175], v[232:235], v[56:59], v[172:175]\n"
"ds_read_b64_tr_b16 v[220:221], v246 offset:16896\n"
"v_mfma_f32_16x16x32_bf16 v[172:175], v[236:239], v[60:63], v[172:175]\n"
"ds_read_b64_tr_b16 v[222:223], v246 offset:25088\n"
"v_mfma_f32_16x16x32_bf16 v[164:167], v[236:239], v[28:31], v[164:167]\n"
"v_mfma_f32_16x16x32_bf16 v[164:167], v[232:235], v[24:27], v[164:167]\n"
"s_waitcnt lgkmcnt(4)\n"
"v_mfma_f32_16x16x32_bf16 a[12:15], v[208:211], v[152:155], a[12:15]\n"
"s_nop 0\n"
"ds_read_b64_tr_b16 v[224:225], v245 offset:17408\n"
"v_mfma_f32_16x16x32_bf16 a[28:31], v[212:215], v[152:155], a[28:31]\n"
"s_nop 0\n"
"v_mfma_f32_16x16x32_bf16 a[24:27], v[212:215], v[144:147], a[24:27]\n"
"ds_read_b64_tr_b16 v[226:227], v245 offset:25600\n"
"v_mfma_f32_16x16x32_bf16 a[8:11], v[208:211], v[144:147], a[8:11]\n"
"v_exp_f32 v184, v184\n"
"v_mfma_f32_16x16x32_bf16 a[4:7], v[208:211], v[136:139], a[4:7]\n"
"ds_read_b64_tr_b16 v[228:229], v246 offset:17408\n"
"v_add_f32 v243, v184, v243\n"
"v_mfma_f32_16x16x32_bf16 a[20:23], v[212:215], v[136:139], a[20:23]\n"
"ds_read_b64_tr_b16 v[230:231], v246 offset:25600\n"
"v_mfma_f32_16x16x32_bf16 a[16:19], v[212:215], v[128:131], a[16:19]\n"
"v_exp_f32 v185, v185\n"
"v_mfma_f32_16x16x32_bf16 a[0:3], v[208:211], v[128:131], a[0:3]\n"
"s_waitcnt lgkmcnt(4)\n"
"v_exp_f32 v186, v186\n"
"v_mfma_f32_16x16x32_bf16 a[32:35], v[216:219], v[128:131], a[32:35]\n"
"s_nop 0\n"
"v_mfma_f32_16x16x32_bf16 a[48:51], v[220:223], v[128:131], a[48:51]\n"
"ds_read_b64_tr_b16 v[232:233], v245 offset:17920\n"
"v_add_f32 v243, v185, v243\n"
"v_mfma_f32_16x16x32_bf16 a[52:55], v[220:223], v[136:139], a[52:55]\n"
"ds_read_b64_tr_b16 v[234:235], v245 offset:26112\n"
"v_add_f32 v243, v186, v243\n"
"v_mfma_f32_16x16x32_bf16 a[36:39], v[216:219], v[136:139], a[36:39]\n"
"v_exp_f32 v187, v187\n"
"v_mfma_f32_16x16x32_bf16 a[40:43], v[216:219], v[144:147], a[40:43]\n"
"ds_read_b64_tr_b16 v[236:237], v246 offset:17920\n"
"v_add_f32 v243, v187, v243\n"
"v_mfma_f32_16x16x32_bf16 a[56:59], v[220:223], v[144:147], a[56:59]\n"
"ds_read_b64_tr_b16 v[238:239], v246 offset:26112\n"
"v_cvt_pk_bf16_f32 v184, v184, v185\n"
"v_mfma_f32_16x16x32_bf16 a[60:63], v[220:223], v[152:155], a[60:63]\n"
"v_exp_f32 v176, v176\n"
"v_mfma_f32_16x16x32_bf16 a[44:47], v[216:219], v[152:155], a[44:47]\n"
"s_waitcnt lgkmcnt(4)\n"
"v_exp_f32 v177, v177\n"
"v_mfma_f32_16x16x32_bf16 a[76:79], v[224:227], v[152:155], a[76:79]\n"
"ds_read_b64_tr_b16 v[208:209], v245 offset:18432\n"
"v_cvt_pk_bf16_f32 v185, v186, v187\n"
"v_mfma_f32_16x16x32_bf16 a[92:95], v[228:231], v[152:155], a[92:95]\n"
"ds_read_b64_tr_b16 v[210:211], v245 offset:26624\n"
"v_add_f32 v242, v176, v242\n"
"v_mfma_f32_16x16x32_bf16 a[88:91], v[228:231], v[144:147], a[88:91]\n"
"v_exp_f32 v178, v178\n"
"v_mfma_f32_16x16x32_bf16 a[72:75], v[224:227], v[144:147], a[72:75]\n"
"v_exp_f32 v179, v179\n"
"v_mfma_f32_16x16x32_bf16 a[68:71], v[224:227], v[136:139], a[68:71]\n"
"s_add_u32 s63, s64, 32768\n"
"ds_read_b64_tr_b16 v[212:213], v246 offset:18432\n"
"v_add_f32 v242, v177, v242\n"
"v_mfma_f32_16x16x32_bf16 a[84:87], v[228:231], v[136:139], a[84:87]\n"
"s_cmp_eq_u32 s63, 163840\n"
"ds_read_b64_tr_b16 v[214:215], v246 offset:26624\n"
"v_add_f32 v242, v178, v242\n"
"v_mfma_f32_16x16x32_bf16 a[80:83], v[228:231], v[128:131], a[80:83]\n"
"s_cselect_b32 s63, 0, s63\n"
"v_add_f32 v242, v179, v242\n"
"v_cvt_pk_bf16_f32 v176, v176, v177\n"
"v_mfma_f32_16x16x32_bf16 a[64:67], v[224:227], v[128:131], a[64:67]\n"
"s_waitcnt lgkmcnt(4)\n"
"v_add_u32 v244, s63, v250\n"
"v_cvt_pk_bf16_f32 v177, v178, v179\n"
"v_mfma_f32_16x16x32_bf16 a[96:99], v[232:235], v[128:131], a[96:99]\n"
"s_nop 0\n"
"ds_read_b64_tr_b16 v[216:217], v245 offset:18944\n"
"v_mfma_f32_16x16x32_bf16 a[112:115], v[236:239], v[128:131], a[112:115]\n"
"s_nop 0\n"
"ds_read_b64_tr_b16 v[218:219], v245 offset:27136\n"
"v_mfma_f32_16x16x32_bf16 a[116:119], v[236:239], v[136:139], a[116:119]\n"
"s_nop 0\n"
"v_mfma_f32_16x16x32_bf16 a[100:103], v[232:235], v[136:139], a[100:103]\n"
"s_nop 0\n"
"v_mfma_f32_16x16x32_bf16 a[104:107], v[232:235], v[144:147], a[104:107]\n"
"s_nop 0\n"
"ds_read_b64_tr_b16 v[220:221], v246 offset:18944\n"
"v_mfma_f32_16x16x32_bf16 a[120:123], v[236:239], v[144:147], a[120:123]\n"
"s_nop 0\n"
"ds_read_b64_tr_b16 v[222:223], v246 offset:27136\n"
"v_mfma_f32_16x16x32_bf16 a[124:127], v[236:239], v[152:155], a[124:127]\n"
"s_sub_u32 s61, s61, 1\n"
"v_exp_f32 v168, v168\n"
"v_mfma_f32_16x16x32_bf16 a[108:111], v[232:235], v[152:155], a[108:111]\n"
"s_waitcnt lgkmcnt(4)\n"
"v_exp_f32 v169, v169\n"
"v_mfma_f32_16x16x32_bf16 a[140:143], v[208:211], v[152:155], a[140:143]\n"
"s_cmp_eq_u32 s61, 0\n"
"ds_read_b64_tr_b16 v[224:225], v245 offset:19456\n"
"v_add_f32 v241, v168, v241\n"
"v_mfma_f32_16x16x32_bf16 a[156:159], v[212:215], v[152:155], a[156:159]\n"
"s_cselect_b32 s47, 1, 0\n"
"ds_read_b64_tr_b16 v[226:227], v245 offset:27648\n"
"v_add_f32 v241, v169, v241\n"
"v_mfma_f32_16x16x32_bf16 a[152:155], v[212:215], v[144:147], a[152:155]\n"
"s_cselect_b32 s61, 12, s61\n"
"v_exp_f32 v170, v170\n"
"v_mfma_f32_16x16x32_bf16 a[136:139], v[208:211], v[144:147], a[136:139]\n"
"s_cmp_lt_u32 s44, 2\n"
"v_exp_f32 v171, v171\n"
"v_mfma_f32_16x16x32_bf16 a[132:135], v[208:211], v[136:139], a[132:135]\n"
"s_cselect_b32 s47, s47, 0\n"
"ds_read_b64_tr_b16 v[228:229], v246 offset:19456\n"
"v_add_f32 v241, v170, v241\n"
"v_mfma_f32_16x16x32_bf16 a[148:151], v[212:215], v[136:139], a[148:151]\n"
"s_add_u32 s18, s18, s35\n"
"ds_read_b64_tr_b16 v[230:231], v246 offset:27648\n"
"v_add_f32 v241, v171, v241\n"
"v_mfma_f32_16x16x32_bf16 a[144:147], v[212:215], v[128:131], a[144:147]\n"
"s_add_u32 s71, s71, 1\n"
"v_cvt_pk_bf16_f32 v168, v168, v169\n"
"v_cvt_pk_bf16_f32 v169, v170, v171\n"
"v_mfma_f32_16x16x32_bf16 a[128:131], v[208:211], v[128:131], a[128:131]\n"
"s_waitcnt lgkmcnt(4)\n"
"v_mfma_f32_16x16x32_bf16 a[160:163], v[216:219], v[128:131], a[160:163]\n"
"s_memrealtime s[16:17]\n"
"ds_read_b64_tr_b16 v[232:233], v245 offset:19968\n"
"v_mfma_f32_16x16x32_bf16 a[176:179], v[220:223], v[128:131], a[176:179]\n"
"ds_read_b64_tr_b16 v[234:235], v245 offset:28160\n"
"v_mfma_f32_16x16x32_bf16 a[180:183], v[220:223], v[136:139], a[180:183]\n"
"v_exp_f32 v160, v160\n"
"v_mfma_f32_16x16x32_bf16 a[164:167], v[216:219], v[136:139], a[164:167]\n"
"v_exp_f32 v161, v161\n"
"v_mfma_f32_16x16x32_bf16 a[168:171], v[216:219], v[144:147], a[168:171]\n"
"ds_read_b64_tr_b16 v[236:237], v246 offset:19968\n"
"v_add_f32 v240, v160, v240\n"
"v_mfma_f32_16x16x32_bf16 a[184:187], v[220:223], v[144:147], a[184:187]\n"
"ds_read_b64_tr_b16 v[238:239], v246 offset:28160\n"
"v_add_f32 v240, v161, v240\n"
"v_mfma_f32_16x16x32_bf16 a[188:191], v[220:223], v[152:155], a[188:191]\n"
"v_exp_f32 v162, v162\n"
"v_mfma_f32_16x16x32_bf16 a[172:175], v[216:219], v[152:155], a[172:175]\n"
"s_waitcnt lgkmcnt(4)\n"
"v_exp_f32 v163, v163\n"
"v_mfma_f32_16x16x32_bf16 a[204:207], v[224:227], v[152:155], a[204:207]\n"
"v_add_f32 v240, v162, v240\n"
"v_add_f32 v240, v163, v240\n"
"v_mfma_f32_16x16x32_bf16 a[220:223], v[228:231], v[152:155], a[220:223]\n"
"s_getreg_b32 s46, hwreg(HW_REG_IB_STS, 0, 4)\n"
"v_cvt_pk_bf16_f32 v160, v160, v161\n"
"v_cvt_pk_bf16_f32 v161, v162, v163\n"
"v_mfma_f32_16x16x32_bf16 a[216:219], v[228:231], v[144:147], a[216:219]\n"
"s_cmp_ge_u32 s46, 8\n"
"v_mfma_f32_16x16x32_bf16 a[200:203], v[224:227], v[144:147], a[200:203]\n"
"s_cselect_b32 s49, 1, s47\n"
"v_mfma_f32_16x16x32_bf16 a[196:199], v[224:227], v[136:139], a[196:199]\n"
"s_or_b32 s48, s48, s49\n"
"s_cbranch_scc1 RSL0_%=\n"
"s_branch RRL0_%=\n"
"IWB11_%=:\n"
"s_cbranch_execz IRB_%=\n"
"ALL0_%=:\n"
"v_mfma_f32_16x16x32_bf16 a[212:215], v[228:231], v[136:139], a[212:215]\n"
"s_waitcnt vmcnt(8) lgkmcnt(0)\n"
"s_barrier\n"
"v_mfma_f32_16x16x32_bf16 a[208:211], v[228:231], v[128:131], a[208:211]\n"
"s_lshl_b32 s16, s16, 2\n"
"ds_read_b128 v[208:211], v244 offset:0\n"
"v_mfma_f32_16x16x32_bf16 a[192:195], v[224:227], v[128:131], a[192:195]\n"
"s_sub_u32 s16, s18, s16\n"
"ds_read_b128 v[212:215], v244 offset:512\n"
"v_mfma_f32_16x16x32_bf16 a[224:227], v[232:235], v[128:131], a[224:227]\n"
"s_min_i32 s19, s16, 0\n"
"ds_read_b128 v[216:219], v244 offset:1024\n"
"v_mfma_f32_16x16x32_bf16 a[240:243], v[236:239], v[128:131], a[240:243]\n"
"s_ashr_i32 s16, s16, 3\n"
"v_exp_f32 v188, v188\n"
"v_mfma_f32_16x16x32_bf16 a[244:247], v[236:239], v[136:139], a[244:247]\n"
"s_min_i32 s16, s16, 32\n"
"v_exp_f32 v189, v189\n"
"v_mfma_f32_16x16x32_bf16 a[228:231], v[232:235], v[136:139], a[228:231]\n"
"s_sub_u32 s16, s16, 1\n"
"s_cmp_gt_i32 s16, 0\n"
"s_cbranch_scc1 65533\n"
"v_mfma_f32_16x16x32_bf16 a[232:235], v[232:235], v[144:147], a[232:235]\n"
"s_and_b32 s19, s19, s43\n"
"ds_read_b128 v[220:223], v244 offset:1536\n"
"v_add_f32 v243, v188, v243\n"
"v_mfma_f32_16x16x32_bf16 a[248:251], v[236:239], v[144:147], a[248:251]\n"
"s_sub_u32 s18, s18, s19\n"
"v_exp_f32 v190, v190\n"
"v_mfma_f32_16x16x32_bf16 a[252:255], v[236:239], v[152:155], a[252:255]\n"
"v_exp_f32 v191, v191\n"
"v_mfma_f32_16x16x32_bf16 a[236:239], v[232:235], v[152:155], a[236:239]\n"
"s_waitcnt lgkmcnt(2)\n"
"v_add_f32 v243, v189, v243\n"
"v_add_f32 v243, v190, v243\n"
"v_mfma_f32_16x16x32_bf16 v[128:131], v[208:211], v[0:3], v[192:195]\n"
"s_nop 1\n"
"v_mfma_f32_16x16x32_bf16 v[128:131], v[212:215], v[4:7], v[128:131]\n"
"s_nop 0\n"
"ds_read_b128 v[224:227], v244 offset:2048\n"
"v_add_f32 v243, v191, v243\n"
"v_mfma_f32_16x16x32_bf16 v[136:139], v[212:215], v[36:39], v[196:199]\n"
"s_nop 0\n"
"v_mfma_f32_16x16x32_bf16 v[136:139], v[208:211], v[32:35], v[136:139]\n"
"v_cvt_pk_bf16_f32 v186, v188, v189\n"
"v_cvt_pk_bf16_f32 v187, v190, v191\n"
"v_mfma_f32_16x16x32_bf16 v[144:147], v[208:211], v[64:67], v[200:203]\n"
"ds_read_b128 v[228:231], v244 offset:2560\n"
"v_mfma_f32_16x16x32_bf16 v[144:147], v[212:215], v[68:71], v[144:147]\n"
"v_mfma_f32_16x16x32_bf16 v[152:155], v[212:215], v[100:103], v[204:207]\n"
"v_exp_f32 v180, v180\n"
"v_mfma_f32_16x16x32_bf16 v[152:155], v[208:211], v[96:99], v[152:155]\n"
"s_waitcnt lgkmcnt(2)\n"
"v_exp_f32 v181, v181\n"
"v_mfma_f32_16x16x32_bf16 v[152:155], v[216:219], v[104:107], v[152:155]\n"
"ds_read_b128 v[232:235], v244 offset:3072\n"
"v_add_u32 v245, s63, v251\n"
"v_mfma_f32_16x16x32_bf16 v[152:155], v[220:223], v[108:111], v[152:155]\n"
"v_xor_b32 v246, 32, v245\n"
"v_add_f32 v242, v180, v242\n"
"v_mfma_f32_16x16x32_bf16 v[144:147], v[220:223], v[76:79], v[144:147]\n"
"s_nop 0\n"
"v_exp_f32 v182, v182\n"
"v_mfma_f32_16x16x32_bf16 v[144:147], v[216:219], v[72:75], v[144:147]\n"
"s_nop 0\n"
"v_mfma_f32_16x16x32_bf16 v[136:139], v[216:219], v[40:43], v[136:139]\n"
"ds_read_b128 v[236:239], v244 offset:3584\n"
"v_add_f32 v242, v181, v242\n"
"v_mfma_f32_16x16x32_bf16 v[136:139], v[220:223], v[44:47], v[136:139]\n"
"v_exp_f32 v183, v183\n"
"v_mfma_f32_16x16x32_bf16 v[128:131], v[220:223], v[12:15], v[128:131]\n"
"v_add_f32 v242, v182, v242\n"
"v_add_f32 v242, v183, v242\n"
"v_mfma_f32_16x16x32_bf16 v[128:131], v[216:219], v[8:11], v[128:131]\n"
"s_waitcnt lgkmcnt(2)\n"
"v_cvt_pk_bf16_f32 v178, v180, v181\n"
"v_cvt_pk_bf16_f32 v179, v182, v183\n"
"v_mfma_f32_16x16x32_bf16 v[128:131], v[224:227], v[16:19], v[128:131]\n"
"ds_read_b128 v[208:211], v244 offset:8192\n"
"v_mfma_f32_16x16x32_bf16 v[128:131], v[228:231], v[20:23], v[128:131]\n"
"v_mfma_f32_16x16x32_bf16 v[136:139], v[228:231], v[52:55], v[136:139]\n"
"v_mfma_f32_16x16x32_bf16 v[136:139], v[224:227], v[48:51], v[136:139]\n"
"v_mfma_f32_16x16x32_bf16 v[144:147], v[224:227], v[80:83], v[144:147]\n"
"s_nop 0\n"
"ds_read_b128 v[212:215], v244 offset:8704\n"
"v_mfma_f32_16x16x32_bf16 v[144:147], v[228:231], v[84:87], v[144:147]\n"
"s_nop 0\n"
"v_mfma_f32_16x16x32_bf16 v[152:155], v[228:231], v[116:119], v[152:155]\n"
"v_exp_f32 v172, v172\n"
"v_mfma_f32_16x16x32_bf16 v[152:155], v[224:227], v[112:115], v[152:155]\n"
"s_waitcnt lgkmcnt(2)\n"
"v_exp_f32 v173, v173\n"
"v_mfma_f32_16x16x32_bf16 v[152:155], v[232:235], v[120:123], v[152:155]\n"
"ds_read_b128 v[216:219], v244 offset:9216\n"
"v_add_f32 v241, v172, v241\n"
"v_mfma_f32_16x16x32_bf16 v[152:155], v[236:239], v[124:127], v[152:155]\n"
"v_exp_f32 v174, v174\n"
"v_mfma_f32_16x16x32_bf16 v[144:147], v[236:239], v[92:95], v[144:147]\n"
"v_exp_f32 v175, v175\n"
"v_mfma_f32_16x16x32_bf16 v[144:147], v[232:235], v[88:91], v[144:147]\n"
"v_add_f32 v241, v173, v241\n"
"v_add_f32 v241, v174, v241\n"
"v_mfma_f32_16x16x32_bf16 v[136:139], v[232:235], v[56:59], v[136:139]\n"
"ds_read_b128 v[220:223], v244 offset:9728\n"
"v_add_f32 v241, v175, v241\n"
"v_mfma_f32_16x16x32_bf16 v[136:139], v[236:239], v[60:63], v[136:139]\n"
"v_cvt_pk_bf16_f32 v170, v172, v173\n"
"v_cvt_pk_bf16_f32 v171, v174, v175\n"
"v_mfma_f32_16x16x32_bf16 v[128:131], v[236:239], v[28:31], v[128:131]\n"
"s_nop 0\n"
"v_mfma_f32_16x16x32_bf16 v[128:131], v[232:235], v[24:27], v[128:131]\n"
"s_waitcnt lgkmcnt(2)\n"
"s_nop 0\n"
"v_mfma_f32_16x16x32_bf16 v[132:135], v[208:211], v[0:3], v[192:195]\n"
"ds_read_b128 v[224:227], v244 offset:10240\n"
"v_mfma_f32_16x16x32_bf16 v[132:135], v[212:215], v[4:7], v[132:135]\n"
"v_mfma_f32_16x16x32_bf16 v[140:143], v[212:215], v[36:39], v[196:199]\n"
"v_exp_f32 v164, v164\n"
"v_mfma_f32_16x16x32_bf16 v[140:143], v[208:211], v[32:35], v[140:143]\n"
"v_exp_f32 v165, v165\n"
"v_mfma_f32_16x16x32_bf16 v[148:151], v[208:211], v[64:67], v[200:203]\n"
"ds_read_b128 v[228:231], v244 offset:10752\n"
"v_add_f32 v240, v164, v240\n"
"v_mfma_f32_16x16x32_bf16 v[148:151], v[212:215], v[68:71], v[148:151]\n"
"v_exp_f32 v166, v166\n"
"v_mfma_f32_16x16x32_bf16 v[156:159], v[212:215], v[100:103], v[204:207]\n"
"v_exp_f32 v167, v167\n"
"v_mfma_f32_16x16x32_bf16 v[156:159], v[208:211], v[96:99], v[156:159]\n"
"s_waitcnt lgkmcnt(2)\n"
"v_add_f32 v240, v165, v240\n"
"v_add_f32 v240, v166, v240\n"
"v_mfma_f32_16x16x32_bf16 v[156:159], v[216:219], v[104:107], v[156:159]\n"
"ds_read_b128 v[232:235], v244 offset:11264\n"
"v_add_f32 v240, v167, v240\n"
"v_mfma_f32_16x16x32_bf16 v[156:159], v[220:223], v[108:111], v[156:159]\n"
"s_nop 0\n"
"v_cvt_pk_bf16_f32 v162, v164, v165\n"
"v_cvt_pk_bf16_f32 v163, v166, v167\n"
"v_mfma_f32_16x16x32_bf16 v[148:151], v[220:223], v[76:79], v[148:151]\n"
"s_nop 0\n"
"v_mfma_f32_16x16x32_bf16 v[148:151], v[216:219], v[72:75], v[148:151]\n"
"v_mfma_f32_16x16x32_bf16 v[140:143], v[216:219], v[40:43], v[140:143]\n"
"ds_read_b128 v[236:239], v244 offset:11776\n"
"v_mfma_f32_16x16x32_bf16 v[140:143], v[220:223], v[44:47], v[140:143]\n"
"v_mfma_f32_16x16x32_bf16 v[132:135], v[220:223], v[12:15], v[132:135]\n"
"v_mfma_f32_16x16x32_bf16 v[132:135], v[216:219], v[8:11], v[132:135]\n"
"s_waitcnt lgkmcnt(2)\n"
"v_mfma_f32_16x16x32_bf16 v[132:135], v[224:227], v[16:19], v[132:135]\n"
"ds_read_b64_tr_b16 v[208:209], v245 offset:16384\n"
"v_mfma_f32_16x16x32_bf16 v[132:135], v[228:231], v[20:23], v[132:135]\n"
"ds_read_b64_tr_b16 v[210:211], v245 offset:24576\n"
"v_mfma_f32_16x16x32_bf16 v[140:143], v[228:231], v[52:55], v[140:143]\n"
"v_mfma_f32_16x16x32_bf16 v[140:143], v[224:227], v[48:51], v[140:143]\n"
"s_nop 0\n"
"v_mfma_f32_16x16x32_bf16 v[148:151], v[224:227], v[80:83], v[148:151]\n"
"s_nop 0\n"
"v_mfma_f32_16x16x32_bf16 v[148:151], v[228:231], v[84:87], v[148:151]\n"
"ds_read_b64_tr_b16 v[212:213], v246 offset:16384\n"
"v_mfma_f32_16x16x32_bf16 v[156:159], v[228:231], v[116:119], v[156:159]\n"
"ds_read_b64_tr_b16 v[214:215], v246 offset:24576\n"
"v_mfma_f32_16x16x32_bf16 v[156:159], v[224:227], v[112:115], v[156:159]\n"
"s_waitcnt lgkmcnt(4)\n"
"v_mfma_f32_16x16x32_bf16 v[156:159], v[232:235], v[120:123], v[156:159]\n"
"ds_read_b64_tr_b16 v[216:217], v245 offset:16896\n"
"v_mfma_f32_16x16x32_bf16 v[156:159], v[236:239], v[124:127], v[156:159]\n"
"ds_read_b64_tr_b16 v[218:219], v245 offset:25088\n"
"v_mfma_f32_16x16x32_bf16 v[148:151], v[236:239], v[92:95], v[148:151]\n"
"v_mfma_f32_16x16x32_bf16 v[148:151], v[232:235], v[88:91], v[148:151]\n"
"v_mfma_f32_16x16x32_bf16 v[140:143], v[232:235], v[56:59], v[140:143]\n"
"ds_read_b64_tr_b16 v[220:221], v246 offset:16896\n"
"v_mfma_f32_16x16x32_bf16 v[140:143], v[236:239], v[60:63], v[140:143]\n"
"s_nop 0\n"
"ds_read_b64_tr_b16 v[222:223], v246 offset:25088\n"
"v_mfma_f32_16x16x32_bf16 v[132:135], v[236:239], v[28:31], v[132:135]\n"
"s_nop 0\n"
"v_mfma_f32_16x16x32_bf16 v[132:135], v[232:235], v[24:27], v[132:135]\n"
"s_waitcnt lgkmcnt(4)\n"
"v_mfma_f32_16x16x32_bf16 a[12:15], v[208:211], v[184:187], a[12:15]\n"
"ds_read_b64_tr_b16 v[224:225], v245 offset:17408\n"
"v_mfma_f32_16x16x32_bf16 a[28:31], v[212:215], v[184:187], a[28:31]\n"
"ds_read_b64_tr_b16 v[226:227], v245 offset:25600\n"
"v_mfma_f32_16x16x32_bf16 a[24:27], v[212:215], v[176:179], a[24:27]\n"
"v_exp_f32 v152, v152\n"
"v_mfma_f32_16x16x32_bf16 a[8:11], v[208:211], v[176:179], a[8:11]\n"
"v_exp_f32 v153, v153\n"
"v_mfma_f32_16x16x32_bf16 a[4:7], v[208:211], v[168:171], a[4:7]\n"
"ds_read_b64_tr_b16 v[228:229], v246 offset:17408\n"
"v_add_f32 v243, v152, v243\n"
"v_mfma_f32_16x16x32_bf16 a[20:23], v[212:215], v[168:171], a[20:23]\n"
"ds_read_b64_tr_b16 v[230:231], v246 offset:25600\n"
"v_add_f32 v243, v153, v243\n"
"v_mfma_f32_16x16x32_bf16 a[16:19], v[212:215], v[160:163], a[16:19]\n"
"v_exp_f32 v154, v154\n"
"v_mfma_f32_16x16x32_bf16 a[0:3], v[208:211], v[160:163], a[0:3]\n"
"s_waitcnt lgkmcnt(4)\n"
"v_exp_f32 v155, v155\n"
"v_mfma_f32_16x16x32_bf16 a[32:35], v[216:219], v[160:163], a[32:35]\n"
"s_nop 0\n"
"ds_read_b64_tr_b16 v[232:233], v245 offset:17920\n"
"v_add_f32 v243, v154, v243\n"
"v_mfma_f32_16x16x32_bf16 a[48:51], v[220:223], v[160:163], a[48:51]\n"
"s_nop 0\n"
"v_mfma_f32_16x16x32_bf16 a[52:55], v[220:223], v[168:171], a[52:55]\n"
"ds_read_b64_tr_b16 v[234:235], v245 offset:26112\n"
"v_add_f32 v243, v155, v243\n"
"v_mfma_f32_16x16x32_bf16 a[36:39], v[216:219], v[168:171], a[36:39]\n"
"v_cvt_pk_bf16_f32 v152, v152, v153\n"
"v_cvt_pk_bf16_f32 v153, v154, v155\n"
"v_mfma_f32_16x16x32_bf16 a[40:43], v[216:219], v[176:179], a[40:43]\n"
"ds_read_b64_tr_b16 v[236:237], v246 offset:17920\n"
"v_mfma_f32_16x16x32_bf16 a[56:59], v[220:223], v[176:179], a[56:59]\n"
"ds_read_b64_tr_b16 v[238:239], v246 offset:26112\n"
"v_mfma_f32_16x16x32_bf16 a[60:63], v[220:223], v[184:187], a[60:63]\n"
"v_exp_f32 v144, v144\n"
"v_mfma_f32_16x16x32_bf16 a[44:47], v[216:219], v[184:187], a[44:47]\n"
"s_waitcnt lgkmcnt(4)\n"
"v_exp_f32 v145, v145\n"
"v_mfma_f32_16x16x32_bf16 a[76:79], v[224:227], v[184:187], a[76:79]\n"
"ds_read_b64_tr_b16 v[208:209], v245 offset:18432\n"
"v_add_f32 v242, v144, v242\n"
"v_mfma_f32_16x16x32_bf16 a[92:95], v[228:231], v[184:187], a[92:95]\n"
"ds_read_b64_tr_b16 v[210:211], v245 offset:26624\n"
"v_add_f32 v242, v145, v242\n"
"v_mfma_f32_16x16x32_bf16 a[88:91], v[228:231], v[176:179], a[88:91]\n"
"v_exp_f32 v146, v146\n"
"v_mfma_f32_16x16x32_bf16 a[72:75], v[224:227], v[176:179], a[72:75]\n"
"v_exp_f32 v147, v147\n"
"v_mfma_f32_16x16x32_bf16 a[68:71], v[224:227], v[168:171], a[68:71]\n"
"s_add_u32 s64, s63, 32768\n"
"ds_read_b64_tr_b16 v[212:213], v246 offset:18432\n"
"v_add_f32 v242, v146, v242\n"
"v_mfma_f32_16x16x32_bf16 a[84:87], v[228:231], v[168:171], a[84:87]\n"
"s_cmp_eq_u32 s64, 163840\n"
"ds_read_b64_tr_b16 v[214:215], v246 offset:26624\n"
"v_add_f32 v242, v147, v242\n"
"v_mfma_f32_16x16x32_bf16 a[80:83], v[228:231], v[160:163], a[80:83]\n"
"s_cselect_b32 s64, 0, s64\n"
"v_cvt_pk_bf16_f32 v144, v144, v145\n"
"v_cvt_pk_bf16_f32 v145, v146, v147\n"
"v_mfma_f32_16x16x32_bf16 a[64:67], v[224:227], v[160:163], a[64:67]\n"
"s_waitcnt lgkmcnt(4)\n"
"v_add_u32 v244, s64, v250\n"
"v_mfma_f32_16x16x32_bf16 a[96:99], v[232:235], v[160:163], a[96:99]\n"
"s_nop 0\n"
"ds_read_b64_tr_b16 v[216:217], v245 offset:18944\n"
"v_mfma_f32_16x16x32_bf16 a[112:115], v[236:239], v[160:163], a[112:115]\n"
"s_nop 0\n"
"ds_read_b64_tr_b16 v[218:219], v245 offset:27136\n"
"v_mfma_f32_16x16x32_bf16 a[116:119], v[236:239], v[168:171], a[116:119]\n"
"s_nop 0\n"
"v_mfma_f32_16x16x32_bf16 a[100:103], v[232:235], v[168:171], a[100:103]\n"
"s_nop 0\n"
"v_mfma_f32_16x16x32_bf16 a[104:107], v[232:235], v[176:179], a[104:107]\n"
"s_nop 0\n"
"ds_read_b64_tr_b16 v[220:221], v246 offset:18944\n"
"v_mfma_f32_16x16x32_bf16 a[120:123], v[236:239], v[176:179], a[120:123]\n"
"s_nop 0\n"
"ds_read_b64_tr_b16 v[222:223], v246 offset:27136\n"
"v_mfma_f32_16x16x32_bf16 a[124:127], v[236:239], v[184:187], a[124:127]\n"
"s_sub_u32 s61, s61, 1\n"
"v_exp_f32 v136, v136\n"
"v_mfma_f32_16x16x32_bf16 a[108:111], v[232:235], v[184:187], a[108:111]\n"
"s_waitcnt lgkmcnt(4)\n"
"v_exp_f32 v137, v137\n"
"v_mfma_f32_16x16x32_bf16 a[140:143], v[208:211], v[184:187], a[140:143]\n"
"s_cmp_eq_u32 s61, 0\n"
"ds_read_b64_tr_b16 v[224:225], v245 offset:19456\n"
"v_add_f32 v241, v136, v241\n"
"v_mfma_f32_16x16x32_bf16 a[156:159], v[212:215], v[184:187], a[156:159]\n"
"s_cselect_b32 s47, 1, 0\n"
"ds_read_b64_tr_b16 v[226:227], v245 offset:27648\n"
"v_add_f32 v241, v137, v241\n"
"v_mfma_f32_16x16x32_bf16 a[152:155], v[212:215], v[176:179], a[152:155]\n"
"s_cselect_b32 s61, 12, s61\n"
"v_exp_f32 v138, v138\n"
"v_mfma_f32_16x16x32_bf16 a[136:139], v[208:211], v[176:179], a[136:139]\n"
"s_cmp_lt_u32 s44, 2\n"
"v_exp_f32 v139, v139\n"
"v_mfma_f32_16x16x32_bf16 a[132:135], v[208:211], v[168:171], a[132:135]\n"
"s_cselect_b32 s47, s47, 0\n"
"ds_read_b64_tr_b16 v[228:229], v246 offset:19456\n"
"v_add_f32 v241, v138, v241\n"
"v_mfma_f32_16x16x32_bf16 a[148:151], v[212:215], v[168:171], a[148:151]\n"
"s_add_u32 s18, s18, s35\n"
"ds_read_b64_tr_b16 v[230:231], v246 offset:27648\n"
"v_add_f32 v241, v139, v241\n"
"v_mfma_f32_16x16x32_bf16 a[144:147], v[212:215], v[160:163], a[144:147]\n"
"v_cvt_pk_bf16_f32 v136, v136, v137\n"
"v_cvt_pk_bf16_f32 v137, v138, v139\n"
"v_mfma_f32_16x16x32_bf16 a[128:131], v[208:211], v[160:163], a[128:131]\n"
"s_waitcnt lgkmcnt(4)\n"
"v_mfma_f32_16x16x32_bf16 a[160:163], v[216:219], v[160:163], a[160:163]\n"
"s_memrealtime s[16:17]\n"
"ds_read_b64_tr_b16 v[232:233], v245 offset:19968\n"
"v_mfma_f32_16x16x32_bf16 a[176:179], v[220:223], v[160:163], a[176:179]\n"
"ds_read_b64_tr_b16 v[234:235], v245 offset:28160\n"
"v_mfma_f32_16x16x32_bf16 a[180:183], v[220:223], v[168:171], a[180:183]\n"
"v_exp_f32 v128, v128\n"
"v_mfma_f32_16x16x32_bf16 a[164:167], v[216:219], v[168:171], a[164:167]\n"
"v_exp_f32 v129, v129\n"
"v_mfma_f32_16x16x32_bf16 a[168:171], v[216:219], v[176:179], a[168:171]\n"
"ds_read_b64_tr_b16 v[236:237], v246 offset:19968\n"
"v_add_f32 v240, v128, v240\n"
"v_mfma_f32_16x16x32_bf16 a[184:187], v[220:223], v[176:179], a[184:187]\n"
"ds_read_b64_tr_b16 v[238:239], v246 offset:28160\n"
"v_add_f32 v240, v129, v240\n"
"v_mfma_f32_16x16x32_bf16 a[188:191], v[220:223], v[184:187], a[188:191]\n"
"v_exp_f32 v130, v130\n"
"v_mfma_f32_16x16x32_bf16 a[172:175], v[216:219], v[184:187], a[172:175]\n"
"s_waitcnt lgkmcnt(4)\n"
"v_exp_f32 v131, v131\n"
"v_mfma_f32_16x16x32_bf16 a[204:207], v[224:227], v[184:187], a[204:207]\n"
"v_add_f32 v240, v130, v240\n"
"v_add_f32 v240, v131, v240\n"
"v_mfma_f32_16x16x32_bf16 a[220:223], v[228:231], v[184:187], a[220:223]\n"
"s_getreg_b32 s46, hwreg(HW_REG_IB_STS, 0, 4)\n"
"v_cvt_pk_bf16_f32 v128, v128, v129\n"
"v_cvt_pk_bf16_f32 v129, v130, v131\n"
"v_mfma_f32_16x16x32_bf16 a[216:219], v[228:231], v[176:179], a[216:219]\n"
"s_cmp_ge_u32 s46, 8\n"
"v_mfma_f32_16x16x32_bf16 a[200:203], v[224:227], v[176:179], a[200:203]\n"
"s_cselect_b32 s49, 1, s47\n"
"v_mfma_f32_16x16x32_bf16 a[196:199], v[224:227], v[168:171], a[196:199]\n"
"s_or_b32 s48, s48, s49\n"
"s_cbranch_scc1 RSL1_%=\n"
"s_branch RRL1_%=\n"
"CE_%=:\n"
"SP0_%=:\n"
"s_mov_b32 s66, 0\n"
"SL0_%=:\n"
"buffer_load_dword v240, v250, s[28:31], 0 offen offset:128 sc1\n"
"s_add_u32 s66, s66, 1\n"
"s_waitcnt vmcnt(0)\n"
"v_cmp_ne_u32 vcc, v240, v244\n"
"s_nop 4\n"
"s_cbranch_vccz SR0_%=\n"
"s_sleep 0\n"
"s_cmp_lt_u32 s66, 1048576\n"
"s_cbranch_scc1 SL0_%=\n"
"s_branch SR0_%=\n"
"SP1_%=:\n"
"s_mov_b32 s66, 0\n"
"SL1_%=:\n"
"buffer_load_dword v240, v250, s[28:31], 0 offen offset:132 sc1\n"
"s_add_u32 s66, s66, 1\n"
"s_waitcnt vmcnt(0)\n"
"v_cmp_ne_u32 vcc, v240, v245\n"
"s_nop 4\n"
"s_cbranch_vccz SR1_%=\n"
"s_sleep 0\n"
"s_cmp_lt_u32 s66, 1048576\n"
"s_cbranch_scc1 SL1_%=\n"
"s_branch SR1_%=\n"
"SP2_%=:\n"
"s_mov_b32 s66, 0\n"
"SL2_%=:\n"
"buffer_load_dword v240, v250, s[28:31], 0 offen offset:136 sc1\n"
"s_add_u32 s66, s66, 1\n"
"s_waitcnt vmcnt(0)\n"
"v_cmp_ne_u32 vcc, v240, v246\n"
"s_nop 4\n"
"s_cbranch_vccz SR2_%=\n"
"s_sleep 0\n"
"s_cmp_lt_u32 s66, 1048576\n"
"s_cbranch_scc1 SL2_%=\n"
"s_branch SR2_%=\n"
"SP3_%=:\n"
"s_mov_b32 s66, 0\n"
"SL3_%=:\n"
"buffer_load_dword v240, v250, s[28:31], 0 offen offset:140 sc1\n"
"s_add_u32 s66, s66, 1\n"
"s_waitcnt vmcnt(0)\n"
"v_cmp_ne_u32 vcc, v240, v247\n"
"s_nop 4\n"
"s_cbranch_vccz SR3_%=\n"
"s_sleep 0\n"
"s_cmp_lt_u32 s66, 1048576\n"
"s_cbranch_scc1 SL3_%=\n"
"s_branch SR3_%=\n"
"FB_%=:\n"
"s_waitcnt vmcnt(0) lgkmcnt(0)\n"
"s_nop 15\n"
"v_mov_b32 v240, 0xff800000\n"
"v_mov_b32 v241, 0xff800000\n"
"v_mov_b32 v242, 0xff800000\n"
"v_mov_b32 v243, 0xff800000\n"
"s_mov_b32 s62, 0\n"
"FBL_%=:\n"
"s_barrier\n"
"s_mov_b32 m0, s52\n"
"s_nop 0\n"
"buffer_load_dwordx4 v247, s[8:11], s62 offen lds\n"
"s_mov_b32 m0, s53\n"
"s_nop 0\n"
"buffer_load_dwordx4 v247, s[8:11], s62 offen offset:128 lds\n"
"s_mov_b32 m0, s54\n"
"s_nop 0\n"
"buffer_load_dwordx4 v247, s[8:11], s62 offen offset:256 lds\n"
"s_mov_b32 m0, s55\n"
"s_nop 0\n"
"buffer_load_dwordx4 v247, s[8:11], s62 offen offset:384 lds\n"
"s_waitcnt vmcnt(0)\n"
"s_barrier\n"
"ds_read_b128 v[208:211], v250 offset:0\n"
"ds_read_b128 v[212:215], v250 offset:512\n"
"ds_read_b128 v[216:219], v250 offset:1024\n"
"ds_read_b128 v[220:223], v250 offset:1536\n"
"ds_read_b128 v[224:227], v250 offset:2048\n"
"ds_read_b128 v[228:231], v250 offset:2560\n"
"ds_read_b128 v[232:235], v250 offset:3072\n"
"ds_read_b128 v[236:239], v250 offset:3584\n"
"s_waitcnt lgkmcnt(0)\n"
"v_mfma_f32_16x16x32_bf16 v[128:131], v[208:211], v[0:3], 0\n"
"v_mfma_f32_16x16x32_bf16 v[136:139], v[208:211], v[32:35], 0\n"
"v_mfma_f32_16x16x32_bf16 v[144:147], v[208:211], v[64:67], 0\n"
"v_mfma_f32_16x16x32_bf16 v[152:155], v[208:211], v[96:99], 0\n"
"v_mfma_f32_16x16x32_bf16 v[128:131], v[212:215], v[4:7], v[128:131]\n"
"v_mfma_f32_16x16x32_bf16 v[136:139], v[212:215], v[36:39], v[136:139]\n"
"v_mfma_f32_16x16x32_bf16 v[144:147], v[212:215], v[68:71], v[144:147]\n"
"v_mfma_f32_16x16x32_bf16 v[152:155], v[212:215], v[100:103], v[152:155]\n"
"v_mfma_f32_16x16x32_bf16 v[128:131], v[216:219], v[8:11], v[128:131]\n"
"v_mfma_f32_16x16x32_bf16 v[136:139], v[216:219], v[40:43], v[136:139]\n"
"v_mfma_f32_16x16x32_bf16 v[144:147], v[216:219], v[72:75], v[144:147]\n"
"v_mfma_f32_16x16x32_bf16 v[152:155], v[216:219], v[104:107], v[152:155]\n"
"v_mfma_f32_16x16x32_bf16 v[128:131], v[220:223], v[12:15], v[128:131]\n"
"v_mfma_f32_16x16x32_bf16 v[136:139], v[220:223], v[44:47], v[136:139]\n"
"v_mfma_f32_16x16x32_bf16 v[144:147], v[220:223], v[76:79], v[144:147]\n"
"v_mfma_f32_16x16x32_bf16 v[152:155], v[220:223], v[108:111], v[152:155]\n"
"v_mfma_f32_16x16x32_bf16 v[128:131], v[224:227], v[16:19], v[128:131]\n"
"v_mfma_f32_16x16x32_bf16 v[136:139], v[224:227], v[48:51], v[136:139]\n"
"v_mfma_f32_16x16x32_bf16 v[144:147], v[224:227], v[80:83], v[144:147]\n"
"v_mfma_f32_16x16x32_bf16 v[152:155], v[224:227], v[112:115], v[152:155]\n"
"v_mfma_f32_16x16x32_bf16 v[128:131], v[228:231], v[20:23], v[128:131]\n"
"v_mfma_f32_16x16x32_bf16 v[136:139], v[228:231], v[52:55], v[136:139]\n"
"v_mfma_f32_16x16x32_bf16 v[144:147], v[228:231], v[84:87], v[144:147]\n"
"v_mfma_f32_16x16x32_bf16 v[152:155], v[228:231], v[116:119], v[152:155]\n"
"v_mfma_f32_16x16x32_bf16 v[128:131], v[232:235], v[24:27], v[128:131]\n"
"v_mfma_f32_16x16x32_bf16 v[136:139], v[232:235], v[56:59], v[136:139]\n"
"v_mfma_f32_16x16x32_bf16 v[144:147], v[232:235], v[88:91], v[144:147]\n"
"v_mfma_f32_16x16x32_bf16 v[152:155], v[232:235], v[120:123], v[152:155]\n"
"v_mfma_f32_16x16x32_bf16 v[128:131], v[236:239], v[28:31], v[128:131]\n"
"v_mfma_f32_16x16x32_bf16 v[136:139], v[236:239], v[60:63], v[136:139]\n"
"v_mfma_f32_16x16x32_bf16 v[144:147], v[236:239], v[92:95], v[144:147]\n"
"v_mfma_f32_16x16x32_bf16 v[152:155], v[236:239], v[124:127], v[152:155]\n"
"ds_read_b128 v[208:211], v250 offset:8192\n"
"ds_read_b128 v[212:215], v250 offset:8704\n"
"ds_read_b128 v[216:219], v250 offset:9216\n"
"ds_read_b128 v[220:223], v250 offset:9728\n"
"ds_read_b128 v[224:227], v250 offset:10240\n"
"ds_read_b128 v[228:231], v250 offset:10752\n"
"ds_read_b128 v[232:235], v250 offset:11264\n"
"ds_read_b128 v[236:239], v250 offset:11776\n"
"s_waitcnt lgkmcnt(0)\n"
"v_mfma_f32_16x16x32_bf16 v[132:135], v[208:211], v[0:3], 0\n"
"v_mfma_f32_16x16x32_bf16 v[140:143], v[208:211], v[32:35], 0\n"
"v_mfma_f32_16x16x32_bf16 v[148:151], v[208:211], v[64:67], 0\n"
"v_mfma_f32_16x16x32_bf16 v[156:159], v[208:211], v[96:99], 0\n"
"v_mfma_f32_16x16x32_bf16 v[132:135], v[212:215], v[4:7], v[132:135]\n"
"v_mfma_f32_16x16x32_bf16 v[140:143], v[212:215], v[36:39], v[140:143]\n"
"v_mfma_f32_16x16x32_bf16 v[148:151], v[212:215], v[68:71], v[148:151]\n"
"v_mfma_f32_16x16x32_bf16 v[156:159], v[212:215], v[100:103], v[156:159]\n"
"v_mfma_f32_16x16x32_bf16 v[132:135], v[216:219], v[8:11], v[132:135]\n"
"v_mfma_f32_16x16x32_bf16 v[140:143], v[216:219], v[40:43], v[140:143]\n"
"v_mfma_f32_16x16x32_bf16 v[148:151], v[216:219], v[72:75], v[148:151]\n"
"v_mfma_f32_16x16x32_bf16 v[156:159], v[216:219], v[104:107], v[156:159]\n"
"v_mfma_f32_16x16x32_bf16 v[132:135], v[220:223], v[12:15], v[132:135]\n"
"v_mfma_f32_16x16x32_bf16 v[140:143], v[220:223], v[44:47], v[140:143]\n"
"v_mfma_f32_16x16x32_bf16 v[148:151], v[220:223], v[76:79], v[148:151]\n"
"v_mfma_f32_16x16x32_bf16 v[156:159], v[220:223], v[108:111], v[156:159]\n"
"v_mfma_f32_16x16x32_bf16 v[132:135], v[224:227], v[16:19], v[132:135]\n"
"v_mfma_f32_16x16x32_bf16 v[140:143], v[224:227], v[48:51], v[140:143]\n"
"v_mfma_f32_16x16x32_bf16 v[148:151], v[224:227], v[80:83], v[148:151]\n"
"v_mfma_f32_16x16x32_bf16 v[156:159], v[224:227], v[112:115], v[156:159]\n"
"v_mfma_f32_16x16x32_bf16 v[132:135], v[228:231], v[20:23], v[132:135]\n"
"v_mfma_f32_16x16x32_bf16 v[140:143], v[228:231], v[52:55], v[140:143]\n"
"v_mfma_f32_16x16x32_bf16 v[148:151], v[228:231], v[84:87], v[148:151]\n"
"v_mfma_f32_16x16x32_bf16 v[156:159], v[228:231], v[116:119], v[156:159]\n"
"v_mfma_f32_16x16x32_bf16 v[132:135], v[232:235], v[24:27], v[132:135]\n"
"v_mfma_f32_16x16x32_bf16 v[140:143], v[232:235], v[56:59], v[140:143]\n"
"v_mfma_f32_16x16x32_bf16 v[148:151], v[232:235], v[88:91], v[148:151]\n"
"v_mfma_f32_16x16x32_bf16 v[156:159], v[232:235], v[120:123], v[156:159]\n"
"v_mfma_f32_16x16x32_bf16 v[132:135], v[236:239], v[28:31], v[132:135]\n"
"v_mfma_f32_16x16x32_bf16 v[140:143], v[236:239], v[60:63], v[140:143]\n"
"v_mfma_f32_16x16x32_bf16 v[148:151], v[236:239], v[92:95], v[148:151]\n"
"v_mfma_f32_16x16x32_bf16 v[156:159], v[236:239], v[124:127], v[156:159]\n"
"v_max3_f32 v240, v240, v128, v129\n"
"v_max3_f32 v240, v240, v130, v131\n"
"s_nop 2\n"
"v_max3_f32 v240, v240, v132, v133\n"
"v_max3_f32 v240, v240, v134, v135\n"
"v_max3_f32 v241, v241, v136, v137\n"
"v_max3_f32 v241, v241, v138, v139\n"
"v_max3_f32 v241, v241, v140, v141\n"
"v_max3_f32 v241, v241, v142, v143\n"
"v_max3_f32 v242, v242, v144, v145\n"
"v_max3_f32 v242, v242, v146, v147\n"
"v_max3_f32 v242, v242, v148, v149\n"
"v_max3_f32 v242, v242, v150, v151\n"
"v_max3_f32 v243, v243, v152, v153\n"
"v_max3_f32 v243, v243, v154, v155\n"
"v_max3_f32 v243, v243, v156, v157\n"
"v_max3_f32 v243, v243, v158, v159\n"
"s_add_u32 s62, s62, 16384\n"
"s_cmp_lt_u32 s62, 524288\n"
"s_cbranch_scc1 FBL_%=\n"
"v_mov_b32 v252, v240\n"
"v_mov_b32 v253, v241\n"
"v_mov_b32 v254, v242\n"
"v_mov_b32 v255, v243\n"
"v_permlane16_swap_b32 v252, v240\n"
"v_permlane16_swap_b32 v253, v241\n"
"v_permlane16_swap_b32 v254, v242\n"
"v_permlane16_swap_b32 v255, v243\n"
"v_max_f32 v240, v240, v252\n"
"v_max_f32 v241, v241, v253\n"
"v_max_f32 v242, v242, v254\n"
"v_max_f32 v243, v243, v255\n"
"v_mov_b32 v252, v240\n"
"v_mov_b32 v253, v241\n"
"v_mov_b32 v254, v242\n"
"v_mov_b32 v255, v243\n"
"v_permlane32_swap_b32 v252, v240\n"
"v_permlane32_swap_b32 v253, v241\n"
"v_permlane32_swap_b32 v254, v242\n"
"v_permlane32_swap_b32 v255, v243\n"
"v_max_f32 v240, v240, v252\n"
"v_max_f32 v241, v241, v253\n"
"v_max_f32 v242, v242, v254\n"
"v_max_f32 v243, v243, v255\n"
"v_xor_b32 v192, 0x80000000, v240\n"
"v_xor_b32 v193, 0x80000000, v240\n"
"v_xor_b32 v194, 0x80000000, v240\n"
"v_xor_b32 v195, 0x80000000, v240\n"
"v_xor_b32 v196, 0x80000000, v241\n"
"v_xor_b32 v197, 0x80000000, v241\n"
"v_xor_b32 v198, 0x80000000, v241\n"
"v_xor_b32 v199, 0x80000000, v241\n"
"v_xor_b32 v200, 0x80000000, v242\n"
"v_xor_b32 v201, 0x80000000, v242\n"
"v_xor_b32 v202, 0x80000000, v242\n"
"v_xor_b32 v203, 0x80000000, v242\n"
"v_xor_b32 v204, 0x80000000, v243\n"
"v_xor_b32 v205, 0x80000000, v243\n"
"v_xor_b32 v206, 0x80000000, v243\n"
"v_xor_b32 v207, 0x80000000, v243\n"
"s_mov_b64 s[68:69], -1\n"
"s_add_u32 s62, s73, 81920\n"
"s_and_b32 s62, s62, 524287\n"
"s_add_u32 s34, s73, 65536\n"
"s_and_b32 s34, s34, 524287\n"
"s_mov_b64 s[44:45], 0\n"
"s_mov_b32 s71, 0\n"
"s_mov_b32 s48, 0\n"
"s_mov_b64 s[50:51], 0\n"
"s_mov_b32 s43, 0\n"
"s_mov_b32 s63, 0\n"
"s_mov_b32 s64, 131072\n"
"v_mov_b32 v244, v250\n"
"s_barrier\n"
"s_add_u32 s66, s73, 0\n"
"s_and_b32 s66, s66, 524287\n"
"s_add_u32 m0, s52, 0\n"
"s_nop 0\n"
"buffer_load_dwordx4 v247, s[8:11], s66 offen nt lds\n"
"s_add_u32 m0, s53, 0\n"
"s_nop 0\n"
"buffer_load_dwordx4 v247, s[8:11], s66 offen offset:128 nt lds\n"
"s_add_u32 m0, s54, 0\n"
"s_nop 0\n"
"buffer_load_dwordx4 v247, s[8:11], s66 offen offset:256 nt lds\n"
"s_add_u32 m0, s55, 0\n"
"s_nop 0\n"
"buffer_load_dwordx4 v247, s[8:11], s66 offen offset:384 nt lds\n"
"s_add_u32 s66, s73, 16384\n"
"s_and_b32 s66, s66, 524287\n"
"s_add_u32 m0, s52, 32768\n"
"s_nop 0\n"
"buffer_load_dwordx4 v247, s[8:11], s66 offen nt lds\n"
"s_add_u32 m0, s53, 32768\n"
"s_nop 0\n"
"buffer_load_dwordx4 v247, s[8:11], s66 offen offset:128 nt lds\n"
"s_add_u32 m0, s54, 32768\n"
"s_nop 0\n"
"buffer_load_dwordx4 v247, s[8:11], s66 offen offset:256 nt lds\n"
"s_add_u32 m0, s55, 32768\n"
"s_nop 0\n"
"buffer_load_dwordx4 v247, s[8:11], s66 offen offset:384 nt lds\n"
"s_add_u32 s66, s73, 0\n"
"s_and_b32 s66, s66, 524287\n"
"s_add_u32 m0, s56, 32768\n"
"s_nop 0\n"
"buffer_load_dwordx4 v248, s[12:15], s66 offen lds\n"
"s_add_u32 m0, s57, 32768\n"
"s_nop 0\n"
"buffer_load_dwordx4 v248, s[12:15], s66 offen offset:128 lds\n"
"s_add_u32 m0, s58, 32768\n"
"s_nop 0\n"
"buffer_load_dwordx4 v248, s[12:15], s66 offen offset:256 lds\n"
"s_add_u32 m0, s59, 32768\n"
"s_nop 0\n"
"buffer_load_dwordx4 v248, s[12:15], s66 offen offset:384 lds\n"
"s_add_u32 s66, s73, 32768\n"
"s_and_b32 s66, s66, 524287\n"
"s_add_u32 m0, s52, 65536\n"
"s_nop 0\n"
"buffer_load_dwordx4 v247, s[8:11], s66 offen nt lds\n"
"s_add_u32 m0, s53, 65536\n"
"s_nop 0\n"
"buffer_load_dwordx4 v247, s[8:11], s66 offen offset:128 nt lds\n"
"s_add_u32 m0, s54, 65536\n"
"s_nop 0\n"
"buffer_load_dwordx4 v247, s[8:11], s66 offen offset:256 nt lds\n"
"s_add_u32 m0, s55, 65536\n"
"s_nop 0\n"
"buffer_load_dwordx4 v247, s[8:11], s66 offen offset:384 nt lds\n"
"s_add_u32 s66, s73, 16384\n"
"s_and_b32 s66, s66, 524287\n"
"s_add_u32 m0, s56, 65536\n"
"s_nop 0\n"
"buffer_load_dwordx4 v248, s[12:15], s66 offen lds\n"
"s_add_u32 m0, s57, 65536\n"
"s_nop 0\n"
"buffer_load_dwordx4 v248, s[12:15], s66 offen offset:128 lds\n"
"s_add_u32 m0, s58, 65536\n"
"s_nop 0\n"
"buffer_load_dwordx4 v248, s[12:15], s66 offen offset:256 lds\n"
"s_add_u32 m0, s59, 65536\n"
"s_nop 0\n"
"buffer_load_dwordx4 v248, s[12:15], s66 offen offset:384 lds\n"
"s_add_u32 s66, s73, 49152\n"
"s_and_b32 s66, s66, 524287\n"
"s_add_u32 m0, s52, 98304\n"
"s_nop 0\n"
"buffer_load_dwordx4 v247, s[8:11], s66 offen nt lds\n"
"s_add_u32 m0, s53, 98304\n"
"s_nop 0\n"
"buffer_load_dwordx4 v247, s[8:11], s66 offen offset:128 nt lds\n"
"s_add_u32 m0, s54, 98304\n"
"s_nop 0\n"
"buffer_load_dwordx4 v247, s[8:11], s66 offen offset:256 nt lds\n"
"s_add_u32 m0, s55, 98304\n"
"s_nop 0\n"
"buffer_load_dwordx4 v247, s[8:11], s66 offen offset:384 nt lds\n"
"s_add_u32 s66, s73, 32768\n"
"s_and_b32 s66, s66, 524287\n"
"s_add_u32 m0, s56, 98304\n"
"s_nop 0\n"
"buffer_load_dwordx4 v248, s[12:15], s66 offen lds\n"
"s_add_u32 m0, s57, 98304\n"
"s_nop 0\n"
"buffer_load_dwordx4 v248, s[12:15], s66 offen offset:128 lds\n"
"s_add_u32 m0, s58, 98304\n"
"s_nop 0\n"
"buffer_load_dwordx4 v248, s[12:15], s66 offen offset:256 lds\n"
"s_add_u32 m0, s59, 98304\n"
"s_nop 0\n"
"buffer_load_dwordx4 v248, s[12:15], s66 offen offset:384 lds\n"
"s_add_u32 s66, s73, 65536\n"
"s_and_b32 s66, s66, 524287\n"
"s_add_u32 m0, s52, 131072\n"
"s_nop 0\n"
"buffer_load_dwordx4 v247, s[8:11], s66 offen nt lds\n"
"s_add_u32 m0, s53, 131072\n"
"s_nop 0\n"
"buffer_load_dwordx4 v247, s[8:11], s66 offen offset:128 nt lds\n"
"s_add_u32 m0, s54, 131072\n"
"s_nop 0\n"
"buffer_load_dwordx4 v247, s[8:11], s66 offen offset:256 nt lds\n"
"s_add_u32 m0, s55, 131072\n"
"s_nop 0\n"
"buffer_load_dwordx4 v247, s[8:11], s66 offen offset:384 nt lds\n"
"s_waitcnt vmcnt(0)\n"
"s_barrier\n"
"ds_read_b128 v[208:211], v244 offset:0\n"
"ds_read_b128 v[212:215], v244 offset:512\n"
"ds_read_b128 v[216:219], v244 offset:1024\n"
"ds_read_b128 v[220:223], v244 offset:1536\n"
"s_waitcnt lgkmcnt(0)\n"
"s_branch A0_%=\n"
  : "+v"(tid) : "s"(__builtin_amdgcn_kernarg_segment_ptr()), "s"(blockIdx.x), "s"((unsigned)(size_t)lds)
  : "v1","v2","v3","v4","v5","v6","v7","v8","v9","v10","v11","v12","v13","v14","v15","v16","v17","v18","v19","v20","v21","v22","v23","v24","v25","v26","v27","v28","v29","v30","v31","v32",
"v33","v34","v35","v36","v37","v38","v39","v40","v41","v42","v43","v44","v45","v46","v47","v48","v49","v50","v51","v52","v53","v54","v55","v56","v57","v58","v59","v60","v61","v62","v63","v64",
"v65","v66","v67","v68","v69","v70","v71","v72","v73","v74","v75","v76","v77","v78","v79","v80","v81","v82","v83","v84","v85","v86","v87","v88","v89","v90","v91","v92","v93","v94","v95","v96",
"v97","v98","v99","v100","v101","v102","v103","v104","v105","v106","v107","v108","v109","v110","v111","v112","v113","v114","v115","v116","v117","v118","v119","v120","v121","v122","v123","v124","v125","v126","v127","v128",
"v129","v130","v131","v132","v133","v134","v135","v136","v137","v138","v139","v140","v141","v142","v143","v144","v145","v146","v147","v148","v149","v150","v151","v152","v153","v154","v155","v156","v157","v158","v159","v160",
"v161","v162","v163","v164","v165","v166","v167","v168","v169","v170","v171","v172","v173","v174","v175","v176","v177","v178","v179","v180","v181","v182","v183","v184","v185","v186","v187","v188","v189","v190","v191","v192",
"v193","v194","v195","v196","v197","v198","v199","v200","v201","v202","v203","v204","v205","v206","v207","v208","v209","v210","v211","v212","v213","v214","v215","v216","v217","v218","v219","v220","v221","v222","v223","v224",
"v225","v226","v227","v228","v229","v230","v231","v232","v233","v234","v235","v236","v237","v238","v239","v240","v241","v242","v243","v244","v245","v246","v247","v248","v249","v250","v251","v252","v253","v254","v255","a0",
"a1","a2","a3","a4","a5","a6","a7","a8","a9","a10","a11","a12","a13","a14","a15","a16","a17","a18","a19","a20","a21","a22","a23","a24","a25","a26","a27","a28","a29","a30","a31","a32",
"a33","a34","a35","a36","a37","a38","a39","a40","a41","a42","a43","a44","a45","a46","a47","a48","a49","a50","a51","a52","a53","a54","a55","a56","a57","a58","a59","a60","a61","a62","a63","a64",
"a65","a66","a67","a68","a69","a70","a71","a72","a73","a74","a75","a76","a77","a78","a79","a80","a81","a82","a83","a84","a85","a86","a87","a88","a89","a90","a91","a92","a93","a94","a95","a96",
"a97","a98","a99","a100","a101","a102","a103","a104","a105","a106","a107","a108","a109","a110","a111","a112","a113","a114","a115","a116","a117","a118","a119","a120","a121","a122","a123","a124","a125","a126","a127","a128",
"a129","a130","a131","a132","a133","a134","a135","a136","a137","a138","a139","a140","a141","a142","a143","a144","a145","a146","a147","a148","a149","a150","a151","a152","a153","a154","a155","a156","a157","a158","a159","a160",
"a161","a162","a163","a164","a165","a166","a167","a168","a169","a170","a171","a172","a173","a174","a175","a176","a177","a178","a179","a180","a181","a182","a183","a184","a185","a186","a187","a188","a189","a190","a191","a192",
"a193","a194","a195","a196","a197","a198","a199","a200","a201","a202","a203","a204","a205","a206","a207","a208","a209","a210","a211","a212","a213","a214","a215","a216","a217","a218","a219","a220","a221","a222","a223","a224",
"a225","a226","a227","a228","a229","a230","a231","a232","a233","a234","a235","a236","a237","a238","a239","a240","a241","a242","a243","a244","a245","a246","a247","a248","a249","a250","a251","a252","a253","a254","a255","s4",
"s5","s6","s7","s8","s9","s10","s11","s12","s13","s14","s15","s16","s17","s18","s19","s20","s21","s22","s23","s24","s25","s26","s27","s28","s29","s30","s31","s33","s34","s35","s36","s37",
"s38","s39","s40","s41","s42","s43","s44","s45","s46","s47","s48","s49","s50","s51","s52","s53","s54","s55","s56","s57","s58","s59","s60","s61","s62","s63","s64","s65","s66","s67","s68","s69",
"s70","s71","s72","s73","s74","s75","s76","s77","s78","s79","s80","s81","s82","s83","s84","s85","s86","s87","s88","s89","s90","s91","s92","s93","s94","s95","s96","s97","s98","s99","vcc","memory");
}

extern "C" void kernel_launch(void* const* d_in, const int* in_sizes, int n_in, void* d_out, int out_size,
                              void* d_ws, size_t ws_size, hipStream_t stream) {
  hipLaunchKernelGGL(concat_heads__gqa_block_cached__gqa_block_cached__near_ridge_kernel, dim3(256), dim3(256), 0, stream, d_in[0], d_in[3], d_in[5], d_in[8],
                     d_in[1], d_in[2], d_in[4], d_ws, d_in[6], d_in[7], d_in[9], d_out);
}
```
